```python
import torch

def _softmax(x, axis=-1):
    return torch.softmax(x, dim=axis)
def _gelu(x):
    return torch.nn.functional.gelu(x, approximate="tanh")
def _silu(x):
    return torch.nn.functional.silu(x)
def _sigmoid(x):
    return torch.sigmoid(x)
def _layernorm(x, axis=-1, eps=1e-5):
    m = x.mean(dim=axis, keepdim=True)
    v = x.var(dim=axis, keepdim=True, unbiased=False)
    return (x - m) / torch.sqrt(v + eps)
def _scatter_add(tgt, idx, src, axis=0):
    return tgt.clone().index_add_(axis, idx, src)
def _segment_sum(src, seg, n):
    out = torch.zeros((n,) + src.shape[1:], dtype=src.dtype)
    return out.index_add_(0, seg, src)
def _pad(x, pw):
    flat = tuple(v for pair in reversed(pw) for v in pair)
    return torch.nn.functional.pad(x, flat)

kernel_name = 'sequential_preLN__linear_attn_block__moe_arch_mod__prompt'

def setup_inputs():

    _w = (lambda a: (lambda t: t.to(torch.bfloat16) if t.is_floating_point() else t)(torch.as_tensor(a)))
    def _A_setup():
        B, S, D = 1, 2048, 1024
        return {
            "x": _w((lambda *s: torch.randn(*s, dtype=torch.bfloat16) * 0.05)(B, S, D)),
            "wqkv": _w((lambda *s: torch.randn(*s, dtype=torch.bfloat16) * 0.05)(D, 3 * D)),
            "wo": _w((lambda *s: torch.randn(*s, dtype=torch.bfloat16) * 0.05)(D, D)),
        }
    _A_in = _A_setup()

    def _B_setup():
        B, S, D, F, C = 1, 2048, 1024, 3584, 2
        return {
            "x": _w((lambda *s: torch.randn(*s, dtype=torch.bfloat16) * 0.05)(B, S, D)),
            "w_r": _w((lambda *s: torch.randn(*s, dtype=torch.bfloat16) * 0.05)(D)),
            "w_gate": _w((lambda *s: torch.randn(*s, dtype=torch.bfloat16) * 0.05)(D, F)),
            "w_up": _w((lambda *s: torch.randn(*s, dtype=torch.bfloat16) * 0.05)(D, F)),
            "w_down": _w((lambda *s: torch.randn(*s, dtype=torch.bfloat16) * 0.05)(F, D)),
        }
    _B_in = _B_setup()

    return {
        "A_x": _A_in["x"],
        "A_wqkv": _A_in["wqkv"],
        "A_wo": _A_in["wo"],
        "B_x": _B_in["x"],
        "B_w_r": _B_in["w_r"],
        "B_w_gate": _B_in["w_gate"],
        "B_w_up": _B_in["w_up"],
        "B_w_down": _B_in["w_down"],
    }

def reference(A_x, A_wqkv, A_wo, B_x, B_w_r, B_w_gate, B_w_up, B_w_down):
    def _A_ref(x, wqkv, wo):
        B, S, D = x.shape
        H = 32; Dh = D // H
        qkv = (lambda x, *d: x.permute(*d))((x @ wqkv).reshape(B, S, 3, H, Dh), 2, 0, 3, 1, 4)
        q, k, v = qkv[0], qkv[1], qkv[2]
        phi = lambda t: torch.where(t > 0, t + 1, torch.exp(t)) + 1e-6
        pq, pk = phi(q), phi(k)
        kv = torch.einsum("bhsd,bhse->bhde", pk, v)
        z = torch.einsum("bhsd->bhd", pk)
        num = torch.einsum("bhqd,bhde->bhqe", pq, kv)
        den = torch.einsum("bhqd,bhd->bhq", pq, z)[..., None] + 1e-6
        o = num / den
        return (lambda x, *d: x.permute(*d))(o, 0, 2, 1, 3).reshape(B, S, D) @ wo

    def _B_ref(x, w_r, w_gate, w_up, w_down):
        B, S, D = x.shape
        C = 2
        r = x @ w_r
        idx = torch.argsort(-r, axis=-1)[:, :C]
        idx_e = (lambda x, n, axis: torch.repeat_interleave(x, n, dim=axis))(idx.reshape(B, C, 1), D, axis=2)
        x_sel = (lambda x, i, axis: torch.take_along_dim(x, i, dim=axis))(x, idx_e, axis=1)
        g = _silu(x_sel @ w_gate)
        u = x_sel @ w_up
        y = (g * u) @ w_down
        r_sel = _sigmoid((lambda x, i, axis: torch.take_along_dim(x, i, dim=axis))(r, idx, axis=1))
        y = y * r_sel.reshape(B, C, 1)
        b_off = (torch.arange(B) * S).reshape(B, 1)
        flat_idx = (idx + b_off).reshape(B * C)
        zero = (x * 0.0).reshape(B * S, D)
        upd = _scatter_add(zero, flat_idx, y.reshape(B * C, D), axis=0)
        return x + upd.reshape(B, S, D)

    x = A_x
    h = x + _A_ref(_layernorm(x, axis=-1), A_wqkv, A_wo)
    h2 = h + _B_ref(_layernorm(h, axis=-1), B_w_r, B_w_gate, B_w_up, B_w_down)
    return h2
```

```cpp
#include <hip/hip_runtime.h>
#include <stdint.h>

#define LAS __attribute__((address_space(3)))
#define GAS __attribute__((address_space(1)))
#define DI __device__ __forceinline__

constexpr int KROT = 2;
constexpr int XSR = 4;
constexpr int XSL1 = 6;
constexpr int YSL0 = 64, YSL1 = 4;
typedef unsigned short u16;
typedef unsigned long long u64;
typedef __attribute__((ext_vector_type(2))) __bf16 bf16x2;
typedef __attribute__((ext_vector_type(8))) __bf16 bf16x8;
typedef __attribute__((ext_vector_type(4))) short s16x4;
typedef __attribute__((ext_vector_type(2))) unsigned short u16x2;
typedef __attribute__((ext_vector_type(16))) float f32x16;
typedef __attribute__((ext_vector_type(4))) float f32x4;
typedef __attribute__((ext_vector_type(2))) float f32x2;
typedef __attribute__((ext_vector_type(4))) unsigned u32x4;
typedef __attribute__((ext_vector_type(2))) unsigned u32x2;
typedef GAS unsigned gu32;
typedef GAS unsigned long long gu64;

constexpr int S_ = 2048, D_ = 1024, F_ = 3584;

constexpr unsigned SY_CH = 0, SY_GO = 1024, SY_RE = 2048, SY_FSR = 4096, SY_FSC = 8192, SY_FA = 12288, SY_FB = 16384,
                   SY_FC = 20480, SY_FD = 28672, SY_FE = 32768;
constexpr unsigned WS_RS  = 65536;
constexpr unsigned WS_CS  = WS_RS + 16 * 128 * 128;
constexpr unsigned WS_KVP = WS_CS + 16 * 192 * 64;
constexpr unsigned WS_O   = WS_KVP + 256 * 8448;
constexpr unsigned WS_ST2 = WS_O + S_ * D_ * 2;
constexpr unsigned WS_YP  = WS_ST2 + 32 * 8 * 64 * 16;
constexpr unsigned WS_GU  = WS_YP + 224 * 8192;
constexpr unsigned WS_XS  = WS_GU + 256 * 1024;
constexpr unsigned WS_END = WS_XS + 4 * 32 * 16384;
constexpr size_t WS_DIAG  = 64u << 20;
#define STAMPT(kid, i, T) do {} while (0)

DI float bflo(unsigned u) { return __uint_as_float(u << 16); }
DI float bfhi(unsigned u) { return __uint_as_float(u & 0xffff0000u); }
DI float rbf(float x) { return (float)(__bf16)x; }
DI unsigned pack2(float a, float b) { bf16x2 c; c[0] = (__bf16)a; c[1] = (__bf16)b; return __builtin_bit_cast(unsigned, c); }
DI f32x16 mfma32(u32x4 a, u32x4 b, f32x16 c) {
  return __builtin_amdgcn_mfma_f32_32x32x16_bf16(__builtin_bit_cast(bf16x8, a), __builtin_bit_cast(bf16x8, b), c, 0, 0, 0);
}
DI u32x2 trread(const char* p) {
  s16x4 v = __builtin_amdgcn_ds_read_tr16_b64_v4i16((LAS s16x4*)p);
  return __builtin_bit_cast(u32x2, v);
}
DI const char* uni(const void* p) {
  const u64 v = (u64)p;
  return (const char*)(((u64)(unsigned)__builtin_amdgcn_readfirstlane((int)(v >> 32)) << 32) | (unsigned)__builtin_amdgcn_readfirstlane((int)v));
}
#define RSV "a119", "a120", "a121", "a122", "a123", "a124", "a125", "a126", "a127"
DI void glds16(unsigned voff, const void* sbase, unsigned lds) {
  asm volatile("s_mov_b32 m0, %2\n\ts_nop 0\n\tglobal_load_lds_dwordx4 %0, %1" ::"v"(voff), "s"(uni(sbase)), "s"(__builtin_amdgcn_readfirstlane(lds)) : "memory");
}
DI void glds16r(unsigned voff, const void* sbase, unsigned lds) {
  asm volatile("s_mov_b32 m0, %2\n\ts_nop 0\n\tglobal_load_lds_dwordx4 %0, %1" ::"v"(voff), "s"(uni(sbase)), "s"(__builtin_amdgcn_readfirstlane(lds)) : "memory", RSV);
}
DI void glds16_sc1(unsigned voff, const void* sbase, unsigned lds) {
  asm volatile("s_mov_b32 m0, %2\n\ts_nop 0\n\tglobal_load_lds_dwordx4 %0, %1 sc1" ::"v"(voff), "s"(uni(sbase)), "s"(__builtin_amdgcn_readfirstlane(lds)) : "memory");
}
DI float xh_sum(float v) {
  auto r = __builtin_amdgcn_permlane32_swap(__float_as_uint(v), __float_as_uint(v), false, false);
  return __uint_as_float(r[0]) + __uint_as_float(r[1]);
}
template <int CTRL> DI unsigned dppu(unsigned v) { return (unsigned)__builtin_amdgcn_update_dpp(0, (int)v, CTRL, 0xf, 0xf, true); }
template <int CTRL> DI float dppf(float v) { return __uint_as_float(dppu<CTRL>(__float_as_uint(v))); }
constexpr int DPP_X1 = 0xB1, DPP_X2 = 0x4E, DPP_HMIR = 0x141, DPP_MIR = 0x140;
DI float quad_sum(float v) { v += dppf<DPP_X1>(v); v += dppf<DPP_X2>(v); return v; }
DI float x16_sum(float v) {
  auto r = __builtin_amdgcn_permlane16_swap(__float_as_uint(v), __float_as_uint(v), false, false);
  return __uint_as_float(r[0]) + __uint_as_float(r[1]);
}
DI float wave_sum(float v) {
  v = quad_sum(v); v += dppf<DPP_HMIR>(v); v += dppf<DPP_MIR>(v);
  return xh_sum(x16_sum(v));
}
DI unsigned phi_pair(f32x2 t, u16x2& tmax) {
  const unsigned tb = pack2(t[0], t[1]);
  tmax = __builtin_elementwise_max(tmax, __builtin_bit_cast(u16x2, tb));
  const f32x2 tr = {bflo(tb), bfhi(tb)};
  const f32x2 u = tr + 1.0f;
  const f32x2 l = tr * 1.44269504088896340736f;
  const float e0 = __builtin_amdgcn_exp2f(l[0]), e1 = __builtin_amdgcn_exp2f(l[1]);
  return pack2(tr[0] > 0.f ? u[0] : e0, tr[1] > 0.f ? u[1] : e1);
}
#define RLX_AGENT __ATOMIC_RELAXED, __HIP_MEMORY_SCOPE_AGENT
DI void st32(char* ws, unsigned off, unsigned v) { __hip_atomic_store((gu32*)(ws + off), v, RLX_AGENT); }
DI void st64(char* ws, unsigned off, u64 v) { __hip_atomic_store((gu64*)(ws + off), v, RLX_AGENT); }
DI unsigned ld32(char* ws, unsigned off) { return __hip_atomic_load((gu32*)(ws + off), RLX_AGENT); }
DI u64 ld64(char* ws, unsigned off) { return __hip_atomic_load((gu64*)(ws + off), RLX_AGENT); }
#define WAIT_V(n) asm volatile("s_waitcnt vmcnt(" #n ")" ::: "memory")
#define WAIT_L0() asm volatile("s_waitcnt lgkmcnt(0)" ::: "memory")
#define RAW_BAR() do { WAIT_L0(); __builtin_amdgcn_s_barrier(); asm volatile("" ::: "memory"); } while (0)
#define SB0() __builtin_amdgcn_sched_barrier(0)
#define WAIT_VR(n) asm volatile("s_waitcnt vmcnt(" #n ")" ::: "memory", RSV)
#define RAW_BARR() do { asm volatile("s_waitcnt lgkmcnt(0)" ::: "memory", RSV); __builtin_amdgcn_s_barrier(); asm volatile("" ::: "memory", RSV); } while (0)
#define SBR() do { asm volatile("" ::: RSV); __builtin_amdgcn_sched_barrier(0); } while (0)
constexpr unsigned SPIN_CAP = 1u << 22;
DI void poll_flags(char* ws, unsigned off, bool act) {
  for (unsigned s = 0; s < SPIN_CAP; ++s) {
    const unsigned v = act ? ld32(ws, off) : 1u;
    if (__all(v != 0u)) break;
  }
}
DI unsigned mk_key(float v, unsigned idx) {
  const unsigned u = __float_as_uint(v) >> 16;
  const unsigned o = (u & 0x8000u) ? (~u & 0xffffu) : (u | 0x8000u);
  return (o << 16) | (0xFFFFu - idx);
}
DI unsigned key_idx(unsigned k) { return 0xFFFFu - (k & 0xFFFFu); }
DI void top2_mix(unsigned& b0, unsigned& b1, unsigned c0, unsigned c1) {
  const unsigned lo = min(b0, c0), mx = max(b1, c1);
  b0 = max(b0, c0); b1 = max(lo, mx);
}
DI void top2_wave(unsigned& b0, unsigned& b1) {
  top2_mix(b0, b1, dppu<DPP_X1>(b0), dppu<DPP_X1>(b1));
  top2_mix(b0, b1, dppu<DPP_X2>(b0), dppu<DPP_X2>(b1));
  top2_mix(b0, b1, dppu<DPP_HMIR>(b0), dppu<DPP_HMIR>(b1));
  top2_mix(b0, b1, dppu<DPP_MIR>(b0), dppu<DPP_MIR>(b1));
  { auto p = __builtin_amdgcn_permlane16_swap(b0, b0, false, false); auto q = __builtin_amdgcn_permlane16_swap(b1, b1, false, false);
    b0 = p[0]; b1 = q[0]; top2_mix(b0, b1, p[1], q[1]); }
  { auto p = __builtin_amdgcn_permlane32_swap(b0, b0, false, false); auto q = __builtin_amdgcn_permlane32_swap(b1, b1, false, false);
    b0 = p[0]; b1 = q[0]; top2_mix(b0, b1, p[1], q[1]); }
}

constexpr int K1_STAGE = 40960;
constexpr int LDS_TOTAL = 4 * K1_STAGE;
constexpr int K2_STAGE = 24576;
constexpr int L_KVL = 147456, L_ZL = L_KVL + 4096, L_MISC = L_ZL + 256;

__global__ __launch_bounds__(512, 2) void fused(const u16* __restrict__ x, const u16* __restrict__ wqkv,
                                                const u16* __restrict__ wo, const u16* __restrict__ w_r,
                                                const u16* __restrict__ wg, const u16* __restrict__ wu,
                                                const u16* __restrict__ wd, u16* __restrict__ out, char* __restrict__ ws) {
  extern __shared__ __attribute__((aligned(16))) char smem[];
  STAMPT(0, 0, 0);
  { float dz = 0.f; asm volatile("" : "+a"(dz)); }
  int tid = threadIdx.x, lane = tid & 63;
  const int wave = __builtin_amdgcn_readfirstlane(tid >> 6);
  const int g = wave >> 2, wr = (wave >> 1) & 1, wc = wave & 1;
  const int bid = blockIdx.x, xcd = bid & 7, idx = bid >> 3;
  const int rb = (xcd & 1) * 8 + (idx & 7);
  const int hp = (xcd >> 1) * 4 + (idx >> 3);
  const int rp = xcd * 4 + (idx >> 3), cb = idx & 7;
  int r = lane & 31, h = lane >> 5;
  const unsigned lds0 = (unsigned)(size_t)smem;
  const __amdgpu_buffer_rsrc_t wsr = __builtin_amdgcn_make_buffer_rsrc(ws, 0, WS_END, 0x00020000);
#define LDW(off) __builtin_amdgcn_raw_buffer_load_b128(wsr, (off), 0, 16)
#define STW(v, off) __builtin_amdgcn_raw_buffer_store_b128((v), wsr, (off), 0, 16)

  unsigned hs_cur = 0, hs_g0 = 0; int hs_state = 0;
  u32x4 ms_exp = {0, 0, 0, 0}; bool ms_done = false;
  const unsigned hs_off = (lane == 1 ? SY_GO : SY_CH) + bid * 4;
  const unsigned ms_off = lane * 16;
  const char* wsu = uni(ws);
#define HS_PROBE() asm volatile("global_load_dword a119, %0, %1 sc1" :: "v"(hs_off), "s"(wsu) : "memory", RSV)
#define MS_PROBE() asm volatile("global_load_dwordx4 a[124:127], %0, %1 offset:2048 sc1" :: "v"(ms_off), "s"(wsu) : "memory", RSV)
  const bool is_master = (bid == 0) && (wave == 1);
  auto hs_begin = [&]() {
    unsigned zo = SY_FA + hp * 256 + rb * 4;
    if (lane == 1) zo = SY_FB + rb * 256 + hp * 4;
    if (lane == 2) zo = SY_FC + rp * 256 + cb * 4;
    if (lane == 3) zo = SY_FE + bid * 4;
    if (lane >= 4) zo = SY_FD + cb * 512 + rp * 8 + (lane & 1) * 4;
    if (lane < 6) st32(ws, zo, 0u);
    STW(((u32x4){0u, 0u, 0u, 0u}), WS_ST2 + (rp * 8 + cb) * 1024 + lane * 16);
  };
  if (is_master) {
    MS_PROBE();
    asm volatile("global_load_dwordx4 a[120:123], %0, %1 offset:1024 sc1" :: "v"(ms_off), "s"(wsu) : "memory", RSV);
  }
  auto hs_step = [&](bool first, bool mfirst) {
    if (wave == 0 && !mfirst) {
      unsigned hv_;
      asm volatile("v_accvgpr_read_b32 %0, a119" : "=v"(hv_) :: RSV);
      const unsigned c = __builtin_amdgcn_readlane(hv_, 0), go = __builtin_amdgcn_readlane(hv_, 1);
      if (first) {
        hs_cur = c;
        if (lane == 0) st32(ws, SY_RE + bid * 4, c);
      } else if (hs_state == 0) {
        if (go == hs_cur && go != hs_g0) hs_state = 2;
        else if (c != hs_cur) { hs_cur = c; if (lane == 0) st32(ws, SY_RE + bid * 4, c); }
      }
      if (hs_state < 2) HS_PROBE();
    }
    if (is_master && !ms_done) {
      u32x4 re;
      asm volatile("v_accvgpr_read_b32 %0, a124\n\tv_accvgpr_read_b32 %1, a125\n\tv_accvgpr_read_b32 %2, a126\n\tv_accvgpr_read_b32 %3, a127"
                   : "=v"(re[0]), "=v"(re[1]), "=v"(re[2]), "=v"(re[3]) :: RSV);
      if (mfirst) {
        u32x4 g0;
        asm volatile("v_accvgpr_read_b32 %0, a120\n\tv_accvgpr_read_b32 %1, a121\n\tv_accvgpr_read_b32 %2, a122\n\tv_accvgpr_read_b32 %3, a123"
                     : "=v"(g0[0]), "=v"(g0[1]), "=v"(g0[2]), "=v"(g0[3]) :: RSV);
#pragma unroll
        for (int j = 0; j < 4; ++j) { const unsigned c = re[j] + 1u; ms_exp[j] = (c == g0[j]) ? c + 1u : c; }
        STW(ms_exp, SY_CH + ms_off);
      } else if (__all(re[0] == ms_exp[0] && re[1] == ms_exp[1] && re[2] == ms_exp[2] && re[3] == ms_exp[3])) {
        STW(ms_exp, SY_GO + ms_off);
        ms_done = true;
      }
      if (!ms_done) MS_PROBE();
    }
  };

  auto hs_pre = [&]() {
    if (wave == 0) {
      unsigned hv_;
      asm volatile("v_accvgpr_read_b32 %0, a119" : "=v"(hv_) :: RSV);
      hs_g0 = __builtin_amdgcn_readlane(hv_, 1);
      HS_PROBE();
    }
  };
  auto stat_dma = [&]() {
    int t2 = threadIdx.x; asm volatile("" : "+v"(t2));
    const unsigned vo = (t2 & 63) * 16;
#pragma unroll
    for (int i = 0; i < 2; ++i)
      asm volatile("s_mov_b32 m0, %2\n\ts_nop 0\n\tglobal_load_lds_dwordx4 %0, %1 sc1" ::"v"(vo), "s"(uni(ws + WS_RS + rb * 16384 + (wave * 2 + i) * 1024)),
                   "s"(__builtin_amdgcn_readfirstlane(lds0 + 98304 + (wave * 2 + i) * 1024)) : "memory", RSV);
#pragma unroll
    for (int i = 0; i < 2; ++i)
      if (wave * 2 + i < 12)
        asm volatile("s_mov_b32 m0, %2\n\ts_nop 0\n\tglobal_load_lds_dwordx4 %0, %1 sc1" ::"v"(vo), "s"(uni(ws + WS_CS + hp * 12288 + (wave * 2 + i) * 1024)),
                     "s"(__builtin_amdgcn_readfirstlane(lds0 + 114688 + (wave * 2 + i) * 1024)) : "memory", RSV);
  };
  const int sigma = (rb + hp) & 15;
  const int krot = KROT * xcd, sg = (sigma - krot) & 15;
  auto korder = [&](int t) { return ((t < 2 ? (t < sg ? t : t + 1) : t == 2 ? sg : (t - 1 < sg ? t - 1 : t)) + krot) & 15; };
#define RS_SLOT(T) (WS_RS + ((rb * 16 + sigma) * 128 + ((T) >> 2)) * 8)
#define CS_SLOT(T) (WS_CS + ((hp * 16 + sigma) * 192 + wave * 32 + ((T) & 31)) * 4)

  unsigned voA0, voA1, voB;
  {
    const int r0 = 16 * wave + (lane >> 3), r1 = r0 + 8;
    const int c0 = (lane & 7) ^ ((r0 >> 1) & 7), c1 = (lane & 7) ^ ((r1 >> 1) & 7);
    voA0 = (unsigned)(r0 * D_ + c0 * 8) * 2u;
    voA1 = (unsigned)(r1 * D_ + c1 * 8) * 2u;
    const int bi = lane >> 4, q = (lane >> 2) & 3, ch = lane & 3;
    voB = (unsigned)((8 * wave + 4 * (bi >> 1) + q) * (3 * D_) + (bi & 1) * 32 + ch * 8) * 2u;
  }
  const char* baseA = uni(x + (size_t)rb * 128 * D_);
  const char* baseB = uni(wqkv + hp * 64);
  const int ft = wc ^ g;
  int trl = ((lane & 15) >> 2) * 64 + ((lane >> 4) & 1) * 32 + (lane & 3) * 8;
  f32x16 acc[2][3];
  {
    auto stage = [&](int t, int sb) {
      const int kt = korder(t);
      const unsigned l = lds0 + sb;
      const char* pa = baseA + kt * 128;
      const char* pb = baseB + (size_t)kt * (64 * 3 * D_ * 2);
      glds16r(voA0, pa, l + (2 * wave) * 1024);
      glds16r(voA1, pa, l + (2 * wave + 1) * 1024);
      glds16r(voB, pb, l + 16384 + (3 * wave) * 1024);
      glds16r(voB + 2048, pb, l + 16384 + (3 * wave + 1) * 1024);
      glds16r(voB + 4096, pb, l + 16384 + (3 * wave + 2) * 1024);
    };
    const int ka = 4 * g + h, kb = 4 * g + 2 + h;
    const int aF0 = (64 * wr + 32 * ft + r) * 128 + ((ka ^ ((r >> 1) & 7)) * 16);
    const int aF1 = (64 * wr + 32 * ft + r) * 128 + ((kb ^ ((r >> 1) & 7)) * 16);
    const int aS0 = aF0 ^ 4096, aS1 = aF1 ^ 4096;
    const int bo0 = 16384 + wc * 256 + trl + ka * 3072, bo1 = 16384 + wc * 256 + trl + kb * 3072;
#pragma unroll
    for (int i = 0; i < 2; ++i)
#pragma unroll
      for (int j = 0; j < 3; ++j)
#pragma unroll
        for (int k = 0; k < 16; ++k) acc[i][j][k] = 0.f;
    struct Frag { u32x4 a[2][2]; u32x2 bl_[2][3], bh_[2][3]; };
    Frag FA, FB;
#define K1_B(F, kl, t) ((u32x4){F.bl_[kl][t][0], F.bl_[kl][t][1], F.bh_[kl][t][0], F.bh_[kl][t][1]})
    auto body = [&](Frag& C, Frag& N, int sn, int t4, int sb, bool ST) {
      const char* nb = smem + sn;
      const int kt = korder(t4 & 15);
      const unsigned l = lds0 + sb;
      const char* pa = baseA + kt * 128;
      const char* pb = baseB + (size_t)kt * (64 * 3 * D_ * 2);
      acc[0][0] = mfma32(K1_B(C, 0, 0), C.a[0][0], acc[0][0]);
      if (ST) glds16r(voA0, pa, l + (2 * wave) * 1024);
      N.a[0][0] = *(const u32x4*)(nb + aF0); N.bl_[0][0] = trread(nb + bo0);
      SBR();
      acc[0][1] = mfma32(C.a[0][0], K1_B(C, 0, 1), acc[0][1]);
      if (ST) glds16r(voA1, pa, l + (2 * wave + 1) * 1024);
      N.bh_[0][0] = trread(nb + bo0 + 512); N.bl_[0][1] = trread(nb + bo0 + 1024);
      SBR();
      acc[0][2] = mfma32(C.a[0][0], K1_B(C, 0, 2), acc[0][2]);
      if (ST) glds16r(voB, pb, l + 16384 + (3 * wave) * 1024);
      N.bh_[0][1] = trread(nb + bo0 + 1536);
      SBR();
      acc[1][0] = mfma32(K1_B(C, 0, 0), C.a[0][1], acc[1][0]);
      if (ST) glds16r(voB + 2048, pb, l + 16384 + (3 * wave + 1) * 1024);
      N.bl_[0][2] = trread(nb + bo0 + 2048);
      SBR();
      acc[1][1] = mfma32(C.a[0][1], K1_B(C, 0, 1), acc[1][1]);
      if (ST) glds16r(voB + 4096, pb, l + 16384 + (3 * wave + 2) * 1024);
      N.bh_[0][2] = trread(nb + bo0 + 2560);
      SBR();
      acc[1][2] = mfma32(C.a[0][1], K1_B(C, 0, 2), acc[1][2]);
      N.a[0][1] = *(const u32x4*)(nb + aS0); N.a[1][0] = *(const u32x4*)(nb + aF1);
      SBR();
      acc[0][0] = mfma32(K1_B(C, 1, 0), C.a[1][0], acc[0][0]);
      N.bl_[1][0] = trread(nb + bo1); N.bh_[1][0] = trread(nb + bo1 + 512);
      SBR();
      acc[0][1] = mfma32(C.a[1][0], K1_B(C, 1, 1), acc[0][1]);
      N.bl_[1][1] = trread(nb + bo1 + 1024); N.bh_[1][1] = trread(nb + bo1 + 1536);
      SBR();
      acc[0][2] = mfma32(C.a[1][0], K1_B(C, 1, 2), acc[0][2]);
      N.bl_[1][2] = trread(nb + bo1 + 2048); N.bh_[1][2] = trread(nb + bo1 + 2560);
      SBR();
      acc[1][0] = mfma32(K1_B(C, 1, 0), C.a[1][1], acc[1][0]);
      N.a[1][1] = *(const u32x4*)(nb + aS1);
      SBR();
      acc[1][1] = mfma32(C.a[1][1], K1_B(C, 1, 1), acc[1][1]);
      acc[1][2] = mfma32(C.a[1][1], K1_B(C, 1, 2), acc[1][2]);
      SBR();
    };
    u64 st_row = 0; unsigned st_col = 0;
    auto stats = [&](int sn) {
      const char* nb = smem + sn;
      int t2 = threadIdx.x; asm volatile("" : "+v"(t2));
      {
        const int row = t2 >> 2, q = t2 & 3, key = (row >> 1) & 7;
        const u32x4 v0 = *(const u32x4*)(nb + row * 128 + (((2 * q) ^ key) * 16));
        const u32x4 v1 = *(const u32x4*)(nb + row * 128 + (((2 * q + 1) ^ key) * 16));
        float s = 0.f, sq = 0.f;
#pragma unroll
        for (int i = 0; i < 4; ++i) {
          const float a0 = bflo(v0[i]), a1 = bfhi(v0[i]), a2 = bflo(v1[i]), a3 = bfhi(v1[i]);
          s += (a0 + a1) + (a2 + a3);
          sq = __builtin_fmaf(a0, a0, sq); sq = __builtin_fmaf(a1, a1, sq); sq = __builtin_fmaf(a2, a2, sq); sq = __builtin_fmaf(a3, a3, sq);
        }
        s = quad_sum(s); sq = quad_sum(sq);
        st_row = ((u64)__float_as_uint(sq) << 32) | __float_as_uint(s);
      }
      if (wave < 6) {
        const int cw = wave >= 3 ? 1 : 0, ct = wave - 3 * cw;
        const u32x4 ONES4 = {0x3F803F80u, 0x3F803F80u, 0x3F803F80u, 0x3F803F80u};
        f32x16 ca;
#pragma unroll
        for (int k = 0; k < 16; ++k) ca[k] = 0.f;
#pragma unroll
        for (int ks = 0; ks < 4; ++ks) {
          const char* bp = nb + 16384 + (2 * ks + ((t2 >> 5) & 1)) * 3072 + ct * 1024 + cw * 256 + (((t2 & 15) >> 2) * 64 + ((t2 >> 4) & 1) * 32 + (t2 & 3) * 8);
          const u32x2 lo = trread(bp), hi = trread(bp + 512);
          ca = mfma32(ONES4, (u32x4){lo[0], lo[1], hi[0], hi[1]}, ca);
        }
        st_col = __float_as_uint(ca[0]);
      }
    };

    stage(0, 0); stage(1, K1_STAGE); stage(2, 2 * K1_STAGE); stage(3, 3 * K1_STAGE);
    if (wave == 0) HS_PROBE();
    if (wave == 7) {
      u64 pc; asm volatile("s_getpc_b64 %0" : "=s"(pc));
      const char* cp = (const char*)(pc - (pc & 127)) + (idx * 10 + (lane < 10 ? lane : 0)) * 128;
      asm volatile("global_load_dword a122, %0, off" :: "v"(cp) : "memory", RSV);
    }
    WAIT_VR(15); RAW_BARR();
    hs_step(false, true);
    STAMPT(0, 1, 0);
    FA.a[0][0] = *(const u32x4*)(smem + aF0); FA.a[0][1] = *(const u32x4*)(smem + aS0);
    FA.a[1][0] = *(const u32x4*)(smem + aF1); FA.a[1][1] = *(const u32x4*)(smem + aS1);
#pragma unroll
    for (int t = 0; t < 3; ++t) {
      FA.bl_[0][t] = trread(smem + bo0 + t * 1024); FA.bh_[0][t] = trread(smem + bo0 + t * 1024 + 512);
      FA.bl_[1][t] = trread(smem + bo1 + t * 1024); FA.bh_[1][t] = trread(smem + bo1 + t * 1024 + 512);
    }
    for (int t = 0; t < 16; t += 4) {
      const bool st = t < 12;
      WAIT_VR(10);
      RAW_BARR(); if (t) hs_step(t == 4, false);
      body(FA, FB, 1 * K1_STAGE, t + 4, 0 * K1_STAGE, st);
      if (st) WAIT_VR(10); else WAIT_VR(5);
      RAW_BARR();
      body(FB, FA, 2 * K1_STAGE, t + 5, 1 * K1_STAGE, st);
      if (t == 0) {
        stats(2 * K1_STAGE);
        int t2 = threadIdx.x; asm volatile("" : "+v"(t2));
        if ((t2 & 3) == 0) st64(ws, RS_SLOT(t2), st_row);
        if (wave < 6 && (t2 & 63) < 32) st32(ws, CS_SLOT(t2), st_col);
        if (wave == 0) hs_begin();
      }
      if (st) WAIT_VR(10); else WAIT_VR(0);
      RAW_BARR(); if (t) hs_step(false, false); else hs_pre();
      body(FA, FB, 3 * K1_STAGE, t + 6, 2 * K1_STAGE, st);
      if (st) WAIT_VR(10); else WAIT_VR(0);
      RAW_BARR();
      if (!st) stat_dma();
      body(FB, FA, 0 * K1_STAGE, t + 7, 3 * K1_STAGE, st);
    }
  }
  STAMPT(0, 2, 0);
  tid = threadIdx.x; asm volatile("" : "+v"(tid));
  lane = tid & 63; r = lane & 31; h = lane >> 5;
  trl = ((lane & 15) >> 2) * 64 + ((lane >> 4) & 1) * 32 + (lane & 3) * 8;
  float* xch = (float*)smem;
  float* stm = (float*)(smem + 144384);
  float* kvr = (float*)(smem + 98304);
  float* zr = (float*)(smem + 143360);
  volatile int* misc = (volatile int*)(smem + L_MISC);
  const int R0 = 64 * wr + 32 * ft;
  auto put_stats = [&]() {
    if (wave < 2) {
      float s = 0.f, sq = 0.f;
#pragma unroll
      for (int i = 0; i < 16; ++i) { const f32x2 v = *(const f32x2*)(smem + 98304 + i * 1024 + tid * 8); s += v[0]; sq += v[1]; }
      const float mean = s * (1.0f / 1024.0f);
      const float var = fmaxf(sq * (1.0f / 1024.0f) - mean * mean, 0.f);
      stm[tid] = rbf(mean);
      stm[128 + tid] = __builtin_amdgcn_rcpf(rbf(__builtin_amdgcn_sqrtf(rbf(rbf(var) + 1e-5f))));
    } else if (wave < 5) {
      float c = 0.f;
#pragma unroll
      for (int i = 0; i < 16; ++i) c += *(const float*)(smem + 114688 + i * 768 + (tid - 128) * 4);
      stm[256 + tid - 128] = c;
    }
  };
#pragma unroll
  for (int t = 0; t < 3; ++t)
#pragma unroll
    for (int q4 = 0; q4 < 4; ++q4)
      *(f32x4*)(xch + wave * 3072 + (t * 4 + q4) * 256 + lane * 4) =
          (f32x4){acc[1][t][4 * q4], acc[1][t][4 * q4 + 1], acc[1][t][4 * q4 + 2], acc[1][t][4 * q4 + 3]};
  if (tid == 0) misc[0] = hs_state;
  if (is_master && !ms_done) {
    for (unsigned s = 0; s < SPIN_CAP; ++s) {
      bool ok = true;
#pragma unroll
      for (int j = 0; j < 4; ++j) ok &= (ld32(ws, SY_RE + lane * 16 + j * 4) == ms_exp[j]);
      if (__all(ok)) break;
    }
    STW(ms_exp, SY_GO + lane * 16);
  }
  if (wave == 0 && hs_state < 2) {
    if (hs_state == 0) {
      for (unsigned s = 0; s < SPIN_CAP; ++s) {
        const unsigned go = ld32(ws, SY_GO + bid * 4);
        if (go == hs_cur && go != hs_g0) break;
        const unsigned c = ld32(ws, SY_CH + bid * 4);
        if (c != hs_cur) { hs_cur = c; if (lane == 0) st32(ws, SY_RE + bid * 4, c); }
      }
    }
  }
  WAIT_V(0);
  STAMPT(1, 0, 0);
  RAW_BAR();
  if (misc[0] != 2) { stat_dma(); WAIT_V(0); RAW_BAR(); }
  put_stats();
  RAW_BAR();
  STAMPT(1, 1, 0);
  if (wave >= 4) {
    const u32x4 z4 = {0u, 0u, 0u, 0u};
    const int sl_ = (bid & 7) * 28 + (bid >> 3);
    if (bid < 224) { STW(z4, WS_YP + sl_ * 8192 + (tid - 256) * 16); STW(z4, WS_YP + sl_ * 8192 + 4096 + (tid - 256) * 16); }
    if (wave == 4) STW(z4, WS_GU + sl_ * 1024 + lane * 16);
    if (wave >= 6) {
#pragma unroll
      for (int rr_ = 0; rr_ < XSR; ++rr_) STW(z4, WS_XS + rr_ * 524288 + rp * 16384 + (wave - 6) * 8192 + cb * 1024 + lane * 16);
    }
  }
  const float m_b = stm[R0 + r], rinv = stm[128 + R0 + r], csk = stm[256 + wc * 96 + 32 + r], csv = stm[256 + wc * 96 + 64 + r];
  STAMPT(0, 3, 0);
  const int head = 2 * hp + wc;
  const int rowtile = rb * 4 + 2 * wr + ft;
  unsigned qp[8];
  f32x4 tq[4];
  {
    unsigned kp[8], vp[8];
    u16x2 tmin = {0, 0};
    const float* px = xch + (wave ^ 4) * 3072 + lane * 4;
#pragma unroll
    for (int q4 = 0; q4 < 4; ++q4) {
      const f32x4 xq = *(const f32x4*)(px + q4 * 256);
      const f32x4 xk = *(const f32x4*)(px + (4 + q4) * 256);
      const f32x4 xv = *(const f32x4*)(px + (8 + q4) * 256);
      tq[q4] = (f32x4){acc[0][0][4 * q4], acc[0][0][4 * q4 + 1], acc[0][0][4 * q4 + 2], acc[0][0][4 * q4 + 3]} + xq;
      const f32x4 m4 = *(const f32x4*)(stm + R0 + 8 * q4 + 4 * h);
      const f32x4 i4 = *(const f32x4*)(stm + 128 + R0 + 8 * q4 + 4 * h);
      const f32x2 ck2 = {csk, csk}, cv2 = {csv, csv};
      const f32x2 ma = {m4[0], m4[1]}, mb = {m4[2], m4[3]}, ia = {i4[0], i4[1]}, ib = {i4[2], i4[3]};
      f32x2 k0 = (((f32x2){acc[0][1][4 * q4], acc[0][1][4 * q4 + 1]} + (f32x2){xk[0], xk[1]}) - ma * ck2) * ia;
      f32x2 k1 = (((f32x2){acc[0][1][4 * q4 + 2], acc[0][1][4 * q4 + 3]} + (f32x2){xk[2], xk[3]}) - mb * ck2) * ib;
      kp[2 * q4] = phi_pair(k0, tmin); kp[2 * q4 + 1] = phi_pair(k1, tmin);
      f32x2 v0 = (((f32x2){acc[0][2][4 * q4], acc[0][2][4 * q4 + 1]} + (f32x2){xv[0], xv[1]}) - ma * cv2) * ia;
      f32x2 v1 = (((f32x2){acc[0][2][4 * q4 + 2], acc[0][2][4 * q4 + 3]} + (f32x2){xv[2], xv[3]}) - mb * cv2) * ib;
      vp[2 * q4] = pack2(v0[0], v0[1]); vp[2 * q4 + 1] = pack2(v1[0], v1[1]);
    }
    if (__any(tmin[0] > 0xC0F0 || tmin[1] > 0xC0F0)) {
#pragma unroll
      for (int i = 0; i < 8; ++i) kp[i] = pack2(bflo(kp[i]) + 1e-6f, bfhi(kp[i]) + 1e-6f);
    }
    f32x16 kv, zv;
#pragma unroll
    for (int k = 0; k < 16; ++k) { kv[k] = 0.f; zv[k] = 0.f; }
    const u32x4 ONES4 = {0x3F803F80u, 0x3F803F80u, 0x3F803F80u, 0x3F803F80u};
    kv = mfma32((u32x4){kp[0], kp[1], kp[2], kp[3]}, (u32x4){vp[0], vp[1], vp[2], vp[3]}, kv);
    zv = mfma32((u32x4){kp[0], kp[1], kp[2], kp[3]}, ONES4, zv);
    kv = mfma32((u32x4){kp[4], kp[5], kp[6], kp[7]}, (u32x4){vp[4], vp[5], vp[6], vp[7]}, kv);
    zv = mfma32((u32x4){kp[4], kp[5], kp[6], kp[7]}, ONES4, zv);
#pragma unroll
    for (int q4 = 0; q4 < 4; ++q4)
      *(f32x4*)(kvr + wave * 1024 + q4 * 256 + lane * 4) = (f32x4){kv[4 * q4], kv[4 * q4 + 1], kv[4 * q4 + 2], kv[4 * q4 + 3]};
    if (r == 0) {
#pragma unroll
      for (int q4 = 0; q4 < 4; ++q4)
        *(f32x4*)(zr + wave * 32 + 8 * q4 + 4 * h) = (f32x4){zv[4 * q4], zv[4 * q4 + 1], zv[4 * q4 + 2], zv[4 * q4 + 3]};
    }
  }
  STAMPT(1, 2, 0);
  RAW_BAR();
  STAMPT(1, 3, 0);
  const int hc = tid >> 8, f = tid & 255;
  {
    f32x4 s = *(const f32x4*)(kvr + hc * 1024 + f * 4);
#pragma unroll
    for (int p = 1; p < 4; ++p) s += *(const f32x4*)(kvr + (hc + 2 * p) * 1024 + f * 4);
    const unsigned o_ = WS_KVP + ((hp * 16 + rb) * 2 + hc) * 4224;
    STW(__builtin_bit_cast(u32x4, s), o_ + f * 16);
    if (f < 8) {
      f32x4 z = *(const f32x4*)(zr + hc * 32 + f * 4);
#pragma unroll
      for (int p = 1; p < 4; ++p) z += *(const f32x4*)(zr + (hc + 2 * p) * 32 + f * 4);
      STW(__builtin_bit_cast(u32x4, z), o_ + 4096 + f * 16);
    }
  }
  WAIT_V(0);
  STAMPT(1, 4, 0);
  RAW_BAR();
  if (tid == 0) st32(ws, SY_FA + hp * 256 + rb * 4, 1u);
  STAMPT(0, 4, 0);

  const int rt = wave & 1, kh = (wave >> 1) & 1;
  unsigned voW;
  {
    const int bi = lane >> 4, q = (lane >> 2) & 3, ch = lane & 3;
    voW = (unsigned)((4 * (bi >> 1) + q) * D_ + (bi & 1) * 32 + ch * 8) * 2u;
  }
  const unsigned voL = lane * 16;
  const char* baseW = uni(wo + (size_t)((16 * kh) * 32 + 16 * rt) * D_ + cb * 128);
  const char* baseO = uni(ws + WS_O + (size_t)(((rp * 2 + rt) * 32 + 16 * kh) * 2) * 1024);
  auto stageW = [&](int t, int sb) {
    const char* sw = baseW + (size_t)t * (32 * D_ * 2);
    const unsigned dw = lds0 + sb + kh * 8192 + (2 * rt) * 2048;
    glds16(voW, sw, dw);
    glds16(voW + 128, sw, dw + 1024);
    glds16(voW + 8 * D_ * 2, sw, dw + 2048);
    glds16(voW + 8 * D_ * 2 + 128, sw, dw + 3072);
  };
  auto stageO = [&](int t, int sb) {
    const char* sp = baseO + (size_t)t * 2048;
    glds16_sc1(voL, sp, lds0 + sb + 16384 + wave * 2048);
    glds16_sc1(voL + 1024, sp, lds0 + sb + 16384 + wave * 2048 + 1024);
  };
  if (wave < 4) {
#pragma unroll
    for (int t = 0; t < 6; ++t) stageW(t, t * K2_STAGE);
  }

  {
    u16x2 tmin = {0, 0};
    const f32x2 mb2 = {m_b, m_b}, ri2 = {rinv, rinv};
#pragma unroll
    for (int q4 = 0; q4 < 4; ++q4) {
      const f32x4 c4 = *(const f32x4*)(stm + 256 + wc * 96 + 8 * q4 + 4 * h);
      const f32x2 t0 = ((f32x2){tq[q4][0], tq[q4][1]} - mb2 * (f32x2){c4[0], c4[1]}) * ri2;
      const f32x2 t1 = ((f32x2){tq[q4][2], tq[q4][3]} - mb2 * (f32x2){c4[2], c4[3]}) * ri2;
      qp[2 * q4] = phi_pair(t0, tmin); qp[2 * q4 + 1] = phi_pair(t1, tmin);
    }
    if (__any(tmin[0] > 0xC0F0 || tmin[1] > 0xC0F0)) {
#pragma unroll
      for (int i = 0; i < 8; ++i) qp[i] = pack2(bflo(qp[i]) + 1e-6f, bfhi(qp[i]) + 1e-6f);
    }
#pragma unroll
    for (int i = 0; i < 8; ++i) asm volatile("" : "+v"(qp[i]));
  }
  if (wave == 4) poll_flags(ws, SY_FA + hp * 256 + (lane & 15) * 4, true);
  RAW_BAR();
  STAMPT(0, 5, 0);
  {
    u32x4 pv[16];
    const unsigned o_ = WS_KVP + (hp * 16 * 2 + hc) * 4224 + f * 16;
#pragma unroll
    for (int p = 0; p < 16; ++p) pv[p] = LDW(o_ + p * 8448);
    f32x4 zs = {0.f, 0.f, 0.f, 0.f};
    if (f < 8) {
#pragma unroll
      for (int p = 0; p < 16; ++p) zs += __builtin_bit_cast(f32x4, LDW(o_ + 4096 + p * 8448));
    }
    f32x4 s = __builtin_bit_cast(f32x4, pv[0]);
#pragma unroll
    for (int p = 1; p < 16; ++p) s += __builtin_bit_cast(f32x4, pv[p]);
    asm volatile("" : "+v"(s));
    STAMPT(1, 5, 0);
    const int q4 = f >> 6, ln = f & 63;
    *(u32x2*)(smem + L_KVL + ((hc * 2 + (q4 >> 1)) * 64 + ln) * 16 + (q4 & 1) * 8) = (u32x2){pack2(s[0], s[1]), pack2(s[2], s[3])};
    if (f < 8)
      *(u32x2*)(smem + L_ZL + hc * 64 + (f & 1) * 32 + (f >> 1) * 8) = (u32x2){pack2(zs[0], zs[1]), pack2(zs[2], zs[3])};
  }
  RAW_BAR();
  {
    const u32x4 k0 = *(const u32x4*)(smem + L_KVL + (wc * 2) * 1024 + lane * 16);
    const u32x4 k1 = *(const u32x4*)(smem + L_KVL + (wc * 2 + 1) * 1024 + lane * 16);
    const u32x4 z0 = *(const u32x4*)(smem + L_ZL + wc * 64 + h * 32), z1 = *(const u32x4*)(smem + L_ZL + wc * 64 + h * 32 + 16);
    f32x16 X, Dn;
#pragma unroll
    for (int k = 0; k < 16; ++k) { X[k] = 0.f; Dn[k] = 0.f; }
    const u32x4 p0 = {qp[0], qp[1], qp[2], qp[3]}, p1 = {qp[4], qp[5], qp[6], qp[7]};
    X = mfma32(k0, p0, X); Dn = mfma32(z0, p0, Dn);
    X = mfma32(k1, p1, X); Dn = mfma32(z1, p1, Dn);
    const float ri = __builtin_amdgcn_rcpf(rbf(rbf(Dn[0]) + 1e-6f));
    u32x4 o0, o1;
#pragma unroll
    for (int i = 0; i < 4; ++i) {
      o0[i] = pack2(rbf(X[2 * i]) * ri, rbf(X[2 * i + 1]) * ri);
      o1[i] = pack2(rbf(X[8 + 2 * i]) * ri, rbf(X[8 + 2 * i + 1]) * ri);
    }
    const unsigned oo = WS_O + ((rowtile * 32 + head) * 2) * 1024 + lane * 16;
    STAMPT(1, 6, 0);
    STW(o0, oo); STW(o1, oo + 1024);
  }
  WAIT_V(0);
  STAMPT(1, 7, 0);
  RAW_BAR();
  if (tid == 0) st32(ws, SY_FB + rb * 256 + hp * 4, 1u);
  STAMPT(0, 6, 0);

  const bool has_mlp = bid < 224;
  float* xs = (float*)smem;
  float* hs = (float*)(smem + 8192);
  float* red = (float*)(smem + 28672);
  float* act = (float*)(smem + 17408);
  float* sgm = (float*)(smem + 17536);
  float* yh = (float*)(smem + 18432);

  if (wave < 4) {
    poll_flags(ws, SY_FB + (rp >> 1) * 256 + (lane & 15) * 4, wave == 0);
    RAW_BAR();
    STAMPT(0, 7, 0);
    float hv[32];
    const int erow = tid >> 2, eseg = tid & 3;
    const size_t ego = (size_t)(rp * 64 + erow) * D_ + cb * 128 + eseg * 32;
#pragma unroll
    for (int t = 0; t < 6; ++t) stageO(t, t * K2_STAGE);
    f32x16 a2[4];
#pragma unroll
    for (int i = 0; i < 4; ++i)
#pragma unroll
      for (int k = 0; k < 16; ++k) a2[i][k] = 0.f;
    const int woff = kh * 8192 + h * 512 + trl;
    const int poff = 16384 + wave * 2048 + lane * 16;
    struct Frag2 { u32x4 o[2]; u32x2 lo[2][4], hi[2][4]; };
    Frag2 GA, GB;
    auto ld2 = [&](Frag2& N, int sn, int st, int nt) {
      const char* base = smem + sn + woff;
      N.lo[st][nt] = trread(base + ((2 * st) * 2 + (nt >> 1)) * 1024 + (nt & 1) * 256);
      N.hi[st][nt] = trread(base + ((2 * st + 1) * 2 + (nt >> 1)) * 1024 + (nt & 1) * 256);
    };
    auto body2 = [&](Frag2& C, Frag2& N, int t, bool LD) {
      const int sn = ((t + 1) % 6) * K2_STAGE, sb = (t % 6) * K2_STAGE;
      const bool ST = t + 6 < 16;
      const char* sw = baseW + (size_t)(t + 6) * (32 * D_ * 2);
      const char* sp = baseO + (size_t)(t + 6) * 2048;
      const unsigned dw = lds0 + sb + kh * 8192 + (2 * rt) * 2048, dp = lds0 + sb + 16384 + wave * 2048;
#pragma unroll
      for (int st = 0; st < 2; ++st)
#pragma unroll
        for (int nt = 0; nt < 4; ++nt) {
          const int i = st * 4 + nt;
          a2[nt] = mfma32((u32x4){C.lo[st][nt][0], C.lo[st][nt][1], C.hi[st][nt][0], C.hi[st][nt][1]}, C.o[st], a2[nt]);
          if (ST) {
            if (i == 0) glds16(voW, sw, dw);
            if (i == 1) glds16(voW + 128, sw, dw + 1024);
            if (i == 2) glds16(voW + 8 * D_ * 2, sw, dw + 2048);
            if (i == 3) glds16(voW + 8 * D_ * 2 + 128, sw, dw + 3072);
            if (i == 4) glds16_sc1(voL, sp, dp);
            if (i == 5) glds16_sc1(voL + 1024, sp, dp + 1024);
          }
          if (LD) {
            if (nt == 0) N.o[st] = *(const u32x4*)(smem + sn + poff + st * 1024);
            ld2(N, sn, st, nt);
          }
          SB0();
        }
    };
    WAIT_V(10); RAW_BAR();
    GA.o[0] = *(const u32x4*)(smem + poff); GA.o[1] = *(const u32x4*)(smem + poff + 1024);
#pragma unroll
    for (int st = 0; st < 2; ++st)
#pragma unroll
      for (int nt = 0; nt < 4; ++nt) ld2(GA, 0, st, nt);
    WAIT_V(8);  RAW_BAR(); body2(GA, GB, 0, true);
    WAIT_V(12); RAW_BAR(); body2(GB, GA, 1, true);
    WAIT_V(16); RAW_BAR(); body2(GA, GB, 2, true);
    WAIT_V(20); RAW_BAR(); body2(GB, GA, 3, true);
    WAIT_V(24); RAW_BAR(); body2(GA, GB, 4, true);
    WAIT_V(24); RAW_BAR(); body2(GB, GA, 5, true);
    WAIT_V(24); RAW_BAR(); body2(GA, GB, 6, true);
    WAIT_V(24); RAW_BAR(); body2(GB, GA, 7, true);
    WAIT_V(24); RAW_BAR(); body2(GA, GB, 8, true);
    WAIT_V(24); RAW_BAR(); body2(GB, GA, 9, true);
    WAIT_V(24); RAW_BAR(); body2(GA, GB, 10, true);
    WAIT_V(18); RAW_BAR(); body2(GB, GA, 11, true);
    WAIT_V(12); RAW_BAR(); body2(GA, GB, 12, true);
    WAIT_V(6);  RAW_BAR(); body2(GB, GA, 13, true);
    WAIT_V(0);  RAW_BAR(); body2(GA, GB, 14, true);
    u32x4 xv[4], wv[4];
#pragma unroll
    for (int c = 0; c < 4; ++c) { xv[c] = *(const u32x4*)(x + ego + c * 8); wv[c] = *(const u32x4*)(w_r + cb * 128 + eseg * 32 + c * 8); }
    RAW_BAR();             body2(GB, GA, 15, false);
    STAMPT(0, 8, 0);
    float* P = (float*)smem;
    {
      float* pr = P + (kh * 64 + 32 * rt + r) * 132 + 4 * h;
#pragma unroll
      for (int nt = 0; nt < 4; ++nt)
#pragma unroll
        for (int q4 = 0; q4 < 4; ++q4)
          *(f32x4*)(pr + 32 * nt + 8 * q4) = (f32x4){a2[nt][4 * q4], a2[nt][4 * q4 + 1], a2[nt][4 * q4 + 2], a2[nt][4 * q4 + 3]};
    }
    RAW_BAR();
    STAMPT(1, 8, 0);
    {
      const float* p0 = P + erow * 132 + eseg * 32;
      const float* p1 = p0 + 64 * 132;
      float s = 0.f, pw = 0.f, sw_ = 0.f;
#pragma unroll
      for (int c = 0; c < 4; ++c) {
        f32x4 a0 = *(const f32x4*)(p0 + c * 8), a1 = *(const f32x4*)(p0 + c * 8 + 4);
        a0 += *(const f32x4*)(p1 + c * 8); a1 += *(const f32x4*)(p1 + c * 8 + 4);
        u32x4 hb;
        hb[0] = pack2(bflo(xv[c][0]) + rbf(a0[0]), bfhi(xv[c][0]) + rbf(a0[1]));
        hb[1] = pack2(bflo(xv[c][1]) + rbf(a0[2]), bfhi(xv[c][1]) + rbf(a0[3]));
        hb[2] = pack2(bflo(xv[c][2]) + rbf(a1[0]), bfhi(xv[c][2]) + rbf(a1[1]));
        hb[3] = pack2(bflo(xv[c][3]) + rbf(a1[2]), bfhi(xv[c][3]) + rbf(a1[3]));
#pragma unroll
        for (int i = 0; i < 4; ++i) {
          const float h0 = bflo(hb[i]), h1 = bfhi(hb[i]), w0 = bflo(wv[c][i]), w1 = bfhi(wv[c][i]);
          hv[c * 8 + 2 * i] = h0; hv[c * 8 + 2 * i + 1] = h1;
          s += h0 + h1; pw = __builtin_fmaf(h0, w0, pw); pw = __builtin_fmaf(h1, w1, pw); sw_ += w0 + w1;
        }
      }
      const float cm = quad_sum(s) * (1.0f / 128.0f);
      float m2 = 0.f;
#pragma unroll
      for (int i = 0; i < 32; ++i) { const float d = hv[i] - cm; m2 = __builtin_fmaf(d, d, m2); }
      m2 = quad_sum(m2); pw = quad_sum(pw); sw_ = quad_sum(sw_);
      if (eseg == 0) STW(((u32x4){__float_as_uint(cm), __float_as_uint(m2) | 1u, __float_as_uint(pw), __float_as_uint(sw_) | 1u}),
                         WS_ST2 + ((rp * 8 + cb) * 64 + erow) * 16);
    }
    STAMPT(0, 9, 0);
    int lc0, lc1;
    {
      u32x4 pu_[8];
      for (unsigned s_ = 0; s_ < SPIN_CAP; ++s_) {
        unsigned ok = 1u;
#pragma unroll
        for (int j = 0; j < 8; ++j) { pu_[j] = LDW(WS_ST2 + ((rp * 8 + j) * 64 + lane) * 16); ok &= pu_[j][1] & pu_[j][3]; }
        if (__all(ok & 1u)) break;
        asm volatile("" ::: "memory");
      }
      STAMPT(0, 10, 0);
      float cs = 0.f, m2 = 0.f, pw = 0.f, sw_ = 0.f;
#pragma unroll
      for (int j = 0; j < 8; ++j) { const f32x4 pt = __builtin_bit_cast(f32x4, pu_[j]); cs += pt[0]; m2 += pt[1]; pw += pt[2]; sw_ += pt[3]; }
      const float mean = cs * 0.125f;
      float dd = 0.f;
#pragma unroll
      for (int j = 0; j < 8; ++j) { const float d = __uint_as_float(pu_[j][0]) - mean; dd = __builtin_fmaf(d, d, dd); }
      const float var = (m2 + 128.0f * dd) * (1.0f / 1024.0f);
      const float mb = rbf(mean), sd = rbf(__builtin_amdgcn_sqrtf(rbf(rbf(var) + 1e-5f)));
      float sc = rbf((pw - mb * sw_) / sd);
      if (!(sc == sc)) sc = -INFINITY;
      unsigned b0 = mk_key(sc, rp * 64 + lane), b1 = 0;
      top2_wave(b0, b1);
      if (tid == 0) st64(ws, SY_FD + cb * 512 + rp * 8, ((u64)b1 << 32) | b0);
      lc0 = key_idx(b0) & (S_ - 1); lc1 = key_idx(b1) & (S_ - 1);
      STAMPT(1, 11, 0);
    }
    float mb, rs;
    {
      u32x4 pa, pb;
      for (unsigned s_ = 0; s_ < SPIN_CAP; ++s_) {
        pa = LDW(WS_ST2 + ((rp * 8 + 2 * eseg) * 64 + erow) * 16);
        pb = LDW(WS_ST2 + ((rp * 8 + 2 * eseg + 1) * 64 + erow) * 16);
        if (__all(pa[1] & pa[3] & pb[1] & pb[3] & 1u)) break;
        asm volatile("" ::: "memory");
      }
      const float ca = __uint_as_float(pa[0]), cb_ = __uint_as_float(pb[0]);
      const float mean = quad_sum(ca + cb_) * 0.125f;
      const float d0 = ca - mean, d1 = cb_ - mean;
      const float var = quad_sum((__uint_as_float(pa[1]) + __uint_as_float(pb[1])) + 128.0f * (d0 * d0 + d1 * d1)) * (1.0f / 1024.0f);
      mb = rbf(mean);
      rs = 1.0f / rbf(__builtin_amdgcn_sqrtf(rbf(rbf(var) + 1e-5f)));
    }
    unsigned nq[16];
#pragma unroll
    for (int i = 0; i < 16; ++i) {
      const unsigned dq = pack2(hv[2 * i] - mb, hv[2 * i + 1] - mb);
      nq[i] = pack2(bflo(dq) * rs, bfhi(dq) * rs);
    }
    const int grow = rp * 64 + erow;
#pragma unroll
    for (int tk = 0; tk < 2; ++tk) {
      const int sel = tk ? lc1 : lc0;
      if (__any(grow == sel)) {
        char* tb = smem + 69632 + wave * 1024;
        if (grow == sel) {
#pragma unroll
          for (int i = 0; i < 16; ++i)
            *(u32x4*)(tb + eseg * 256 + i * 16) = (u32x4){nq[i] << 16, __float_as_uint(hv[2 * i]) | 1u, nq[i] & 0xffff0000u, __float_as_uint(hv[2 * i + 1]) | 1u};
        }
        __builtin_amdgcn_wave_barrier();
        const u32x4 v4 = *(const u32x4*)(tb + lane * 16);
#pragma unroll
        for (int rr_ = 0; rr_ < XSR; ++rr_) STW(v4, WS_XS + rr_ * 524288 + rp * 16384 + tk * 8192 + cb * 1024 + lane * 16);
        __builtin_amdgcn_wave_barrier();
      }
    }
    if (wave == 0) {
      u64 cc = 0;
      for (unsigned s_ = 0; s_ < SPIN_CAP; ++s_) {
        cc = ld64(ws, SY_FD + cb * 512 + (lane & 31) * 8);
        if (__all(cc != 0ull)) break;
      }
      STAMPT(1, 13, 0);
      unsigned c0 = lane < 32 ? (unsigned)cc : 0u, c1 = lane < 32 ? (unsigned)(cc >> 32) : 0u;
      top2_wave(c0, c1);
      const unsigned w0 = key_idx(c0) & (S_ - 1), w1 = key_idx(c1) & (S_ - 1);
      const unsigned k0 = __builtin_amdgcn_readlane((unsigned)cc, w0 >> 6), k1 = __builtin_amdgcn_readlane((unsigned)cc, w1 >> 6);
      if (lane == 0) { misc[3] = w0; misc[4] = w1; misc[5] = (w0 >> 6) * 2 + (k0 == c0 ? 0 : 1); misc[6] = (w1 >> 6) * 2 + (k1 == c1 ? 0 : 1); }
    }
    RAW_BAR();
    STAMPT(0, 11, 0);
    const int sel0 = misc[3], sel1 = misc[4];
    RAW_BAR();
    if (grow != sel0 && grow != sel1) {
#pragma unroll
      for (int c = 0; c < 4; ++c) {
        u32x4 ob;
#pragma unroll
        for (int i = 0; i < 4; ++i) ob[i] = pack2(hv[8 * c + 2 * i] + bflo(nq[4 * c + i]), hv[8 * c + 2 * i + 1] + bfhi(nq[4 * c + i]));
        __builtin_amdgcn_raw_buffer_store_b128(ob, __builtin_amdgcn_make_buffer_rsrc(out, 0, S_ * D_ * 2, 0x00020000), (unsigned)(ego + c * 8) * 2u, 0, 16);
      }
    }
    STAMPT(0, 12, 0);
    if (has_mlp) { RAW_BAR(); RAW_BAR(); }
  } else {
    const int fs = has_mlp ? (bid & 7) * 28 + (bid >> 3) : (bid & 7) * 28;
    const int mt = tid & 255;
    u32x4 wgr[8], wur[8], wdr[8];
    const int krow = (fs & 3) * 256 + (mt >> 6) * 64 + (lane >> 3);
    const u16* pg = wg + (size_t)krow * F_ + (fs >> 2) * 64 + (lane & 7) * 8;
    const u16* pu = wu + (size_t)krow * F_ + (fs >> 2) * 64 + (lane & 7) * 8;
    const u16* pd = wd + (size_t)(fs * 16 + h * 8) * D_ + ((mt >> 6) * 32 + r) * 8;
    RAW_BAR(); RAW_BAR();
#pragma unroll
    for (int i = 0; i < 8; ++i) {
      asm volatile("global_load_dwordx4 %0, %1, off" : "=a"(wgr[i]) : "v"(pg + (size_t)i * 8 * F_) : "memory");
      asm volatile("global_load_dwordx4 %0, %1, off" : "=a"(wdr[i]) : "v"(pd + (size_t)i * D_) : "memory");
      RAW_BAR();
      asm volatile("global_load_dwordx4 %0, %1, off" : "=a"(wur[i]) : "v"(pu + (size_t)i * 8 * F_) : "memory");
      RAW_BAR();
    }
    RAW_BAR();
    WAIT_V(0);
#pragma unroll
    for (int i = 0; i < 8; ++i) asm volatile("" : "+a"(wgr[i]), "+a"(wur[i]), "+a"(wdr[i]));
    unsigned wr2[8];
    if (wave < 6) {
#pragma unroll
      for (int j = 0; j < 8; ++j) wr2[j] = ((const unsigned*)w_r)[j * 64 + lane];
    }
    RAW_BAR();
    if (wave < 6) {
      const int tk = wave - 4;
      u32x4 q[8];
      const unsigned xo = WS_XS + ((bid >> 3) & (XSR - 1)) * 524288u + (unsigned)misc[5 + tk] * 8192u;
      for (unsigned s_ = 0; s_ < SPIN_CAP; ++s_) {
        unsigned ok = 1u;
#pragma unroll
        for (int j = 0; j < 8; ++j) { q[j] = LDW(xo + j * 1024 + lane * 16); ok &= q[j][1] & q[j][3]; }
        if (__all(ok & 1u)) break;
        asm volatile("" ::: "memory");
        __builtin_amdgcn_s_sleep(XSL1);
      }
      float rr = 0.f;
#pragma unroll
      for (int j = 0; j < 8; ++j) {
        rr = __builtin_fmaf(__uint_as_float(q[j][0]), bflo(wr2[j]), rr); rr = __builtin_fmaf(__uint_as_float(q[j][2]), bfhi(wr2[j]), rr);
        *(u32x2*)(xs + tk * 1024 + j * 128 + lane * 2) = (u32x2){q[j][0], q[j][2]};
        *(u32x2*)(hs + tk * 1024 + j * 128 + lane * 2) = (u32x2){q[j][1] & ~1u, q[j][3] & ~1u};
      }
      rr = rbf(wave_sum(rr));
      if (lane == 0) sgm[tk] = rbf(1.0f / (1.0f + __expf(-rr)));
    }
    RAW_BAR();
    if (has_mlp) {
      {
        f32x2 ag[8], au[8];
#pragma unroll
        for (int c = 0; c < 8; ++c) { ag[c] = (f32x2){0.f, 0.f}; au[c] = (f32x2){0.f, 0.f}; }
#pragma unroll
        for (int i = 0; i < 8; ++i) {
          const f32x2 xx = {xs[krow + 8 * i], xs[1024 + krow + 8 * i]};
#pragma unroll
          for (int c = 0; c < 4; ++c) {
            ag[2 * c] += xx * bflo(wgr[i][c]); ag[2 * c + 1] += xx * bfhi(wgr[i][c]);
            au[2 * c] += xx * bflo(wur[i][c]); au[2 * c + 1] += xx * bfhi(wur[i][c]);
          }
        }
        float v[32];
#pragma unroll
        for (int c = 0; c < 8; ++c) { v[c] = ag[c][0]; v[8 + c] = ag[c][1]; v[16 + c] = au[c][0]; v[24 + c] = au[c][1]; }
#pragma unroll
        for (int i = 0; i < 16; ++i) {
          auto sw = __builtin_amdgcn_permlane32_swap(__float_as_uint(v[i]), __float_as_uint(v[16 + i]), false, false);
          v[i] = __uint_as_float(sw[0]) + __uint_as_float(sw[1]);
        }
#pragma unroll
        for (int i = 0; i < 8; ++i) {
          auto sw = __builtin_amdgcn_permlane16_swap(__float_as_uint(v[i]), __float_as_uint(v[8 + i]), false, false);
          v[i] = __uint_as_float(sw[0]) + __uint_as_float(sw[1]);
        }
        const bool up = (lane & 8) != 0;
#pragma unroll
        for (int i = 0; i < 4; ++i) {
          const float keep = up ? v[4 + i] : v[i], give = up ? v[i] : v[4 + i];
          v[i] = keep + dppf<0x128>(give);
        }
        *(f32x4*)(red + (wave - 4) * 256 + (lane >> 5) * 128 + ((lane >> 4) & 1) * 64 + (lane & 7) * 8 + ((lane >> 3) & 1) * 4) = (f32x4){v[0], v[1], v[2], v[3]};
      }
      STAMPT(1, 14, 256);
      RAW_BAR();
      if (wave == 4) {
        f32x4 s4 = *(const f32x4*)(red + lane * 4);
#pragma unroll
        for (int w2 = 1; w2 < 4; ++w2) s4 += *(const f32x4*)(red + w2 * 256 + lane * 4);
        u32x4 t4 = __builtin_bit_cast(u32x4, s4); t4[1] |= 1u; t4[3] |= 1u;
        STW(t4, WS_GU + fs * 1024 + lane * 16);
        u32x4 q[4];
        for (unsigned s_ = 0; s_ < SPIN_CAP; ++s_) {
          unsigned ok = 1u;
#pragma unroll
          for (int j = 0; j < 4; ++j) { q[j] = LDW(WS_GU + ((fs & ~3) + j) * 1024 + lane * 16); ok &= q[j][1] & q[j][3]; }
          if (__all(ok & 1u)) break;
          asm volatile("" ::: "memory");
        }
        const f32x4 tot = (__builtin_bit_cast(f32x4, q[0]) + __builtin_bit_cast(f32x4, q[1])) + (__builtin_bit_cast(f32x4, q[2]) + __builtin_bit_cast(f32x4, q[3]));
        f32x4 a4;
#pragma unroll
        for (int i = 0; i < 4; ++i) {
          auto sw = __builtin_amdgcn_permlane32_swap(__float_as_uint(tot[i]), __float_as_uint(tot[i]), false, false);
          const float gs = rbf(__uint_as_float(sw[0])), us = rbf(__uint_as_float(sw[1]));
          const float sg = rbf(gs / (1.0f + __expf(-gs)));
          a4[i] = rbf(sg * us);
        }
        if (lane < 32 && ((lane & 15) >> 2) == (fs & 3)) *(f32x4*)(act + (lane >> 4) * 16 + (lane & 3) * 4) = a4;
      }
      RAW_BAR();
      float y0[8], y1[8];
#pragma unroll
      for (int c = 0; c < 8; ++c) { y0[c] = 0.f; y1[c] = 0.f; }
#pragma unroll
      for (int i = 0; i < 8; ++i) {
        const float a0 = act[h * 8 + i], a1 = act[16 + h * 8 + i];
#pragma unroll
        for (int c = 0; c < 4; ++c) {
          const float dl = bflo(wdr[i][c]), dh = bfhi(wdr[i][c]);
          y0[2 * c] += a0 * dl; y0[2 * c + 1] += a0 * dh; y1[2 * c] += a1 * dl; y1[2 * c + 1] += a1 * dh;
        }
      }
      u32x4 ya, yb;
#pragma unroll
      for (int c = 0; c < 8; ++c) {
        auto sw = __builtin_amdgcn_permlane32_swap(__float_as_uint(y0[c]), __float_as_uint(y1[c]), false, false);
        const unsigned v = __float_as_uint(__uint_as_float(sw[0]) + __uint_as_float(sw[1])) | (c & 1);
        if (c < 4) ya[c] = v; else yb[c - 4] = v;
      }
      const unsigned yo = WS_YP + fs * 8192 + h * 4096 + ((mt >> 6) * 32 + r) * 32;
      STW(ya, yo); STW(yb, yo + 16);
      STAMPT(1, 15, 256);
      STAMPT(0, 13, 256);
    }
  }
  if (!has_mlp) {
    const int j = bid - 224, tk = j >> 4, c0 = (j & 15) * 64;
    const int pg = tid >> 4, c4 = tid & 15;
    u32x4 pv[7];
    __builtin_amdgcn_s_sleep(YSL0);
    for (unsigned s_ = 0; s_ < SPIN_CAP; ++s_) {
      unsigned ok = 1u;
#pragma unroll
      for (int p = 0; p < 7; ++p) { pv[p] = LDW(WS_YP + (pg * 7 + p) * 8192 + tk * 4096 + c0 * 4 + c4 * 16); ok &= pv[p][1] & pv[p][3]; }
      if (__all(ok & 1u)) break;
      asm volatile("" ::: "memory");
      __builtin_amdgcn_s_sleep(YSL1);
    }
    STAMPT(0, 13, 0);
    f32x4 s = {0.f, 0.f, 0.f, 0.f};
#pragma unroll
    for (int p = 0; p < 7; ++p) s += __builtin_bit_cast(f32x4, pv[p]);
    float* rd = (float*)(smem + 32768);
    *(f32x4*)(rd + pg * 64 + c4 * 4) = s;
    RAW_BAR();
    if (tid < 64) {
      float y = 0.f;
#pragma unroll
      for (int p = 0; p < 32; ++p) y += rd[p * 64 + tid];
      const float yy = rbf(rbf(y) * sgm[tk]);
      const float t = rbf(xs[tk * 1024 + c0 + tid] + yy);
      out[(size_t)misc[3 + tk] * D_ + c0 + tid] = (u16)(pack2(hs[tk * 1024 + c0 + tid] + t, 0.f) & 0xffffu);
    }
    STAMPT(0, 14, 0);
  }
}

extern "C" void dbg_layout(long long* l) {
  l[0] = -1; l[1] = -1; l[2] = -1; l[3] = -1; l[4] = -1;
}

extern "C" void kernel_launch(void* const* d_in, const int* in_sizes, int n_in, void* d_out, int out_size,
                              void* d_ws, size_t ws_size, hipStream_t stream) {
  (void)hipFuncSetAttribute((const void*)fused, hipFuncAttributeMaxDynamicSharedMemorySize, LDS_TOTAL);
  fused<<<256, 512, LDS_TOTAL, stream>>>((const u16*)d_in[0], (const u16*)d_in[1], (const u16*)d_in[2], (const u16*)d_in[4],
                                         (const u16*)d_in[5], (const u16*)d_in[6], (const u16*)d_in[7], (u16*)d_out, (char*)d_ws);
}
```

```cpp
#include <hip/hip_runtime.h>
#include <stdint.h>

#define LAS __attribute__((address_space(3)))
#define GAS __attribute__((address_space(1)))
#define DI __device__ __forceinline__

typedef unsigned short u16;
typedef unsigned long long u64;
typedef __attribute__((ext_vector_type(2))) __bf16 bf16x2;
typedef __attribute__((ext_vector_type(8))) __bf16 bf16x8;
typedef __attribute__((ext_vector_type(4))) short s16x4;
typedef __attribute__((ext_vector_type(2))) unsigned short u16x2;
typedef __attribute__((ext_vector_type(16))) float f32x16;
typedef __attribute__((ext_vector_type(4))) float f32x4;
typedef __attribute__((ext_vector_type(2))) float f32x2;
typedef __attribute__((ext_vector_type(4))) unsigned u32x4;
typedef __attribute__((ext_vector_type(2))) unsigned u32x2;
typedef GAS unsigned gu32;
typedef GAS unsigned long long gu64;

constexpr int S_ = 2048, D_ = 1024, F_ = 3584;

constexpr unsigned SY_CH = 0, SY_GO = 1024, SY_RE = 2048;
constexpr unsigned WS_FD  = 98304;
constexpr unsigned WS_ZF  = 8192;
constexpr unsigned WS_KVF = 16384;
constexpr unsigned WS_RS  = 131072;
constexpr unsigned WS_CS  = WS_RS + 16 * 16 * 1024;
constexpr unsigned WS_ZP  = WS_CS + 16 * 16 * 768;
constexpr unsigned WS_FB  = WS_ZP + 32 * 16 * 128;
constexpr unsigned WS_ST2 = WS_FB + 32 * 32 * 128;
constexpr unsigned WS_XS  = WS_ST2 + 32 * 8 * 1024;
constexpr unsigned WS_GU  = WS_XS + 64 * 2048;
constexpr unsigned WS_YP  = WS_GU + 256 * 1024;
constexpr unsigned WS_KVP = WS_YP + 2 * 8 * 64 * 512;
constexpr unsigned WS_O   = WS_KVP + 32 * 16 * 4096;
constexpr unsigned WS_END = WS_O + S_ * D_ * 2;
constexpr unsigned OOB = 0x80000000u;
constexpr size_t WS_DIAG  = 64u << 20;
#define CNT(i) do {} while (0)
#define STAMPT(kid, i, T) do {} while (0)

#define TSL(idx) do {} while (0)
#define TSDUMP(k) do {} while (0)
template <int V> struct IC { static constexpr int v = V; };
template <int B, int E, class F> DI void sfor(F&& f) { if constexpr (B < E) { f(IC<B>{}); sfor<B + 1, E>(f); } }

DI float bflo(unsigned u) { return __uint_as_float(u << 16); }
DI float bfhi(unsigned u) { return __uint_as_float(u & 0xffff0000u); }
DI float rbf(float x) { return (float)(__bf16)x; }
DI unsigned pack2(float a, float b) { bf16x2 c; c[0] = (__bf16)a; c[1] = (__bf16)b; return __builtin_bit_cast(unsigned, c); }
DI unsigned pack2o(float a, float b) { unsigned p = pack2(a, b); asm("" : "+v"(p)); return p; }
DI f32x16 mfma32(u32x4 a, u32x4 b, f32x16 c) {
  return __builtin_amdgcn_mfma_f32_32x32x16_bf16(__builtin_bit_cast(bf16x8, a), __builtin_bit_cast(bf16x8, b), c, 0, 0, 0);
}
DI f32x4 mfma16(u32x4 a, u32x4 b, f32x4 c) {
  return __builtin_amdgcn_mfma_f32_16x16x32_bf16(__builtin_bit_cast(bf16x8, a), __builtin_bit_cast(bf16x8, b), c, 0, 0, 0);
}
DI f32x2 unpk(unsigned u) { return (f32x2){bflo(u), bfhi(u)}; }
DI u32x2 trread(const char* p) {
  s16x4 v = __builtin_amdgcn_ds_read_tr16_b64_v4i16((LAS s16x4*)p);
  return __builtin_bit_cast(u32x2, v);
}
DI const char* uni(const void* p) {
  const u64 v = (u64)p;
  return (const char*)(((u64)(unsigned)__builtin_amdgcn_readfirstlane((int)(v >> 32)) << 32) | (unsigned)__builtin_amdgcn_readfirstlane((int)v));
}
#define RV4(a, b, c, d) "v" #a, "v" #b, "v" #c, "v" #d
#define RV10(p) "v" #p "0", "v" #p "1", "v" #p "2", "v" #p "3", "v" #p "4", "v" #p "5", "v" #p "6", "v" #p "7", "v" #p "8", "v" #p "9"
#define RSV2 RV4(224, 225, 226, 227), "v228", "v229", RV10(23), RV10(24), RV4(250, 251, 252, 253), "v254", "v255"
#define RSV1 RV4(192, 193, 194, 195), RV4(196, 197, 198, 199), RV10(20), RV10(21), RV4(220, 221, 222, 223), RSV2
#define RSV "v148", "v149", RV10(15), RV10(16), RV10(17), RV10(18), "v190", "v191", RSV1
#define NTW " nt"
#define NTA ""
#define NTB ""
#define NTO ""
#define GL_(v, s, o) "global_load_lds_dwordx4 " v ", " s " offset:" #o
#define GLDS4(SC, CL...) asm volatile("s_add_u32 m0, %5, %c6\n\ts_nop 3\n\t" GL_("%0", "%4", 0) SC "\n\t" GL_("%1", "%4", 1024) SC "\n\t" \
  GL_("%2", "%4", 2048) SC "\n\t" GL_("%3", "%4", 3072) SC :: "v"(v0), "v"(v1), "v"(v2), "v"(v3), "s"(uni(sbase)), "s"(lds), "i"(IMM) : "memory", "scc", ##CL)
#define GLDS3(SC, CL...) asm volatile("s_add_u32 m0, %4, %c5\n\ts_nop 3\n\t" GL_("%0", "%3", 0) SC "\n\t" GL_("%1", "%3", 1024) SC "\n\t" \
  GL_("%2", "%3", 2048) SC :: "v"(v0), "v"(v1), "v"(v2), "s"(uni(sbase)), "s"(lds), "i"(IMM) : "memory", "scc", ##CL)
template <int IMM> DI void glds4(unsigned v0, unsigned v1, unsigned v2, unsigned v3, const void* sbase, unsigned lds) { GLDS4(NTO); }
template <int IMM> DI void glds4o(unsigned v0, unsigned v1, unsigned v2, unsigned v3, const void* sbase, unsigned lds) { GLDS4(NTO, RSV); }
template <int IMM> DI void glds4r(unsigned v0, unsigned v1, unsigned v2, unsigned v3, const void* sbase, unsigned lds) { GLDS4(NTA, RSV); }
template <int IMM> DI void glds4n(unsigned v0, unsigned v1, unsigned v2, unsigned v3, const void* sbase, unsigned lds) { GLDS4(" nt", RSV); }
template <int IMM> DI void glds3n(unsigned v0, unsigned v1, unsigned v2, const void* sbase, unsigned lds) { GLDS3(" nt", RSV); }
template <int IMM> DI void glds3r(unsigned v0, unsigned v1, unsigned v2, const void* sbase, unsigned lds) { GLDS3(NTB, RSV); }
template <int R> DI void sload(unsigned voff, const void* sbase) {
  asm volatile("s_nop 4\n\tglobal_load_dwordx4 v[%c0:%c1], %2, %3 sc1" ::"i"(R), "i"(R + 3), "v"(voff), "s"(uni(sbase)) : "memory", RSV);
}
DI float fadd(float a, float b) { float o; asm("v_add_f32 %0, %1, %2" : "=v"(o) : "v"(a), "v"(b)); return o; }
DI float fmul(float a, float b) { float o; asm("v_mul_f32 %0, %1, %2" : "=v"(o) : "v"(a), "v"(b)); return o; }
DI float fsq(float a, float c) { asm("v_fmac_f32 %0, %1, %1" : "+v"(c) : "v"(a)); return c; }
#define RS_(e, a, b, c, d) "v_add_f32 %" #e ", v[%c5+" #a "], v[%c5+" #b "]\n\tv_max3_u32 %4, v[%c5+" #a "], v[%c5+" #b "], %4\n\t" \
  "v_add_f32 %" #e ", v[%c5+" #c "], %" #e "\n\tv_max3_u32 %4, v[%c5+" #c "], v[%c5+" #d "], %4\n\tv_add_f32 %" #e ", v[%c5+" #d "], %" #e "\n\t"
template <int R> DI void rsum4(float* a, unsigned& m) {
  asm volatile(RS_(0, 0, 4, 8, 12) RS_(1, 1, 5, 9, 13) RS_(2, 2, 6, 10, 14) RS_(3, 3, 7, 11, 15)
               : "=&v"(a[0]), "=&v"(a[1]), "=&v"(a[2]), "=&v"(a[3]), "+v"(m) : "i"(R) : RSV);
}
template <int R> DI void wload2(unsigned voff, const void* sbase) {
  asm volatile("s_nop 4\n\tglobal_load_dwordx4 v[%c0:%c1], %2, %3" NTW ::"i"(R), "i"(R + 3), "v"(voff), "s"(uni(sbase)) : "memory", RSV2);
}
template <int R> DI void wload(unsigned voff, const void* sbase) {
  asm volatile("s_nop 4\n\tglobal_load_dwordx4 v[%c0:%c1], %2, %3" NTW ::"i"(R), "i"(R + 3), "v"(voff), "s"(uni(sbase)) : "memory", RSV);
}
template <int RA, int RB> DI unsigned wperm(unsigned sel) {
  unsigned o;
  if (RA < 192) asm volatile("v_perm_b32 %0, v%c2, v%c1, %3" : "=v"(o) : "i"(RA), "i"(RB), "s"(sel) : RSV);
  else if (RA < 224) asm volatile("v_perm_b32 %0, v%c2, v%c1, %3" : "=v"(o) : "i"(RA), "i"(RB), "s"(sel) : RSV1);
  else asm volatile("v_perm_b32 %0, v%c2, v%c1, %3" : "=v"(o) : "i"(RA), "i"(RB), "s"(sel) : RSV2);
  return o;
}
template <int R> DI unsigned rget() { unsigned o; asm volatile("v_mov_b32 %0, v%c1" : "=v"(o) : "i"(R) : RSV); return o; }
DI float xh_sum(float v) {
  auto r = __builtin_amdgcn_permlane32_swap(__float_as_uint(v), __float_as_uint(v), false, false);
  return __uint_as_float(r[0]) + __uint_as_float(r[1]);
}
template <int CTRL> DI unsigned dppu(unsigned v) { return (unsigned)__builtin_amdgcn_update_dpp(0, (int)v, CTRL, 0xf, 0xf, true); }
template <int CTRL> DI float dppf(float v) { return __uint_as_float(dppu<CTRL>(__float_as_uint(v))); }
constexpr int DPP_X1 = 0xB1, DPP_X2 = 0x4E, DPP_HMIR = 0x141, DPP_MIR = 0x140, DPP_ROR8 = 0x128;
DI float quad_sum(float v) { v += dppf<DPP_X1>(v); v += dppf<DPP_X2>(v); return v; }
DI float oct_sum(float v) { v = quad_sum(v); v += dppf<DPP_HMIR>(v); return v; }
DI float x16_sum(float v) {
  auto r = __builtin_amdgcn_permlane16_swap(__float_as_uint(v), __float_as_uint(v), false, false);
  return __uint_as_float(r[0]) + __uint_as_float(r[1]);
}
DI float sw16(float a, float b) {
  auto r = __builtin_amdgcn_permlane16_swap(__float_as_uint(a), __float_as_uint(b), false, false);
  return fadd(__uint_as_float(r[0]), __uint_as_float(r[1]));
}
DI float sw32(float a, float b) {
  auto r = __builtin_amdgcn_permlane32_swap(__float_as_uint(a), __float_as_uint(b), false, false);
  return fadd(__uint_as_float(r[0]), __uint_as_float(r[1]));
}
DI float hi_sum(float v) {
  auto r = __builtin_amdgcn_permlane16_swap(__float_as_uint(v), __float_as_uint(v), false, false);
  v = fadd(__uint_as_float(r[0]), __uint_as_float(r[1]));
  auto q = __builtin_amdgcn_permlane32_swap(__float_as_uint(v), __float_as_uint(v), false, false);
  return fadd(__uint_as_float(q[0]), __uint_as_float(q[1]));
}
template <int N> DI void phi_n(const f32x2* t, unsigned* p) {
  unsigned tb[N]; f32x2 tr[N], e[N];
#pragma unroll
  for (int i = 0; i < N; ++i) tb[i] = pack2o(t[i][0], t[i][1]);
  __builtin_amdgcn_sched_barrier(0);
#pragma unroll
  for (int i = 0; i < N; ++i) tr[i] = unpk(tb[i]);
  __builtin_amdgcn_sched_barrier(0);
#pragma unroll
  for (int i = 0; i < N; ++i) e[i] = tr[i] * 1.44269504088896340736f;
  __builtin_amdgcn_sched_barrier(0);
  u16x2 tm[2] = {{0, 0}, {0, 0}};
#pragma unroll
  for (int i = 0; i < N; ++i) {
    e[i][0] = __builtin_amdgcn_fmed3f(__builtin_amdgcn_exp2f(e[i][0]), 0.f, 1.f);
    tm[i & 1] = __builtin_elementwise_max(tm[i & 1], __builtin_bit_cast(u16x2, tb[i]));
    e[i][1] = __builtin_amdgcn_fmed3f(__builtin_amdgcn_exp2f(e[i][1]), 0.f, 1.f);
    __builtin_amdgcn_sched_barrier(0);
  }
#pragma unroll
  for (int i = 0; i < N; ++i) tr[i] += e[i];
  __builtin_amdgcn_sched_barrier(0);
#pragma unroll
  for (int i = 0; i < N; ++i) { tr[i][0] = fmaxf(tr[i][0], e[i][0]); tr[i][1] = fmaxf(tr[i][1], e[i][1]); }
  const u16x2 tx = __builtin_elementwise_max(tm[0], tm[1]);
  __builtin_amdgcn_sched_barrier(0);
#pragma unroll
  for (int i = 0; i < N; ++i) p[i] = pack2(tr[i][0], tr[i][1]);
  if (__any(tx[0] > 0xC0F0 || tx[1] > 0xC0F0)) {
#pragma unroll
    for (int i = 0; i < N; ++i) p[i] = pack2(bflo(p[i]) + 1e-6f, bfhi(p[i]) + 1e-6f);
  }
}
#define RLX_AGENT __ATOMIC_RELAXED, __HIP_MEMORY_SCOPE_AGENT
DI void st32(char* ws, unsigned off, unsigned v) { __hip_atomic_store((gu32*)(ws + off), v, RLX_AGENT); }
DI void st64(char* ws, unsigned off, u64 v) { __hip_atomic_store((gu64*)(ws + off), v, RLX_AGENT); }
DI unsigned ld32(char* ws, unsigned off) { return __hip_atomic_load((gu32*)(ws + off), RLX_AGENT); }
DI u64 ld64(char* ws, unsigned off) { return __hip_atomic_load((gu64*)(ws + off), RLX_AGENT); }
#define WAIT_V(n) asm volatile("s_waitcnt vmcnt(%c0)" ::"i"(n) : "memory")
#define RAW_BAR() do { asm volatile("s_waitcnt lgkmcnt(0)" ::: "memory"); __builtin_amdgcn_s_barrier(); asm volatile("" ::: "memory"); } while (0)
#define SB0() __builtin_amdgcn_sched_barrier(0)
#define WAIT_VR(n) asm volatile("s_waitcnt vmcnt(%c0)" ::"i"(n) : "memory", RSV)
#define RAW_BARR() do { asm volatile("s_waitcnt lgkmcnt(0)" ::: "memory", RSV); __builtin_amdgcn_s_barrier(); asm volatile("" ::: "memory", RSV); } while (0)
#define KEEP2() asm volatile("" ::: "memory", RSV2)
#define RAW_BAR2() do { asm volatile("s_waitcnt lgkmcnt(0)" ::: "memory", RSV2); __builtin_amdgcn_s_barrier(); KEEP2(); } while (0)
constexpr unsigned SPIN_CAP = 1u << 18;
#define SPIN _Pragma("nounroll") for (unsigned s_ = 0; s_ < SPIN_CAP; ++s_)
constexpr unsigned SENT = 0xFFFFFFFFu;
DI unsigned sane1(unsigned v) { return min(v, 0xFFFFFFFEu); }
DI u32x4 sane(u32x4 v) { v[1] = sane1(v[1]); v[3] = sane1(v[3]); return v; }
DI unsigned chk(unsigned m, u32x4 v) { return max(max(m, v[1]), v[3]); }
DI bool wave_ok(unsigned m) { return !__any(m == SENT); }
DI unsigned key_idx(unsigned k) { return (0xFFFFu - (k & 0xFFFFu)) & (S_ - 1); }
DI float key_val(unsigned k) { const unsigned o = k >> 16; return __uint_as_float(((o & 0x8000u) ? (o & 0x7fffu) : (~o & 0xffffu)) << 16); }
DI void top2_mix(unsigned& b0, unsigned& b1, unsigned c0, unsigned c1) {
  const unsigned lo = min(b0, c0), mx = max(b1, c1);
  b0 = max(b0, c0); b1 = max(lo, mx);
}
DI void top2_lo(unsigned& b0, unsigned& b1) {
  top2_mix(b0, b1, dppu<DPP_X1>(b0), dppu<DPP_X1>(b1));
  top2_mix(b0, b1, dppu<DPP_X2>(b0), dppu<DPP_X2>(b1));
  top2_mix(b0, b1, dppu<DPP_HMIR>(b0), dppu<DPP_HMIR>(b1));
}
DI void top2_mid(unsigned& b0, unsigned& b1) {
  top2_mix(b0, b1, dppu<DPP_MIR>(b0), dppu<DPP_MIR>(b1));
  auto p = __builtin_amdgcn_permlane16_swap(b0, b0, false, false); auto q = __builtin_amdgcn_permlane16_swap(b1, b1, false, false);
  b0 = p[0]; b1 = q[0]; top2_mix(b0, b1, p[1], q[1]);
}
DI void top2_hi(unsigned& b0, unsigned& b1) {
  top2_mid(b0, b1);
  auto p = __builtin_amdgcn_permlane32_swap(b0, b0, false, false); auto q = __builtin_amdgcn_permlane32_swap(b1, b1, false, false);
  b0 = p[0]; b1 = q[0]; top2_mix(b0, b1, p[1], q[1]);
}

constexpr int K1_STAGE = 40960;
constexpr int LDS_TOTAL = 4 * K1_STAGE;
constexpr int L_STM = 0;
constexpr int L_MISC = 2048;
constexpr int L_XK = 5120;
constexpr int L_KX = 3328;
constexpr int L_ACT = 2304;
constexpr int L_CAND = 4096;
constexpr int L_WO = 16384;
constexpr int L_P = 16384, L_PK = 7 * 16384;
constexpr int L_RED = 65536;
constexpr int wo_slot(int j) { return L_WO + (j % 9) * 16384; }
constexpr int CODE_LINES = 10;
constexpr int OW = 8;
#define W1A_ 16
constexpr int W1A = W1A_;
#define PRIO_M 1
#define PRIO_S 3
#define GSCHED 7, 10, 14, 18, 21, 24, 27, 30, 33, 36, 39, 42, 45, 48, 48
#define NT_FIRST 0
constexpr int GS[15] = {GSCHED};
constexpr int gops(int g) { return (g / 3) * 10 + (g % 3 == 0 ? 0 : g % 3 == 1 ? 4 : 7); }
constexpr bool gs_ok() {
  bool ok = GS[0] >= 3 && GS[1] >= 6 && GS[1] <= 12 && GS[0] <= GS[1] && GS[13] == 48 && GS[14] == 48;
  for (int t = 0; t < 12; ++t) ok = ok && GS[t + 2] <= 3 * (t + 5) && GS[t + 2] >= 3 * (t + 3) && GS[t + 2] >= GS[t + 1] + 3;
  return ok;
}
static_assert(gs_ok(), "staging schedule");
#define WSCHED 0, 0, 0, 0, 0, 0, 0, 0, 0, 2, 4, 6, 8
constexpr int WS_[13] = {WSCHED};
#define TCH0 100
#define KPH 8
#define PACE 1
#define PSLP 0
#define PFM 1
#define PFS 4
#define SLOWPF 0
#define P_KVP 6
#define L_KVP 0
#define P_KVF 12
#define L_KVF 0
#define P_FB 8
#define L_FB 0
#define P_C 14
#define P_D 2
#define L_D 0
#define P_GU 12
#define P_E 50
#define L_E 0
#define SLEEP(n) do { if (n) __builtin_amdgcn_s_sleep(n); } while (0)
#define OUTE 1
constexpr int VW = 160;

__global__ __launch_bounds__(512, 2) void fused(const u16* __restrict__ x, const u16* __restrict__ wqkv,
                                                const u16* __restrict__ wo, const u16* __restrict__ w_r,
                                                const u16* __restrict__ wg, const u16* __restrict__ wu,
                                                const u16* __restrict__ wd, u16* __restrict__ out, char* __restrict__ ws) {
  extern __shared__ __attribute__((aligned(16))) char smem[];
  STAMPT(0, 0, 0);
  u64 pc0; asm volatile("s_getpc_b64 %0" : "=s"(pc0));
  const int wave = __builtin_amdgcn_readfirstlane(threadIdx.x >> 6);
  const int bid = blockIdx.x, xcd = bid & 7, idx = bid >> 3;
  const int rb = (xcd & 1) * 8 + (idx & 7);
  const int hp = (xcd >> 1) * 4 + (idx >> 3);
  const int rp = xcd * 4 + (idx >> 3), cb = idx & 7;
  const int hs_ = (hp + 4 + (int)(((size_t)wqkv >> 7) & 7)) & 7;
  const int g8 = ((hp >> 3) * 4 + (rb >> 2)) * 8 + hs_, kq = rb & 3;
  const int krot = (4 * ((hp >> 2) + (rb >> 3) + 2 * ((rb >> 2) & 1))) & 15, posA = 1 + (hp & 3), posB = 1 + (rb & 3);
  const unsigned rs_slot = WS_RS + (rb * 16 + ((krot + posA) & 15)) * 1024, cs_slot = WS_CS + (hp * 16 + ((krot + posB) & 15)) * 768;
  const unsigned lds0 = (unsigned)(size_t)smem;
  const __amdgpu_buffer_rsrc_t wsr = __builtin_amdgcn_make_buffer_rsrc(ws, 0, WS_END, 0x00020000);
#define LDW(off) __builtin_amdgcn_raw_buffer_load_b128(wsr, (off), 0, 16)
#define STW(v, off) __builtin_amdgcn_raw_buffer_store_b128((v), wsr, (off), 0, 16)
#define STW1(v, off) __builtin_amdgcn_raw_buffer_store_b32((v), wsr, (off), 0, 16)
  float* stm = (float*)(smem + L_STM);
  volatile LAS int* misc = (volatile LAS int*)(smem + L_MISC);
#define LANES() int tid = threadIdx.x; asm volatile("" : "+v"(tid)); const int lane = tid & 63, r = lane & 31, h = lane >> 5; (void)r; (void)h
#define EPI() const int erow = tid >> 3, eseg = tid & 7; const size_t ego = (size_t)(rp * 64 + erow) * D_ + cb * 128 + eseg * 8
  u32x4 xv[2], wv[2];
  f32x2 xf[8], wf2[8];
  float sw_;
  auto unpack_xw = [&]() {
    f32x2 a = {0.f, 0.f};
#pragma unroll
    for (int i = 0; i < 8; ++i) { xf[i] = unpk(xv[i >> 2][i & 3]); wf2[i] = unpk(wv[i >> 2][i & 3]); a += wf2[i]; }
    sw_ = oct_sum(a[0] + a[1]);
#pragma unroll
    for (int i = 0; i < 8; ++i) asm volatile("" : "+v"(xf[i]), "+v"(wf2[i]));
    asm volatile("" : "+v"(sw_));
  };
  unsigned tsv = 0; (void)tsv;
  u32x4 wf[24];

  auto wtr = [&](auto I) {
    constexpr int m = I.v >> 3, c = I.v & 7, R = VW + 32 * m + (c >> 1);
    const unsigned sel = (c & 1) ? 0x07060302u : 0x05040100u;
    wf[I.v] = (u32x4){wperm<R, R + 4>(sel), wperm<R + 8, R + 12>(sel), wperm<R + 16, R + 20>(sel), wperm<R + 24, R + 28>(sel)};
  };

  if (wave < 4) {
    LANES();
    const int trl = ((lane & 15) >> 2) * 64 + ((lane >> 4) & 1) * 32 + (lane & 3) * 8;
    const int wr = wave >> 1, wc = wave & 1;
    const int head = 2 * hp + wc;
    const unsigned kvp_off = WS_KVP + (head * 16 + rb) * 4096 + wr * 2048 + lane * 16;
    const unsigned zp_off = WS_ZP + (head * 16 + rb) * 128;
    const unsigned o_off = WS_O + ((rb * 4 + 2 * wr) * 32 + head) * 2048 + lane * 16;
    const unsigned fb_off = lane < 8 ? WS_FB + ((rb * 2 + wr) * 32 + head) * 128 + lane * 16 : OOB;
    unsigned pfa, pfb, pfc = lane < 8 ? fb_off : (lane < 16 && wr == 0) ? zp_off + (lane & 7) * 16 : OOB;
    if (wave == 0) { pfa = WS_ST2 + (rp * 8 + cb) * 1024 + lane * 16; pfb = WS_GU + (g8 * 4 + kq) * 1024 + lane * 16; }
    else if (wave == 1) { pfa = WS_YP + ((2 * kq + (lane >> 5)) * 64 + g8) * 512 + (lane & 31) * 16; pfb = pfa + 8 * 64 * 512; }
    else if (wave == 2) {
      pfa = lane < 32 ? WS_XS + (rp * 2 + (lane >> 4)) * 2048 + cb * 256 + (lane & 15) * 16
          : lane < 48 ? WS_KVF + hp * 4096 + rb * 256 + (lane & 15) * 16
          : lane < 56 ? WS_FD + (cb * 32 + rp) * 128 + (lane & 7) * 16
          : rb < 2 ? WS_ZF + (2 * hp + rb) * 128 + (lane & 7) * 16 : OOB;
      pfb = OOB;
    } else { pfa = rs_slot + lane * 16; pfb = lane < 48 ? cs_slot + lane * 16 : OOB; }
    u32x4 S4 = {SENT, SENT, SENT, SENT};
    asm volatile("" : "+v"(pfa), "+v"(pfb), "+v"(pfc), "+v"(S4));
    if (PFM == 0) { __builtin_amdgcn_s_sleep(PFS); STW(S4, pfa); STW(S4, pfc); STW(S4, pfb); STW(S4, kvp_off); STW(S4, kvp_off + 1024); }
    __builtin_amdgcn_s_setprio(PRIO_M);
    f32x16 acc[2][3];
    {
      int ao[2][4], bo[4];
#pragma unroll
      for (int ks = 0; ks < 4; ++ks) {
        ao[0][ks] = (64 * wr + r) * 128 + (((2 * ks + h) ^ ((r >> 1) & 7)) * 16); ao[1][ks] = ao[0][ks] + 2 * K1_STAGE;
        bo[ks] = ks * K1_STAGE + 16384 + wc * 256 + trl + h * 3072;
        asm volatile("" : "+v"(ao[0][ks]), "+v"(ao[1][ks]), "+v"(bo[ks]));
      }
#pragma unroll
      for (int i = 0; i < 2; ++i)
#pragma unroll
        for (int j = 0; j < 3; ++j)
#pragma unroll
          for (int k = 0; k < 16; ++k) acc[i][j][k] = 0.f;
      asm volatile("" : "+v"(acc[0][0]), "+v"(acc[0][1]), "+v"(acc[0][2]), "+v"(acc[1][0]), "+v"(acc[1][1]), "+v"(acc[1][2]));
      struct Frag { u32x4 a[2][2]; u32x2 bl[2][3], bh[2][3]; };
      Frag FA, FB;
#define K1_B(F, kl, t) ((u32x4){F.bl[kl][t][0], F.bl[kl][t][1], F.bh[kl][t][0], F.bh[kl][t][1]})
#define K1_LA(kl, rt) N.a[kl][rt] = *(const u32x4*)(smem + ao[sn >> 1][2 * hf + kl] + (sn & 1) * K1_STAGE + rt * 4096)
#define K1_LB(kl, t) do { N.bl[kl][t] = trread(smem + bo[sn] + (2 * hf + kl) * 6144 + t * 1024); N.bh[kl][t] = trread(smem + bo[sn] + (2 * hf + kl) * 6144 + t * 1024 + 512); } while (0)
#define sub_unused sub
      auto sub_unused = [&](Frag& C, Frag& N, int sn, int hf) {
        acc[0][0] = mfma32(K1_B(C, 0, 0), C.a[0][0], acc[0][0]);
        K1_LB(0, 0); SB0();
        acc[0][1] = mfma32(C.a[0][0], K1_B(C, 0, 1), acc[0][1]);
        K1_LA(0, 0); K1_LA(0, 1); SB0();
        acc[0][2] = mfma32(C.a[0][0], K1_B(C, 0, 2), acc[0][2]);
        K1_LB(0, 1); SB0();
        acc[1][0] = mfma32(K1_B(C, 0, 0), C.a[0][1], acc[1][0]);
        K1_LB(0, 2); SB0();
        acc[1][1] = mfma32(C.a[0][1], K1_B(C, 0, 1), acc[1][1]);
        K1_LB(1, 0); SB0();
        acc[1][2] = mfma32(C.a[0][1], K1_B(C, 0, 2), acc[1][2]);
        K1_LA(1, 0); K1_LA(1, 1); SB0();
        acc[0][0] = mfma32(K1_B(C, 1, 0), C.a[1][0], acc[0][0]);
        K1_LB(1, 1); SB0();
        acc[0][1] = mfma32(C.a[1][0], K1_B(C, 1, 1), acc[0][1]);
        K1_LB(1, 2); SB0();
        acc[0][2] = mfma32(C.a[1][0], K1_B(C, 1, 2), acc[0][2]);
        SB0();
        acc[1][0] = mfma32(K1_B(C, 1, 0), C.a[1][1], acc[1][0]);
        SB0();
        acc[1][1] = mfma32(C.a[1][1], K1_B(C, 1, 1), acc[1][1]);
        SB0();
        acc[1][2] = mfma32(C.a[1][1], K1_B(C, 1, 2), acc[1][2]);
        SB0();
      };
      RAW_BAR();
      STAMPT(0, 1, 0);
      {
        Frag& N = FA; const int sn = 0, hf = 0;
        K1_LB(0, 0); K1_LA(0, 0); K1_LA(0, 1); K1_LB(0, 1); K1_LB(0, 2); K1_LB(1, 0); K1_LA(1, 0); K1_LA(1, 1); K1_LB(1, 1); K1_LB(1, 2);
      }
#pragma unroll 1
      for (int it = 0; it < 4; ++it) {
#pragma unroll
        for (int s = 0; s < 4; ++s) {
          sub(FA, FB, s, 1);
          if (s == (PFM ? 0 : 2)) WAIT_V(0);
          TSL(it * 4 + s);
          if (!(s & 1) || it < 3) RAW_BAR();
          else if (s == 3) { asm volatile("s_waitcnt lgkmcnt(0)" ::: "memory"); misc[8 + wave] = 3; }
          if (PFM == 1 && it == 0) {
            if (s == 0) { STW(S4, pfa); STW(S4, pfc); }
            if (s == 1) { STW(S4, pfb); STW(S4, kvp_off); }
            if (s == 2) STW(S4, kvp_off + 1024);
          }
          sub(FB, FA, (s + 1) & 3, 0);
        }
      }
    }
    TSL(16); if (wave == 0) TSDUMP(0);
    STAMPT(0, 2, 0);
    {
      f32x4 m4[8], i4[8]; float csk, csv;
      SPIN {
        const int ok = misc[12 + (lane & 3)];
        asm volatile("" ::: "memory");
        csk = stm[256 + wc * 96 + 32 + r]; csv = stm[256 + wc * 96 + 64 + r];
#pragma unroll
        for (int i = 0; i < 8; ++i) { m4[i] = *(const f32x4*)(stm + 64 * wr + 8 * i + 4 * h); i4[i] = *(const f32x4*)(stm + 128 + 64 * wr + 8 * i + 4 * h); }
        SB0();
        if (__all(ok != 0)) break;
      }
      const u32x4 ONES4 = {0x3F803F80u, 0x3F803F80u, 0x3F803F80u, 0x3F803F80u};
      f32x2 tk[16], tv[16];
#define PR(v, i) ((f32x2){(v)[2 * (i)], (v)[2 * (i) + 1]})
#pragma unroll
      for (int i = 0; i < 16; ++i) { tk[i] = PR(acc[i >> 3][1], i & 7) - PR(m4[i >> 1], i & 1) * csk; tv[i] = PR(acc[i >> 3][2], i & 7) - PR(m4[i >> 1], i & 1) * csv; }
      SB0();
#pragma unroll
      for (int i = 0; i < 16; ++i) { tk[i] *= PR(i4[i >> 1], i & 1); tv[i] *= PR(i4[i >> 1], i & 1); }
      SB0();
      unsigned kp[16], vp[16];
#pragma unroll
      for (int i = 0; i < 16; ++i) vp[i] = pack2o(tv[i][0], tv[i][1]);
      phi_n<16>(tk, kp);
      f32x16 kv, zv;
#pragma unroll
      for (int k = 0; k < 16; ++k) { kv[k] = 0.f; zv[k] = 0.f; }
#pragma unroll
      for (int i = 0; i < 4; ++i) kv = mfma32((u32x4){kp[4 * i], kp[4 * i + 1], kp[4 * i + 2], kp[4 * i + 3]}, (u32x4){vp[4 * i], vp[4 * i + 1], vp[4 * i + 2], vp[4 * i + 3]}, kv);
#pragma unroll
      for (int i = 0; i < 4; ++i) zv = mfma32(ONES4, (u32x4){kp[4 * i], kp[4 * i + 1], kp[4 * i + 2], kp[4 * i + 3]}, zv);
      SB0();
      f32x4 keep[2], give[2];
#pragma unroll
      for (int i = 0; i < 2; ++i) {
        const f32x4 lo = {kv[4 * i], kv[4 * i + 1], kv[4 * i + 2], kv[4 * i + 3]}, hi = {kv[8 + 4 * i], kv[9 + 4 * i], kv[10 + 4 * i], kv[11 + 4 * i]};
        keep[i] = wr ? hi : lo; give[i] = wr ? lo : hi;
      }
      char* xk = smem + L_XK + wave * 2304 + lane * 16;
      const char* xp = smem + L_XK + (wave ^ 2) * 2304 + lane * 16;
      *(f32x4*)xk = give[0]; *(f32x4*)(xk + 1024) = give[1]; *(float*)(xk + 2048 - lane * 12) = zv[0];
      asm volatile("s_waitcnt lgkmcnt(0)" ::: "memory");
      misc[16 + wave] = 1;
      SPIN { if (misc[16 + (wave ^ 2)] == 1) break; }
      asm volatile("" ::: "memory");
      STW(sane(__builtin_bit_cast(u32x4, keep[0] + *(const f32x4*)xp)), kvp_off);
      STW(sane(__builtin_bit_cast(u32x4, keep[1] + *(const f32x4*)(xp + 1024))), kvp_off + 1024);
      STW1(sane1(__float_as_uint(zv[0] + *(const float*)(xp + 2048 - lane * 12))), (lane < 32 && wr == 0) ? zp_off + lane * 4 : OOB);
    }
    STAMPT(0, 3, 0);
    unsigned qp[2][8];
    const unsigned po = WS_KVP + ((2 * hp + (rb >> 3)) * 16 + h) * 4096 + (2 * ((rb >> 2) & 1) + ((lane >> 4) & 1)) * 1024 + (16 * (rb & 3) + (lane & 15)) * 16;
    u32x4 pv[8] = {};
    {
      f32x4 c4[4]; float m_b[2], rinv[2];
#pragma unroll
      for (int i = 0; i < 4; ++i) c4[i] = *(const f32x4*)(stm + 256 + wc * 96 + 8 * i + 4 * h);
#pragma unroll
      for (int i = 0; i < 2; ++i) { m_b[i] = stm[64 * wr + 32 * i + r]; rinv[i] = stm[128 + 64 * wr + 32 * i + r]; }
      SB0();
      f32x2 tq[16];
#pragma unroll
      for (int i = 0; i < 16; ++i) tq[i] = PR(acc[i >> 3][0], i & 7) - PR(c4[(i & 7) >> 1], i & 1) * m_b[i >> 3];
      SB0();
#pragma unroll
      for (int i = 0; i < 16; ++i) tq[i] *= rinv[i >> 3];
      SB0();
      phi_n<16>(tq, &qp[0][0]);
#pragma unroll
      for (int i = 0; i < 16; ++i) asm volatile("" : "+v"(qp[i >> 3][i & 7]));
      if (wave == 0) {
        SLEEP(P_KVP);
#pragma unroll
        for (int p = 0; p < 8; ++p) pv[p] = LDW(po + p * 8192);
      }
      asm volatile("" ::: "memory");
    }
    STAMPT(0, 4, 0);
    if (wave == 1 && rb < 2) {
      SPIN {
        unsigned m = 0;
#pragma unroll
        for (int p = 0; p < 2; ++p) { pv[p] = LDW(WS_ZP + (2 * hp + rb) * 2048 + p * 1024 + lane * 16); m = chk(max(max(m, pv[p][0]), pv[p][2]), pv[p]); }
        if (wave_ok(m)) break;
        asm volatile("" ::: "memory");
      }
      f32x4 s = __builtin_bit_cast(f32x4, pv[0]) + __builtin_bit_cast(f32x4, pv[1]);
#pragma unroll
      for (int i = 0; i < 4; ++i) { s[i] += dppf<DPP_ROR8>(s[i]); s[i] = xh_sum(x16_sum(s[i])); }
      if (lane < 16) st64(ws, WS_ZF + (2 * hp + rb) * 128 + (lane >> 3) * 64 + (lane & 1) * 32 + ((lane & 7) >> 1) * 8,
                          ((u64)sane1(pack2(s[2], s[3])) << 32) | pack2(s[0], s[1]));
    }
    if (wave == 0) {
      SPIN {
        unsigned m = 0;
#pragma unroll
        for (int p = 0; p < 8; ++p) m = chk(m, pv[p]);
        CNT(0);
        if (wave_ok(m)) break;
        asm volatile("" ::: "memory");
        SLEEP(L_KVP);
#pragma unroll
        for (int p = 0; p < 8; ++p) pv[p] = LDW(po + p * 8192);
      }
      f32x4 s = __builtin_bit_cast(f32x4, pv[0]);
#pragma unroll
      for (int p = 1; p < 8; ++p) s += __builtin_bit_cast(f32x4, pv[p]);
      const unsigned p0 = pack2(xh_sum(s[0]), xh_sum(s[1])), p1 = pack2(xh_sum(s[2]), xh_sum(s[3]));
      auto a = __builtin_amdgcn_permlane16_swap(p0, p0, false, false);
      auto b = __builtin_amdgcn_permlane16_swap(p1, p1, false, false);
      STW(sane((u32x4){a[0], b[0], a[1], b[1]}), lane < 16 ? WS_KVF + hp * 4096 + rb * 256 + lane * 16 : OOB);
      STAMPT(1, 2, 0);
      misc[7] = 1;
    } else {
      SPIN { if (misc[7] == 1) break; __builtin_amdgcn_s_sleep(1); }
    }
    SLEEP(P_KVF);
    {
      u32x4 k0, k1, z0, z1;
      SPIN {
        k0 = LDW(WS_KVF + head * 2048 + lane * 16); k1 = LDW(WS_KVF + head * 2048 + 1024 + lane * 16);
        z0 = LDW(WS_ZF + head * 128 + h * 32); z1 = LDW(WS_ZF + head * 128 + h * 32 + 16);
        CNT(1);
        if (wave_ok(chk(chk(chk(chk(0u, k0), k1), z0), z1))) break;
        asm volatile("" ::: "memory");
        SLEEP(L_KVF);
      }
      STAMPT(0, 5, 0);
      f32x16 X[2]; f32x16 Dn[2];
#pragma unroll
      for (int rt = 0; rt < 2; ++rt) {
#pragma unroll
        for (int k = 0; k < 16; ++k) { X[rt][k] = 0.f; Dn[rt][k] = 0.f; }
        const u32x4 p0 = {qp[rt][0], qp[rt][1], qp[rt][2], qp[rt][3]}, p1 = {qp[rt][4], qp[rt][5], qp[rt][6], qp[rt][7]};
        Dn[rt] = mfma32(z0, p0, Dn[rt]); Dn[rt] = mfma32(z1, p1, Dn[rt]);
        X[rt] = mfma32(k0, p0, X[rt]); X[rt] = mfma32(k1, p1, X[rt]);
      }
      SB0();
#pragma unroll
      for (int rt = 0; rt < 2; ++rt) {
        const float ri = __builtin_amdgcn_rcpf(rbf(rbf(Dn[rt][0]) + 1e-6f));
        unsigned a[8]; float f[16];
#pragma unroll
        for (int i = 0; i < 8; ++i) a[i] = pack2o(X[rt][2 * i], X[rt][2 * i + 1]);
        SB0();
#pragma unroll
        for (int i = 0; i < 8; ++i) { f[2 * i] = bflo(a[i]); f[2 * i + 1] = bfhi(a[i]); }
        SB0();
#pragma unroll
        for (int i = 0; i < 16; ++i) f[i] = rt ? f[i] * ri : fmul(f[i], ri);
        SB0();
#pragma unroll
        for (int i = 0; i < 8; ++i) a[i] = pack2(f[2 * i], f[2 * i + 1]);
        STW(((u32x4){a[0], a[1], a[2], a[3]}), o_off + rt * 65536); STW(((u32x4){a[4], a[5], a[6], a[7]}), o_off + rt * 65536 + 1024);
        SB0();
      }
    }
    STAMPT(0, 6, 0);
    {
      EPI();
      const int rt = wave & 1, kh = wave >> 1;
      const unsigned ob = WS_O + ((rp * 2 + rt) * 32 + 16 * kh) * 2048 + lane * 16;
      auto oadr = [&](int t, int st) { return ob + (((t >> 2) + rp) & 3) * 8192 + (t & 3) * 2048 + st * 1024; };
      const int woff = kh * 8192 + h * 512 + trl;
      struct Frag2 { u32x2 lo[2][4], hi[2][4]; };
      Frag2 G[2];
      auto ld2 = [&](Frag2& N, int sn, int st, int nt) {
        const char* base = smem + sn + woff;
        N.lo[st][nt] = trread(base + ((2 * st) * 2 + (nt >> 1)) * 1024 + (nt & 1) * 256);
        N.hi[st][nt] = trread(base + ((2 * st + 1) * 2 + (nt >> 1)) * 1024 + (nt & 1) * 256);
      };
      WAIT_V(0);
      STW(((u32x4){1u, 1u, 1u, 1u}), fb_off);
      RAW_BAR();
#pragma unroll
      for (int st = 0; st < 2; ++st)
#pragma unroll
        for (int nt = 0; nt < 4; ++nt) ld2(G[0], wo_slot(0), st, nt);
      f32x16 a2[4];
#pragma unroll
      for (int i = 0; i < 4; ++i)
#pragma unroll
        for (int k = 0; k < 16; ++k) a2[i][k] = 0.f;
      SLEEP(P_FB);
      SPIN { CNT(2); if (wave_ok(ld32(ws, WS_FB + (rp * 32 + 16 * kh + (lane & 15)) * 128))) break; SLEEP(L_FB); }
      asm volatile("" ::: "memory");
      STAMPT(0, 7, 0);
      u32x4 of[16][2];
#pragma unroll
      for (int t = 0; t < OW; ++t) { of[t][0] = LDW(oadr(t, 0)); of[t][1] = LDW(oadr(t, 1)); }
      SB0();
      sfor<0, 16>([&](auto T) {
        constexpr int t = T.v;
        Frag2& C = G[t & 1]; Frag2& N = G[(t + 1) & 1];
#pragma unroll
        for (int st = 0; st < 2; ++st)
#pragma unroll
          for (int nt = 0; nt < 4; ++nt) {
            a2[nt] = mfma32((u32x4){C.lo[st][nt][0], C.lo[st][nt][1], C.hi[st][nt][0], C.hi[st][nt][1]}, of[t][st], a2[nt]);
            if ((t & 1) && t <= 11 && st == 1 && nt == 0) {
              asm volatile("s_waitcnt lgkmcnt(8)" ::: "memory"); __builtin_amdgcn_s_barrier(); asm volatile("" ::: "memory");
            }
            if (t < 15) ld2(N, wo_slot(t + 1), st, nt);
            if (t + OW < 16 && nt == 3) of[(t + OW) & 15][st] = LDW(oadr(t + OW, st));
            if (t + OW == 16 && nt == 3) { xv[st] = *(const u32x4*)(x + ego + st * 64); wv[st] = *(const u32x4*)(w_r + cb * 128 + eseg * 8 + st * 64); }
            SB0();
          }
      });
      STAMPT(0, 8, 0);
      char* pr = smem + L_P + kh * L_PK + (32 * rt + r) * 512;
#pragma unroll
      for (int q4 = 0; q4 < 4; ++q4)
#pragma unroll
        for (int nt = 0; nt < 4; ++nt)
          *(f32x4*)(pr + (((2 * q4 + h) ^ (r & 7)) * 16) + nt * 128) = (f32x4){a2[nt][4 * q4], a2[nt][4 * q4 + 1], a2[nt][4 * q4 + 2], a2[nt][4 * q4 + 3]};
    }
    unpack_xw();
    __builtin_amdgcn_s_setprio(0);
  } else {
    LANES();
    const int sw = wave - 4, t2 = tid - 256;
    __builtin_amdgcn_s_setprio(PRIO_S);
    unsigned hs_g0 = 0, hs_cur = 0;
    u32x4 ms_exp = {0, 0, 0, 0};
    bool hs_act = true, ms_act = (bid == 0) && (sw == 1);
    const unsigned hs_off = (lane == 1 ? SY_GO : SY_CH) + bid * 4;
    const unsigned re_off = t2 == 0 ? SY_RE + bid * 4 : OOB;
    const unsigned ms_off = lane * 16;
    const char* wsu = uni(ws);
#define HS_PROBE() asm volatile("s_nop 4\n\tglobal_load_dword v156, %0, %1 sc1" :: "v"(hs_off), "s"(wsu) : "memory", RSV)
#define MS_PROBE() asm volatile("s_nop 4\n\tglobal_load_dwordx4 v[152:155], %0, %1 offset:2048 sc1" :: "v"(ms_off), "s"(wsu) : "memory", RSV)
    auto hs_step = [&](bool first) {
      if (hs_act) {
        const unsigned hv_ = rget<156>();
        hs_act = __builtin_amdgcn_readlane(hv_, 1) == hs_g0;
        const unsigned c = __builtin_amdgcn_readlane(hv_, 0);
        if (sw == 0 && (first || c != hs_cur)) STW1(hv_, re_off);
        hs_cur = c;
        HS_PROBE();
      }
      if (ms_act) {
        const unsigned d = ((rget<152>() ^ ms_exp[0]) | (rget<153>() ^ ms_exp[1])) | ((rget<154>() ^ ms_exp[2]) | (rget<155>() ^ ms_exp[3]));
        ms_act = __any(d != 0u);
        STW(ms_exp, ms_act ? OOB : SY_GO + ms_off);
        MS_PROBE();
      }
    };
    auto korder = [&](int t) { return (t + krot) & 15; };
    unsigned voA0, voA1, voB;
    {
      const int r0 = 32 * sw + (lane >> 3), r1 = r0 + 8;
      const int c0 = (lane & 7) ^ ((r0 >> 1) & 7), c1 = (lane & 7) ^ ((r1 >> 1) & 7);
      voA0 = (unsigned)(r0 * D_ + c0 * 8) * 2u;
      voA1 = (unsigned)(r1 * D_ + c1 * 8) * 2u;
      const int bi = lane >> 4, q = (lane >> 2) & 3, ch = lane & 3;
      voB = (unsigned)((16 * sw + 4 * (bi >> 1) + q) * (3 * D_) + (bi & 1) * 32 + ch * 8) * 2u;
    }
    const char* baseA = (const char*)(x + (size_t)rb * 128 * D_);
    const char* baseB = (const char*)(wqkv + hp * 64);
    const unsigned l4 = __builtin_amdgcn_readfirstlane(lds0 + sw * 4096);
    const unsigned l6 = __builtin_amdgcn_readfirstlane(lds0 + sw * 6144);
    auto group = [&](auto G) {
      constexpr int t = G.v / 3, p = G.v % 3, sl = (t & 3) * K1_STAGE;
      const int kt = korder(t);
      if (p == 0) {
        if (t < NT_FIRST) glds4n<sl>(voA0, voA1 - 1024, voA0 + 16 * D_ * 2 - 2048, voA1 + 16 * D_ * 2 - 3072, baseA + kt * 128, l4);
        else glds4r<sl>(voA0, voA1 - 1024, voA0 + 16 * D_ * 2 - 2048, voA1 + 16 * D_ * 2 - 3072, baseA + kt * 128, l4);
      } else {
        const char* pb = baseB + (size_t)kt * (64 * 3 * D_ * 2) + (p - 1) * (8 * 3 * D_ * 2);
        if (t < NT_FIRST) glds3n<sl + 16384 + (p - 1) * 3072>(voB, voB + 1024, voB + 2048, pb, l6);
        else glds3r<sl + 16384 + (p - 1) * 3072>(voB, voB + 1024, voB + 2048, pb, l6);
      }
    };
    HS_PROBE();
    if (ms_act) {
      MS_PROBE();
      asm volatile("s_nop 4\n\tglobal_load_dwordx4 v[148:151], %0, %1 offset:1024 sc1" :: "v"(ms_off), "s"(wsu) : "memory", RSV);
    }
    sfor<0, GS[0]>(group);
    if (sw == 3) {
      const char* cp = (const char*)(pc0 - (pc0 & 127)) + (idx * CODE_LINES + (lane < CODE_LINES ? lane : 0)) * 128;
      asm volatile("global_load_dword v157, %0, off" :: "v"(cp) : "memory", RSV);
    }
    {
      const int hq = (hp & ~3) + ((hp + 7 - hs_) & 3);
      if (SLOWPF && (sw == 1 || sw == 2) && ((hq + 4 + (int)(((size_t)wqkv >> 7) & 7)) & 7) == 7)
        asm volatile("s_nop 4\n\tglobal_load_dword v157, %0, %1" :: "v"(min(lane, 47) * 2048), "s"(uni((const char*)wqkv + hq * 128 + (size_t)(((rb & 7) * 4 + (hp & 3)) * 96 + (sw - 1) * 48) * 2048)) : "memory", RSV);
    }
    const int gr = (g8 >> 3) * 7 + min(hs_, 6);
    const bool real = hs_ != 7;
    const unsigned voG = (unsigned)((lane >> 3) * 8 * F_ + (lane & 7) * 8) * 2u, voD = (unsigned)((lane >> 3) * 8 * D_ + (lane & 7) * 8) * 2u;
    const size_t gofs = ((size_t)(kq * 256 + sw * 64) * F_ + gr * 64) * 2;
    const char* pG = (const char*)wg + gofs;
    const char* pU = (const char*)wu + gofs;
    const char* pD = (const char*)(wd + (size_t)(gr * 64) * D_ + kq * 256 + sw * 64);
    auto wstep = [&](auto I) {
      constexpr int i = I.v;
      if (i < 8) wload<VW + 4 * i>(voG, pG + (i & 7) * (F_ * 2));
      else if (i < 16) wload<VW + 4 * i>(voG, pU + (i & 7) * (F_ * 2));
      else wload2<VW + 4 * i>(voD, pD + (i & 7) * (D_ * 2));
    };
    unsigned voW;
    {
      const int bi = lane >> 4, q = (lane >> 2) & 3, ch = lane & 3;
      voW = (unsigned)((4 * (bi >> 1) + q) * D_ + (bi & 1) * 32 + ch * 8) * 2u;
    }
    const char* baseW = (const char*)(wo + (size_t)((16 * (sw >> 1)) * 32 + 16 * (sw & 1)) * D_ + cb * 128) - 1024;
    auto stageW = [&](auto J, bool rsv) {
      const char* s_ = baseW + (size_t)((J.v + 4 * (rp & 3)) & 15) * (32 * D_ * 2);
      if (rsv) glds4o<wo_slot(J.v)>(voW + 1024, voW + 128, voW + 8 * D_ * 2 - 1024, voW + 8 * D_ * 2 + 128 - 2048, s_, l4);
      else glds4<wo_slot(J.v)>(voW + 1024, voW + 128, voW + 8 * D_ * 2 - 1024, voW + 8 * D_ * 2 + 128 - 2048, s_, l4);
    };
    int sta[4], stb[3];
    {
      const int row = t2 >> 1, key = (row >> 1) & 7;
#pragma unroll
      for (int c = 0; c < 4; ++c) { sta[c] = (posA & 3) * K1_STAGE + row * 128 + (((4 * (t2 & 1) + c) ^ key) * 16); asm volatile("" : "+v"(sta[c]) :: RSV); }
#pragma unroll
      for (int i = 0; i < 3; ++i) {
        const int G = sw * 3 + i, hd = G >= 6 ? 1 : 0, mat = (G - 6 * hd) >> 1;
        stb[i] = (posB & 3) * K1_STAGE + 16384 + (lane >> 4) * 6144 + mat * 1024 + hd * 256 + ((lane & 15) >> 2) * 64 + (G & 1) * 32 + (lane & 3) * 8;
        asm volatile("" : "+v"(stb[i]) :: RSV);
      }
    }
    unsigned rs_dst = rs_slot + t2 * 4, cs_dst = lane < 48 ? cs_slot + (sw * 48 + lane) * 4 : OOB;
    unsigned rs_off = WS_RS + rb * 16384 + (lane >> 4) * 1024 + sw * 256 + (lane & 15) * 16;
    unsigned cs_off = WS_CS + hp * 12288 + (lane >> 4) * 768 + sw * 192 + min(lane & 15, 11) * 16;
    asm volatile("" : "+v"(rs_dst), "+v"(cs_dst), "+v"(rs_off), "+v"(cs_off) :: RSV);
    WAIT_VR(gops(GS[0]) - 10); RAW_BARR();
    hs_g0 = __builtin_amdgcn_readlane(rget<156>(), 1);
    if (ms_act) {
      const u32x4 re = {rget<152>(), rget<153>(), rget<154>(), rget<155>()}, g0 = {rget<148>(), rget<149>(), rget<150>(), rget<151>()};
#pragma unroll
      for (int j = 0; j < 4; ++j) { const unsigned c = re[j] + 1u; ms_exp[j] = (c == g0[j]) ? c + 1u : c; }
      STW(ms_exp, SY_CH + ms_off);
    }
    sfor<GS[0], GS[1]>(group);
    WAIT_VR(gops(GS[1]) - 20); RAW_BARR();
    u32x4 ra[4] = {}; u32x2 rc[3][4] = {};
    float sa = 0.f, sb = 0.f, qa = 0.f, qb = 0.f, ca[3] = {0.f, 0.f, 0.f}, cb_[3] = {0.f, 0.f, 0.f};
    auto chunk = [&](auto C) {
      constexpr int c = C.v;
      if constexpr (c < 4) {
        asm volatile("" : "+v"(ra[c]) :: RSV);
#pragma unroll
        for (int i = 0; i < 4; ++i) { const float lo = bflo(ra[c][i]), hi = bfhi(ra[c][i]); sa = fadd(sa, lo); sb = fadd(sb, hi); qa = fsq(lo, qa); qb = fsq(hi, qb); }
        asm volatile("" : "+v"(sa), "+v"(sb), "+v"(qa), "+v"(qb) :: RSV);
      } else if constexpr (c == 4) {
        float s = fadd(sa, sb), sq = fadd(qa, qb);
        s = fadd(s, dppf<DPP_X1>(s)); sq = fadd(sq, dppf<DPP_X1>(sq));
        STW1(sane1(__float_as_uint((t2 & 1) ? sq : s)), rs_dst);
      } else if constexpr (c < 11) {
        constexpr int i = (c - 5) >> 1, j = ((c - 5) & 1) * 2;
        asm volatile("" : "+v"(rc[i][j]), "+v"(rc[i][j + 1]) :: RSV);
#pragma unroll
        for (int e = 0; e < 4; ++e) { const unsigned u = rc[i][j + (e >> 1)][e & 1]; ca[i] = fadd(ca[i], bflo(u)); cb_[i] = fadd(cb_[i], bfhi(u)); }
        asm volatile("" : "+v"(ca[i]), "+v"(cb_[i]) :: RSV);
      } else {
        const float c2 = fadd(ca[2], cb_[2]);
        STW1(sane1(__float_as_uint(sw32(sw16(fadd(ca[0], cb_[0]), fadd(ca[1], cb_[1])), sw16(c2, c2)))), cs_dst);
      }
    };
    sfor<0, 12>([&](auto T) {
      constexpr int t = T.v;
      TSL(t);
      if (t < 4 && posA == t + 1) {
#pragma unroll
        for (int c = 0; c < 4; ++c) ra[c] = *(const u32x4*)(smem + sta[c]);
      }
      if (t < 4 && posB == t + 1) {
#pragma unroll
        for (int i = 0; i < 3; ++i)
#pragma unroll
          for (int j = 0; j < 4; ++j) rc[i][j] = trread(smem + stb[i] + (j >> 1) * 3072 + (j & 1) * 512);
      }
      if (t == (PFM ? 2 : 0)) HS_PROBE();
      if (t >= (PFM ? 4 : 2)) hs_step(t == (PFM ? 4 : 2));
      sfor<GS[t + 1], GS[t + 2]>([&](auto G) {
        group(G);
        constexpr int u = t - 4, p = G.v - GS[t + 1];
        constexpr int ord[12] = {0, 5, 6, 1, 7, 8, 2, 9, 10, 3, 11, 4};
        if constexpr (u >= 0 && u < 4 && p < 3) chunk(IC<ord[(3 * u + p) % 12]>{});
      });
      if (t >= TCH0 && t < TCH0 + 8 && sw == ((t - TCH0) & 3))
        asm volatile("s_nop 4\n\tglobal_load_dword v157, %0, %1" :: "v"(lane * 128), "s"(uni((const char*)wo + idx * 65536 + (t - TCH0) * 8192)) : "memory", RSV);
      if (real) sfor<WS_[t], WS_[t + 1]>(wstep);
      TSL(16 + t);
      WAIT_VR(t < 11 ? gops(GS[t + 2]) - 10 * (t + 3) : 10);
      TSL(32 + t);
      RAW_BARR();
    });
    TSL(12);
    hs_step(false);
    auto st_load = [&]() { sfor<0, 4>([&](auto I) { sload<224 + 4 * I.v>(rs_off, wsu + I.v * 4096); sload<240 + 4 * I.v>(cs_off, wsu + I.v * 3072); }); };
    auto st_put = [&]() -> bool {
      float a[8]; unsigned m = 0;
      rsum4<224>(a, m); rsum4<240>(a + 4, m);
      const float s1 = sw16(a[0], a[2]), q1 = sw16(a[1], a[3]);
      const float cs = sw32(sw16(a[4], a[5]), sw16(a[6], a[7]));
      const float mean = sw32(s1, s1) * (1.0f / 1024.0f);
      const float var = fmaxf(__builtin_fmaf(sw32(q1, q1), 1.0f / 1024.0f, -(mean * mean)), 0.f);
      const float rinv = __builtin_amdgcn_rcpf(rbf(__builtin_amdgcn_sqrtf(rbf(rbf(var) + 1e-5f))));
      stm[(lane >> 5) * 128 + sw * 32 + (lane & 15) * 2 + ((lane >> 4) & 1)] = (lane & 32) ? rinv : rbf(mean);
      if ((lane & 15) < 12) stm[256 + sw * 48 + (lane & 15) * 4 + (lane >> 4)] = cs;
      return wave_ok(m);
    };
    st_load();
    if (real) sfor<WS_[12], W1A>(wstep);
    TSL(28);
    if (real) WAIT_VR(W1A - WS_[12]); else WAIT_VR(0);
    const bool st_ok = st_put() && !hs_act;
    if (lane == 0) { misc[8 + sw] = 0; misc[12 + sw] = st_ok; misc[16 + sw] = 0; misc[7] = 0; }
    TSL(44);
    RAW_BARR();
    TSL(13); TSDUMP(1 + sw);
    STAMPT(1, 0, 256);
    if (!st_ok) {
      if (ms_act) {
        SPIN {
          bool ok = true;
#pragma unroll
          for (int j = 0; j < 4; ++j) ok &= (ld32(ws, SY_RE + lane * 16 + j * 4) == ms_exp[j]);
          if (__all(ok)) break;
        }
        STW(ms_exp, SY_GO + lane * 16);
      }
      if (hs_act) {
        SPIN {
          if (ld32(ws, SY_GO + bid * 4) != hs_g0) break;
          const unsigned c = ld32(ws, SY_CH + bid * 4);
          if (c != hs_cur) { hs_cur = c; STW1(c, re_off); }
        }
      }
      SPIN { st_load(); WAIT_VR(0); if (st_put()) break; }
      asm volatile("s_waitcnt lgkmcnt(0)" ::: "memory", RSV);
      if (lane == 0) misc[12 + sw] = 1;
    }
    __builtin_amdgcn_s_setprio(0);
    if (real) sfor<W1A, 16>(wstep);
    auto mwait = [&](int v) { SPIN { const int a = misc[8 + (lane & 3)]; if (__all(a >= v)) break; __builtin_amdgcn_s_sleep(1); } asm volatile("" ::: "memory"); };
    sfor<0, 9>([&](auto J) {
      if (J.v == 6) mwait(3);
      stageW(J, J.v < 2);
      if (J.v >= 1) WAIT_V(4 * PACE);
      if (J.v == 1) { WAIT_VR(8); sfor<0, 16>(wtr); }
      if (J.v >= 2 && PSLP) __builtin_amdgcn_s_sleep(PSLP);
    });
    STAMPT(1, 1, 256);
    EPI();
#pragma unroll
    for (int i = 0; i < 2; ++i) { xv[i] = *(const u32x4*)(x + ego + i * 64); wv[i] = *(const u32x4*)(w_r + cb * 128 + eseg * 8 + i * 64); }
    asm volatile("s_waitcnt vmcnt(0)" : "+v"(xv[0]), "+v"(xv[1]), "+v"(wv[0]), "+v"(wv[1]) :: "memory");
    unpack_xw();
    RAW_BAR();
    RAW_BAR(); stageW(IC<9>{}, false); stageW(IC<10>{}, false);
    RAW_BAR(); stageW(IC<11>{}, false); stageW(IC<12>{}, false);
    RAW_BAR(); stageW(IC<13>{}, false); stageW(IC<14>{}, false);
    WAIT_V(16); RAW_BAR(); stageW(IC<15>{}, false);
    WAIT_V(12); RAW_BAR();
    WAIT_V(0); RAW_BAR();
    if (real) sfor<16, 24>(wstep);
  }
  LANES(); EPI();
  const int grow = rp * 64 + erow;
  int pa0 = L_P + erow * 512 + ((2 * eseg) ^ (erow & 7)) * 16, pa1 = pa0 ^ 16;
  unsigned st_dst = eseg == 0 ? WS_ST2 + ((rp * 8 + cb) * 64 + erow) * 16 : OOB, st_src = WS_ST2 + rp * 8192 + lane * 16;
  unsigned fd_dst = lane < 8 ? WS_FD + (cb * 32 + rp) * 128 + lane * 16 : OOB, kidx = 0xFFFFu - grow;
  asm volatile("" : "+v"(pa0), "+v"(pa1), "+v"(st_dst), "+v"(st_src), "+v"(fd_dst), "+v"(kidx));
  RAW_BAR2();
  STAMPT(0, 9, 0);
  unsigned hb[8];
  f32x2 hv[8];
  {
    f32x4 pp[2][4];
#pragma unroll
    for (int k = 0; k < 2; ++k)
#pragma unroll
      for (int i = 0; i < 4; ++i) pp[k][i] = *(const f32x4*)(smem + ((i & 1) ? pa1 : pa0) + (i >> 1) * 256 + k * L_PK);
    SB0();
    f32x2 s = {0.f, 0.f}, q = {0.f, 0.f}, p2 = {0.f, 0.f};
#pragma unroll
    for (int i = 0; i < 8; ++i) {
      const f32x4 a = pp[0][i >> 1] + pp[1][i >> 1];
      const f32x2 u = xf[i] + unpk(pack2o(a[2 * (i & 1)], a[2 * (i & 1) + 1]));
      hb[i] = pack2o(u[0], u[1]);
      hv[i] = unpk(hb[i]);
      s += hv[i]; q += hv[i] * hv[i]; p2 += hv[i] * wf2[i];
    }
    STW(sane((u32x4){__float_as_uint(oct_sum(s[0] + s[1])), __float_as_uint(oct_sum(q[0] + q[1])), __float_as_uint(oct_sum(p2[0] + p2[1])), __float_as_uint(sw_)}), st_dst);
  }
  KEEP2();
  STAMPT(0, 10, 0);
  const int sw = wave - 4;
  const unsigned fd_src = WS_FD + xcd * 4096 + r * 128;
  u32x2 kpk = {SENT, SENT};
  if (wave == 0) {
    u32x4 pa[8];
    SLEEP(P_C);
    SPIN {
      unsigned m = 0;
#pragma unroll
      for (int j = 0; j < 8; ++j) { pa[j] = LDW(st_src + j * 1024); m = chk(m, pa[j]); }
      CNT(3);
      if (wave_ok(m)) break;
      KEEP2();
    }
    STAMPT(0, 11, 0);
#define F4(v) __builtin_bit_cast(f32x4, v)
    const f32x4 t = ((F4(pa[0]) + F4(pa[1])) + (F4(pa[2]) + F4(pa[3]))) + ((F4(pa[4]) + F4(pa[5])) + (F4(pa[6]) + F4(pa[7])));
    const float mean = t[0] * (1.0f / 1024.0f);
    const float var = fmaxf(__builtin_fmaf(t[1], 1.0f / 1024.0f, -(mean * mean)), 0.f);
    const float mb = rbf(mean), rs = __builtin_amdgcn_rcpf(rbf(__builtin_amdgcn_sqrtf(rbf(rbf(var) + 1e-5f))));
    *(f32x2*)(smem + L_KX + lane * 8) = (f32x2){mb, rs};
    const float sc = fmaxf((t[2] - mb * t[3]) * rs, -INFINITY);
    const int u = (int)(pack2(0.f, sc) & 0xffff0000u);
    unsigned b0 = ((unsigned)(u ^ ((u >> 31) | (int)0x80000000)) & 0xffff0000u) | (0xFFFFu - (rp * 64 + lane)), b1 = 0;
    top2_lo(b0, b1); top2_hi(b0, b1);
    STW(((u32x4){b0, b1, b0, b1}), fd_dst);
    if (lane == 0) *(u32x2*)(smem + L_MISC + 112) = (u32x2){b0, b1};
  } else if (wave >= 4) {
    asm volatile("s_waitcnt vmcnt(0)" ::: "memory", RSV2);
    sfor<16, 24>(wtr);
  }
  RAW_BAR();
  bool cand;
  u32x4 ob[2];
  {
    const f32x2 mr = *(const f32x2*)(smem + L_KX + erow * 8);
    const u32x2 kk = *(const u32x2*)(smem + L_MISC + 112);
    const float mb = mr[0], rs = mr[1];
    const unsigned b0 = kk[0], b1 = kk[1];
    u32x4 nq[2];
#pragma unroll
    for (int i = 0; i < 8; ++i) {
      const f32x2 d = hv[i] - mb;
      const f32x2 n = unpk(pack2o(d[0], d[1])) * rs;
      nq[i >> 2][i & 3] = pack2o(n[0], n[1]);
    }
    const bool c2 = (b1 & 0xffffu) == kidx;
    cand = c2 || (b0 & 0xffffu) == kidx;
    if (cand) {
      const unsigned xo = WS_XS + (rp * 2 + (c2 ? 1 : 0)) * 2048 + cb * 256 + eseg * 16;
      STW(sane(nq[0]), xo); STW(sane(nq[1]), xo + 128);
      char* cp = smem + L_CAND + (c2 ? 512 : 0) + eseg * 16;
      *(u32x4*)cp = (u32x4){hb[0], hb[1], hb[2], hb[3]}; *(u32x4*)(cp + 128) = (u32x4){hb[4], hb[5], hb[6], hb[7]};
      *(u32x4*)(cp + 256) = nq[0]; *(u32x4*)(cp + 384) = nq[1];
    }
    asm volatile("" ::: "memory");
    if (wave >= 4) { SLEEP(P_D); kpk = __builtin_amdgcn_raw_buffer_load_b64(wsr, fd_src, 0, 16); }
    asm volatile("" ::: "memory");
#pragma unroll
    for (int i = 0; i < 8; ++i) { const f32x2 o = hv[i] + unpk(nq[i >> 2][i & 3]); ob[i >> 2][i & 3] = pack2(o[0], o[1]); }
  }
  STAMPT(0, 12, 0);
  if (wave < 4) {
    if (!cand) { *(u32x4*)(out + ego) = ob[0]; *(u32x4*)(out + ego + 64) = ob[1]; }
    RAW_BAR();
    const unsigned w0 = misc[24], w1 = misc[25];
    if (cand && grow != (int)key_idx(w0) && grow != (int)key_idx(w1)) { *(u32x4*)(out + ego) = ob[0]; *(u32x4*)(out + ego + 64) = ob[1]; }
#pragma unroll 1
    for (int tk = 0; tk < 2; ++tk) {
      const unsigned wk = tk ? w1 : w0, slot = misc[26 + tk];
      if ((int)(slot >> 1) != rp) continue;
      u32x4 pv[7];
      const unsigned yo = WS_YP + ((tk * 8 + cb) * 64 + (lane >> 3) * 8) * 512 + wave * 128 + (lane & 7) * 16;
      const int col = (wave >> 1) * 64 + (lane & 7) * 8 + (wave & 1) * 4 + (lane >> 4);
      float sg = rbf(__builtin_amdgcn_rcpf(1.0f + __expf(-key_val(wk))));
      float hh = bflo(*(const u16*)(smem + L_CAND + (slot & 1) * 512 + col * 2)), nn = bflo(*(const u16*)(smem + L_CAND + (slot & 1) * 512 + 256 + col * 2));
      u16* op = out + (size_t)key_idx(wk) * D_ + cb * 128 + col;
      asm volatile("" : "+v"(sg), "+v"(hh), "+v"(nn), "+v"(op));
      if (tk == 0 || (int)(misc[26] >> 1) != rp) SLEEP(P_E);
      SPIN {
        unsigned m = 0;
#pragma unroll
        for (int p = 0; p < 7; ++p) { pv[p] = LDW(yo + p * 512); m = chk(m, pv[p]); }
        CNT(6);
        if (wave_ok(m)) break;
        asm volatile("" ::: "memory");
        SLEEP(L_E);
      }
      STAMPT(0, 13, 0);
      f32x4 s = __builtin_bit_cast(f32x4, pv[0]);
#pragma unroll
      for (int p = 1; p < 7; ++p) s += __builtin_bit_cast(f32x4, pv[p]);
      float y = sw32(sw16(s[0], s[1]), sw16(s[2], s[3]));
      y += dppf<DPP_ROR8>(y);
      if (!(lane & 8)) *(GAS u16*)op = (u16)pack2(hh + rbf(nn + rbf(rbf(y) * sg)), 0.f);
      STAMPT(0, 14, 0);
    }
  } else {
    unsigned xl = WS_XS + (kq * 256 + sw * 64 + (lane >> 4) * 16 + ((lane >> 2) & 1) * 8) * 2;
    int red_dst = (lane < 8 || (lane >= 24 && lane < 32)) ? L_RED + sw * 2048 + ((lane >> 3) & 1) * 1024 + (lane & 7) * 16 : L_RED + 8192 + lane * 16;
    unsigned gu_src = WS_GU + g8 * 4096 + lane * 16;
    int act_dst = L_ACT + sw * 256 + ((lane & 7) * 8 + (lane >> 3)) * 2, act_src = L_ACT + sw * 256 + (lane & 1) * 128 + (lane >> 4) * 32 + ((lane >> 1) & 1) * 16;
    unsigned yp_dst = lane < 16 ? WS_YP + (((lane >> 3) * 8 + 2 * kq + (sw >> 1)) * 64 + g8) * 512 + (sw & 1) * 256 + (lane & 7) * 16 : OOB;
    asm volatile("" : "+v"(xl), "+v"(red_dst), "+v"(gu_src), "+v"(act_dst), "+v"(act_src), "+v"(yp_dst));
    unsigned w0, w1; int s0 = -1, s1 = -1;
    u32x4 xa = {SENT, SENT, SENT, SENT};
    SPIN {
      CNT(4);
      const bool all = wave_ok(kpk[1]);
      const unsigned k0 = kpk[1] == SENT ? 0u : kpk[0], k1 = kpk[1] == SENT ? 0u : kpk[1];
      w0 = k0; w1 = k1;
      top2_lo(w0, w1); top2_mid(w0, w1);
      const int n0 = 2 * __builtin_ctzll(__ballot(k0 == w0));
      const u64 m1 = __ballot(k0 == w1);
      const int n1 = m1 ? 2 * __builtin_ctzll(m1) : n0 + 1;
      const bool same = n0 == s0 && n1 == s1;
      s0 = n0; s1 = n1;
      asm volatile("" ::: "memory");
      if (all && same) break;
      if (!all) { SLEEP(L_D); kpk = __builtin_amdgcn_raw_buffer_load_b64(wsr, fd_src, 0, 16); }
      xa = LDW(xl + ((lane & 1) ? s1 : s0) * 2048);
      if (all) break;
    }
    SPIN {
      if (wave_ok(chk(0u, xa))) break;
      asm volatile("" ::: "memory");
      xa = LDW(xl + ((lane & 1) ? s1 : s0) * 2048);
    }
    STAMPT(1, 3, 256);
    {
      const u32x4 z4 = {0u, 0u, 0u, 0u};
      const u32x4 xg = (lane & 2) ? z4 : xa, xu = (lane & 2) ? xa : z4;
      f32x4 d[8];
#pragma unroll
      for (int c = 0; c < 8; ++c) d[c] = mfma16(xg, wf[c], (f32x4){0.f, 0.f, 0.f, 0.f});
      if (tid == 256) *(u32x4*)(smem + L_MISC + 96) = (u32x4){w0, w1, (unsigned)s0, (unsigned)s1};
      SB0();
#pragma unroll
      for (int c = 0; c < 10; ++c) {
        if (c < 8) d[c & 7] = mfma16(xu, wf[8 + (c & 7)], d[c & 7]);
        if (c >= 2) *(f32x4*)(smem + red_dst + (c - 2) * 128) = d[(c - 2) & 7];
        SB0();
      }
    }
    RAW_BAR();
    if (sw == 0) {
      f32x4 t[8];
#pragma unroll
      for (int p = 0; p < 8; ++p) t[p] = *(const f32x4*)(smem + L_RED + p * 1024 + lane * 16);
      SB0();
      STW(sane(__builtin_bit_cast(u32x4, ((t[0] + t[1]) + (t[2] + t[3])) + ((t[4] + t[5]) + (t[6] + t[7])))), gu_src + kq * 1024);
    } else __builtin_amdgcn_s_sleep(2);
    STAMPT(1, 4, 256);
    {
      u32x4 q[4];
      if (OUTE && grow != (int)key_idx(w0) && grow != (int)key_idx(w1)) { *(u32x4*)(out + ego) = ob[0]; *(u32x4*)(out + ego + 64) = ob[1]; }
      asm volatile("" ::: "memory");
      SLEEP(P_GU);
      SPIN {
        unsigned m = 0;
#pragma unroll
        for (int j = 0; j < 4; ++j) { q[j] = LDW(gu_src + j * 1024); m = chk(m, q[j]); }
        CNT(5);
        if (wave_ok(m)) break;
        asm volatile("" ::: "memory");
      }
      STAMPT(1, 5, 256);
      const f32x4 tot = (__builtin_bit_cast(f32x4, q[0]) + __builtin_bit_cast(f32x4, q[1])) + (__builtin_bit_cast(f32x4, q[2]) + __builtin_bit_cast(f32x4, q[3]));
      const f32x2 gs = unpk(pack2o(tot[0], tot[1])), us = unpk(pack2o(tot[2], tot[3]));
      const f32x2 sg = unpk(pack2o(gs[0] * __builtin_amdgcn_rcpf(1.0f + __expf(-gs[0])), gs[1] * __builtin_amdgcn_rcpf(1.0f + __expf(-gs[1]))));
      const unsigned a2 = pack2o(sg[0] * us[0], sg[1] * us[1]);
      *(u16*)(smem + act_dst) = (u16)a2; *(u16*)(smem + act_dst + 128) = (u16)(a2 >> 16);
      __builtin_amdgcn_fence(__ATOMIC_RELEASE, "wavefront"); __builtin_amdgcn_wave_barrier(); __builtin_amdgcn_fence(__ATOMIC_ACQUIRE, "wavefront");
      const u32x4 aa = *(const u32x4*)(smem + act_src);
      f32x4 d[8];
#pragma unroll
      for (int c = 0; c < 8; ++c) d[c] = mfma16(aa, wf[16 + c], (f32x4){0.f, 0.f, 0.f, 0.f});
      SB0();
      u32x4 y[2];
#pragma unroll
      for (int c = 0; c < 8; ++c) {
        const float t0 = d[c][0] + dppf<DPP_ROR8>(d[c][2]), t1 = d[c][3] + dppf<DPP_ROR8>(d[c][1]);
        y[c >> 2][c & 3] = __float_as_uint((lane & 8) ? t1 : t0);
      }
      STW(sane(y[0]), yp_dst); STW(sane(y[1]), yp_dst + 128);
    }
    STAMPT(1, 6, 256);
    if (!OUTE && grow != (int)key_idx(w0) && grow != (int)key_idx(w1)) { *(u32x4*)(out + ego) = ob[0]; *(u32x4*)(out + ego + 64) = ob[1]; }
  }
}

extern "C" void kernel_launch(void* const* d_in, const int* in_sizes, int n_in, void* d_out, int out_size,
                              void* d_ws, size_t ws_size, hipStream_t stream) {
  (void)hipFuncSetAttribute((const void*)fused, hipFuncAttributeMaxDynamicSharedMemorySize, LDS_TOTAL);
  fused<<<256, 512, LDS_TOTAL, stream>>>((const u16*)d_in[0], (const u16*)d_in[1], (const u16*)d_in[2], (const u16*)d_in[4],
                                         (const u16*)d_in[5], (const u16*)d_in[6], (const u16*)d_in[7], (u16*)d_out, (char*)d_ws);
}
```

```cpp
#include <hip/hip_runtime.h>
#include <stdint.h>

#define LAS __attribute__((address_space(3)))
#define GAS __attribute__((address_space(1)))
#define DI __device__ __forceinline__

typedef unsigned short u16;
typedef unsigned long long u64;
typedef __attribute__((ext_vector_type(2))) __bf16 bf16x2;
typedef __attribute__((ext_vector_type(8))) __bf16 bf16x8;
typedef __attribute__((ext_vector_type(4))) short s16x4;
typedef __attribute__((ext_vector_type(2))) unsigned short u16x2;
typedef __attribute__((ext_vector_type(16))) float f32x16;
typedef __attribute__((ext_vector_type(4))) float f32x4;
typedef __attribute__((ext_vector_type(2))) float f32x2;
typedef __attribute__((ext_vector_type(4))) unsigned u32x4;
typedef __attribute__((ext_vector_type(2))) unsigned u32x2;
typedef GAS unsigned gu32;
typedef GAS unsigned long long gu64;

constexpr int S_ = 2048, D_ = 1024, F_ = 3584;

constexpr unsigned SY_CH = 0, SY_GO = 1024, SY_RE = 2048;
constexpr unsigned WS_FD  = 98304;
constexpr unsigned WS_ZF  = 8192;
constexpr unsigned WS_KVF = 16384;
constexpr unsigned WS_RS  = 131072;
constexpr unsigned WS_CS  = WS_RS + 16 * 16 * 1024;
constexpr unsigned WS_ZP  = WS_CS + 16 * 16 * 768;
constexpr unsigned WS_FB  = WS_ZP + 32 * 16 * 128;
constexpr unsigned WS_ST2 = WS_FB + 32 * 32 * 128;
constexpr unsigned WS_XS  = WS_ST2 + 32 * 8 * 1024;
constexpr unsigned WS_GU  = WS_XS + 64 * 2048;
constexpr unsigned WS_YP  = WS_GU + 256 * 1024;
constexpr unsigned WS_KVP = WS_YP + 2 * 8 * 64 * 512;
constexpr unsigned WS_O   = WS_KVP + 32 * 16 * 4096;
constexpr unsigned WS_END = WS_O + S_ * D_ * 2;
constexpr unsigned OOB = 0x80000000u;
constexpr size_t WS_DIAG  = 64u << 20;
#define CNT(i) do {} while (0)
#define STAMPT(kid, i, T) do {} while (0)

#define TSL(idx) do {} while (0)
#define TSDUMP(k) do {} while (0)
template <int V> struct IC { static constexpr int v = V; };
template <int B, int E, class F> DI void sfor(F&& f) { if constexpr (B < E) { f(IC<B>{}); sfor<B + 1, E>(f); } }

DI float bflo(unsigned u) { return __uint_as_float(u << 16); }
DI float bfhi(unsigned u) { return __uint_as_float(u & 0xffff0000u); }
DI float rbf(float x) { return (float)(__bf16)x; }
DI unsigned pack2(float a, float b) { bf16x2 c; c[0] = (__bf16)a; c[1] = (__bf16)b; return __builtin_bit_cast(unsigned, c); }
DI unsigned pack2o(float a, float b) { unsigned p = pack2(a, b); asm("" : "+v"(p)); return p; }
DI f32x16 mfma32(u32x4 a, u32x4 b, f32x16 c) {
  return __builtin_amdgcn_mfma_f32_32x32x16_bf16(__builtin_bit_cast(bf16x8, a), __builtin_bit_cast(bf16x8, b), c, 0, 0, 0);
}
DI f32x4 mfma16(u32x4 a, u32x4 b, f32x4 c) {
  return __builtin_amdgcn_mfma_f32_16x16x32_bf16(__builtin_bit_cast(bf16x8, a), __builtin_bit_cast(bf16x8, b), c, 0, 0, 0);
}
DI void mfma16i(u32x4 a, u32x4 b, f32x4& c) { asm volatile("v_mfma_f32_16x16x32_bf16 %0, %1, %2, %0" : "+v"(c) : "v"(a), "v"(b)); }
DI f32x2 unpk(unsigned u) { return (f32x2){bflo(u), bfhi(u)}; }
DI u32x2 trread(const char* p) {
  s16x4 v = __builtin_amdgcn_ds_read_tr16_b64_v4i16((LAS s16x4*)p);
  return __builtin_bit_cast(u32x2, v);
}
DI const char* uni(const void* p) {
  const u64 v = (u64)p;
  return (const char*)(((u64)(unsigned)__builtin_amdgcn_readfirstlane((int)(v >> 32)) << 32) | (unsigned)__builtin_amdgcn_readfirstlane((int)v));
}
#define RV4(a, b, c, d) "v" #a, "v" #b, "v" #c, "v" #d
#define RV10(p) "v" #p "0", "v" #p "1", "v" #p "2", "v" #p "3", "v" #p "4", "v" #p "5", "v" #p "6", "v" #p "7", "v" #p "8", "v" #p "9"
#define RSV2 RV4(224, 225, 226, 227), "v228", "v229", RV10(23), RV10(24), RV4(250, 251, 252, 253), "v254", "v255"
#define RSV1 RV4(192, 193, 194, 195), RV4(196, 197, 198, 199), RV10(20), RV10(21), RV4(220, 221, 222, 223), RSV2
#define RSV "v148", "v149", RV10(15), RV10(16), RV10(17), RV10(18), "v190", "v191", RSV1
#define NTW " nt"
#define NTA ""
#define NTB ""
#define NTO ""
#define GL_(v, s, o) "global_load_lds_dwordx4 " v ", " s " offset:" #o
#define GLDS4(SC, CL...) asm volatile("s_add_u32 m0, %5, %c6\n\ts_nop 3\n\t" GL_("%0", "%4", 0) SC "\n\t" GL_("%1", "%4", 1024) SC "\n\t" \
  GL_("%2", "%4", 2048) SC "\n\t" GL_("%3", "%4", 3072) SC :: "v"(v0), "v"(v1), "v"(v2), "v"(v3), "s"(uni(sbase)), "s"(lds), "i"(IMM) : "memory", "scc", ##CL)
#define GLDS3(SC, CL...) asm volatile("s_add_u32 m0, %4, %c5\n\ts_nop 3\n\t" GL_("%0", "%3", 0) SC "\n\t" GL_("%1", "%3", 1024) SC "\n\t" \
  GL_("%2", "%3", 2048) SC :: "v"(v0), "v"(v1), "v"(v2), "s"(uni(sbase)), "s"(lds), "i"(IMM) : "memory", "scc", ##CL)
template <int IMM> DI void glds4(unsigned v0, unsigned v1, unsigned v2, unsigned v3, const void* sbase, unsigned lds) { GLDS4(NTO); }
template <int IMM> DI void glds4o(unsigned v0, unsigned v1, unsigned v2, unsigned v3, const void* sbase, unsigned lds) { GLDS4(NTO, RSV); }
template <int IMM> DI void glds4r(unsigned v0, unsigned v1, unsigned v2, unsigned v3, const void* sbase, unsigned lds) { GLDS4(NTA, RSV); }
template <int IMM> DI void glds4n(unsigned v0, unsigned v1, unsigned v2, unsigned v3, const void* sbase, unsigned lds) { GLDS4(" nt", RSV); }
template <int IMM> DI void glds3n(unsigned v0, unsigned v1, unsigned v2, const void* sbase, unsigned lds) { GLDS3(" nt", RSV); }
template <int IMM> DI void glds3r(unsigned v0, unsigned v1, unsigned v2, const void* sbase, unsigned lds) { GLDS3(NTB, RSV); }
template <int R> DI void sload(unsigned voff, const void* sbase) {
  asm volatile("s_nop 4\n\tglobal_load_dwordx4 v[%c0:%c1], %2, %3 sc1" ::"i"(R), "i"(R + 3), "v"(voff), "s"(uni(sbase)) : "memory", RSV);
}
DI float fadd(float a, float b) { float o; asm("v_add_f32 %0, %1, %2" : "=v"(o) : "v"(a), "v"(b)); return o; }
DI float fmul(float a, float b) { float o; asm("v_mul_f32 %0, %1, %2" : "=v"(o) : "v"(a), "v"(b)); return o; }
DI float fsq(float a, float c) { asm("v_fmac_f32 %0, %1, %1" : "+v"(c) : "v"(a)); return c; }
#define RA_(o, a, b) "v_add_f32 %" #o ", v[%c6+" #a "], v[%c6+" #b "]\n\t"
#define RB2_(o, a) "v_add_f32 %" #o ", v[%c6+" #a "], %" #o "\n\t"
#define RM_(o, a, b) "v_max3_u32 %" #o ", v[%c6+" #a "], v[%c6+" #b "], %" #o "\n\t"
template <int R> DI void rsum4(float* a, unsigned& m, unsigned& n) {
  asm volatile(RA_(0, 0, 4) RA_(1, 1, 5) RM_(4, 0, 1) RA_(2, 2, 6) RA_(3, 3, 7) RM_(5, 2, 3) RB2_(0, 8) RM_(4, 4, 5) RB2_(1, 9) RM_(5, 6, 7)
               RB2_(2, 10) RM_(4, 8, 9) RB2_(3, 11) RM_(5, 10, 11) RB2_(0, 12) RB2_(1, 13) RM_(4, 12, 13) RB2_(2, 14) RB2_(3, 15) RM_(5, 14, 15)
               : "=&v"(a[0]), "=&v"(a[1]), "=&v"(a[2]), "=&v"(a[3]), "+v"(m), "+v"(n) : "i"(R) : RSV);
}
template <int R> DI void wload2(unsigned voff, const void* sbase) {
  asm volatile("s_nop 4\n\tglobal_load_dwordx4 v[%c0:%c1], %2, %3" NTW ::"i"(R), "i"(R + 3), "v"(voff), "s"(uni(sbase)) : "memory", RSV2);
}
template <int R> DI void wload(unsigned voff, const void* sbase) {
  asm volatile("s_nop 4\n\tglobal_load_dwordx4 v[%c0:%c1], %2, %3" NTW ::"i"(R), "i"(R + 3), "v"(voff), "s"(uni(sbase)) : "memory", RSV);
}
template <int RA, int RB> DI unsigned wperm(unsigned sel) {
  unsigned o;
  if (RA < 192) asm volatile("v_perm_b32 %0, v%c2, v%c1, %3" : "=v"(o) : "i"(RA), "i"(RB), "s"(sel) : RSV);
  else if (RA < 224) asm volatile("v_perm_b32 %0, v%c2, v%c1, %3" : "=v"(o) : "i"(RA), "i"(RB), "s"(sel) : RSV1);
  else asm volatile("v_perm_b32 %0, v%c2, v%c1, %3" : "=v"(o) : "i"(RA), "i"(RB), "s"(sel) : RSV2);
  return o;
}
template <int R> DI unsigned rget() { unsigned o; asm volatile("v_mov_b32 %0, v%c1" : "=v"(o) : "i"(R) : RSV); return o; }
DI float xh_sum(float v) {
  auto r = __builtin_amdgcn_permlane32_swap(__float_as_uint(v), __float_as_uint(v), false, false);
  return __uint_as_float(r[0]) + __uint_as_float(r[1]);
}
template <int CTRL> DI unsigned dppu(unsigned v) { return (unsigned)__builtin_amdgcn_update_dpp(0, (int)v, CTRL, 0xf, 0xf, true); }
template <int CTRL> DI float dppf(float v) { return __uint_as_float(dppu<CTRL>(__float_as_uint(v))); }
constexpr int DPP_X1 = 0xB1, DPP_X2 = 0x4E, DPP_HMIR = 0x141, DPP_MIR = 0x140, DPP_ROR8 = 0x128;
DI float quad_sum(float v) { v += dppf<DPP_X1>(v); v += dppf<DPP_X2>(v); return v; }
DI float oct_sum(float v) { v = quad_sum(v); v += dppf<DPP_HMIR>(v); return v; }
DI float x16_sum(float v) {
  auto r = __builtin_amdgcn_permlane16_swap(__float_as_uint(v), __float_as_uint(v), false, false);
  return __uint_as_float(r[0]) + __uint_as_float(r[1]);
}
DI float sw16(float a, float b) {
  auto r = __builtin_amdgcn_permlane16_swap(__float_as_uint(a), __float_as_uint(b), false, false);
  return fadd(__uint_as_float(r[0]), __uint_as_float(r[1]));
}
DI float sw32(float a, float b) {
  auto r = __builtin_amdgcn_permlane32_swap(__float_as_uint(a), __float_as_uint(b), false, false);
  return fadd(__uint_as_float(r[0]), __uint_as_float(r[1]));
}
DI float hi_sum(float v) {
  auto r = __builtin_amdgcn_permlane16_swap(__float_as_uint(v), __float_as_uint(v), false, false);
  v = fadd(__uint_as_float(r[0]), __uint_as_float(r[1]));
  auto q = __builtin_amdgcn_permlane32_swap(__float_as_uint(v), __float_as_uint(v), false, false);
  return fadd(__uint_as_float(q[0]), __uint_as_float(q[1]));
}
template <int N> DI void phi_n(const f32x2* t, unsigned* p) {
  unsigned tb[N]; f32x2 tr[N], e[N];
#pragma unroll
  for (int i = 0; i < N; ++i) tb[i] = pack2o(t[i][0], t[i][1]);
  __builtin_amdgcn_sched_barrier(0);
#pragma unroll
  for (int i = 0; i < N; ++i) tr[i] = unpk(tb[i]);
  __builtin_amdgcn_sched_barrier(0);
#pragma unroll
  for (int i = 0; i < N; ++i) e[i] = tr[i] * 1.44269504088896340736f;
  __builtin_amdgcn_sched_barrier(0);
  u16x2 tm[2] = {{0, 0}, {0, 0}};
#pragma unroll
  for (int i = 0; i < N; ++i) {
    e[i][0] = __builtin_amdgcn_fmed3f(__builtin_amdgcn_exp2f(e[i][0]), 0.f, 1.f);
    tm[i & 1] = __builtin_elementwise_max(tm[i & 1], __builtin_bit_cast(u16x2, tb[i]));
    e[i][1] = __builtin_amdgcn_fmed3f(__builtin_amdgcn_exp2f(e[i][1]), 0.f, 1.f);
    __builtin_amdgcn_sched_barrier(0);
  }
#pragma unroll
  for (int i = 0; i < N; ++i) tr[i] += e[i];
  __builtin_amdgcn_sched_barrier(0);
#pragma unroll
  for (int i = 0; i < N; ++i) { tr[i][0] = fmaxf(tr[i][0], e[i][0]); tr[i][1] = fmaxf(tr[i][1], e[i][1]); }
  const u16x2 tx = __builtin_elementwise_max(tm[0], tm[1]);
  __builtin_amdgcn_sched_barrier(0);
#pragma unroll
  for (int i = 0; i < N; ++i) p[i] = pack2(tr[i][0], tr[i][1]);
  if (__any(tx[0] > 0xC0F0 || tx[1] > 0xC0F0)) {
#pragma unroll
    for (int i = 0; i < N; ++i) p[i] = pack2(bflo(p[i]) + 1e-6f, bfhi(p[i]) + 1e-6f);
  }
}
#define RLX_AGENT __ATOMIC_RELAXED, __HIP_MEMORY_SCOPE_AGENT
DI void st32(char* ws, unsigned off, unsigned v) { __hip_atomic_store((gu32*)(ws + off), v, RLX_AGENT); }
DI void st64(char* ws, unsigned off, u64 v) { __hip_atomic_store((gu64*)(ws + off), v, RLX_AGENT); }
DI unsigned ld32(char* ws, unsigned off) { return __hip_atomic_load((gu32*)(ws + off), RLX_AGENT); }
DI u64 ld64(char* ws, unsigned off) { return __hip_atomic_load((gu64*)(ws + off), RLX_AGENT); }
#define WAIT_V(n) asm volatile("s_waitcnt vmcnt(%c0)" ::"i"(n) : "memory")
#define RAW_BAR() do { asm volatile("s_waitcnt lgkmcnt(0)" ::: "memory"); __builtin_amdgcn_s_barrier(); asm volatile("" ::: "memory"); } while (0)
#define SB0() __builtin_amdgcn_sched_barrier(0)
#define WAIT_VR(n) asm volatile("s_waitcnt vmcnt(%c0)" ::"i"(n) : "memory", RSV)
#define RAW_BARR() do { asm volatile("s_waitcnt lgkmcnt(0)" ::: "memory", RSV); __builtin_amdgcn_s_barrier(); asm volatile("" ::: "memory", RSV); } while (0)
#define KEEP2() asm volatile("" ::: "memory", RSV2)
#define RAW_BAR2() do { asm volatile("s_waitcnt lgkmcnt(0)" ::: "memory", RSV2); __builtin_amdgcn_s_barrier(); KEEP2(); } while (0)
constexpr unsigned SPIN_CAP = 1u << 18;
#define SPIN _Pragma("nounroll") for (unsigned s_ = 0; s_ < SPIN_CAP; ++s_)
constexpr unsigned SENT = 0xFFFFFFFFu;
DI unsigned sane1(unsigned v) { return min(v, 0xFFFFFFFEu); }
DI u32x4 sane(u32x4 v) { v[1] = sane1(v[1]); v[3] = sane1(v[3]); return v; }
DI unsigned chk(unsigned m, u32x4 v) { return max(max(m, v[1]), v[3]); }
DI bool wave_ok(unsigned m) { return !__any(m == SENT); }
DI unsigned key_idx(unsigned k) { return (0xFFFFu - (k & 0xFFFFu)) & (S_ - 1); }
DI float key_val(unsigned k) { const unsigned o = k >> 16; return __uint_as_float(((o & 0x8000u) ? (o & 0x7fffu) : (~o & 0xffffu)) << 16); }
DI void top2_mix(unsigned& b0, unsigned& b1, unsigned c0, unsigned c1) {
  const unsigned lo = min(b0, c0), mx = max(b1, c1);
  b0 = max(b0, c0); b1 = max(lo, mx);
}
DI void top2_lo(unsigned& b0, unsigned& b1) {
  top2_mix(b0, b1, dppu<DPP_X1>(b0), dppu<DPP_X1>(b1));
  top2_mix(b0, b1, dppu<DPP_X2>(b0), dppu<DPP_X2>(b1));
  top2_mix(b0, b1, dppu<DPP_HMIR>(b0), dppu<DPP_HMIR>(b1));
}
DI void top2_mid(unsigned& b0, unsigned& b1) {
  top2_mix(b0, b1, dppu<DPP_MIR>(b0), dppu<DPP_MIR>(b1));
  auto p = __builtin_amdgcn_permlane16_swap(b0, b0, false, false); auto q = __builtin_amdgcn_permlane16_swap(b1, b1, false, false);
  b0 = p[0]; b1 = q[0]; top2_mix(b0, b1, p[1], q[1]);
}
DI void top2_hi(unsigned& b0, unsigned& b1) {
  top2_mid(b0, b1);
  auto p = __builtin_amdgcn_permlane32_swap(b0, b0, false, false); auto q = __builtin_amdgcn_permlane32_swap(b1, b1, false, false);
  b0 = p[0]; b1 = q[0]; top2_mix(b0, b1, p[1], q[1]);
}

constexpr int K1_STAGE = 40960;
constexpr int LDS_TOTAL = 4 * K1_STAGE;
constexpr int L_STM = 0;
constexpr int L_MISC = 2048;
constexpr int L_XK = 5120;
constexpr int L_KX = 3328;
constexpr int L_ACT = 2304;
constexpr int L_CAND = 4096;
constexpr int L_WO = 16384;
constexpr int L_P = 16384, L_PK = 7 * 16384;
constexpr int L_RED = 65536;
constexpr int wo_slot(int j) { return L_WO + (j % 9) * 16384; }
constexpr int CODE_LINES = 10;
#define OW_ 8
constexpr int OW = OW_;
#define W1A_ 16
constexpr int W1A = W1A_;
#define PRIO_M 1
#define PRIO_S 3
#define GSCHED 5, 9, 13, 17, 21, 24, 27, 30, 33, 36, 39, 42, 45, 48, 48
constexpr int GS[15] = {GSCHED};
constexpr int gops(int g) { return (g / 3) * 10 + (g % 3 == 0 ? 0 : g % 3 == 1 ? 4 : 7); }
constexpr bool gs_ok() {
  bool ok = GS[0] >= 3 && GS[1] >= 6 && GS[1] <= 12 && GS[0] <= GS[1] && GS[12] == 45 && GS[13] == 48 && GS[14] == 48;
  for (int t = 0; t < 12; ++t) ok = ok && GS[t + 2] <= 3 * (t + 5) && GS[t + 2] >= 3 * (t + 3) && GS[t + 2] >= GS[t + 1] + 3;
  return ok;
}
static_assert(gs_ok(), "staging schedule");
#define WSCHED 0, 0, 0, 0, 0, 0, 0, 0, 0, 2, 4, 6, 8
constexpr int WS_[13] = {WSCHED};
#define PACE 1
#define PSLP 0
#define P_KVP 6
#define L_KVP 0
#define P_KVF 12
#define L_KVF 0
#define P_FB 8
#define L_FB 0
#define P_C 14
#define P_D 2
#define L_D 0
#define P_GU 12
#define P_E 10
#define L_E 0
#define SLEEP(n) do { if (n) __builtin_amdgcn_s_sleep(n); } while (0)
#define OUTE 1
#define WROT (4 * rp + (xcd >> 1))
#define WDIR ((xcd & 1) ? -1 : 1)
#define OUTWT 0
#define OUTWB 0
#define PFO 0
constexpr int VW = 160;

__global__ __launch_bounds__(512, 2) void fused(const u16* __restrict__ x, const u16* __restrict__ wqkv,
                                                const u16* __restrict__ wo, const u16* __restrict__ w_r,
                                                const u16* __restrict__ wg, const u16* __restrict__ wu,
                                                const u16* __restrict__ wd, u16* __restrict__ out, char* __restrict__ ws) {
  extern __shared__ __attribute__((aligned(16))) char smem[];
  STAMPT(0, 0, 0);
  u64 pc0; asm volatile("s_getpc_b64 %0" : "=s"(pc0));
  const int wave = __builtin_amdgcn_readfirstlane(threadIdx.x >> 6);
  const unsigned lds0 = (unsigned)(size_t)smem;
#define RB_(b) (((b) & 1) * 8 + (((b) >> 3) & 7))
#define HP_(b) ((((b) & 7) >> 1) * 4 + ((b) >> 6))
#define KROT_(b) ((4 * ((HP_(b) >> 2) + (RB_(b) >> 3) + 2 * ((RB_(b) >> 2) & 1))) & 15)
  unsigned voA0 = 0, voA1 = 0, voA2 = 0, voA3 = 0, voB0 = 0, voB1 = 0, voB2 = 0, voB3 = 0, voB4 = 0, voB5 = 0;
  const unsigned l4 = __builtin_amdgcn_readfirstlane(lds0 + (wave - 4) * 4096), l6 = __builtin_amdgcn_readfirstlane(lds0 + (wave - 4) * 6144);
  const int krot0 = KROT_(blockIdx.x);
  auto group = [&](auto G) {
    constexpr int t = G.v / 3, p = G.v % 3, sl = (t & 3) * K1_STAGE;
    const int kt = (t + krot0) & 15;
    if (p == 0) glds4r<sl>(voA0, voA1, voA2, voA3, (const char*)x + kt * 128, l4);
    else if (p == 1) glds3r<sl + 16384>(voB0, voB1, voB2, (const char*)wqkv + kt * 64 * (3 * D_ * 2), l6);
    else glds3r<sl + 16384 + 3072>(voB3, voB4, voB5, (const char*)wqkv + kt * 64 * (3 * D_ * 2), l6);
  };
  if (wave >= 4) {
    const int lane = threadIdx.x & 63, sw = wave - 4;
    const int r0 = RB_(blockIdx.x) * 128 + 32 * sw + (lane >> 3);
    voA0 = (unsigned)(r0 * D_ + ((lane & 7) ^ (lane >> 4)) * 8) * 2u;
    voA1 = ((voA0 + 8 * D_ * 2) ^ 64) - 1024; voA2 = voA0 + 16 * D_ * 2 - 2048; voA3 = voA1 + 16 * D_ * 2 - 2048;
    voB0 = (unsigned)((16 * sw + 4 * (lane >> 5) + ((lane >> 2) & 3)) * (3 * D_) + HP_(blockIdx.x) * 64 + ((lane >> 4) & 1) * 32 + (lane & 3) * 8) * 2u;
    voB1 = voB0 + 1024; voB2 = voB0 + 2048;
    voB3 = (voB0 ^ 32) + 8 * 3 * D_ * 2; voB4 = voB3 + 1024; voB5 = voB3 + 2048;
    asm volatile("" : "+v"(voA0), "+v"(voA1), "+v"(voA2), "+v"(voA3), "+v"(voB0), "+v"(voB1), "+v"(voB2), "+v"(voB3), "+v"(voB4), "+v"(voB5));
    sfor<0, 3>(group);
  }
  int bid = blockIdx.x; asm volatile("" : "+s"(bid));
  const int xcd = bid & 7, idx = bid >> 3;
  const int rb = RB_(bid), hp = HP_(bid);
  const int rp = xcd * 4 + (idx >> 3), cb = idx & 7;
  const int hs_ = (hp + 4 + (int)(((size_t)wqkv >> 7) & 7)) & 7;
  const int g8 = ((hp >> 3) * 4 + (rb >> 2)) * 8 + hs_, kq = rb & 3;
  const int krot = KROT_(bid), posA = 1 + (hp & 3), posB = 1 + (rb & 3);
  const int wrot = WROT, wdir = WDIR;
  auto whead = [&](int t) { return (wrot + wdir * t) & 15; };
  const unsigned rs_slot = WS_RS + (rb * 16 + ((krot + posA) & 15)) * 1024, cs_slot = WS_CS + (hp * 16 + ((krot + posB) & 15)) * 768;
  const __amdgpu_buffer_rsrc_t wsr = __builtin_amdgcn_make_buffer_rsrc(ws, 0, WS_END, 0x00020000);
#define LDW(off) __builtin_amdgcn_raw_buffer_load_b128(wsr, (off), 0, 16)
#define STW(v, off) __builtin_amdgcn_raw_buffer_store_b128((v), wsr, (off), 0, 16)
#define STW1(v, off) __builtin_amdgcn_raw_buffer_store_b32((v), wsr, (off), 0, 16)
  float* stm = (float*)(smem + L_STM);
  volatile LAS int* misc = (volatile LAS int*)(smem + L_MISC);
#define LANES() int tid = threadIdx.x; asm volatile("" : "+v"(tid)); const int lane = tid & 63, r = lane & 31, h = lane >> 5; (void)r; (void)h
#define EPI() const int erow = tid >> 3, eseg = tid & 7; const size_t ego = (size_t)(rp * 64 + erow) * D_ + cb * 128 + eseg * 8
  u32x4 xv[2], wv[2];
  f32x2 xf[8], wf2[8];
  float sw_;
  auto unpack_xw = [&]() {
    f32x2 a = {0.f, 0.f};
#pragma unroll
    for (int i = 0; i < 8; ++i) { xf[i] = unpk(xv[i >> 2][i & 3]); wf2[i] = unpk(wv[i >> 2][i & 3]); a += wf2[i]; }
    sw_ = oct_sum(a[0] + a[1]);
#pragma unroll
    for (int i = 0; i < 8; ++i) asm volatile("" : "+v"(xf[i]), "+v"(wf2[i]));
    asm volatile("" : "+v"(sw_));
  };
  unsigned tsv = 0; (void)tsv;
  u32x4 wf[24];

  auto wtr = [&](auto I) {
    constexpr int m = I.v >> 3, c = I.v & 7, R = VW + 32 * m + (c >> 1);
    const unsigned sel = (c & 1) ? 0x07060302u : 0x05040100u;
    wf[I.v] = (u32x4){wperm<R, R + 4>(sel), wperm<R + 8, R + 12>(sel), wperm<R + 16, R + 20>(sel), wperm<R + 24, R + 28>(sel)};
  };

  if (wave < 4) {
    LANES();
    const int c16 = lane & 15, g4 = lane >> 4;
    const int trl = (c16 >> 2) * 64 + (c16 & 3) * 8;
    const int wr = wave >> 1, wc = wave & 1;
    const int head = 2 * hp + wc;
    const unsigned kvp_off = WS_KVP + (head * 16 + rb) * 4096 + wr * 2048 + lane * 16;
    const unsigned zp_off = WS_ZP + (head * 16 + rb) * 128;
    const unsigned o_off = WS_O + ((rb * 4 + 2 * wr) * 32 + head) * 2048 + lane * 16;
    const unsigned fb_off = lane < 8 ? WS_FB + ((rb * 2 + wr) * 32 + head) * 128 + lane * 16 : OOB;
    unsigned pfa, pfb = OOB;
    unsigned pfc = lane < 8 ? fb_off : lane < 24 ? rs_slot + wave * 256 + (lane - 8) * 16
        : lane < 40 ? (wave < 3 ? cs_slot + wave * 256 : WS_KVF + hp * 4096 + rb * 256) + (lane - 24) * 16
        : lane < 48 ? (wave < 2 ? zp_off + (lane & 7) * 16 : (wave == 2 && rb < 2) ? WS_ZF + (2 * hp + rb) * 128 + (lane & 7) * 16 : OOB) : OOB;
    if (wave == 0) pfa = WS_ST2 + (rp * 8 + cb) * 1024 + lane * 16;
    else if (wave == 1) pfa = WS_GU + (g8 * 4 + kq) * 1024 + lane * 16;
    else pfa = WS_YP + (((wave - 2) * 8 + 2 * kq + (lane >> 5)) * 64 + g8) * 512 + (lane & 31) * 16;
    if (wave == 0) pfb = lane < 32 ? WS_XS + (rp * 2 + (lane >> 4)) * 2048 + cb * 256 + (lane & 15) * 16 : lane < 40 ? WS_FD + (cb * 32 + rp) * 128 + (lane & 7) * 16 : OOB;
    u32x4 S4 = {SENT, SENT, SENT, SENT};
    asm volatile("" : "+v"(pfa), "+v"(pfb), "+v"(pfc), "+v"(S4));
    __builtin_amdgcn_s_setprio(PRIO_M);
    f32x4 acc[4][6];
    {
      int ao[2][2], bo[2][2];
#pragma unroll
      for (int i = 0; i < 2; ++i) {
        ao[0][i] = (64 * wr + c16) * 128 + (((4 * i + g4) ^ (c16 >> 1)) * 16); ao[1][i] = ao[0][i] + 2 * K1_STAGE;
        bo[0][i] = 16384 + g4 * 3072 + wc * 256 + trl + ((i ^ (g4 & 1)) * 32); bo[1][i] = bo[0][i] + 2 * K1_STAGE;
        asm volatile("" : "+v"(ao[0][i]), "+v"(ao[1][i]), "+v"(bo[0][i]), "+v"(bo[1][i]));
      }
#pragma unroll
      for (int i = 0; i < 4; ++i)
#pragma unroll
        for (int j = 0; j < 6; ++j) { acc[i][j] = (f32x4){0.f, 0.f, 0.f, 0.f}; asm volatile("" : "+v"(acc[i][j])); }
      struct Frag { u32x4 a[4]; u32x2 bl[6], bh[6]; };
      Frag FA, FB;
#define K1_B(j) ((u32x4){C.bl[j][0], C.bl[j][1], C.bh[j][0], C.bh[j][1]})
#define K1_LA(i) N.a[i] = *(const u32x4*)(smem + ao[sn >> 1][hf] + (sn & 1) * K1_STAGE + (i) * 2048)
#define K1_LL(j) N.bl[j] = trread(smem + bo[sn >> 1][(j) & 1] + (sn & 1) * K1_STAGE + hf * 12288 + ((j) >> 1) * 1024)
#define K1_LH(j) N.bh[j] = trread(smem + bo[sn >> 1][(j) & 1] + (sn & 1) * K1_STAGE + hf * 12288 + ((j) >> 1) * 1024 + 512)
#define K1_M(i, j) if ((j) < 2) mfma16i(K1_B(j), C.a[i], acc[i][j]); else mfma16i(C.a[i], K1_B(j), acc[i][j]); SB0()
#define K1_Q(i, j, ld) K1_M(i, j); ld; SB0()
      auto sub = [&](Frag& C, Frag& N, int sn, int hf) {
        K1_Q(0, 0, K1_LL(0)); K1_Q(1, 0, K1_LH(0)); K1_Q(0, 1, K1_LA(0)); K1_Q(1, 1, K1_LA(1)); K1_Q(0, 2, K1_LL(1)); K1_Q(1, 2, K1_LH(1));
        K1_Q(0, 3, K1_LL(2)); K1_Q(1, 3, K1_LH(2)); K1_Q(2, 0, K1_LL(3)); K1_Q(3, 0, K1_LH(3)); K1_Q(2, 1, K1_LA(2)); K1_Q(3, 1, K1_LA(3));
        K1_Q(2, 2, K1_LL(4)); K1_Q(3, 2, K1_LH(4)); K1_Q(2, 3, K1_LL(5)); K1_Q(3, 3, K1_LH(5));
        K1_M(0, 4); K1_M(1, 4); K1_M(2, 4); K1_M(3, 4); K1_M(0, 5); K1_M(1, 5); K1_M(2, 5);
        asm volatile("s_waitcnt lgkmcnt(0)" : "+v"(N.bh[5]));
        K1_M(3, 5);
      };
      RAW_BAR();
      STAMPT(0, 1, 0);
      {
        Frag& N = FA; const int sn = 0, hf = 0;
        K1_LL(0); K1_LH(0); K1_LA(0); K1_LA(1); K1_LL(1); K1_LH(1); K1_LL(2); K1_LH(2); K1_LL(3); K1_LH(3); K1_LA(2); K1_LA(3); K1_LL(4); K1_LH(4); K1_LL(5); K1_LH(5);
      }
#pragma unroll 1
      for (int it = 0; it < 4; ++it) {
#pragma unroll
        for (int s = 0; s < 4; ++s) {
          sub(FA, FB, s, 1);
          if (s == 0) WAIT_V(0);
          if (it == 0) { if (s == PFO) STW(S4, pfc); if (s == PFO + 1) STW(S4, kvp_off); if (s == PFO + 2) STW(S4, kvp_off + 1024); }
          if (it == 1) { if (s == 0) STW(S4, pfa); if (s == 1) STW(S4, pfb); }
          TSL(it * 4 + s);
          if (!(s & 1) || it < 3) RAW_BAR();
          else if (s == 3) { asm volatile("s_waitcnt lgkmcnt(0)" ::: "memory"); misc[8 + wave] = 3; }
          sub(FB, FA, (s + 1) & 3, 0);
        }
      }
    }
    asm volatile("s_nop 15" ::: "memory");
    TSL(16); if (wave == 0) TSDUMP(0);
    STAMPT(0, 2, 0);
    {
      f32x4 m4[4], i4[4]; float cs[4];
      SPIN {
        const int ok = misc[12 + (lane & 3)];
        asm volatile("" ::: "memory");
#pragma unroll
        for (int i = 0; i < 4; ++i) {
          cs[i] = stm[256 + wc * 96 + 32 + 16 * i + c16];
          m4[i] = *(const f32x4*)(stm + 64 * wr + 16 * i + 4 * g4); i4[i] = *(const f32x4*)(stm + 128 + 64 * wr + 16 * i + 4 * g4);
        }
        SB0();
        if (__all(ok != 0)) break;
      }
      const u32x4 ONES4 = {0x3F803F80u, 0x3F803F80u, 0x3F803F80u, 0x3F803F80u};
      f32x2 tk[16], tv[16];
#define PR(v, i) ((f32x2){(v)[2 * (i)], (v)[2 * (i) + 1]})
#pragma unroll
      for (int i = 0; i < 16; ++i) {
        tk[i] = PR(acc[(i >> 1) & 3][2 + (i >> 3)], i & 1) - PR(m4[(i >> 1) & 3], i & 1) * cs[i >> 3];
        tv[i] = PR(acc[(i >> 1) & 3][4 + (i >> 3)], i & 1) - PR(m4[(i >> 1) & 3], i & 1) * cs[2 + (i >> 3)];
      }
      SB0();
#pragma unroll
      for (int i = 0; i < 16; ++i) { tk[i] *= PR(i4[(i >> 1) & 3], i & 1); tv[i] *= PR(i4[(i >> 1) & 3], i & 1); }
      SB0();
      unsigned kp[16], vp[16];
#pragma unroll
      for (int i = 0; i < 16; ++i) vp[i] = pack2o(tv[i][0], tv[i][1]);
      phi_n<16>(tk, kp);
#define F4_(p, i) ((u32x4){(p)[4 * (i)], (p)[4 * (i) + 1], (p)[4 * (i) + 2], (p)[4 * (i) + 3]})
      const f32x4 Z4 = {0.f, 0.f, 0.f, 0.f};
      u32x4 vk[2], vg[2];
#pragma unroll
      for (int j = 0; j < 2; ++j) { vk[j] = wr ? F4_(vp, 2 + j) : F4_(vp, j); vg[j] = wr ? F4_(vp, j) : F4_(vp, 2 + j); }
      f32x4 keep[2], give[2], zq[2];
#pragma unroll
      for (int d = 0; d < 2; ++d) { give[d] = mfma16(F4_(kp, 2 * d), vg[0], Z4); zq[d] = mfma16(ONES4, F4_(kp, 2 * d), Z4); }
#pragma unroll
      for (int d = 0; d < 2; ++d) { give[d] = mfma16(F4_(kp, 2 * d + 1), vg[1], give[d]); zq[d] = mfma16(ONES4, F4_(kp, 2 * d + 1), zq[d]); }
#pragma unroll
      for (int d = 0; d < 2; ++d) keep[d] = mfma16(F4_(kp, 2 * d), vk[0], Z4);
#pragma unroll
      for (int d = 0; d < 2; ++d) keep[d] = mfma16(F4_(kp, 2 * d + 1), vk[1], keep[d]);
      SB0();
      const float zv = (lane & 16) ? zq[1][0] : zq[0][0];
      char* xk = smem + L_XK + wave * 2304 + lane * 16;
      const char* xp = smem + L_XK + (wave ^ 2) * 2304 + lane * 16;
      *(f32x4*)xk = give[0]; *(f32x4*)(xk + 1024) = give[1]; *(float*)(xk + 2048 - lane * 12) = zv;
      asm volatile("" ::: "memory");
      misc[16 + wave] = 1;
      f32x4 p0, p1; float pz;
      SPIN {
        const int ok = misc[16 + (wave ^ 2)];
        asm volatile("" ::: "memory");
        p0 = *(const f32x4*)xp; p1 = *(const f32x4*)(xp + 1024); pz = *(const float*)(xp + 2048 - lane * 12);
        SB0();
        if (ok == 1) break;
      }
      STW(sane(__builtin_bit_cast(u32x4, keep[0] + p0)), kvp_off);
      STW(sane(__builtin_bit_cast(u32x4, keep[1] + p1)), kvp_off + 1024);
      STW1(sane1(__float_as_uint(zv + pz)), (lane < 32 && wr == 0) ? zp_off + lane * 4 : OOB);
    }
    STAMPT(0, 3, 0);
    unsigned qp[16];
    const unsigned po = WS_KVP + ((2 * hp + (rb >> 3)) * 16 + h) * 4096 + (2 * ((rb >> 2) & 1) + ((lane >> 4) & 1)) * 1024 + (16 * (rb & 3) + (lane & 15)) * 16;
    u32x4 pv[8] = {};
    {
      f32x4 c4[2]; float m_b[4], rinv[4];
#pragma unroll
      for (int i = 0; i < 2; ++i) c4[i] = *(const f32x4*)(stm + 256 + wc * 96 + 16 * i + 4 * g4);
#pragma unroll
      for (int i = 0; i < 4; ++i) { m_b[i] = stm[64 * wr + 16 * i + c16]; rinv[i] = stm[128 + 64 * wr + 16 * i + c16]; }
      SB0();
      f32x2 tq[16];
#pragma unroll
      for (int i = 0; i < 16; ++i) tq[i] = PR(acc[i >> 2][(i >> 1) & 1], i & 1) - PR(c4[(i >> 1) & 1], i & 1) * m_b[i >> 2];
      SB0();
#pragma unroll
      for (int i = 0; i < 16; ++i) tq[i] *= rinv[i >> 2];
      SB0();
      phi_n<16>(tq, qp);
#pragma unroll
      for (int i = 0; i < 16; ++i) asm volatile("" : "+v"(qp[i]));
      if (wave == 0) {
        SLEEP(P_KVP);
#pragma unroll
        for (int p = 0; p < 8; ++p) pv[p] = LDW(po + p * 8192);
      }
      asm volatile("" ::: "memory");
    }
    STAMPT(0, 4, 0);
    if (wave == 1 && rb < 2) {
      SPIN {
        unsigned m = 0;
#pragma unroll
        for (int p = 0; p < 2; ++p) { pv[p] = LDW(WS_ZP + (2 * hp + rb) * 2048 + p * 1024 + lane * 16); m = chk(max(max(m, pv[p][0]), pv[p][2]), pv[p]); }
        if (wave_ok(m)) break;
        asm volatile("" ::: "memory");
      }
      f32x4 s = __builtin_bit_cast(f32x4, pv[0]) + __builtin_bit_cast(f32x4, pv[1]);
#pragma unroll
      for (int i = 0; i < 4; ++i) { s[i] += dppf<DPP_ROR8>(s[i]); s[i] = xh_sum(x16_sum(s[i])); }
      if (lane < 16) st64(ws, WS_ZF + (2 * hp + rb) * 128 + (lane >> 3) * 64 + (lane & 3) * 16 + ((lane >> 2) & 1) * 8,
                          ((u64)sane1(pack2(s[2], s[3])) << 32) | pack2(s[0], s[1]));
    }
    if (wave == 0) {
      SPIN {
        unsigned m = 0;
#pragma unroll
        for (int p = 0; p < 8; ++p) m = chk(m, pv[p]);
        CNT(0);
        if (wave_ok(m)) break;
        asm volatile("" ::: "memory");
        SLEEP(L_KVP);
#pragma unroll
        for (int p = 0; p < 8; ++p) pv[p] = LDW(po + p * 8192);
      }
      f32x4 s = __builtin_bit_cast(f32x4, pv[0]);
#pragma unroll
      for (int p = 1; p < 8; ++p) s += __builtin_bit_cast(f32x4, pv[p]);
      const unsigned p0 = pack2(xh_sum(s[0]), xh_sum(s[1])), p1 = pack2(xh_sum(s[2]), xh_sum(s[3]));
      auto a = __builtin_amdgcn_permlane16_swap(p0, p0, false, false);
      auto b = __builtin_amdgcn_permlane16_swap(p1, p1, false, false);
      STW(sane((u32x4){a[0], b[0], a[1], b[1]}), lane < 16 ? WS_KVF + hp * 4096 + rb * 256 + lane * 16 : OOB);
      STAMPT(1, 2, 0);
      misc[7] = 1;
    } else {
      SPIN { if (misc[7] == 1) break; __builtin_amdgcn_s_sleep(1); }
    }
    SLEEP(P_KVF);
    {
      u32x4 k0, k1, z0;
      SPIN {
        k0 = LDW(WS_KVF + head * 2048 + lane * 16); k1 = LDW(WS_KVF + head * 2048 + 1024 + lane * 16);
        z0 = LDW(WS_ZF + head * 128 + g4 * 16);
        CNT(1);
        if (wave_ok(chk(chk(chk(0u, k0), k1), z0))) break;
        asm volatile("" ::: "memory");
        SLEEP(L_KVF);
      }
      STAMPT(0, 5, 0);
      const f32x4 Z4 = {0.f, 0.f, 0.f, 0.f};
      f32x4 X[8], Dn[4];
#pragma unroll
      for (int i = 0; i < 4; ++i) Dn[i] = mfma16(z0, F4_(qp, i), Z4);
#pragma unroll
      for (int i = 0; i < 8; ++i) X[i] = mfma16((i & 1) ? k1 : k0, F4_(qp, i >> 1), Z4);
      SB0();
      float ri[4]; unsigned a[16]; f32x2 f[16];
#pragma unroll
      for (int i = 0; i < 4; ++i) ri[i] = __builtin_amdgcn_rcpf(rbf(rbf(Dn[i][0]) + 1e-6f));
      SB0();
#pragma unroll
      for (int i = 0; i < 16; ++i) a[i] = pack2o(X[i >> 1][2 * (i & 1)], X[i >> 1][2 * (i & 1) + 1]);
      SB0();
#pragma unroll
      for (int i = 0; i < 16; ++i) f[i] = unpk(a[i]);
      SB0();
#pragma unroll
      for (int i = 0; i < 16; ++i) f[i] *= ri[i >> 2];
      SB0();
#pragma unroll
      for (int i = 0; i < 4; ++i) {
        STW(((u32x4){pack2(f[4 * i][0], f[4 * i][1]), pack2(f[4 * i + 1][0], f[4 * i + 1][1]), pack2(f[4 * i + 2][0], f[4 * i + 2][1]), pack2(f[4 * i + 3][0], f[4 * i + 3][1])}),
            o_off + (i >> 1) * 65536 + (i & 1) * 1024);
        SB0();
      }
    }
    STAMPT(0, 6, 0);
    {
      EPI();
      const int rt = wave & 1, kh = wave >> 1;
      const unsigned ob = WS_O + ((rp * 2 + rt) * 32 + 16 * kh) * 2048 + lane * 16;
#define LDO(t, st) __builtin_amdgcn_raw_buffer_load_b128(wsr, ob + (st) * 1024, whead(t) * 2048, 16)
      int woff[2];
#pragma unroll
      for (int i = 0; i < 2; ++i) woff[i] = kh * 8192 + (g4 >> 1) * 2048 + (g4 & 1) * 512 + trl + ((i ^ (g4 & 1)) * 32);
      struct Frag2 { u32x2 lo[8], hi[8]; };
      Frag2 G[2];
      auto ld2 = [&](Frag2& N, int sn, int nt, int hi) {
        const char* p = smem + sn + woff[nt & 1] + (nt >> 2) * 1024 + ((nt >> 1) & 1) * 256;
        if (hi) N.hi[nt] = trread(p + 4096); else N.lo[nt] = trread(p);
      };
      WAIT_V(0);
      STW(((u32x4){1u, 1u, 1u, 1u}), fb_off);
      RAW_BAR();
#pragma unroll
      for (int i = 0; i < 16; ++i) ld2(G[0], wo_slot(0), i >> 1, i & 1);
      f32x4 a2[2][8];
#pragma unroll
      for (int i = 0; i < 16; ++i) a2[i & 1][i >> 1] = (f32x4){0.f, 0.f, 0.f, 0.f};
      SLEEP(P_FB);
      SPIN { CNT(2); if (wave_ok(ld32(ws, WS_FB + (rp * 32 + 16 * kh + (lane & 15)) * 128))) break; SLEEP(L_FB); }
      asm volatile("" ::: "memory");
      STAMPT(0, 7, 0);
      char* pr[2];
#pragma unroll
      for (int i = 0; i < 2; ++i) pr[i] = smem + L_P + kh * L_PK + (32 * rt + c16) * 512 + (((4 * i + g4) ^ (c16 & 7)) * 16);
      u32x4 of[16][2];
#pragma unroll
      for (int t = 0; t < OW; ++t) { of[t][0] = LDO(t, 0); of[t][1] = LDO(t, 1); }
      SB0();
      sfor<0, 16>([&](auto T) {
        constexpr int t = T.v;
        Frag2& C = G[t & 1]; Frag2& N = G[(t + 1) & 1];
#pragma unroll
        for (int i = 0; i < 16; ++i) {
          const int nt = i >> 1, st = i & 1;
          a2[st][nt] = mfma16((u32x4){C.lo[nt][0], C.lo[nt][1], C.hi[nt][0], C.hi[nt][1]}, of[t][st], a2[st][nt]);
          SB0();
          if ((t & 1) && t <= 11 && i == 8) {
            asm volatile("s_waitcnt lgkmcnt(8)" ::: "memory"); __builtin_amdgcn_s_barrier(); asm volatile("" ::: "memory");
          }
          if (t < 15) ld2(N, wo_slot(t + 1), nt, st);
          if (t > 0 && t - 1 + OW < 16 && (i == 3 || i == 11)) of[(t - 1 + OW) & 15][i >> 3] = LDO(t - 1 + OW, i >> 3);
          if (t - 1 + OW == 16 && (i == 3 || i == 11)) { xv[i >> 3] = *(const u32x4*)(x + ego + (i >> 3) * 64); wv[i >> 3] = *(const u32x4*)(w_r + cb * 128 + eseg * 8 + (i >> 3) * 64); }
          if (t == 15 && i >= 4) *(f32x4*)(pr[(nt - 2) & 1] + ((nt - 2) >> 1) * 128 + st * 8192) = a2[st][(nt - 2) & 7];
          SB0();
        }
      });
      STAMPT(0, 8, 0);
#pragma unroll
      for (int i = 12; i < 16; ++i) *(f32x4*)(pr[(i >> 1) & 1] + (i >> 2) * 128 + (i & 1) * 8192) = a2[i & 1][i >> 1];
    }
    unpack_xw();
    __builtin_amdgcn_s_setprio(0);
  } else {
    LANES();
    const int sw = wave - 4, t2 = tid - 256;
    __builtin_amdgcn_s_setprio(PRIO_S);
    unsigned hs_g0 = 0, hs_cur = 0;
    u32x4 ms_exp = {0, 0, 0, 0};
    bool hs_act = true, ms_act = (bid == 0) && (sw == 1);
    const unsigned hs_off = (lane == 1 ? SY_GO : SY_CH) + bid * 4;
    const unsigned re_off = t2 == 0 ? SY_RE + bid * 4 : OOB;
    const unsigned ms_off = lane * 16;
    const char* wsu = uni(ws);
#define HS_PROBE() asm volatile("s_nop 4\n\tglobal_load_dword v156, %0, %1 sc1" :: "v"(hs_off), "s"(wsu) : "memory", RSV)
#define MS_PROBE() asm volatile("s_nop 4\n\tglobal_load_dwordx4 v[152:155], %0, %1 offset:2048 sc1" :: "v"(ms_off), "s"(wsu) : "memory", RSV)
    auto hs_step = [&](bool first) {
      if (hs_act) {
        const unsigned hv_ = rget<156>();
        hs_act = __builtin_amdgcn_readlane(hv_, 1) == hs_g0;
        const unsigned c = __builtin_amdgcn_readlane(hv_, 0);
        if (sw == 0 && (first || c != hs_cur)) STW1(hv_, re_off);
        hs_cur = c;
        HS_PROBE();
      }
      if (ms_act) {
        const unsigned d = ((rget<152>() ^ ms_exp[0]) | (rget<153>() ^ ms_exp[1])) | ((rget<154>() ^ ms_exp[2]) | (rget<155>() ^ ms_exp[3]));
        ms_act = __any(d != 0u);
        STW(ms_exp, ms_act ? OOB : SY_GO + ms_off);
        MS_PROBE();
      }
    };
    HS_PROBE();
    if (ms_act) {
      MS_PROBE();
      asm volatile("s_nop 4\n\tglobal_load_dwordx4 v[148:151], %0, %1 offset:1024 sc1" :: "v"(ms_off), "s"(wsu) : "memory", RSV);
    }
    if (sw == 3) {
      const char* cp = (const char*)(pc0 - (pc0 & 127)) + (idx * CODE_LINES + (lane < CODE_LINES ? lane : 0)) * 128;
      asm volatile("global_load_dword v157, %0, off" :: "v"(cp) : "memory", RSV);
    }
    const int gr = (g8 >> 3) * 7 + min(hs_, 6);
    const bool real = hs_ != 7;
    const unsigned voG = (unsigned)((lane >> 3) * 8 * F_ + (lane & 7) * 8) * 2u, voD = (unsigned)((lane >> 3) * 8 * D_ + (lane & 7) * 8) * 2u;
    const size_t gofs = ((size_t)(kq * 256 + sw * 64) * F_ + gr * 64) * 2;
    const char* pG = (const char*)wg + gofs;
    const char* pU = (const char*)wu + gofs;
    const char* pD = (const char*)(wd + (size_t)(gr * 64) * D_ + kq * 256 + sw * 64);
    auto wstep = [&](auto I) {
      constexpr int i = I.v;
      if (i < 8) wload<VW + 4 * i>(voG, pG + (i & 7) * (F_ * 2));
      else if (i < 16) wload<VW + 4 * i>(voG, pU + (i & 7) * (F_ * 2));
      else wload2<VW + 4 * i>(voD, pD + (i & 7) * (D_ * 2));
    };
    unsigned voW;
    {
      const int bi = lane >> 4, q = (lane >> 2) & 3, ch = lane & 3;
      voW = (unsigned)((4 * (bi >> 1) + q) * D_ + (bi & 1) * 32 + (ch ^ (bi & 2)) * 8) * 2u;
    }
    const char* baseW = (const char*)(wo + (size_t)((16 * (sw >> 1)) * 32 + 16 * (sw & 1)) * D_ + cb * 128) - 1024;
    auto stageW = [&](auto J, bool rsv) {
      const char* s_ = baseW + (size_t)whead(J.v) * (32 * D_ * 2);
      if (rsv) glds4o<wo_slot(J.v)>(voW + 1024, voW + 128, voW + 8 * D_ * 2 - 1024, voW + 8 * D_ * 2 + 128 - 2048, s_, l4);
      else glds4<wo_slot(J.v)>(voW + 1024, voW + 128, voW + 8 * D_ * 2 - 1024, voW + 8 * D_ * 2 + 128 - 2048, s_, l4);
    };
    int sta[4], stb[3];
    {
      const int row = t2 >> 1, key = (row >> 1) & 7;
#pragma unroll
      for (int c = 0; c < 4; ++c) { sta[c] = (posA & 3) * K1_STAGE + row * 128 + (((4 * (t2 & 1) + c) ^ key) * 16); asm volatile("" : "+v"(sta[c]) :: RSV); }
#pragma unroll
      for (int i = 0; i < 3; ++i) {
        const int G = sw * 3 + i, hd = G >= 6 ? 1 : 0, mat = (G - 6 * hd) >> 1;
        stb[i] = (posB & 3) * K1_STAGE + 16384 + (lane >> 4) * 6144 + mat * 1024 + hd * 256 + ((lane & 15) >> 2) * 64 + (G & 1) * 32 + (lane & 3) * 8;
        asm volatile("" : "+v"(stb[i]) :: RSV);
      }
    }
    unsigned rs_dst = rs_slot + t2 * 4, cs_dst = lane < 48 ? cs_slot + (sw * 48 + lane) * 4 : OOB;
    unsigned rs_off = WS_RS + rb * 16384 + (lane >> 4) * 1024 + sw * 256 + (lane & 15) * 16;
    unsigned cs_off = WS_CS + hp * 12288 + (lane >> 4) * 768 + sw * 192 + min(lane & 15, 11) * 16;
    asm volatile("" : "+v"(rs_dst), "+v"(cs_dst), "+v"(rs_off), "+v"(cs_off) :: RSV);
    int stm_dst = ((lane >> 5) * 128 + sw * 32 + (lane & 15) * 2 + ((lane >> 4) & 1)) * 4, cs_put = (lane & 15) < 12 ? (256 + sw * 48 + (lane & 15) * 4 + (lane >> 4)) * 4 : 1792 + lane * 4;
    asm volatile("" : "+v"(stm_dst), "+v"(cs_put) :: RSV);
    auto st_load = [&]() { sfor<0, 4>([&](auto I) { sload<224 + 4 * I.v>(rs_off, wsu + I.v * 4096); sload<240 + 4 * I.v>(cs_off, wsu + I.v * 3072); }); };
    sfor<3, GS[0]>(group);
    WAIT_VR(gops(GS[0]) - 10 + 1); RAW_BARR();
    TSL(14);
    sfor<GS[0], GS[1]>(group);
    WAIT_VR(gops(GS[1]) - 20); RAW_BARR();
    hs_g0 = __builtin_amdgcn_readlane(rget<156>(), 1);
    if (ms_act) {
      const u32x4 re = {rget<152>(), rget<153>(), rget<154>(), rget<155>()}, g0 = {rget<148>(), rget<149>(), rget<150>(), rget<151>()};
#pragma unroll
      for (int j = 0; j < 4; ++j) { const unsigned c = re[j] + 1u; ms_exp[j] = (c == g0[j]) ? c + 1u : c; }
      STW(ms_exp, SY_CH + ms_off);
    }
    u32x4 ra[4] = {}; u32x2 rc[3][4] = {};
    float sa = 0.f, sb = 0.f, qa = 0.f, qb = 0.f, ca[3] = {0.f, 0.f, 0.f}, cb_[3] = {0.f, 0.f, 0.f};
    auto chunk = [&](auto C) {
      constexpr int c = C.v;
      if constexpr (c < 4) {
        asm volatile("" : "+v"(ra[c]) :: RSV);
#pragma unroll
        for (int i = 0; i < 4; ++i) { const float lo = bflo(ra[c][i]), hi = bfhi(ra[c][i]); sa = fadd(sa, lo); sb = fadd(sb, hi); qa = fsq(lo, qa); qb = fsq(hi, qb); }
        asm volatile("" : "+v"(sa), "+v"(sb), "+v"(qa), "+v"(qb) :: RSV);
      } else if constexpr (c == 4) {
        float s = fadd(sa, sb), sq = fadd(qa, qb);
        s = fadd(s, dppf<DPP_X1>(s)); sq = fadd(sq, dppf<DPP_X1>(sq));
        STW1(sane1(__float_as_uint((t2 & 1) ? sq : s)), rs_dst);
      } else if constexpr (c < 11) {
        constexpr int i = (c - 5) >> 1, j = ((c - 5) & 1) * 2;
        asm volatile("" : "+v"(rc[i][j]), "+v"(rc[i][j + 1]) :: RSV);
#pragma unroll
        for (int e = 0; e < 4; ++e) { const unsigned u = rc[i][j + (e >> 1)][e & 1]; ca[i] = fadd(ca[i], bflo(u)); cb_[i] = fadd(cb_[i], bfhi(u)); }
        asm volatile("" : "+v"(ca[i]), "+v"(cb_[i]) :: RSV);
      } else {
        const float c2 = fadd(ca[2], cb_[2]);
        STW1(sane1(__float_as_uint(sw32(sw16(fadd(ca[0], cb_[0]), fadd(ca[1], cb_[1])), sw16(c2, c2)))), cs_dst);
      }
    };
    sfor<0, 12>([&](auto T) {
      constexpr int t = T.v;
      TSL(t);
      if (t < 4 && posA == t + 1) {
#pragma unroll
        for (int c = 0; c < 4; ++c) ra[c] = *(const u32x4*)(smem + sta[c]);
      }
      if (t < 4 && posB == t + 1) {
#pragma unroll
        for (int i = 0; i < 3; ++i)
#pragma unroll
          for (int j = 0; j < 4; ++j) rc[i][j] = trread(smem + (stb[i] ^ ((j >> 1) * 32)) + (j >> 1) * 3072 + (j & 1) * 512);
      }
      if (t == 2) HS_PROBE();
      if (t >= 4) hs_step(t == 4);
      sfor<GS[t + 1], GS[t + 2]>([&](auto G) {
        group(G);
        constexpr int u = t - 4, p = G.v - GS[t + 1];
        constexpr int ord[12] = {0, 5, 6, 1, 7, 8, 2, 9, 10, 3, 11, 4};
        if constexpr (u >= 0 && u < 4 && p < 3) chunk(IC<ord[(3 * u + p) % 12]>{});
      });
      if (real) sfor<WS_[t], WS_[t + 1]>(wstep);
      TSL(16 + t);
      WAIT_VR(t < 11 ? gops(GS[t + 2]) - 10 * (t + 3) : gops(GS[13]) - gops(GS[12]));
      TSL(32 + t);
      RAW_BARR();
    });
    TSL(12);
    hs_step(false);
    auto st_put = [&]() -> bool {
      float a[8]; unsigned m = 0, n = 0;
      rsum4<224>(a, m, n); rsum4<240>(a + 4, m, n);
      const float s1 = sw16(a[0], a[2]), q1 = sw16(a[1], a[3]);
      const float cs = sw32(sw16(a[4], a[5]), sw16(a[6], a[7]));
      const float mean = sw32(s1, s1) * (1.0f / 1024.0f);
      const float var = fmaxf(__builtin_fmaf(sw32(q1, q1), 1.0f / 1024.0f, -(mean * mean)), 0.f);
      const float rinv = __builtin_amdgcn_rcpf(rbf(__builtin_amdgcn_sqrtf(rbf(rbf(var) + 1e-5f))));
      *(float*)(smem + L_STM + stm_dst) = (lane & 32) ? rinv : rbf(mean);
      *(float*)(smem + L_STM + cs_put) = cs;
      return wave_ok(max(m, n));
    };
    st_load();
    if (real) sfor<WS_[12], W1A>(wstep);
    TSL(28);
    if (real) WAIT_VR(8 + W1A - WS_[11]); else WAIT_VR(8);
    if (lane == 0) { misc[8 + sw] = 0; misc[12 + sw] = 0; misc[16 + sw] = 0; misc[7] = 0; }
    TSL(44);
    RAW_BARR();
    TSL(13); TSDUMP(1 + sw);
    STAMPT(1, 0, 256);
    if (real) WAIT_VR(W1A - WS_[12]); else WAIT_VR(0);
    if (!st_put() || hs_act) {
      if (ms_act) {
        SPIN {
          bool ok = true;
#pragma unroll
          for (int j = 0; j < 4; ++j) ok &= (ld32(ws, SY_RE + lane * 16 + j * 4) == ms_exp[j]);
          if (__all(ok)) break;
        }
        STW(ms_exp, SY_GO + lane * 16);
      }
      if (hs_act) {
        SPIN {
          if (ld32(ws, SY_GO + bid * 4) != hs_g0) break;
          const unsigned c = ld32(ws, SY_CH + bid * 4);
          if (c != hs_cur) { hs_cur = c; STW1(c, re_off); }
        }
      }
      SPIN { st_load(); WAIT_VR(0); if (st_put()) break; }
    }
    asm volatile("" ::: "memory", RSV);
    if (lane == 0) misc[12 + sw] = 1;
    __builtin_amdgcn_s_setprio(0);
    if (real) sfor<W1A, 16>(wstep);
    auto mwait = [&](int v) { SPIN { const int a = misc[8 + (lane & 3)]; if (__all(a >= v)) break; __builtin_amdgcn_s_sleep(1); } asm volatile("" ::: "memory"); };
    sfor<0, 9>([&](auto J) {
      if (J.v == 6) mwait(3);
      stageW(J, J.v < 2);
      if (J.v >= 1) WAIT_V(4 * PACE);
      if (J.v == 1) { WAIT_VR(8); sfor<0, 16>(wtr); }
      if (J.v >= 2 && PSLP) __builtin_amdgcn_s_sleep(PSLP);
    });
    STAMPT(1, 1, 256);
    EPI();
#pragma unroll
    for (int i = 0; i < 2; ++i) { xv[i] = *(const u32x4*)(x + ego + i * 64); wv[i] = *(const u32x4*)(w_r + cb * 128 + eseg * 8 + i * 64); }
    asm volatile("s_waitcnt vmcnt(0)" : "+v"(xv[0]), "+v"(xv[1]), "+v"(wv[0]), "+v"(wv[1]) :: "memory");
    unpack_xw();
    RAW_BAR();
    RAW_BAR(); stageW(IC<9>{}, false); stageW(IC<10>{}, false);
    RAW_BAR(); stageW(IC<11>{}, false); stageW(IC<12>{}, false);
    RAW_BAR(); stageW(IC<13>{}, false); stageW(IC<14>{}, false);
    WAIT_V(16); RAW_BAR(); stageW(IC<15>{}, false);
    WAIT_V(12); RAW_BAR();
    WAIT_V(0); RAW_BAR();
    if (real) sfor<16, 24>(wstep);
  }
  LANES(); EPI();
  const int grow = rp * 64 + erow;
  int pa0 = L_P + erow * 512 + ((2 * eseg) ^ (erow & 7)) * 16, pa1 = pa0 ^ 16;
  unsigned st_dst = eseg == 0 ? WS_ST2 + ((rp * 8 + cb) * 64 + erow) * 16 : OOB, st_src = WS_ST2 + rp * 8192 + lane * 16;
  unsigned fd_dst = lane < 8 ? WS_FD + (cb * 32 + rp) * 128 + lane * 16 : OOB, kidx = 0xFFFFu - grow;
  asm volatile("" : "+v"(pa0), "+v"(pa1), "+v"(st_dst), "+v"(st_src), "+v"(fd_dst), "+v"(kidx));
  RAW_BAR2();
  STAMPT(0, 9, 0);
  unsigned hb[8];
  f32x2 hv[8];
  {
    f32x4 pp[2][4];
#pragma unroll
    for (int k = 0; k < 2; ++k)
#pragma unroll
      for (int i = 0; i < 4; ++i) pp[k][i] = *(const f32x4*)(smem + ((i & 1) ? pa1 : pa0) + (i >> 1) * 256 + k * L_PK);
    SB0();
    f32x2 s = {0.f, 0.f}, q = {0.f, 0.f}, p2 = {0.f, 0.f};
#pragma unroll
    for (int i = 0; i < 8; ++i) {
      const f32x4 a = pp[0][i >> 1] + pp[1][i >> 1];
      const f32x2 u = xf[i] + unpk(pack2o(a[2 * (i & 1)], a[2 * (i & 1) + 1]));
      hb[i] = pack2o(u[0], u[1]);
      hv[i] = unpk(hb[i]);
      s += hv[i]; q += hv[i] * hv[i]; p2 += hv[i] * wf2[i];
    }
    STW(sane((u32x4){__float_as_uint(oct_sum(s[0] + s[1])), __float_as_uint(oct_sum(q[0] + q[1])), __float_as_uint(oct_sum(p2[0] + p2[1])), __float_as_uint(sw_)}), st_dst);
  }
  KEEP2();
  STAMPT(0, 10, 0);
  const int sw = wave - 4;
  const unsigned fd_src = WS_FD + xcd * 4096 + r * 128;
  u32x2 kpk = {SENT, SENT};
  if (wave == 0) {
    u32x4 pa[8];
    SLEEP(P_C);
    SPIN {
      unsigned m = 0;
#pragma unroll
      for (int j = 0; j < 8; ++j) { pa[j] = LDW(st_src + j * 1024); m = chk(m, pa[j]); }
      CNT(3);
      if (wave_ok(m)) break;
      KEEP2();
    }
    STAMPT(0, 11, 0);
#define F4(v) __builtin_bit_cast(f32x4, v)
    const f32x4 t = ((F4(pa[0]) + F4(pa[1])) + (F4(pa[2]) + F4(pa[3]))) + ((F4(pa[4]) + F4(pa[5])) + (F4(pa[6]) + F4(pa[7])));
    const float mean = t[0] * (1.0f / 1024.0f);
    const float var = fmaxf(__builtin_fmaf(t[1], 1.0f / 1024.0f, -(mean * mean)), 0.f);
    const float mb = rbf(mean), rs = __builtin_amdgcn_rcpf(rbf(__builtin_amdgcn_sqrtf(rbf(rbf(var) + 1e-5f))));
    *(f32x2*)(smem + L_KX + lane * 8) = (f32x2){mb, rs};
    const float sc = fmaxf((t[2] - mb * t[3]) * rs, -INFINITY);
    const int u = (int)(pack2(0.f, sc) & 0xffff0000u);
    unsigned b0 = ((unsigned)(u ^ ((u >> 31) | (int)0x80000000)) & 0xffff0000u) | (0xFFFFu - (rp * 64 + lane)), b1 = 0;
    top2_lo(b0, b1); top2_hi(b0, b1);
    STW(((u32x4){b0, b1, b0, b1}), fd_dst);
    if (lane == 0) *(u32x2*)(smem + L_MISC + 112) = (u32x2){b0, b1};
  } else if (wave >= 4) {
    asm volatile("s_waitcnt vmcnt(0)" ::: "memory", RSV2);
    sfor<16, 24>(wtr);
  }
  RAW_BAR();
  bool cand;
  u32x4 ob[2];
  {
    const f32x2 mr = *(const f32x2*)(smem + L_KX + erow * 8);
    const u32x2 kk = *(const u32x2*)(smem + L_MISC + 112);
    const float mb = mr[0], rs = mr[1];
    const unsigned b0 = kk[0], b1 = kk[1];
    u32x4 nq[2];
#pragma unroll
    for (int i = 0; i < 8; ++i) {
      const f32x2 d = hv[i] - mb;
      const f32x2 n = unpk(pack2o(d[0], d[1])) * rs;
      nq[i >> 2][i & 3] = pack2o(n[0], n[1]);
    }
    const bool c2 = (b1 & 0xffffu) == kidx;
    cand = c2 || (b0 & 0xffffu) == kidx;
    if (cand) {
      const unsigned xo = WS_XS + (rp * 2 + (c2 ? 1 : 0)) * 2048 + cb * 256 + eseg * 16;
      STW(sane(nq[0]), xo); STW(sane(nq[1]), xo + 128);
      char* cp = smem + L_CAND + (c2 ? 512 : 0) + eseg * 16;
      *(u32x4*)cp = (u32x4){hb[0], hb[1], hb[2], hb[3]}; *(u32x4*)(cp + 128) = (u32x4){hb[4], hb[5], hb[6], hb[7]};
      *(u32x4*)(cp + 256) = nq[0]; *(u32x4*)(cp + 384) = nq[1];
    }
    asm volatile("" ::: "memory");
    if (wave >= 4) { SLEEP(P_D); kpk = __builtin_amdgcn_raw_buffer_load_b64(wsr, fd_src, 0, 16); }
    asm volatile("" ::: "memory");
#pragma unroll
    for (int i = 0; i < 8; ++i) { const f32x2 o = hv[i] + unpk(nq[i >> 2][i & 3]); ob[i >> 2][i & 3] = pack2(o[0], o[1]); }
  }
#define OUT_ST() do { *(u32x4*)(out + ego) = ob[0]; *(u32x4*)(out + ego + 64) = ob[1]; } while (0)
  STAMPT(0, 12, 0);
  if (wave < 4) {
    if (!cand) OUT_ST();
    RAW_BAR();
    const unsigned w0 = misc[24], w1 = misc[25];
    if (cand && grow != (int)key_idx(w0) && grow != (int)key_idx(w1)) OUT_ST();
#pragma unroll 1
    for (int tk = 0; tk < 2; ++tk) {
      const unsigned wk = tk ? w1 : w0, slot = misc[26 + tk];
      if ((int)(slot >> 1) != rp) continue;
      u32x4 pv[7];
      const unsigned yo = WS_YP + ((tk * 8 + cb) * 64 + (lane >> 3) * 8) * 512 + wave * 128 + (lane & 7) * 16;
      const int col = (wave >> 1) * 64 + (lane & 7) * 8 + (wave & 1) * 4 + (lane >> 4);
      float sg = rbf(__builtin_amdgcn_rcpf(1.0f + __expf(-key_val(wk))));
      float hh = bflo(*(const u16*)(smem + L_CAND + (slot & 1) * 512 + col * 2)), nn = bflo(*(const u16*)(smem + L_CAND + (slot & 1) * 512 + 256 + col * 2));
      u16* op = out + (size_t)key_idx(wk) * D_ + cb * 128 + col;
      asm volatile("" : "+v"(sg), "+v"(hh), "+v"(nn), "+v"(op));
      if (tk == 0 || (int)(misc[26] >> 1) != rp) {
        SPIN { if (misc[30] == (int)w0) break; __builtin_amdgcn_s_sleep(1); }
        SLEEP(P_E);
      }
      SPIN {
        unsigned m = 0;
#pragma unroll
        for (int p = 0; p < 7; ++p) { pv[p] = LDW(yo + p * 512); m = chk(m, pv[p]); }
        CNT(6);
        if (wave_ok(m)) break;
        asm volatile("" ::: "memory");
        SLEEP(L_E);
      }
      STAMPT(0, 13, 0);
      f32x4 s = __builtin_bit_cast(f32x4, pv[0]);
#pragma unroll
      for (int p = 1; p < 7; ++p) s += __builtin_bit_cast(f32x4, pv[p]);
      float y = sw32(sw16(s[0], s[1]), sw16(s[2], s[3]));
      y += dppf<DPP_ROR8>(y);
      if (!(lane & 8)) *(GAS u16*)op = (u16)pack2(hh + rbf(nn + rbf(rbf(y) * sg)), 0.f);
      STAMPT(0, 14, 0);
    }
  } else {
    unsigned xl = WS_XS + (kq * 256 + sw * 64 + (lane >> 4) * 16 + ((lane >> 2) & 1) * 8) * 2;
    int red_dst = (lane < 8 || (lane >= 24 && lane < 32)) ? L_RED + sw * 2048 + ((lane >> 3) & 1) * 1024 + (lane & 7) * 16 : L_RED + 8192 + lane * 16;
    unsigned gu_src = WS_GU + g8 * 4096 + lane * 16;
    int act_dst = L_ACT + sw * 256 + ((lane & 7) * 8 + (lane >> 3)) * 2, act_src = L_ACT + sw * 256 + (lane & 1) * 128 + (lane >> 4) * 32 + ((lane >> 1) & 1) * 16;
    unsigned yp_dst = lane < 16 ? WS_YP + (((lane >> 3) * 8 + 2 * kq + (sw >> 1)) * 64 + g8) * 512 + (sw & 1) * 256 + (lane & 7) * 16 : OOB;
    asm volatile("" : "+v"(xl), "+v"(red_dst), "+v"(gu_src), "+v"(act_dst), "+v"(act_src), "+v"(yp_dst));
    unsigned w0, w1; int s0 = -1, s1 = -1;
    u32x4 xa = {SENT, SENT, SENT, SENT};
    SPIN {
      CNT(4);
      const bool all = wave_ok(kpk[1]);
      const unsigned k0 = kpk[1] == SENT ? 0u : kpk[0], k1 = kpk[1] == SENT ? 0u : kpk[1];
      w0 = k0; w1 = k1;
      top2_lo(w0, w1); top2_mid(w0, w1);
      const int n0 = 2 * __builtin_ctzll(__ballot(k0 == w0));
      const u64 m1 = __ballot(k0 == w1);
      const int n1 = m1 ? 2 * __builtin_ctzll(m1) : n0 + 1;
      const bool same = n0 == s0 && n1 == s1;
      s0 = n0; s1 = n1;
      asm volatile("" ::: "memory");
      if (all && same) break;
      if (!all) { SLEEP(L_D); kpk = __builtin_amdgcn_raw_buffer_load_b64(wsr, fd_src, 0, 16); }
      xa = LDW(xl + ((lane & 1) ? s1 : s0) * 2048);
      if (all) break;
    }
    SPIN {
      if (wave_ok(chk(0u, xa))) break;
      asm volatile("" ::: "memory");
      xa = LDW(xl + ((lane & 1) ? s1 : s0) * 2048);
    }
    STAMPT(1, 3, 256);
    {
      const u32x4 z4 = {0u, 0u, 0u, 0u};
      const u32x4 xg = (lane & 2) ? z4 : xa, xu = (lane & 2) ? xa : z4;
      f32x4 d[8];
#pragma unroll
      for (int c = 0; c < 8; ++c) d[c] = mfma16(xg, wf[c], (f32x4){0.f, 0.f, 0.f, 0.f});
      if (tid == 256) { *(u32x4*)(smem + L_MISC + 96) = (u32x4){w0, w1, (unsigned)s0, (unsigned)s1}; misc[30] = 0; }
      SB0();
#pragma unroll
      for (int c = 0; c < 10; ++c) {
        if (c < 8) d[c & 7] = mfma16(xu, wf[8 + (c & 7)], d[c & 7]);
        if (c >= 2) *(f32x4*)(smem + red_dst + (c - 2) * 128) = d[(c - 2) & 7];
        SB0();
      }
    }
    RAW_BAR();
    if (sw == 0) {
      f32x4 t[8];
#pragma unroll
      for (int p = 0; p < 8; ++p) t[p] = *(const f32x4*)(smem + L_RED + p * 1024 + lane * 16);
      SB0();
      STW(sane(__builtin_bit_cast(u32x4, ((t[0] + t[1]) + (t[2] + t[3])) + ((t[4] + t[5]) + (t[6] + t[7])))), gu_src + kq * 1024);
    } else __builtin_amdgcn_s_sleep(2);
    STAMPT(1, 4, 256);
    {
      u32x4 q[4];
      if (OUTE && grow != (int)key_idx(w0) && grow != (int)key_idx(w1)) OUT_ST();
      asm volatile("" ::: "memory");
      SLEEP(P_GU);
      SPIN {
        unsigned m = 0;
#pragma unroll
        for (int j = 0; j < 4; ++j) { q[j] = LDW(gu_src + j * 1024); m = chk(m, q[j]); }
        CNT(5);
        if (wave_ok(m)) break;
        asm volatile("" ::: "memory");
      }
      STAMPT(1, 5, 256);
      const f32x4 tot = (__builtin_bit_cast(f32x4, q[0]) + __builtin_bit_cast(f32x4, q[1])) + (__builtin_bit_cast(f32x4, q[2]) + __builtin_bit_cast(f32x4, q[3]));
      const f32x2 gs = unpk(pack2o(tot[0], tot[1])), us = unpk(pack2o(tot[2], tot[3]));
      const f32x2 sg = unpk(pack2o(gs[0] * __builtin_amdgcn_rcpf(1.0f + __expf(-gs[0])), gs[1] * __builtin_amdgcn_rcpf(1.0f + __expf(-gs[1]))));
      const unsigned a2 = pack2o(sg[0] * us[0], sg[1] * us[1]);
      *(u16*)(smem + act_dst) = (u16)a2; *(u16*)(smem + act_dst + 128) = (u16)(a2 >> 16);
      __builtin_amdgcn_fence(__ATOMIC_RELEASE, "wavefront"); __builtin_amdgcn_wave_barrier(); __builtin_amdgcn_fence(__ATOMIC_ACQUIRE, "wavefront");
      const u32x4 aa = *(const u32x4*)(smem + act_src);
      f32x4 d[8];
#pragma unroll
      for (int c = 0; c < 8; ++c) d[c] = mfma16(aa, wf[16 + c], (f32x4){0.f, 0.f, 0.f, 0.f});
      SB0();
      u32x4 y[2];
#pragma unroll
      for (int c = 0; c < 8; ++c) {
        const float t0 = d[c][0] + dppf<DPP_ROR8>(d[c][2]), t1 = d[c][3] + dppf<DPP_ROR8>(d[c][1]);
        y[c >> 2][c & 3] = __float_as_uint((lane & 8) ? t1 : t0);
      }
      STW(sane(y[0]), yp_dst); STW(sane(y[1]), yp_dst + 128);
      if (tid == 256) misc[30] = w0;
    }
    STAMPT(1, 6, 256);
    if (!OUTE && grow != (int)key_idx(w0) && grow != (int)key_idx(w1)) OUT_ST();
  }
}

extern "C" void kernel_launch(void* const* d_in, const int* in_sizes, int n_in, void* d_out, int out_size,
                              void* d_ws, size_t ws_size, hipStream_t stream) {
  (void)hipFuncSetAttribute((const void*)fused, hipFuncAttributeMaxDynamicSharedMemorySize, LDS_TOTAL);
  fused<<<256, 512, LDS_TOTAL, stream>>>((const u16*)d_in[0], (const u16*)d_in[1], (const u16*)d_in[2], (const u16*)d_in[4],
                                         (const u16*)d_in[5], (const u16*)d_in[6], (const u16*)d_in[7], (u16*)d_out, (char*)d_ws);
}
```

```cpp
#include <hip/hip_runtime.h>
#include <stdint.h>

#define LAS __attribute__((address_space(3)))
#define GAS __attribute__((address_space(1)))
#define DI __device__ __forceinline__

typedef unsigned short u16;
typedef unsigned long long u64;
typedef __attribute__((ext_vector_type(2))) __bf16 bf16x2;
typedef __attribute__((ext_vector_type(8))) __bf16 bf16x8;
typedef __attribute__((ext_vector_type(4))) short s16x4;
typedef __attribute__((ext_vector_type(2))) unsigned short u16x2;
typedef __attribute__((ext_vector_type(16))) float f32x16;
typedef __attribute__((ext_vector_type(4))) float f32x4;
typedef __attribute__((ext_vector_type(2))) float f32x2;
typedef __attribute__((ext_vector_type(4))) unsigned u32x4;
typedef __attribute__((ext_vector_type(2))) unsigned u32x2;
typedef GAS unsigned gu32;
typedef GAS unsigned long long gu64;

constexpr int S_ = 2048, D_ = 1024, F_ = 3584;

constexpr unsigned SY_CH = 0, SY_GO = 1024, SY_RE = 2048;
constexpr unsigned WS_FD  = 98304;
constexpr unsigned WS_ZF  = 8192;
constexpr unsigned WS_KVF = 16384;
constexpr unsigned WS_RS  = 131072;
constexpr unsigned WS_CS  = WS_RS + 16 * 16 * 1024;
constexpr unsigned WS_ZP  = WS_CS + 16 * 16 * 768;
constexpr unsigned WS_FB  = WS_ZP + 32 * 16 * 128;
constexpr unsigned WS_ST2 = WS_FB + 32 * 32 * 128;
constexpr unsigned WS_XS  = WS_ST2 + 32 * 8 * 1024;
constexpr unsigned WS_GU  = WS_XS + 64 * 2048;
constexpr unsigned WS_YP  = WS_GU + 256 * 1024;
constexpr unsigned WS_KVP = WS_YP + 2 * 8 * 64 * 512;
constexpr unsigned WS_O   = WS_KVP + 32 * 16 * 4096;
constexpr unsigned WS_END = WS_O + S_ * D_ * 2;
constexpr unsigned OOB = 0x80000000u;
constexpr size_t WS_DIAG  = 64u << 20;
#define CNT(i) do {} while (0)
#define STAMPT(kid, i, T) do {} while (0)

#define TSL(idx) do {} while (0)
#define TSDUMP(k) do {} while (0)
template <int V> struct IC { static constexpr int v = V; };
template <int B, int E, class F> DI void sfor(F&& f) { if constexpr (B < E) { f(IC<B>{}); sfor<B + 1, E>(f); } }

DI float bflo(unsigned u) { return __uint_as_float(u << 16); }
DI float bfhi(unsigned u) { return __uint_as_float(u & 0xffff0000u); }
DI float rbf(float x) { return (float)(__bf16)x; }
DI unsigned pack2(float a, float b) { bf16x2 c; c[0] = (__bf16)a; c[1] = (__bf16)b; return __builtin_bit_cast(unsigned, c); }
DI unsigned pack2o(float a, float b) { unsigned p = pack2(a, b); asm("" : "+v"(p)); return p; }
DI f32x16 mfma32(u32x4 a, u32x4 b, f32x16 c) {
  return __builtin_amdgcn_mfma_f32_32x32x16_bf16(__builtin_bit_cast(bf16x8, a), __builtin_bit_cast(bf16x8, b), c, 0, 0, 0);
}
DI f32x4 mfma16(u32x4 a, u32x4 b, f32x4 c) {
  return __builtin_amdgcn_mfma_f32_16x16x32_bf16(__builtin_bit_cast(bf16x8, a), __builtin_bit_cast(bf16x8, b), c, 0, 0, 0);
}
DI void mfma16i(u32x4 a, u32x4 b, f32x4& c) { asm volatile("v_mfma_f32_16x16x32_bf16 %0, %1, %2, %0" : "+v"(c) : "v"(a), "v"(b)); }
DI f32x2 unpk(unsigned u) { return (f32x2){bflo(u), bfhi(u)}; }
DI u32x2 trread(const char* p) {
  s16x4 v = __builtin_amdgcn_ds_read_tr16_b64_v4i16((LAS s16x4*)p);
  return __builtin_bit_cast(u32x2, v);
}
DI const char* uni(const void* p) {
  const u64 v = (u64)p;
  return (const char*)(((u64)(unsigned)__builtin_amdgcn_readfirstlane((int)(v >> 32)) << 32) | (unsigned)__builtin_amdgcn_readfirstlane((int)v));
}
#define RV4(a, b, c, d) "v" #a, "v" #b, "v" #c, "v" #d
#define RV10(p) "v" #p "0", "v" #p "1", "v" #p "2", "v" #p "3", "v" #p "4", "v" #p "5", "v" #p "6", "v" #p "7", "v" #p "8", "v" #p "9"
#define RSV2 RV4(224, 225, 226, 227), "v228", "v229", RV10(23), RV10(24), RV4(250, 251, 252, 253), "v254", "v255"
#define RSV1 RV4(192, 193, 194, 195), RV4(196, 197, 198, 199), RV10(20), RV10(21), RV4(220, 221, 222, 223), RSV2
#define RSV "v148", "v149", RV10(15), RV10(16), RV10(17), RV10(18), "v190", "v191", RSV1
#define NTW " nt"
#define NTA ""
#define NTB ""
#define NTO ""
#define GL_(v, s, o) "global_load_lds_dwordx4 " v ", " s " offset:" #o
#define GLDS4(SC, CL...) asm volatile("s_add_u32 m0, %5, %c6\n\ts_nop 3\n\t" GL_("%0", "%4", 0) SC "\n\t" GL_("%1", "%4", 1024) SC "\n\t" \
  GL_("%2", "%4", 2048) SC "\n\t" GL_("%3", "%4", 3072) SC :: "v"(v0), "v"(v1), "v"(v2), "v"(v3), "s"(uni(sbase)), "s"(lds), "i"(IMM) : "memory", "scc", ##CL)
#define GLDS3(SC, CL...) asm volatile("s_add_u32 m0, %4, %c5\n\ts_nop 3\n\t" GL_("%0", "%3", 0) SC "\n\t" GL_("%1", "%3", 1024) SC "\n\t" \
  GL_("%2", "%3", 2048) SC :: "v"(v0), "v"(v1), "v"(v2), "s"(uni(sbase)), "s"(lds), "i"(IMM) : "memory", "scc", ##CL)
template <int IMM> DI void glds4(unsigned v0, unsigned v1, unsigned v2, unsigned v3, const void* sbase, unsigned lds) { GLDS4(NTO); }
template <int IMM> DI void glds4o(unsigned v0, unsigned v1, unsigned v2, unsigned v3, const void* sbase, unsigned lds) { GLDS4(NTO, RSV); }
template <int IMM> DI void glds4r(unsigned v0, unsigned v1, unsigned v2, unsigned v3, const void* sbase, unsigned lds) { GLDS4(NTA, RSV); }
template <int IMM> DI void glds4n(unsigned v0, unsigned v1, unsigned v2, unsigned v3, const void* sbase, unsigned lds) { GLDS4(" nt", RSV); }
template <int IMM> DI void glds3n(unsigned v0, unsigned v1, unsigned v2, const void* sbase, unsigned lds) { GLDS3(" nt", RSV); }
template <int IMM> DI void glds3r(unsigned v0, unsigned v1, unsigned v2, const void* sbase, unsigned lds) { GLDS3(NTB, RSV); }
template <int R> DI void sload(unsigned voff, const void* sbase) {
  asm volatile("s_nop 4\n\tglobal_load_dwordx4 v[%c0:%c1], %2, %3 sc1" ::"i"(R), "i"(R + 3), "v"(voff), "s"(uni(sbase)) : "memory", RSV);
}
DI float fadd(float a, float b) { float o; asm("v_add_f32 %0, %1, %2" : "=v"(o) : "v"(a), "v"(b)); return o; }
DI float fmul(float a, float b) { float o; asm("v_mul_f32 %0, %1, %2" : "=v"(o) : "v"(a), "v"(b)); return o; }
DI float fsq(float a, float c) { asm("v_fmac_f32 %0, %1, %1" : "+v"(c) : "v"(a)); return c; }
#define RA_(o, a, b) "v_add_f32 %" #o ", v[%c6+" #a "], v[%c6+" #b "]\n\t"
#define RB2_(o, a) "v_add_f32 %" #o ", v[%c6+" #a "], %" #o "\n\t"
#define RM_(o, a, b) "v_max3_u32 %" #o ", v[%c6+" #a "], v[%c6+" #b "], %" #o "\n\t"
template <int R> DI void rsum4(float* a, unsigned& m, unsigned& n) {
  asm volatile(RA_(0, 0, 4) RA_(1, 1, 5) RM_(4, 0, 1) RA_(2, 2, 6) RA_(3, 3, 7) RM_(5, 2, 3) RB2_(0, 8) RM_(4, 4, 5) RB2_(1, 9) RM_(5, 6, 7)
               RB2_(2, 10) RM_(4, 8, 9) RB2_(3, 11) RM_(5, 10, 11) RB2_(0, 12) RB2_(1, 13) RM_(4, 12, 13) RB2_(2, 14) RB2_(3, 15) RM_(5, 14, 15)
               : "=&v"(a[0]), "=&v"(a[1]), "=&v"(a[2]), "=&v"(a[3]), "+v"(m), "+v"(n) : "i"(R) : RSV);
}
template <int R> DI void wload2(unsigned voff, const void* sbase) {
  asm volatile("s_nop 4\n\tglobal_load_dwordx4 v[%c0:%c1], %2, %3" NTW ::"i"(R), "i"(R + 3), "v"(voff), "s"(uni(sbase)) : "memory", RSV2);
}
template <int R> DI void wload(unsigned voff, const void* sbase) {
  asm volatile("s_nop 4\n\tglobal_load_dwordx4 v[%c0:%c1], %2, %3" NTW ::"i"(R), "i"(R + 3), "v"(voff), "s"(uni(sbase)) : "memory", RSV);
}
template <int RA, int RB> DI unsigned wperm(unsigned sel) {
  unsigned o;
  if (RA < 192) asm volatile("v_perm_b32 %0, v%c2, v%c1, %3" : "=v"(o) : "i"(RA), "i"(RB), "s"(sel) : RSV);
  else if (RA < 224) asm volatile("v_perm_b32 %0, v%c2, v%c1, %3" : "=v"(o) : "i"(RA), "i"(RB), "s"(sel) : RSV1);
  else asm volatile("v_perm_b32 %0, v%c2, v%c1, %3" : "=v"(o) : "i"(RA), "i"(RB), "s"(sel) : RSV2);
  return o;
}
template <int R> DI unsigned rget() { unsigned o; asm volatile("v_mov_b32 %0, v%c1" : "=v"(o) : "i"(R) : RSV); return o; }
DI float xh_sum(float v) {
  auto r = __builtin_amdgcn_permlane32_swap(__float_as_uint(v), __float_as_uint(v), false, false);
  return __uint_as_float(r[0]) + __uint_as_float(r[1]);
}
template <int CTRL> DI unsigned dppu(unsigned v) { return (unsigned)__builtin_amdgcn_update_dpp(0, (int)v, CTRL, 0xf, 0xf, true); }
template <int CTRL> DI float dppf(float v) { return __uint_as_float(dppu<CTRL>(__float_as_uint(v))); }
constexpr int DPP_X1 = 0xB1, DPP_X2 = 0x4E, DPP_HMIR = 0x141, DPP_MIR = 0x140, DPP_ROR8 = 0x128;
DI float quad_sum(float v) { v += dppf<DPP_X1>(v); v += dppf<DPP_X2>(v); return v; }
DI float oct_sum(float v) { v = quad_sum(v); v += dppf<DPP_HMIR>(v); return v; }
DI float x16_sum(float v) {
  auto r = __builtin_amdgcn_permlane16_swap(__float_as_uint(v), __float_as_uint(v), false, false);
  return __uint_as_float(r[0]) + __uint_as_float(r[1]);
}
DI float sw16(float a, float b) {
  auto r = __builtin_amdgcn_permlane16_swap(__float_as_uint(a), __float_as_uint(b), false, false);
  return fadd(__uint_as_float(r[0]), __uint_as_float(r[1]));
}
DI float sw32(float a, float b) {
  auto r = __builtin_amdgcn_permlane32_swap(__float_as_uint(a), __float_as_uint(b), false, false);
  return fadd(__uint_as_float(r[0]), __uint_as_float(r[1]));
}
DI float hi_sum(float v) {
  auto r = __builtin_amdgcn_permlane16_swap(__float_as_uint(v), __float_as_uint(v), false, false);
  v = fadd(__uint_as_float(r[0]), __uint_as_float(r[1]));
  auto q = __builtin_amdgcn_permlane32_swap(__float_as_uint(v), __float_as_uint(v), false, false);
  return fadd(__uint_as_float(q[0]), __uint_as_float(q[1]));
}
template <int N> struct Phi { unsigned tb[N]; f32x2 tr[N], e[N]; };
template <int N> DI void phi_a(const f32x2* t, Phi<N>& s) {
#pragma unroll
  for (int i = 0; i < N; ++i) s.tb[i] = pack2o(t[i][0], t[i][1]);
  __builtin_amdgcn_sched_barrier(0);
#pragma unroll
  for (int i = 0; i < N; ++i) s.tr[i] = unpk(s.tb[i]);
  __builtin_amdgcn_sched_barrier(0);
#pragma unroll
  for (int i = 0; i < N; ++i) s.e[i] = s.tr[i] * 1.44269504088896340736f;
  __builtin_amdgcn_sched_barrier(0);
}
template <int N> DI void phi_b(Phi<N>& s, unsigned* p) {
  u16x2 tm[2] = {{0, 0}, {0, 0}};
#pragma unroll
  for (int i = 0; i < N; ++i) {
    s.e[i][0] = __builtin_amdgcn_fmed3f(__builtin_amdgcn_exp2f(s.e[i][0]), 0.f, 1.f);
    tm[i & 1] = __builtin_elementwise_max(tm[i & 1], __builtin_bit_cast(u16x2, s.tb[i]));
    s.e[i][1] = __builtin_amdgcn_fmed3f(__builtin_amdgcn_exp2f(s.e[i][1]), 0.f, 1.f);
    __builtin_amdgcn_sched_barrier(0);
  }
#pragma unroll
  for (int i = 0; i < N; ++i) s.tr[i] += s.e[i];
  __builtin_amdgcn_sched_barrier(0);
#pragma unroll
  for (int i = 0; i < N; ++i) { s.tr[i][0] = fmaxf(s.tr[i][0], s.e[i][0]); s.tr[i][1] = fmaxf(s.tr[i][1], s.e[i][1]); }
  const u16x2 tx = __builtin_elementwise_max(tm[0], tm[1]);
  __builtin_amdgcn_sched_barrier(0);
#pragma unroll
  for (int i = 0; i < N; ++i) p[i] = pack2(s.tr[i][0], s.tr[i][1]);
  if (__any(tx[0] > 0xC0F0 || tx[1] > 0xC0F0)) {
#pragma unroll
    for (int i = 0; i < N; ++i) p[i] = pack2(bflo(p[i]) + 1e-6f, bfhi(p[i]) + 1e-6f);
  }
}
template <int N> DI void phi_n(const f32x2* t, unsigned* p) { Phi<N> s; phi_a<N>(t, s); phi_b<N>(s, p); }
#define RLX_AGENT __ATOMIC_RELAXED, __HIP_MEMORY_SCOPE_AGENT
DI void st32(char* ws, unsigned off, unsigned v) { __hip_atomic_store((gu32*)(ws + off), v, RLX_AGENT); }
DI void st64(char* ws, unsigned off, u64 v) { __hip_atomic_store((gu64*)(ws + off), v, RLX_AGENT); }
DI unsigned ld32(char* ws, unsigned off) { return __hip_atomic_load((gu32*)(ws + off), RLX_AGENT); }
DI u64 ld64(char* ws, unsigned off) { return __hip_atomic_load((gu64*)(ws + off), RLX_AGENT); }
#define WAIT_V(n) asm volatile("s_waitcnt vmcnt(%c0)" ::"i"(n) : "memory")
#define RAW_BAR() do { asm volatile("s_waitcnt lgkmcnt(0)" ::: "memory"); __builtin_amdgcn_s_barrier(); asm volatile("" ::: "memory"); } while (0)
#define SB0() __builtin_amdgcn_sched_barrier(0)
#define WAIT_VR(n) asm volatile("s_waitcnt vmcnt(%c0)" ::"i"(n) : "memory", RSV)
#define RAW_BARR() do { asm volatile("s_waitcnt lgkmcnt(0)" ::: "memory", RSV); __builtin_amdgcn_s_barrier(); asm volatile("" ::: "memory", RSV); } while (0)
#define KEEP2() asm volatile("" ::: "memory", RSV2)
#define RAW_BAR2() do { asm volatile("s_waitcnt lgkmcnt(0)" ::: "memory", RSV2); __builtin_amdgcn_s_barrier(); KEEP2(); } while (0)
constexpr unsigned SPIN_CAP = 1u << 18;
#define SPIN _Pragma("nounroll") for (unsigned s_ = 0; s_ < SPIN_CAP; ++s_)
constexpr unsigned SENT = 0xFFFFFFFFu;
DI unsigned sane1(unsigned v) { return min(v, 0xFFFFFFFEu); }
DI u32x4 sane(u32x4 v) { v[1] = sane1(v[1]); v[3] = sane1(v[3]); return v; }
DI unsigned chk(unsigned m, u32x4 v) { return max(max(m, v[1]), v[3]); }
DI bool wave_ok(unsigned m) { return !__any(m == SENT); }
DI unsigned key_idx(unsigned k) { return (0xFFFFu - (k & 0xFFFFu)) & (S_ - 1); }
DI float key_val(unsigned k) { const unsigned o = k >> 16; return __uint_as_float(((o & 0x8000u) ? (o & 0x7fffu) : (~o & 0xffffu)) << 16); }
DI void top2_mix(unsigned& b0, unsigned& b1, unsigned c0, unsigned c1) {
  const unsigned lo = min(b0, c0), mx = max(b1, c1);
  b0 = max(b0, c0); b1 = max(lo, mx);
}
DI void top2_lo(unsigned& b0, unsigned& b1) {
  top2_mix(b0, b1, dppu<DPP_X1>(b0), dppu<DPP_X1>(b1));
  top2_mix(b0, b1, dppu<DPP_X2>(b0), dppu<DPP_X2>(b1));
  top2_mix(b0, b1, dppu<DPP_HMIR>(b0), dppu<DPP_HMIR>(b1));
}
DI void top2_mid(unsigned& b0, unsigned& b1) {
  top2_mix(b0, b1, dppu<DPP_MIR>(b0), dppu<DPP_MIR>(b1));
  auto p = __builtin_amdgcn_permlane16_swap(b0, b0, false, false); auto q = __builtin_amdgcn_permlane16_swap(b1, b1, false, false);
  b0 = p[0]; b1 = q[0]; top2_mix(b0, b1, p[1], q[1]);
}
DI void top2_hi(unsigned& b0, unsigned& b1) {
  top2_mid(b0, b1);
  auto p = __builtin_amdgcn_permlane32_swap(b0, b0, false, false); auto q = __builtin_amdgcn_permlane32_swap(b1, b1, false, false);
  b0 = p[0]; b1 = q[0]; top2_mix(b0, b1, p[1], q[1]);
}

constexpr int K1_STAGE = 40960;
constexpr int LDS_TOTAL = 4 * K1_STAGE;
constexpr int L_STM = 0;
constexpr int L_MISC = 2048;
constexpr int L_XK = 5120;
constexpr int L_KX = 3328;
constexpr int L_ACT = 2304;
constexpr int L_CAND = 4096;
constexpr int L_WO = 16384;
constexpr int L_P = 16384, L_PK = 7 * 16384;
constexpr int L_RED = 65536;
constexpr int wo_slot(int j) { return L_WO + (j % 9) * 16384; }
constexpr int CODE_LINES = 12;
#define OW_ 8
constexpr int OW = OW_;
#define W1A_ 16
constexpr int W1A = W1A_;
#define PRIO_M 1
#define PRIO_S 3
#define GSCHED 5, 9, 13, 17, 21, 24, 27, 30, 33, 36, 39, 42, 45, 48, 48
constexpr int GS[15] = {GSCHED};
constexpr int gops(int g) { return (g / 3) * 10 + (g % 3 == 0 ? 0 : g % 3 == 1 ? 4 : 7); }
constexpr bool gs_ok() {
  bool ok = GS[0] >= 3 && GS[1] >= 6 && GS[1] <= 12 && GS[0] <= GS[1] && GS[12] == 45 && GS[13] == 48 && GS[14] == 48;
  for (int t = 0; t < 12; ++t) ok = ok && GS[t + 2] <= 3 * (t + 5) && GS[t + 2] >= 3 * (t + 3) && GS[t + 2] >= GS[t + 1] + 3;
  return ok;
}
static_assert(gs_ok(), "staging schedule");
#define WSCHED 0, 0, 0, 0, 0, 0, 0, 0, 0, 2, 4, 6, 8
constexpr int WS_[13] = {WSCHED};
#define PACE 1
#define PSLP 0
#define P_KVP 10
#define L_KVP 0
#define P_KVF 1
#define L_KVF 0
#define P_FB 8
#define L_FB 0
#define P_C 14
#define P_D 2
#define L_D 0
#define P_GU 12
#define P_E 10
#define L_E 0
#define SLEEP(n) do { if (n) __builtin_amdgcn_s_sleep(n); } while (0)
#define OUTE 1
#define P2MAP 0
#define TCH0 100
#define OUTWT 0
#define OUTWB 0
#define PFO 1
#define WOD 100
#define WODF 6
#define WODS 0
constexpr int VW = 160;

__global__ __launch_bounds__(512, 2) void fused(const u16* __restrict__ x, const u16* __restrict__ wqkv,
                                                const u16* __restrict__ wo, const u16* __restrict__ w_r,
                                                const u16* __restrict__ wg, const u16* __restrict__ wu,
                                                const u16* __restrict__ wd, u16* __restrict__ out, char* __restrict__ ws) {
  extern __shared__ __attribute__((aligned(16))) char smem[];
  STAMPT(0, 0, 0);
  u64 pc0; asm volatile("s_getpc_b64 %0" : "=s"(pc0));
  const int wave = __builtin_amdgcn_readfirstlane(threadIdx.x >> 6);
  const unsigned lds0 = (unsigned)(size_t)smem;
#define RB_(b) (((b) & 1) * 8 + (((b) >> 3) & 7))
#define HP_(b) ((((b) & 7) >> 1) * 4 + ((b) >> 6))
#define KROT_(b) ((4 * ((HP_(b) >> 2) + (RB_(b) >> 3) + 2 * ((RB_(b) >> 2) & 1))) & 15)
  unsigned voA0 = 0, voA1 = 0, voA2 = 0, voA3 = 0, voB0 = 0, voB1 = 0, voB2 = 0, voB3 = 0, voB4 = 0, voB5 = 0;
  const unsigned l4 = __builtin_amdgcn_readfirstlane(lds0 + (wave - 4) * 4096), l6 = __builtin_amdgcn_readfirstlane(lds0 + (wave - 4) * 6144);
  const int krot0 = KROT_(blockIdx.x);
  auto group = [&](auto G) {
    constexpr int t = G.v / 3, p = G.v % 3, sl = (t & 3) * K1_STAGE;
    const int kt = (t + krot0) & 15;
    if (p == 0) glds4r<sl>(voA0, voA1, voA2, voA3, (const char*)x + kt * 128, l4);
    else if (p == 1) glds3r<sl + 16384>(voB0, voB1, voB2, (const char*)wqkv + kt * 64 * (3 * D_ * 2), l6);
    else glds3r<sl + 16384 + 3072>(voB3, voB4, voB5, (const char*)wqkv + kt * 64 * (3 * D_ * 2), l6);
  };
  if (wave >= 4) {
    const int lane = threadIdx.x & 63, sw = wave - 4;
    const int r0 = RB_(blockIdx.x) * 128 + 32 * sw + (lane >> 3);
    voA0 = (unsigned)(r0 * D_ + ((lane & 7) ^ (lane >> 4)) * 8) * 2u;
    voA1 = ((voA0 + 8 * D_ * 2) ^ 64) - 1024; voA2 = voA0 + 16 * D_ * 2 - 2048; voA3 = voA1 + 16 * D_ * 2 - 2048;
    voB0 = (unsigned)((16 * sw + 4 * (lane >> 5) + ((lane >> 2) & 3)) * (3 * D_) + HP_(blockIdx.x) * 64 + ((lane >> 4) & 1) * 32 + (lane & 3) * 8) * 2u;
    voB1 = voB0 + 1024; voB2 = voB0 + 2048;
    voB3 = (voB0 ^ 32) + 8 * 3 * D_ * 2; voB4 = voB3 + 1024; voB5 = voB3 + 2048;
    asm volatile("" : "+v"(voA0), "+v"(voA1), "+v"(voA2), "+v"(voA3), "+v"(voB0), "+v"(voB1), "+v"(voB2), "+v"(voB3), "+v"(voB4), "+v"(voB5));
    sfor<0, 3>(group);
  }
  int bid = blockIdx.x; asm volatile("" : "+s"(bid));
  const int xcd = bid & 7, idx = bid >> 3;
  const int rb = RB_(bid), hp = HP_(bid);
  const int rp = xcd * 4 + (idx >> 3), cb = idx & 7;
#define WROT (4 * rp + (xcd >> 1))
#define WDIR ((xcd & 1) ? -1 : 1)
  const int hs_ = (hp + 4 + (int)(((size_t)wqkv >> 7) & 7)) & 7;
  const int g8 = ((hp >> 3) * 4 + (rb >> 2)) * 8 + hs_, kq = rb & 3;
  const int krot = KROT_(bid), posA = 1 + (hp & 3), posB = 1 + (rb & 3);
  const int wrot = WROT, wdir = WDIR;
  auto whead = [&](int t) { return (wrot + wdir * t) & 15; };
  const unsigned rs_slot = WS_RS + (rb * 16 + ((krot + posA) & 15)) * 1024, cs_slot = WS_CS + (hp * 16 + ((krot + posB) & 15)) * 768;
  const __amdgpu_buffer_rsrc_t wsr = __builtin_amdgcn_make_buffer_rsrc(ws, 0, WS_END, 0x00020000);
#define LDW(off) __builtin_amdgcn_raw_buffer_load_b128(wsr, (off), 0, 16)
#define STW(v, off) __builtin_amdgcn_raw_buffer_store_b128((v), wsr, (off), 0, 16)
#define STW1(v, off) __builtin_amdgcn_raw_buffer_store_b32((v), wsr, (off), 0, 16)
  float* stm = (float*)(smem + L_STM);
  volatile LAS int* misc = (volatile LAS int*)(smem + L_MISC);
#define LANES() int tid = threadIdx.x; asm volatile("" : "+v"(tid)); const int lane = tid & 63, r = lane & 31, h = lane >> 5; (void)r; (void)h
#define EPI() const int erow = tid >> 3, eseg = tid & 7; const size_t ego = (size_t)(rp * 64 + erow) * D_ + cb * 128 + eseg * 8
  u32x4 xv[2], wv[2];
  f32x2 xf[8], wf2[8];
  float sw_;
  auto unpack_xw = [&]() {
    f32x2 a = {0.f, 0.f};
#pragma unroll
    for (int i = 0; i < 8; ++i) { xf[i] = unpk(xv[i >> 2][i & 3]); wf2[i] = unpk(wv[i >> 2][i & 3]); a += wf2[i]; }
    sw_ = oct_sum(a[0] + a[1]);
#pragma unroll
    for (int i = 0; i < 8; ++i) asm volatile("" : "+v"(xf[i]), "+v"(wf2[i]));
    asm volatile("" : "+v"(sw_));
  };
  unsigned tsv = 0; (void)tsv;
  u32x4 wf[24];

  auto wtr = [&](auto I) {
    constexpr int m = I.v >> 3, c = I.v & 7, R = VW + 32 * m + (c >> 1);
    const unsigned sel = (c & 1) ? 0x07060302u : 0x05040100u;
    wf[I.v] = (u32x4){wperm<R, R + 4>(sel), wperm<R + 8, R + 12>(sel), wperm<R + 16, R + 20>(sel), wperm<R + 24, R + 28>(sel)};
  };

  if (wave < 4) {
    LANES();
    const int c16 = lane & 15, g4 = lane >> 4;
    const int trl = (c16 >> 2) * 64 + (c16 & 3) * 8;
    const int wr = wave >> 1, wc = wave & 1;
    const int head = 2 * hp + wc;
    const unsigned kvp_off = WS_KVP + (head * 16 + rb) * 4096 + wr * 2048 + lane * 16;
    const unsigned zp_off = WS_ZP + (head * 16 + rb) * 128;
    const unsigned o_off = WS_O + ((rb * 4 + 2 * wr) * 32 + head) * 2048 + lane * 16;
    const unsigned fb_off = lane < 8 ? WS_FB + ((rb * 2 + wr) * 32 + head) * 128 + lane * 16 : OOB;
    unsigned pfa, pfb = OOB;
    unsigned pfc = lane < 8 ? fb_off : lane < 24 ? rs_slot + wave * 256 + (lane - 8) * 16
        : lane < 40 ? (wave < 3 ? cs_slot + wave * 256 : WS_KVF + hp * 4096 + rb * 256) + (lane - 24) * 16
        : lane < 48 ? (wave < 2 ? zp_off + (lane & 7) * 16 : (wave == 2 && rb < 2) ? WS_ZF + (2 * hp + rb) * 128 + (lane & 7) * 16 : OOB) : OOB;
    if (wave == 0) pfa = WS_ST2 + (rp * 8 + cb) * 1024 + lane * 16;
    else if (wave == 1) pfa = WS_GU + (g8 * 4 + kq) * 1024 + lane * 16;
    else pfa = WS_YP + (((wave - 2) * 8 + 2 * kq + (lane >> 5)) * 64 + g8) * 512 + (lane & 31) * 16;
    if (wave == 0) pfb = lane < 32 ? WS_XS + (rp * 2 + (lane >> 4)) * 2048 + cb * 256 + (lane & 15) * 16 : lane < 40 ? WS_FD + (cb * 32 + rp) * 128 + (lane & 7) * 16 : OOB;
    u32x4 S4 = {SENT, SENT, SENT, SENT};
    asm volatile("" : "+v"(pfa), "+v"(pfb), "+v"(pfc), "+v"(S4));
    __builtin_amdgcn_s_setprio(PRIO_M);
    f32x4 acc[4][6];
    {
      int ao[2][2], bo[2][2];
#pragma unroll
      for (int i = 0; i < 2; ++i) {
        ao[0][i] = (64 * wr + c16) * 128 + (((4 * i + g4) ^ (c16 >> 1)) * 16); ao[1][i] = ao[0][i] + 2 * K1_STAGE;
        bo[0][i] = 16384 + g4 * 3072 + wc * 256 + trl + ((i ^ (g4 & 1)) * 32); bo[1][i] = bo[0][i] + 2 * K1_STAGE;
        asm volatile("" : "+v"(ao[0][i]), "+v"(ao[1][i]), "+v"(bo[0][i]), "+v"(bo[1][i]));
      }
#pragma unroll
      for (int i = 0; i < 4; ++i)
#pragma unroll
        for (int j = 0; j < 6; ++j) { acc[i][j] = (f32x4){0.f, 0.f, 0.f, 0.f}; asm volatile("" : "+v"(acc[i][j])); }
      struct Frag { u32x4 a[4]; u32x2 bl[6], bh[6]; };
      Frag FA, FB;
#define K1_B(j) ((u32x4){C.bl[j][0], C.bl[j][1], C.bh[j][0], C.bh[j][1]})
#define K1_LA(i) N.a[i] = *(const u32x4*)(smem + ao[sn >> 1][hf] + (sn & 1) * K1_STAGE + (i) * 2048)
#define K1_LL(j) N.bl[j] = trread(smem + bo[sn >> 1][(j) & 1] + (sn & 1) * K1_STAGE + hf * 12288 + ((j) >> 1) * 1024)
#define K1_LH(j) N.bh[j] = trread(smem + bo[sn >> 1][(j) & 1] + (sn & 1) * K1_STAGE + hf * 12288 + ((j) >> 1) * 1024 + 512)
#define K1_M(i, j) if ((j) < 2) mfma16i(K1_B(j), C.a[i], acc[i][j]); else mfma16i(C.a[i], K1_B(j), acc[i][j]); SB0()
#define K1_Q(i, j, ld) K1_M(i, j); ld; SB0()
      auto sub = [&](Frag& C, Frag& N, int sn, int hf) {
        K1_Q(0, 0, K1_LL(0)); K1_Q(1, 0, K1_LH(0)); K1_Q(0, 1, K1_LA(0)); K1_Q(1, 1, K1_LA(1)); K1_Q(0, 2, K1_LL(1)); K1_Q(1, 2, K1_LH(1));
        K1_Q(0, 3, K1_LL(2)); K1_Q(1, 3, K1_LH(2)); K1_Q(2, 0, K1_LL(3)); K1_Q(3, 0, K1_LH(3)); K1_Q(2, 1, K1_LA(2)); K1_Q(3, 1, K1_LA(3));
        K1_Q(2, 2, K1_LL(4)); K1_Q(3, 2, K1_LH(4)); K1_Q(2, 3, K1_LL(5)); K1_Q(3, 3, K1_LH(5));
        K1_M(0, 4); K1_M(1, 4); K1_M(2, 4); K1_M(3, 4); K1_M(0, 5); K1_M(1, 5); K1_M(2, 5);
        asm volatile("s_waitcnt lgkmcnt(0)" : "+v"(N.bh[5]));
        K1_M(3, 5);
      };
      RAW_BAR();
      STAMPT(0, 1, 0);
      {
        Frag& N = FA; const int sn = 0, hf = 0;
        K1_LL(0); K1_LH(0); K1_LA(0); K1_LA(1); K1_LL(1); K1_LH(1); K1_LL(2); K1_LH(2); K1_LL(3); K1_LH(3); K1_LA(2); K1_LA(3); K1_LL(4); K1_LH(4); K1_LL(5); K1_LH(5);
      }
#pragma unroll 1
      for (int it = 0; it < 4; ++it) {
#pragma unroll
        for (int s = 0; s < 4; ++s) {
          sub(FA, FB, s, 1);
          if (s == 0) WAIT_V(0);
          if (it == 0) { if (s == PFO) STW(S4, pfc); if (s == PFO + 1) STW(S4, kvp_off); if (s == PFO + 2) STW(S4, kvp_off + 1024); }
          if (it == 1) { if (s == 0) STW(S4, pfa); if (s == 1) STW(S4, pfb); }
          TSL(it * 4 + s);
          if (!(s & 1) || it < 3) RAW_BAR();
          else if (s == 3) { asm volatile("s_waitcnt lgkmcnt(0)" ::: "memory"); misc[8 + wave] = 3; }
          sub(FB, FA, (s + 1) & 3, 0);
        }
      }
    }
    asm volatile("s_nop 15" ::: "memory");
    TSL(16); if (wave == 0) TSDUMP(0);
    STAMPT(0, 2, 0);
    {
      f32x4 m4[4], i4[4]; float cs[4];
      SPIN {
        const int ok = misc[12 + (lane & 3)];
        asm volatile("" ::: "memory");
#pragma unroll
        for (int i = 0; i < 4; ++i) {
          cs[i] = stm[256 + wc * 96 + 32 + 16 * i + c16];
          m4[i] = *(const f32x4*)(stm + 64 * wr + 16 * i + 4 * g4); i4[i] = *(const f32x4*)(stm + 128 + 64 * wr + 16 * i + 4 * g4);
        }
        SB0();
        if (__all(ok != 0)) break;
      }
      const u32x4 ONES4 = {0x3F803F80u, 0x3F803F80u, 0x3F803F80u, 0x3F803F80u};
      f32x2 tk[16], tv[16];
#define PR(v, i) ((f32x2){(v)[2 * (i)], (v)[2 * (i) + 1]})
#pragma unroll
      for (int i = 0; i < 16; ++i) {
        tk[i] = PR(acc[(i >> 1) & 3][2 + (i >> 3)], i & 1) - PR(m4[(i >> 1) & 3], i & 1) * cs[i >> 3];
        tv[i] = PR(acc[(i >> 1) & 3][4 + (i >> 3)], i & 1) - PR(m4[(i >> 1) & 3], i & 1) * cs[2 + (i >> 3)];
      }
      SB0();
#pragma unroll
      for (int i = 0; i < 16; ++i) { tk[i] *= PR(i4[(i >> 1) & 3], i & 1); tv[i] *= PR(i4[(i >> 1) & 3], i & 1); }
      SB0();
      unsigned kp[16], vp[16];
#pragma unroll
      for (int i = 0; i < 16; ++i) vp[i] = pack2o(tv[i][0], tv[i][1]);
      phi_n<16>(tk, kp);
#define F4_(p, i) ((u32x4){(p)[4 * (i)], (p)[4 * (i) + 1], (p)[4 * (i) + 2], (p)[4 * (i) + 3]})
      const f32x4 Z4 = {0.f, 0.f, 0.f, 0.f};
      u32x4 vk[2], vg[2];
#pragma unroll
      for (int j = 0; j < 2; ++j) { vk[j] = wr ? F4_(vp, 2 + j) : F4_(vp, j); vg[j] = wr ? F4_(vp, j) : F4_(vp, 2 + j); }
      f32x4 keep[2], give[2], zq[2];
#pragma unroll
      for (int d = 0; d < 2; ++d) { give[d] = mfma16(F4_(kp, 2 * d), vg[0], Z4); zq[d] = mfma16(ONES4, F4_(kp, 2 * d), Z4); }
#pragma unroll
      for (int d = 0; d < 2; ++d) { give[d] = mfma16(F4_(kp, 2 * d + 1), vg[1], give[d]); zq[d] = mfma16(ONES4, F4_(kp, 2 * d + 1), zq[d]); }
#pragma unroll
      for (int d = 0; d < 2; ++d) keep[d] = mfma16(F4_(kp, 2 * d), vk[0], Z4);
#pragma unroll
      for (int d = 0; d < 2; ++d) keep[d] = mfma16(F4_(kp, 2 * d + 1), vk[1], keep[d]);
      SB0();
      const float zv = (lane & 16) ? zq[1][0] : zq[0][0];
      char* xk = smem + L_XK + wave * 2304 + lane * 16;
      const char* xp = smem + L_XK + (wave ^ 2) * 2304 + lane * 16;
      *(f32x4*)xk = give[0]; *(f32x4*)(xk + 1024) = give[1]; *(float*)(xk + 2048 - lane * 12) = zv;
      asm volatile("" ::: "memory");
      misc[16 + wave] = 1;
      f32x4 p0, p1; float pz;
      SPIN {
        const int ok = misc[16 + (wave ^ 2)];
        asm volatile("" ::: "memory");
        p0 = *(const f32x4*)xp; p1 = *(const f32x4*)(xp + 1024); pz = *(const float*)(xp + 2048 - lane * 12);
        SB0();
        if (ok == 1) break;
      }
      STW(sane(__builtin_bit_cast(u32x4, keep[0] + p0)), kvp_off);
      STW(sane(__builtin_bit_cast(u32x4, keep[1] + p1)), kvp_off + 1024);
      STW1(sane1(__float_as_uint(zv + pz)), (lane < 32 && wr == 0) ? zp_off + lane * 4 : OOB);
    }
    STAMPT(0, 3, 0);
    unsigned qp[16];
    Phi<16> qs;
    const unsigned po = WS_KVP + ((2 * hp + (rb >> 3)) * 16 + h) * 4096 + (2 * ((rb >> 2) & 1) + ((lane >> 4) & 1)) * 1024 + (16 * (rb & 3) + (lane & 15)) * 16;
    u32x4 pv[8] = {};
    {
      f32x4 c4[2]; float m_b[4], rinv[4];
#pragma unroll
      for (int i = 0; i < 2; ++i) c4[i] = *(const f32x4*)(stm + 256 + wc * 96 + 16 * i + 4 * g4);
#pragma unroll
      for (int i = 0; i < 4; ++i) { m_b[i] = stm[64 * wr + 16 * i + c16]; rinv[i] = stm[128 + 64 * wr + 16 * i + c16]; }
      SB0();
      f32x2 tq[16];
#pragma unroll
      for (int i = 0; i < 16; ++i) tq[i] = PR(acc[i >> 2][(i >> 1) & 1], i & 1) - PR(c4[(i >> 1) & 1], i & 1) * m_b[i >> 2];
      SB0();
#pragma unroll
      for (int i = 0; i < 16; ++i) tq[i] *= rinv[i >> 2];
      SB0();
      phi_a<16>(tq, qs);
#pragma unroll
      for (int i = 0; i < 16; ++i) asm volatile("" : "+v"(qs.tb[i]), "+v"(qs.tr[i]), "+v"(qs.e[i]));
      if (wave == 0) {
        SLEEP(P_KVP);
#pragma unroll
        for (int p = 0; p < 8; ++p) pv[p] = LDW(po + p * 8192);
      }
      asm volatile("" ::: "memory");
    }
    STAMPT(0, 4, 0);
    if (wave == 1 && rb < 2) {
      SLEEP(P_KVP);
      SPIN {
        unsigned m = 0;
#pragma unroll
        for (int p = 0; p < 2; ++p) { pv[p] = LDW(WS_ZP + (2 * hp + rb) * 2048 + p * 1024 + lane * 16); m = chk(max(max(m, pv[p][0]), pv[p][2]), pv[p]); }
        if (wave_ok(m)) break;
        asm volatile("" ::: "memory");
      }
      f32x4 s = __builtin_bit_cast(f32x4, pv[0]) + __builtin_bit_cast(f32x4, pv[1]);
#pragma unroll
      for (int i = 0; i < 4; ++i) { s[i] += dppf<DPP_ROR8>(s[i]); s[i] = xh_sum(x16_sum(s[i])); }
      if (lane < 16) st64(ws, WS_ZF + (2 * hp + rb) * 128 + (lane >> 3) * 64 + (lane & 3) * 16 + ((lane >> 2) & 1) * 8,
                          ((u64)sane1(pack2(s[2], s[3])) << 32) | pack2(s[0], s[1]));
    }
    if (wave == 0) {
      SPIN {
        unsigned m = 0;
#pragma unroll
        for (int p = 0; p < 8; ++p) m = chk(m, pv[p]);
        CNT(0);
        if (wave_ok(m)) break;
        asm volatile("" ::: "memory");
        SLEEP(L_KVP);
#pragma unroll
        for (int p = 0; p < 8; ++p) pv[p] = LDW(po + p * 8192);
      }
      f32x4 s = __builtin_bit_cast(f32x4, pv[0]);
#pragma unroll
      for (int p = 1; p < 8; ++p) s += __builtin_bit_cast(f32x4, pv[p]);
      const unsigned p0 = pack2(xh_sum(s[0]), xh_sum(s[1])), p1 = pack2(xh_sum(s[2]), xh_sum(s[3]));
      auto a = __builtin_amdgcn_permlane16_swap(p0, p0, false, false);
      auto b = __builtin_amdgcn_permlane16_swap(p1, p1, false, false);
      STW(sane((u32x4){a[0], b[0], a[1], b[1]}), lane < 16 ? WS_KVF + hp * 4096 + rb * 256 + lane * 16 : OOB);
      STAMPT(1, 2, 0);
    }
    phi_b<16>(qs, qp);
#pragma unroll
    for (int i = 0; i < 16; ++i) asm volatile("" : "+v"(qp[i]));
    if (wave == 0) misc[7] = 1;
    else SPIN { if (misc[7] == 1) break; __builtin_amdgcn_s_sleep(1); }
    SLEEP(P_KVF);
    {
      u32x4 k0, k1, z0;
      SPIN {
        k0 = LDW(WS_KVF + head * 2048 + lane * 16); k1 = LDW(WS_KVF + head * 2048 + 1024 + lane * 16);
        z0 = LDW(WS_ZF + head * 128 + g4 * 16);
        CNT(1);
        if (wave_ok(chk(chk(chk(0u, k0), k1), z0))) break;
        asm volatile("" ::: "memory");
        SLEEP(L_KVF);
      }
      STAMPT(0, 5, 0);
      const f32x4 Z4 = {0.f, 0.f, 0.f, 0.f};
      f32x4 X[8], Dn[4];
#pragma unroll
      for (int i = 0; i < 4; ++i) Dn[i] = mfma16(z0, F4_(qp, i), Z4);
#pragma unroll
      for (int i = 0; i < 8; ++i) X[i] = mfma16((i & 1) ? k1 : k0, F4_(qp, i >> 1), Z4);
      SB0();
      float ri[4]; unsigned a[16]; f32x2 f[16];
#pragma unroll
      for (int i = 0; i < 4; ++i) ri[i] = __builtin_amdgcn_rcpf(rbf(rbf(Dn[i][0]) + 1e-6f));
      SB0();
#pragma unroll
      for (int i = 0; i < 16; ++i) a[i] = pack2o(X[i >> 1][2 * (i & 1)], X[i >> 1][2 * (i & 1) + 1]);
      SB0();
#pragma unroll
      for (int i = 0; i < 16; ++i) f[i] = unpk(a[i]);
      SB0();
#pragma unroll
      for (int i = 0; i < 16; ++i) f[i] *= ri[i >> 2];
      SB0();
#pragma unroll
      for (int i = 0; i < 4; ++i) {
        STW(((u32x4){pack2(f[4 * i][0], f[4 * i][1]), pack2(f[4 * i + 1][0], f[4 * i + 1][1]), pack2(f[4 * i + 2][0], f[4 * i + 2][1]), pack2(f[4 * i + 3][0], f[4 * i + 3][1])}),
            o_off + (i >> 1) * 65536 + (i & 1) * 1024);
        SB0();
      }
    }
    if (wave == 0) misc[6] = 1;
    STAMPT(0, 6, 0);
    {
      EPI();
      const int rt = wave & 1, kh = wave >> 1;
      const unsigned ob = WS_O + ((rp * 2 + rt) * 32 + 16 * kh) * 2048 + lane * 16;
#define LDO(t, st) __builtin_amdgcn_raw_buffer_load_b128(wsr, ob + (st) * 1024, whead(t) * 2048, 16)
      int woff[2];
#pragma unroll
      for (int i = 0; i < 2; ++i) woff[i] = kh * 8192 + (g4 >> 1) * 2048 + (g4 & 1) * 512 + trl + ((i ^ (g4 & 1)) * 32);
      struct Frag2 { u32x2 lo[8], hi[8]; };
      Frag2 G[2];
      auto ld2 = [&](Frag2& N, int sn, int nt, int hi) {
        const char* p = smem + sn + woff[nt & 1] + (nt >> 2) * 1024 + ((nt >> 1) & 1) * 256;
        if (hi) N.hi[nt] = trread(p + 4096); else N.lo[nt] = trread(p);
      };
      WAIT_V(0);
      STW(((u32x4){1u, 1u, 1u, 1u}), fb_off);
      RAW_BAR();
#pragma unroll
      for (int i = 0; i < 16; ++i) ld2(G[0], wo_slot(0), i >> 1, i & 1);
      f32x4 a2[2][8];
#pragma unroll
      for (int i = 0; i < 16; ++i) a2[i & 1][i >> 1] = (f32x4){0.f, 0.f, 0.f, 0.f};
      SLEEP(P_FB);
      SPIN { CNT(2); if (wave_ok(ld32(ws, WS_FB + (rp * 32 + 16 * kh + (lane & 15)) * 128))) break; SLEEP(L_FB); }
      asm volatile("" ::: "memory");
      STAMPT(0, 7, 0);
      char* pr[2];
#pragma unroll
      for (int i = 0; i < 2; ++i) pr[i] = smem + L_P + kh * L_PK + (32 * rt + c16) * 512 + (((4 * i + g4) ^ (c16 & 7)) * 16);
      u32x4 of[16][2];
#pragma unroll
      for (int t = 0; t < OW; ++t) { of[t][0] = LDO(t, 0); of[t][1] = LDO(t, 1); }
      SB0();
      sfor<0, 16>([&](auto T) {
        constexpr int t = T.v;
        Frag2& C = G[t & 1]; Frag2& N = G[(t + 1) & 1];
#pragma unroll
        for (int i = 0; i < 16; ++i) {
          const int nt = i >> 1, st = i & 1;
          a2[st][nt] = mfma16((u32x4){C.lo[nt][0], C.lo[nt][1], C.hi[nt][0], C.hi[nt][1]}, of[t][st], a2[st][nt]);
          SB0();
          if ((t & 1) && t <= 11 && i == 8) {
            asm volatile("s_waitcnt lgkmcnt(8)" ::: "memory"); __builtin_amdgcn_s_barrier(); asm volatile("" ::: "memory");
          }
          if (t < 15) ld2(N, wo_slot(t + 1), nt, st);
          if (t > 0 && t - 1 + OW < 16 && (i == 3 || i == 11)) of[(t - 1 + OW) & 15][i >> 3] = LDO(t - 1 + OW, i >> 3);
          if (t - 1 + OW == 16 && (i == 3 || i == 11)) { xv[i >> 3] = *(const u32x4*)(x + ego + (i >> 3) * 64); wv[i >> 3] = *(const u32x4*)(w_r + cb * 128 + eseg * 8 + (i >> 3) * 64); }
          if (t == 15 && i >= 4) *(f32x4*)(pr[(nt - 2) & 1] + ((nt - 2) >> 1) * 128 + st * 8192) = a2[st][(nt - 2) & 7];
          SB0();
        }
      });
      STAMPT(0, 8, 0);
#pragma unroll
      for (int i = 12; i < 16; ++i) *(f32x4*)(pr[(i >> 1) & 1] + (i >> 2) * 128 + (i & 1) * 8192) = a2[i & 1][i >> 1];
    }
    unpack_xw();
    __builtin_amdgcn_s_setprio(0);
  } else {
    LANES();
    const int sw = wave - 4, t2 = tid - 256;
    __builtin_amdgcn_s_setprio(PRIO_S);
    unsigned hs_g0 = 0, hs_cur = 0;
    u32x4 ms_exp = {0, 0, 0, 0};
    bool hs_act = true, ms_act = (bid == 0) && (sw == 1);
    const unsigned hs_off = (lane == 1 ? SY_GO : SY_CH) + bid * 4;
    const unsigned re_off = t2 == 0 ? SY_RE + bid * 4 : OOB;
    const unsigned ms_off = lane * 16;
    const char* wsu = uni(ws);
#define HS_PROBE() asm volatile("s_nop 4\n\tglobal_load_dword v156, %0, %1 sc1" :: "v"(hs_off), "s"(wsu) : "memory", RSV)
#define MS_PROBE() asm volatile("s_nop 4\n\tglobal_load_dwordx4 v[152:155], %0, %1 offset:2048 sc1" :: "v"(ms_off), "s"(wsu) : "memory", RSV)
    auto hs_step = [&](bool first) {
      if (hs_act) {
        const unsigned hv_ = rget<156>();
        hs_act = __builtin_amdgcn_readlane(hv_, 1) == hs_g0;
        const unsigned c = __builtin_amdgcn_readlane(hv_, 0);
        if (sw == 0 && (first || c != hs_cur)) STW1(hv_, re_off);
        hs_cur = c;
        HS_PROBE();
      }
      if (ms_act) {
        const unsigned d = ((rget<152>() ^ ms_exp[0]) | (rget<153>() ^ ms_exp[1])) | ((rget<154>() ^ ms_exp[2]) | (rget<155>() ^ ms_exp[3]));
        ms_act = __any(d != 0u);
        STW(ms_exp, ms_act ? OOB : SY_GO + ms_off);
        MS_PROBE();
      }
    };
    HS_PROBE();
    if (ms_act) {
      MS_PROBE();
      asm volatile("s_nop 4\n\tglobal_load_dwordx4 v[148:151], %0, %1 offset:1024 sc1" :: "v"(ms_off), "s"(wsu) : "memory", RSV);
    }
    if (sw == 3) {
      const char* cp = (const char*)(pc0 - (pc0 & 127)) + (idx * CODE_LINES + (lane < CODE_LINES ? lane : 0)) * 128;
      asm volatile("global_load_dword v157, %0, off" :: "v"(cp) : "memory", RSV);
    }
    const int gr = (g8 >> 3) * 7 + min(hs_, 6);
    const bool real = hs_ != 7;
    const unsigned voG = (unsigned)((lane >> 3) * 8 * F_ + (lane & 7) * 8) * 2u, voD = (unsigned)((lane >> 3) * 8 * D_ + (lane & 7) * 8) * 2u;
    const size_t gofs = ((size_t)(kq * 256 + sw * 64) * F_ + gr * 64) * 2;
    const char* pG = (const char*)wg + gofs;
    const char* pU = (const char*)wu + gofs;
    const char* pD = (const char*)(wd + (size_t)(gr * 64) * D_ + kq * 256 + sw * 64);
    auto wstep = [&](auto I) {
      constexpr int i = I.v;
      if (i < 8) wload<VW + 4 * i>(voG, pG + (i & 7) * (F_ * 2));
      else if (i < 16) wload<VW + 4 * i>(voG, pU + (i & 7) * (F_ * 2));
      else wload2<VW + 4 * i>(voD, pD + (i & 7) * (D_ * 2));
    };
    unsigned voW;
    {
      const int bi = lane >> 4, q = (lane >> 2) & 3, ch = lane & 3;
      voW = (unsigned)((4 * (bi >> 1) + q) * D_ + (bi & 1) * 32 + (ch ^ (bi & 2)) * 8) * 2u;
    }
    const char* baseW = (const char*)(wo + (size_t)((16 * (sw >> 1)) * 32 + 16 * (sw & 1)) * D_ + cb * 128) - 1024;
    auto stageW = [&](auto J, bool rsv) {
      const char* s_ = baseW + (size_t)whead(J.v) * (32 * D_ * 2);
      if (rsv) glds4o<wo_slot(J.v)>(voW + 1024, voW + 128, voW + 8 * D_ * 2 - 1024, voW + 8 * D_ * 2 + 128 - 2048, s_, l4);
      else glds4<wo_slot(J.v)>(voW + 1024, voW + 128, voW + 8 * D_ * 2 - 1024, voW + 8 * D_ * 2 + 128 - 2048, s_, l4);
    };
    int sta[4], stb[3];
    {
      const int row = t2 >> 1, key = (row >> 1) & 7;
#pragma unroll
      for (int c = 0; c < 4; ++c) { sta[c] = (posA & 3) * K1_STAGE + row * 128 + (((4 * (t2 & 1) + c) ^ key) * 16); asm volatile("" : "+v"(sta[c]) :: RSV); }
#pragma unroll
      for (int i = 0; i < 3; ++i) {
        const int G = sw * 3 + i, hd = G >= 6 ? 1 : 0, mat = (G - 6 * hd) >> 1;
        stb[i] = (posB & 3) * K1_STAGE + 16384 + (lane >> 4) * 6144 + mat * 1024 + hd * 256 + ((lane & 15) >> 2) * 64 + (G & 1) * 32 + (lane & 3) * 8;
        asm volatile("" : "+v"(stb[i]) :: RSV);
      }
    }
    unsigned rs_dst = rs_slot + t2 * 4, cs_dst = lane < 48 ? cs_slot + (sw * 48 + lane) * 4 : OOB;
    unsigned rs_off = WS_RS + rb * 16384 + (lane >> 4) * 1024 + sw * 256 + (lane & 15) * 16;
    unsigned cs_off = WS_CS + hp * 12288 + (lane >> 4) * 768 + sw * 192 + min(lane & 15, 11) * 16;
    asm volatile("" : "+v"(rs_dst), "+v"(cs_dst), "+v"(rs_off), "+v"(cs_off) :: RSV);
    int stm_dst = ((lane >> 5) * 128 + sw * 32 + (lane & 15) * 2 + ((lane >> 4) & 1)) * 4, cs_put = (lane & 15) < 12 ? (256 + sw * 48 + (lane & 15) * 4 + (lane >> 4)) * 4 : 1792 + lane * 4;
    asm volatile("" : "+v"(stm_dst), "+v"(cs_put) :: RSV);
    auto st_load = [&]() { sfor<0, 4>([&](auto I) { sload<224 + 4 * I.v>(rs_off, wsu + I.v * 4096); sload<240 + 4 * I.v>(cs_off, wsu + I.v * 3072); }); };
    sfor<3, GS[0]>(group);
    WAIT_VR(gops(GS[0]) - 10 + 1); RAW_BARR();
    TSL(14);
    sfor<GS[0], GS[1]>(group);
    WAIT_VR(gops(GS[1]) - 20); RAW_BARR();
    hs_g0 = __builtin_amdgcn_readlane(rget<156>(), 1);
    if (ms_act) {
      const u32x4 re = {rget<152>(), rget<153>(), rget<154>(), rget<155>()}, g0 = {rget<148>(), rget<149>(), rget<150>(), rget<151>()};
#pragma unroll
      for (int j = 0; j < 4; ++j) { const unsigned c = re[j] + 1u; ms_exp[j] = (c == g0[j]) ? c + 1u : c; }
      STW(ms_exp, SY_CH + ms_off);
    }
    u32x4 ra[4] = {}; u32x2 rc[3][4] = {};
    float sa = 0.f, sb = 0.f, qa = 0.f, qb = 0.f, ca[3] = {0.f, 0.f, 0.f}, cb_[3] = {0.f, 0.f, 0.f};
    auto chunk = [&](auto C) {
      constexpr int c = C.v;
      if constexpr (c < 4) {
        asm volatile("" : "+v"(ra[c]) :: RSV);
#pragma unroll
        for (int i = 0; i < 4; ++i) { const float lo = bflo(ra[c][i]), hi = bfhi(ra[c][i]); sa = fadd(sa, lo); sb = fadd(sb, hi); qa = fsq(lo, qa); qb = fsq(hi, qb); }
        asm volatile("" : "+v"(sa), "+v"(sb), "+v"(qa), "+v"(qb) :: RSV);
      } else if constexpr (c == 4) {
        float s = fadd(sa, sb), sq = fadd(qa, qb);
        s = fadd(s, dppf<DPP_X1>(s)); sq = fadd(sq, dppf<DPP_X1>(sq));
        STW1(sane1(__float_as_uint((t2 & 1) ? sq : s)), rs_dst);
      } else if constexpr (c < 11) {
        constexpr int i = (c - 5) >> 1, j = ((c - 5) & 1) * 2;
        asm volatile("" : "+v"(rc[i][j]), "+v"(rc[i][j + 1]) :: RSV);
#pragma unroll
        for (int e = 0; e < 4; ++e) { const unsigned u = rc[i][j + (e >> 1)][e & 1]; ca[i] = fadd(ca[i], bflo(u)); cb_[i] = fadd(cb_[i], bfhi(u)); }
        asm volatile("" : "+v"(ca[i]), "+v"(cb_[i]) :: RSV);
      } else {
        const float c2 = fadd(ca[2], cb_[2]);
        STW1(sane1(__float_as_uint(sw32(sw16(fadd(ca[0], cb_[0]), fadd(ca[1], cb_[1])), sw16(c2, c2)))), cs_dst);
      }
    };
    sfor<0, 12>([&](auto T) {
      constexpr int t = T.v;
      TSL(t);
      if (t < 4 && posA == t + 1) {
#pragma unroll
        for (int c = 0; c < 4; ++c) ra[c] = *(const u32x4*)(smem + sta[c]);
      }
      if (t < 4 && posB == t + 1) {
#pragma unroll
        for (int i = 0; i < 3; ++i)
#pragma unroll
          for (int j = 0; j < 4; ++j) rc[i][j] = trread(smem + (stb[i] ^ ((j >> 1) * 32)) + (j >> 1) * 3072 + (j & 1) * 512);
      }
      if (t == 2) HS_PROBE();
      if (t >= 4) hs_step(t == 4);
      sfor<GS[t + 1], GS[t + 2]>([&](auto G) {
        group(G);
        constexpr int u = t - 4, p = G.v - GS[t + 1];
        constexpr int ord[12] = {0, 5, 6, 1, 7, 8, 2, 9, 10, 3, 11, 4};
        if constexpr (u >= 0 && u < 4 && p < 3) chunk(IC<ord[(3 * u + p) % 12]>{});
      });
      if (P2MAP == 0 && t >= TCH0 && t < TCH0 + 8 && sw == ((t - TCH0) & 3))
        asm volatile("s_nop 4\n\tglobal_load_dword v157, %0, %1" :: "v"(lane * 128), "s"(uni((const char*)wo + idx * 65536 + ((t - TCH0 + xcd) & 7) * 8192)) : "memory", RSV);
      if (P2MAP == 1 && t >= TCH0 && t < TCH0 + 4 && sw == ((t - TCH0) & 3))
        asm volatile("s_nop 4\n\tglobal_load_dword v157, %0, %1" :: "v"((lane >> 3) * 2048 + (lane & 7) * 128), "s"(uni((const char*)wo + idx * 65536 + ((t - TCH0 + (rp >> 3)) & 3) * 16384 + (cb & 4) * 256)) : "memory", RSV);
      if (real) sfor<WS_[t], WS_[t + 1]>(wstep);
      TSL(16 + t);
      WAIT_VR(t < 11 ? gops(GS[t + 2]) - 10 * (t + 3) : gops(GS[13]) - gops(GS[12]));
      TSL(32 + t);
      RAW_BARR();
    });
    TSL(12);
    hs_step(false);
    auto st_put = [&]() -> bool {
      float a[8]; unsigned m = 0, n = 0;
      rsum4<224>(a, m, n); rsum4<240>(a + 4, m, n);
      const float s1 = sw16(a[0], a[2]), q1 = sw16(a[1], a[3]);
      const float cs = sw32(sw16(a[4], a[5]), sw16(a[6], a[7]));
      const float mean = sw32(s1, s1) * (1.0f / 1024.0f);
      const float var = fmaxf(__builtin_fmaf(sw32(q1, q1), 1.0f / 1024.0f, -(mean * mean)), 0.f);
      const float rinv = __builtin_amdgcn_rcpf(rbf(__builtin_amdgcn_sqrtf(rbf(rbf(var) + 1e-5f))));
      *(float*)(smem + L_STM + stm_dst) = (lane & 32) ? rinv : rbf(mean);
      *(float*)(smem + L_STM + cs_put) = cs;
      return wave_ok(max(m, n));
    };
    st_load();
    if (real) sfor<WS_[12], W1A>(wstep);
    TSL(28);
    if (real) WAIT_VR(8 + W1A - WS_[11]); else WAIT_VR(8);
    if (lane == 0) { misc[8 + sw] = 0; misc[12 + sw] = 0; misc[16 + sw] = 0; misc[7] = 0; misc[6] = 0; }
    TSL(44);
    RAW_BARR();
    TSL(13); TSDUMP(1 + sw);
    STAMPT(1, 0, 256);
    if (real) WAIT_VR(W1A - WS_[12]); else WAIT_VR(0);
    if (!st_put() || hs_act) {
      if (ms_act) {
        SPIN {
          bool ok = true;
#pragma unroll
          for (int j = 0; j < 4; ++j) ok &= (ld32(ws, SY_RE + lane * 16 + j * 4) == ms_exp[j]);
          if (__all(ok)) break;
        }
        STW(ms_exp, SY_GO + lane * 16);
      }
      if (hs_act) {
        SPIN {
          if (ld32(ws, SY_GO + bid * 4) != hs_g0) break;
          const unsigned c = ld32(ws, SY_CH + bid * 4);
          if (c != hs_cur) { hs_cur = c; STW1(c, re_off); }
        }
      }
      SPIN { st_load(); WAIT_VR(0); if (st_put()) break; }
    }
    asm volatile("" ::: "memory", RSV);
    if (lane == 0) misc[12 + sw] = 1;
    __builtin_amdgcn_s_setprio(0);
    if (real) sfor<W1A, 16>(wstep);
    auto mwait = [&](int v) { SPIN { const int a = misc[8 + (lane & 3)]; if (__all(a >= v)) break; __builtin_amdgcn_s_sleep(1); } asm volatile("" ::: "memory"); };
    sfor<0, 9>([&](auto J) {
      if (J.v == 6) mwait(3);
      if (J.v == WOD) { SPIN { if (misc[WODF] == 1) break; __builtin_amdgcn_s_sleep(2); } SLEEP(WODS); }
      stageW(J, J.v < 2);
      if (J.v >= 1 && J.v < WOD) WAIT_V(4 * PACE);
      if (J.v == 1) { WAIT_VR(8); sfor<0, 16>(wtr); }
      if (J.v >= 2 && PSLP) __builtin_amdgcn_s_sleep(PSLP);
    });
    STAMPT(1, 1, 256);
    EPI();
#pragma unroll
    for (int i = 0; i < 2; ++i) { xv[i] = *(const u32x4*)(x + ego + i * 64); wv[i] = *(const u32x4*)(w_r + cb * 128 + eseg * 8 + i * 64); }
    asm volatile("s_waitcnt vmcnt(0)" : "+v"(xv[0]), "+v"(xv[1]), "+v"(wv[0]), "+v"(wv[1]) :: "memory");
    unpack_xw();
    RAW_BAR();
    RAW_BAR(); stageW(IC<9>{}, false); stageW(IC<10>{}, false);
    RAW_BAR(); stageW(IC<11>{}, false); stageW(IC<12>{}, false);
    RAW_BAR(); stageW(IC<13>{}, false); stageW(IC<14>{}, false);
    WAIT_V(16); RAW_BAR(); stageW(IC<15>{}, false);
    WAIT_V(12); RAW_BAR();
    WAIT_V(0); RAW_BAR();
    if (real) sfor<16, 24>(wstep);
  }
  LANES(); EPI();
  const int grow = rp * 64 + erow;
  int pa0 = L_P + erow * 512 + ((2 * eseg) ^ (erow & 7)) * 16, pa1 = pa0 ^ 16;
  unsigned st_dst = eseg == 0 ? WS_ST2 + ((rp * 8 + cb) * 64 + erow) * 16 : OOB, st_src = WS_ST2 + rp * 8192 + lane * 16;
  unsigned fd_dst = lane < 8 ? WS_FD + (cb * 32 + rp) * 128 + lane * 16 : OOB, kidx = 0xFFFFu - grow;
  asm volatile("" : "+v"(pa0), "+v"(pa1), "+v"(st_dst), "+v"(st_src), "+v"(fd_dst), "+v"(kidx));
  RAW_BAR2();
  STAMPT(0, 9, 0);
  unsigned hb[8];
  f32x2 hv[8];
  {
    f32x4 pp[2][4];
#pragma unroll
    for (int k = 0; k < 2; ++k)
#pragma unroll
      for (int i = 0; i < 4; ++i) pp[k][i] = *(const f32x4*)(smem + ((i & 1) ? pa1 : pa0) + (i >> 1) * 256 + k * L_PK);
    SB0();
    f32x2 s = {0.f, 0.f}, q = {0.f, 0.f}, p2 = {0.f, 0.f};
#pragma unroll
    for (int i = 0; i < 8; ++i) {
      const f32x4 a = pp[0][i >> 1] + pp[1][i >> 1];
      const f32x2 u = xf[i] + unpk(pack2o(a[2 * (i & 1)], a[2 * (i & 1) + 1]));
      hb[i] = pack2o(u[0], u[1]);
      hv[i] = unpk(hb[i]);
      s += hv[i]; q += hv[i] * hv[i]; p2 += hv[i] * wf2[i];
    }
    STW(sane((u32x4){__float_as_uint(oct_sum(s[0] + s[1])), __float_as_uint(oct_sum(q[0] + q[1])), __float_as_uint(oct_sum(p2[0] + p2[1])), __float_as_uint(sw_)}), st_dst);
  }
  KEEP2();
  STAMPT(0, 10, 0);
  const int sw = wave - 4;
  const unsigned fd_src = WS_FD + xcd * 4096 + r * 128;
  u32x2 kpk = {SENT, SENT};
  if (wave == 0) {
    u32x4 pa[8];
    SLEEP(P_C);
    SPIN {
      unsigned m = 0;
#pragma unroll
      for (int j = 0; j < 8; ++j) { pa[j] = LDW(st_src + j * 1024); m = chk(m, pa[j]); }
      CNT(3);
      if (wave_ok(m)) break;
      KEEP2();
    }
    STAMPT(0, 11, 0);
#define F4(v) __builtin_bit_cast(f32x4, v)
    const f32x4 t = ((F4(pa[0]) + F4(pa[1])) + (F4(pa[2]) + F4(pa[3]))) + ((F4(pa[4]) + F4(pa[5])) + (F4(pa[6]) + F4(pa[7])));
    const float mean = t[0] * (1.0f / 1024.0f);
    const float var = fmaxf(__builtin_fmaf(t[1], 1.0f / 1024.0f, -(mean * mean)), 0.f);
    const float mb = rbf(mean), rs = __builtin_amdgcn_rcpf(rbf(__builtin_amdgcn_sqrtf(rbf(rbf(var) + 1e-5f))));
    *(f32x2*)(smem + L_KX + lane * 8) = (f32x2){mb, rs};
    const float sc = fmaxf((t[2] - mb * t[3]) * rs, -INFINITY);
    const int u = (int)(pack2(0.f, sc) & 0xffff0000u);
    unsigned b0 = ((unsigned)(u ^ ((u >> 31) | (int)0x80000000)) & 0xffff0000u) | (0xFFFFu - (rp * 64 + lane)), b1 = 0;
    top2_lo(b0, b1); top2_hi(b0, b1);
    STW(((u32x4){b0, b1, b0, b1}), fd_dst);
    if (lane == 0) *(u32x2*)(smem + L_MISC + 112) = (u32x2){b0, b1};
  } else if (wave >= 4) {
    asm volatile("s_waitcnt vmcnt(0)" ::: "memory", RSV2);
    sfor<16, 24>(wtr);
  }
  RAW_BAR();
  bool cand;
  u32x4 ob[2];
  {
    const f32x2 mr = *(const f32x2*)(smem + L_KX + erow * 8);
    const u32x2 kk = *(const u32x2*)(smem + L_MISC + 112);
    const float mb = mr[0], rs = mr[1];
    const unsigned b0 = kk[0], b1 = kk[1];
    u32x4 nq[2];
#pragma unroll
    for (int i = 0; i < 8; ++i) {
      const f32x2 d = hv[i] - mb;
      const f32x2 n = unpk(pack2o(d[0], d[1])) * rs;
      nq[i >> 2][i & 3] = pack2o(n[0], n[1]);
    }
    const bool c2 = (b1 & 0xffffu) == kidx;
    cand = c2 || (b0 & 0xffffu) == kidx;
    if (cand) {
      const unsigned xo = WS_XS + (rp * 2 + (c2 ? 1 : 0)) * 2048 + cb * 256 + eseg * 16;
      STW(sane(nq[0]), xo); STW(sane(nq[1]), xo + 128);
      char* cp = smem + L_CAND + (c2 ? 512 : 0) + eseg * 16;
      *(u32x4*)cp = (u32x4){hb[0], hb[1], hb[2], hb[3]}; *(u32x4*)(cp + 128) = (u32x4){hb[4], hb[5], hb[6], hb[7]};
      *(u32x4*)(cp + 256) = nq[0]; *(u32x4*)(cp + 384) = nq[1];
    }
    asm volatile("" ::: "memory");
    if (wave >= 4) { SLEEP(P_D); kpk = __builtin_amdgcn_raw_buffer_load_b64(wsr, fd_src, 0, 16); }
    asm volatile("" ::: "memory");
#pragma unroll
    for (int i = 0; i < 8; ++i) { const f32x2 o = hv[i] + unpk(nq[i >> 2][i & 3]); ob[i >> 2][i & 3] = pack2(o[0], o[1]); }
  }
#define OUT_ST() do { *(u32x4*)(out + ego) = ob[0]; *(u32x4*)(out + ego + 64) = ob[1]; } while (0)
  STAMPT(0, 12, 0);
  if (wave < 4) {
    if (!cand) OUT_ST();
    RAW_BAR();
    const unsigned w0 = misc[24], w1 = misc[25];
    if (cand && grow != (int)key_idx(w0) && grow != (int)key_idx(w1)) OUT_ST();
#pragma unroll 1
    for (int tk = 0; tk < 2; ++tk) {
      const unsigned wk = tk ? w1 : w0, slot = misc[26 + tk];
      if ((int)(slot >> 1) != rp) continue;
      u32x4 pv[7];
      const unsigned yo = WS_YP + ((tk * 8 + cb) * 64 + (lane >> 3) * 8) * 512 + wave * 128 + (lane & 7) * 16;
      const int col = (wave >> 1) * 64 + (lane & 7) * 8 + (wave & 1) * 4 + (lane >> 4);
      float sg = rbf(__builtin_amdgcn_rcpf(1.0f + __expf(-key_val(wk))));
      float hh = bflo(*(const u16*)(smem + L_CAND + (slot & 1) * 512 + col * 2)), nn = bflo(*(const u16*)(smem + L_CAND + (slot & 1) * 512 + 256 + col * 2));
      u16* op = out + (size_t)key_idx(wk) * D_ + cb * 128 + col;
      asm volatile("" : "+v"(sg), "+v"(hh), "+v"(nn), "+v"(op));
      if (tk == 0 || (int)(misc[26] >> 1) != rp) {
        SPIN { if (misc[30] == (int)w0) break; __builtin_amdgcn_s_sleep(1); }
        SLEEP(P_E);
      }
      SPIN {
        unsigned m = 0;
#pragma unroll
        for (int p = 0; p < 7; ++p) { pv[p] = LDW(yo + p * 512); m = chk(m, pv[p]); }
        CNT(6);
        if (wave_ok(m)) break;
        asm volatile("" ::: "memory");
        SLEEP(L_E);
      }
      STAMPT(0, 13, 0);
      f32x4 s = __builtin_bit_cast(f32x4, pv[0]);
#pragma unroll
      for (int p = 1; p < 7; ++p) s += __builtin_bit_cast(f32x4, pv[p]);
      float y = sw32(sw16(s[0], s[1]), sw16(s[2], s[3]));
      y += dppf<DPP_ROR8>(y);
      if (!(lane & 8)) *(GAS u16*)op = (u16)pack2(hh + rbf(nn + rbf(rbf(y) * sg)), 0.f);
      STAMPT(0, 14, 0);
    }
  } else {
    unsigned xl = WS_XS + (kq * 256 + sw * 64 + (lane >> 4) * 16 + ((lane >> 2) & 1) * 8) * 2;
    int red_dst = (lane < 8 || (lane >= 24 && lane < 32)) ? L_RED + sw * 2048 + ((lane >> 3) & 1) * 1024 + (lane & 7) * 16 : L_RED + 8192 + lane * 16;
    unsigned gu_src = WS_GU + g8 * 4096 + lane * 16;
    int act_dst = L_ACT + sw * 256 + ((lane & 7) * 8 + (lane >> 3)) * 2, act_src = L_ACT + sw * 256 + (lane & 1) * 128 + (lane >> 4) * 32 + ((lane >> 1) & 1) * 16;
    unsigned yp_dst = lane < 16 ? WS_YP + (((lane >> 3) * 8 + 2 * kq + (sw >> 1)) * 64 + g8) * 512 + (sw & 1) * 256 + (lane & 7) * 16 : OOB;
    asm volatile("" : "+v"(xl), "+v"(red_dst), "+v"(gu_src), "+v"(act_dst), "+v"(act_src), "+v"(yp_dst));
    unsigned w0, w1; int s0 = -1, s1 = -1;
    u32x4 xa = {SENT, SENT, SENT, SENT};
    SPIN {
      CNT(4);
      const bool all = wave_ok(kpk[1]);
      const unsigned k0 = kpk[1] == SENT ? 0u : kpk[0], k1 = kpk[1] == SENT ? 0u : kpk[1];
      w0 = k0; w1 = k1;
      top2_lo(w0, w1); top2_mid(w0, w1);
      const int n0 = 2 * __builtin_ctzll(__ballot(k0 == w0));
      const u64 m1 = __ballot(k0 == w1);
      const int n1 = m1 ? 2 * __builtin_ctzll(m1) : n0 + 1;
      const bool same = n0 == s0 && n1 == s1;
      s0 = n0; s1 = n1;
      asm volatile("" ::: "memory");
      if (all && same) break;
      if (!all) { SLEEP(L_D); kpk = __builtin_amdgcn_raw_buffer_load_b64(wsr, fd_src, 0, 16); }
      xa = LDW(xl + ((lane & 1) ? s1 : s0) * 2048);
      if (all) break;
    }
    SPIN {
      if (wave_ok(chk(0u, xa))) break;
      asm volatile("" ::: "memory");
      xa = LDW(xl + ((lane & 1) ? s1 : s0) * 2048);
    }
    STAMPT(1, 3, 256);
    {
      const u32x4 z4 = {0u, 0u, 0u, 0u};
      const u32x4 xg = (lane & 2) ? z4 : xa, xu = (lane & 2) ? xa : z4;
      f32x4 d[8];
#pragma unroll
      for (int c = 0; c < 8; ++c) d[c] = mfma16(xg, wf[c], (f32x4){0.f, 0.f, 0.f, 0.f});
      if (tid == 256) { *(u32x4*)(smem + L_MISC + 96) = (u32x4){w0, w1, (unsigned)s0, (unsigned)s1}; misc[30] = 0; }
      SB0();
#pragma unroll
      for (int c = 0; c < 10; ++c) {
        if (c < 8) d[c & 7] = mfma16(xu, wf[8 + (c & 7)], d[c & 7]);
        if (c >= 2) *(f32x4*)(smem + red_dst + (c - 2) * 128) = d[(c - 2) & 7];
        SB0();
      }
    }
    RAW_BAR();
    if (sw == 0) {
      f32x4 t[8];
#pragma unroll
      for (int p = 0; p < 8; ++p) t[p] = *(const f32x4*)(smem + L_RED + p * 1024 + lane * 16);
      SB0();
      STW(sane(__builtin_bit_cast(u32x4, ((t[0] + t[1]) + (t[2] + t[3])) + ((t[4] + t[5]) + (t[6] + t[7])))), gu_src + kq * 1024);
    } else __builtin_amdgcn_s_sleep(2);
    STAMPT(1, 4, 256);
    {
      u32x4 q[4];
      if (OUTE && grow != (int)key_idx(w0) && grow != (int)key_idx(w1)) OUT_ST();
      asm volatile("" ::: "memory");
      SLEEP(P_GU);
      SPIN {
        unsigned m = 0;
#pragma unroll
        for (int j = 0; j < 4; ++j) { q[j] = LDW(gu_src + j * 1024); m = chk(m, q[j]); }
        CNT(5);
        if (wave_ok(m)) break;
        asm volatile("" ::: "memory");
      }
      STAMPT(1, 5, 256);
      const f32x4 tot = (__builtin_bit_cast(f32x4, q[0]) + __builtin_bit_cast(f32x4, q[1])) + (__builtin_bit_cast(f32x4, q[2]) + __builtin_bit_cast(f32x4, q[3]));
      const f32x2 gs = unpk(pack2o(tot[0], tot[1])), us = unpk(pack2o(tot[2], tot[3]));
      const f32x2 sg = unpk(pack2o(gs[0] * __builtin_amdgcn_rcpf(1.0f + __expf(-gs[0])), gs[1] * __builtin_amdgcn_rcpf(1.0f + __expf(-gs[1]))));
      const unsigned a2 = pack2o(sg[0] * us[0], sg[1] * us[1]);
      *(u16*)(smem + act_dst) = (u16)a2; *(u16*)(smem + act_dst + 128) = (u16)(a2 >> 16);
      __builtin_amdgcn_fence(__ATOMIC_RELEASE, "wavefront"); __builtin_amdgcn_wave_barrier(); __builtin_amdgcn_fence(__ATOMIC_ACQUIRE, "wavefront");
      const u32x4 aa = *(const u32x4*)(smem + act_src);
      f32x4 d[8];
#pragma unroll
      for (int c = 0; c < 8; ++c) d[c] = mfma16(aa, wf[16 + c], (f32x4){0.f, 0.f, 0.f, 0.f});
      SB0();
      u32x4 y[2];
#pragma unroll
      for (int c = 0; c < 8; ++c) {
        const float t0 = d[c][0] + dppf<DPP_ROR8>(d[c][2]), t1 = d[c][3] + dppf<DPP_ROR8>(d[c][1]);
        y[c >> 2][c & 3] = __float_as_uint((lane & 8) ? t1 : t0);
      }
      STW(sane(y[0]), yp_dst); STW(sane(y[1]), yp_dst + 128);
      if (tid == 256) misc[30] = w0;
    }
    STAMPT(1, 6, 256);
    if (!OUTE && grow != (int)key_idx(w0) && grow != (int)key_idx(w1)) OUT_ST();
  }
}

extern "C" void kernel_launch(void* const* d_in, const int* in_sizes, int n_in, void* d_out, int out_size,
                              void* d_ws, size_t ws_size, hipStream_t stream) {
  (void)hipFuncSetAttribute((const void*)fused, hipFuncAttributeMaxDynamicSharedMemorySize, LDS_TOTAL);
  fused<<<256, 512, LDS_TOTAL, stream>>>((const u16*)d_in[0], (const u16*)d_in[1], (const u16*)d_in[2], (const u16*)d_in[4],
                                         (const u16*)d_in[5], (const u16*)d_in[6], (const u16*)d_in[7], (u16*)d_out, (char*)d_ws);
}
```

```cpp
#include <hip/hip_runtime.h>
#include <stdint.h>

#define LAS __attribute__((address_space(3)))
#define GAS __attribute__((address_space(1)))
#define DI __device__ __forceinline__

typedef unsigned short u16;
typedef unsigned long long u64;
typedef __attribute__((ext_vector_type(2))) __bf16 bf16x2;
typedef __attribute__((ext_vector_type(8))) __bf16 bf16x8;
typedef __attribute__((ext_vector_type(4))) short s16x4;
typedef __attribute__((ext_vector_type(2))) unsigned short u16x2;
typedef __attribute__((ext_vector_type(16))) float f32x16;
typedef __attribute__((ext_vector_type(4))) float f32x4;
typedef __attribute__((ext_vector_type(2))) float f32x2;
typedef __attribute__((ext_vector_type(4))) unsigned u32x4;
typedef __attribute__((ext_vector_type(2))) unsigned u32x2;
typedef GAS unsigned gu32;
typedef GAS unsigned long long gu64;

constexpr int S_ = 2048, D_ = 1024, F_ = 3584;

constexpr unsigned SY_CH = 0, SY_GO = 1024, SY_RE = 2048;
constexpr unsigned WS_FD  = 98304;
constexpr unsigned WS_ZF  = 8192;
constexpr unsigned WS_KVF = 16384;
constexpr unsigned WS_RS  = 131072;
constexpr unsigned WS_CS  = WS_RS + 16 * 16 * 1024;
constexpr unsigned WS_ZP  = WS_CS + 16 * 16 * 768;
constexpr unsigned WS_FB  = WS_ZP + 32 * 16 * 128;
constexpr unsigned WS_ST2 = WS_FB + 32 * 32 * 128;
constexpr unsigned WS_XS  = WS_ST2 + 32 * 8 * 1024;
constexpr unsigned WS_GU  = WS_XS + 64 * 2048;
constexpr unsigned WS_YP  = WS_GU + 256 * 1024;
constexpr unsigned WS_KVP = WS_YP + 2 * 8 * 64 * 512;
constexpr unsigned WS_O   = WS_KVP + 32 * 16 * 4096;
constexpr unsigned WS_END = WS_O + S_ * D_ * 2;
constexpr unsigned OOB = 0x80000000u;
constexpr size_t WS_DIAG  = 64u << 20;
#define CNT(i) do {} while (0)
#define STAMPT(kid, i, T) do {} while (0)

#define TSL(idx) do {} while (0)
#define TSDUMP(k) do {} while (0)
template <int V> struct IC { static constexpr int v = V; };
template <int B, int E, class F> DI void sfor(F&& f) { if constexpr (B < E) { f(IC<B>{}); sfor<B + 1, E>(f); } }

DI float bflo(unsigned u) { return __uint_as_float(u << 16); }
DI float bfhi(unsigned u) { return __uint_as_float(u & 0xffff0000u); }
DI float rbf(float x) { return (float)(__bf16)x; }
DI unsigned pack2(float a, float b) { bf16x2 c; c[0] = (__bf16)a; c[1] = (__bf16)b; return __builtin_bit_cast(unsigned, c); }
DI unsigned pack2o(float a, float b) { unsigned p = pack2(a, b); asm("" : "+v"(p)); return p; }
DI f32x16 mfma32(u32x4 a, u32x4 b, f32x16 c) {
  return __builtin_amdgcn_mfma_f32_32x32x16_bf16(__builtin_bit_cast(bf16x8, a), __builtin_bit_cast(bf16x8, b), c, 0, 0, 0);
}
DI f32x4 mfma16(u32x4 a, u32x4 b, f32x4 c) {
  return __builtin_amdgcn_mfma_f32_16x16x32_bf16(__builtin_bit_cast(bf16x8, a), __builtin_bit_cast(bf16x8, b), c, 0, 0, 0);
}
DI void mfma16i(u32x4 a, u32x4 b, f32x4& c) { asm volatile("v_mfma_f32_16x16x32_bf16 %0, %1, %2, %0" : "+v"(c) : "v"(a), "v"(b)); }
DI f32x2 unpk(unsigned u) { return (f32x2){bflo(u), bfhi(u)}; }
DI u32x2 trread(const char* p) {
  s16x4 v = __builtin_amdgcn_ds_read_tr16_b64_v4i16((LAS s16x4*)p);
  return __builtin_bit_cast(u32x2, v);
}
DI const char* uni(const void* p) {
  const u64 v = (u64)p;
  return (const char*)(((u64)(unsigned)__builtin_amdgcn_readfirstlane((int)(v >> 32)) << 32) | (unsigned)__builtin_amdgcn_readfirstlane((int)v));
}
#define RV4(a, b, c, d) "v" #a, "v" #b, "v" #c, "v" #d
#define RV10(p) "v" #p "0", "v" #p "1", "v" #p "2", "v" #p "3", "v" #p "4", "v" #p "5", "v" #p "6", "v" #p "7", "v" #p "8", "v" #p "9"
#define RSV2 RV4(224, 225, 226, 227), "v228", "v229", RV10(23), RV10(24), RV4(250, 251, 252, 253), "v254", "v255"
#define RSV1 RV4(192, 193, 194, 195), RV4(196, 197, 198, 199), RV10(20), RV10(21), RV4(220, 221, 222, 223), RSV2
#define RSV "v148", "v149", RV10(15), RV10(16), RV10(17), RV10(18), "v190", "v191", RSV1
#define NTW " nt"
#define NTA ""
#define NTB ""
#define NTO ""
#define GL_(v, s, o) "global_load_lds_dwordx4 " v ", " s " offset:" #o
#define GLDS4(SC, CL...) asm volatile("s_add_u32 m0, %5, %c6\n\ts_nop 3\n\t" GL_("%0", "%4", 0) SC "\n\t" GL_("%1", "%4", 1024) SC "\n\t" \
  GL_("%2", "%4", 2048) SC "\n\t" GL_("%3", "%4", 3072) SC :: "v"(v0), "v"(v1), "v"(v2), "v"(v3), "s"(uni(sbase)), "s"(lds), "i"(IMM) : "memory", "scc", ##CL)
#define GLDS3(SC, CL...) asm volatile("s_add_u32 m0, %4, %c5\n\ts_nop 3\n\t" GL_("%0", "%3", 0) SC "\n\t" GL_("%1", "%3", 1024) SC "\n\t" \
  GL_("%2", "%3", 2048) SC :: "v"(v0), "v"(v1), "v"(v2), "s"(uni(sbase)), "s"(lds), "i"(IMM) : "memory", "scc", ##CL)
template <int IMM> DI void glds4(unsigned v0, unsigned v1, unsigned v2, unsigned v3, const void* sbase, unsigned lds) { GLDS4(NTO); }
template <int IMM> DI void glds4o(unsigned v0, unsigned v1, unsigned v2, unsigned v3, const void* sbase, unsigned lds) { GLDS4(NTO, RSV); }
template <int IMM> DI void glds4r(unsigned v0, unsigned v1, unsigned v2, unsigned v3, const void* sbase, unsigned lds) { GLDS4(NTA, RSV); }
template <int IMM> DI void glds4n(unsigned v0, unsigned v1, unsigned v2, unsigned v3, const void* sbase, unsigned lds) { GLDS4(" nt", RSV); }
template <int IMM> DI void glds3n(unsigned v0, unsigned v1, unsigned v2, const void* sbase, unsigned lds) { GLDS3(" nt", RSV); }
template <int IMM> DI void glds3r(unsigned v0, unsigned v1, unsigned v2, const void* sbase, unsigned lds) { GLDS3(NTB, RSV); }
template <int R> DI void sload(unsigned voff, const void* sbase) {
  asm volatile("s_nop 4\n\tglobal_load_dwordx4 v[%c0:%c1], %2, %3 sc1" ::"i"(R), "i"(R + 3), "v"(voff), "s"(uni(sbase)) : "memory", RSV);
}
DI float fadd(float a, float b) { float o; asm("v_add_f32 %0, %1, %2" : "=v"(o) : "v"(a), "v"(b)); return o; }
DI float fmul(float a, float b) { float o; asm("v_mul_f32 %0, %1, %2" : "=v"(o) : "v"(a), "v"(b)); return o; }
DI float fsq(float a, float c) { asm("v_fmac_f32 %0, %1, %1" : "+v"(c) : "v"(a)); return c; }
#define RA_(o, a, b) "v_add_f32 %" #o ", v[%c6+" #a "], v[%c6+" #b "]\n\t"
#define RB2_(o, a) "v_add_f32 %" #o ", v[%c6+" #a "], %" #o "\n\t"
#define RM_(o, a, b) "v_max3_u32 %" #o ", v[%c6+" #a "], v[%c6+" #b "], %" #o "\n\t"
template <int R> DI void rsum4(float* a, unsigned& m, unsigned& n) {
  asm volatile(RA_(0, 0, 4) RA_(1, 1, 5) RM_(4, 0, 1) RA_(2, 2, 6) RA_(3, 3, 7) RM_(5, 2, 3) RB2_(0, 8) RM_(4, 4, 5) RB2_(1, 9) RM_(5, 6, 7)
               RB2_(2, 10) RM_(4, 8, 9) RB2_(3, 11) RM_(5, 10, 11) RB2_(0, 12) RB2_(1, 13) RM_(4, 12, 13) RB2_(2, 14) RB2_(3, 15) RM_(5, 14, 15)
               : "=&v"(a[0]), "=&v"(a[1]), "=&v"(a[2]), "=&v"(a[3]), "+v"(m), "+v"(n) : "i"(R) : RSV);
}
template <int R> DI void wload2(unsigned voff, const void* sbase) {
  asm volatile("s_nop 4\n\tglobal_load_dwordx4 v[%c0:%c1], %2, %3" NTW ::"i"(R), "i"(R + 3), "v"(voff), "s"(uni(sbase)) : "memory", RSV2);
}
template <int R> DI void wload(unsigned voff, const void* sbase) {
  asm volatile("s_nop 4\n\tglobal_load_dwordx4 v[%c0:%c1], %2, %3" NTW ::"i"(R), "i"(R + 3), "v"(voff), "s"(uni(sbase)) : "memory", RSV);
}
template <int RA, int RB> DI unsigned wperm(unsigned sel) {
  unsigned o;
  if (RA < 192) asm volatile("v_perm_b32 %0, v%c2, v%c1, %3" : "=v"(o) : "i"(RA), "i"(RB), "s"(sel) : RSV);
  else if (RA < 224) asm volatile("v_perm_b32 %0, v%c2, v%c1, %3" : "=v"(o) : "i"(RA), "i"(RB), "s"(sel) : RSV1);
  else asm volatile("v_perm_b32 %0, v%c2, v%c1, %3" : "=v"(o) : "i"(RA), "i"(RB), "s"(sel) : RSV2);
  return o;
}
template <int R> DI unsigned rget() { unsigned o; asm volatile("v_mov_b32 %0, v%c1" : "=v"(o) : "i"(R) : RSV); return o; }
DI float xh_sum(float v) {
  auto r = __builtin_amdgcn_permlane32_swap(__float_as_uint(v), __float_as_uint(v), false, false);
  return __uint_as_float(r[0]) + __uint_as_float(r[1]);
}
template <int CTRL> DI unsigned dppu(unsigned v) { return (unsigned)__builtin_amdgcn_update_dpp(0, (int)v, CTRL, 0xf, 0xf, true); }
template <int CTRL> DI float dppf(float v) { return __uint_as_float(dppu<CTRL>(__float_as_uint(v))); }
constexpr int DPP_X1 = 0xB1, DPP_X2 = 0x4E, DPP_HMIR = 0x141, DPP_MIR = 0x140, DPP_ROR8 = 0x128;
DI float quad_sum(float v) { v += dppf<DPP_X1>(v); v += dppf<DPP_X2>(v); return v; }
DI float oct_sum(float v) { v = quad_sum(v); v += dppf<DPP_HMIR>(v); return v; }
DI float x16_sum(float v) {
  auto r = __builtin_amdgcn_permlane16_swap(__float_as_uint(v), __float_as_uint(v), false, false);
  return __uint_as_float(r[0]) + __uint_as_float(r[1]);
}
DI float sw16(float a, float b) {
  auto r = __builtin_amdgcn_permlane16_swap(__float_as_uint(a), __float_as_uint(b), false, false);
  return fadd(__uint_as_float(r[0]), __uint_as_float(r[1]));
}
DI float sw32(float a, float b) {
  auto r = __builtin_amdgcn_permlane32_swap(__float_as_uint(a), __float_as_uint(b), false, false);
  return fadd(__uint_as_float(r[0]), __uint_as_float(r[1]));
}
DI float hi_sum(float v) {
  auto r = __builtin_amdgcn_permlane16_swap(__float_as_uint(v), __float_as_uint(v), false, false);
  v = fadd(__uint_as_float(r[0]), __uint_as_float(r[1]));
  auto q = __builtin_amdgcn_permlane32_swap(__float_as_uint(v), __float_as_uint(v), false, false);
  return fadd(__uint_as_float(q[0]), __uint_as_float(q[1]));
}
template <int N> struct Phi { unsigned tb[N]; f32x2 tr[N], e[N]; };
template <int N> DI void phi_a(const f32x2* t, Phi<N>& s) {
#pragma unroll
  for (int i = 0; i < N; ++i) s.tb[i] = pack2o(t[i][0], t[i][1]);
  __builtin_amdgcn_sched_barrier(0);
#pragma unroll
  for (int i = 0; i < N; ++i) s.tr[i] = unpk(s.tb[i]);
  __builtin_amdgcn_sched_barrier(0);
#pragma unroll
  for (int i = 0; i < N; ++i) s.e[i] = s.tr[i] * 1.44269504088896340736f;
  __builtin_amdgcn_sched_barrier(0);
}
template <int N> DI void phi_b(Phi<N>& s, unsigned* p) {
  u16x2 tm[2] = {{0, 0}, {0, 0}};
#pragma unroll
  for (int i = 0; i < N; ++i) {
    s.e[i][0] = __builtin_amdgcn_fmed3f(__builtin_amdgcn_exp2f(s.e[i][0]), 0.f, 1.f);
    tm[i & 1] = __builtin_elementwise_max(tm[i & 1], __builtin_bit_cast(u16x2, s.tb[i]));
    s.e[i][1] = __builtin_amdgcn_fmed3f(__builtin_amdgcn_exp2f(s.e[i][1]), 0.f, 1.f);
    __builtin_amdgcn_sched_barrier(0);
  }
#pragma unroll
  for (int i = 0; i < N; ++i) s.tr[i] += s.e[i];
  __builtin_amdgcn_sched_barrier(0);
#pragma unroll
  for (int i = 0; i < N; ++i) { s.tr[i][0] = fmaxf(s.tr[i][0], s.e[i][0]); s.tr[i][1] = fmaxf(s.tr[i][1], s.e[i][1]); }
  const u16x2 tx = __builtin_elementwise_max(tm[0], tm[1]);
  __builtin_amdgcn_sched_barrier(0);
#pragma unroll
  for (int i = 0; i < N; ++i) p[i] = pack2(s.tr[i][0], s.tr[i][1]);
  if (__any(tx[0] > 0xC0F0 || tx[1] > 0xC0F0)) {
#pragma unroll
    for (int i = 0; i < N; ++i) p[i] = pack2(bflo(p[i]) + 1e-6f, bfhi(p[i]) + 1e-6f);
  }
}
template <int N> DI void phi_n(const f32x2* t, unsigned* p) { Phi<N> s; phi_a<N>(t, s); phi_b<N>(s, p); }
#define RLX_AGENT __ATOMIC_RELAXED, __HIP_MEMORY_SCOPE_AGENT
DI void st32(char* ws, unsigned off, unsigned v) { __hip_atomic_store((gu32*)(ws + off), v, RLX_AGENT); }
DI void st64(char* ws, unsigned off, u64 v) { __hip_atomic_store((gu64*)(ws + off), v, RLX_AGENT); }
DI unsigned ld32(char* ws, unsigned off) { return __hip_atomic_load((gu32*)(ws + off), RLX_AGENT); }
DI u64 ld64(char* ws, unsigned off) { return __hip_atomic_load((gu64*)(ws + off), RLX_AGENT); }
#define WAIT_V(n) asm volatile("s_waitcnt vmcnt(%c0)" ::"i"(n) : "memory")
#define RAW_BAR() do { asm volatile("s_waitcnt lgkmcnt(0)" ::: "memory"); __builtin_amdgcn_s_barrier(); asm volatile("" ::: "memory"); } while (0)
#define SB0() __builtin_amdgcn_sched_barrier(0)
#define WAIT_VR(n) asm volatile("s_waitcnt vmcnt(%c0)" ::"i"(n) : "memory", RSV)
#define RAW_BARR() do { asm volatile("s_waitcnt lgkmcnt(0)" ::: "memory", RSV); __builtin_amdgcn_s_barrier(); asm volatile("" ::: "memory", RSV); } while (0)
#define KEEP2() asm volatile("" ::: "memory", RSV2)
#define RAW_BAR2() do { asm volatile("s_waitcnt lgkmcnt(0)" ::: "memory", RSV2); __builtin_amdgcn_s_barrier(); KEEP2(); } while (0)
constexpr unsigned SPIN_CAP = 1u << 18;
#define SPIN _Pragma("nounroll") for (unsigned s_ = 0; s_ < SPIN_CAP; ++s_)
constexpr unsigned SENT = 0xFFFFFFFFu;
DI unsigned sane1(unsigned v) { return min(v, 0xFFFFFFFEu); }
DI u32x4 sane(u32x4 v) { v[1] = sane1(v[1]); v[3] = sane1(v[3]); return v; }
DI unsigned chk(unsigned m, u32x4 v) { return max(max(m, v[1]), v[3]); }
DI bool wave_ok(unsigned m) { return !__any(m == SENT); }
DI unsigned key_idx(unsigned k) { return (0xFFFFu - (k & 0xFFFFu)) & (S_ - 1); }
DI float key_val(unsigned k) { const unsigned o = k >> 16; return __uint_as_float(((o & 0x8000u) ? (o & 0x7fffu) : (~o & 0xffffu)) << 16); }
DI void top2_mix(unsigned& b0, unsigned& b1, unsigned c0, unsigned c1) {
  const unsigned lo = min(b0, c0), mx = max(b1, c1);
  b0 = max(b0, c0); b1 = max(lo, mx);
}
DI void top2_lo(unsigned& b0, unsigned& b1) {
  top2_mix(b0, b1, dppu<DPP_X1>(b0), dppu<DPP_X1>(b1));
  top2_mix(b0, b1, dppu<DPP_X2>(b0), dppu<DPP_X2>(b1));
  top2_mix(b0, b1, dppu<DPP_HMIR>(b0), dppu<DPP_HMIR>(b1));
}
DI void top2_mid(unsigned& b0, unsigned& b1) {
  top2_mix(b0, b1, dppu<DPP_MIR>(b0), dppu<DPP_MIR>(b1));
  auto p = __builtin_amdgcn_permlane16_swap(b0, b0, false, false); auto q = __builtin_amdgcn_permlane16_swap(b1, b1, false, false);
  b0 = p[0]; b1 = q[0]; top2_mix(b0, b1, p[1], q[1]);
}
DI void top2_hi(unsigned& b0, unsigned& b1) {
  top2_mid(b0, b1);
  auto p = __builtin_amdgcn_permlane32_swap(b0, b0, false, false); auto q = __builtin_amdgcn_permlane32_swap(b1, b1, false, false);
  b0 = p[0]; b1 = q[0]; top2_mix(b0, b1, p[1], q[1]);
}

constexpr int K1_STAGE = 40960;
constexpr int LDS_TOTAL = 4 * K1_STAGE;
constexpr int L_STM = 0;
constexpr int L_MISC = 2048;
constexpr int L_XK = 5120;
constexpr int L_KX = 3328;
constexpr int L_ACT = 2304;
constexpr int L_CAND = 4096;
constexpr int L_WO = 16384;
constexpr int L_P = 16384, L_PK = 7 * 16384;
constexpr int L_RED = 65536;
constexpr int wo_slot(int j) { return L_WO + (j % 9) * 16384; }
constexpr int CODE_LINES = 12;
#define OW_ 8
constexpr int OW = OW_;
#define W1A_ 16
constexpr int W1A = W1A_;
#define PRIO_M 1
#define PRIO_S 3
#define GSCHED 5, 9, 13, 17, 21, 24, 27, 30, 33, 36, 39, 42, 45, 48, 48
constexpr int GS[15] = {GSCHED};
constexpr int gops(int g) { return (g / 3) * 10 + (g % 3 == 0 ? 0 : g % 3 == 1 ? 4 : 7); }
constexpr bool gs_ok() {
  bool ok = GS[0] >= 3 && GS[1] >= 6 && GS[1] <= 12 && GS[0] <= GS[1] && GS[12] == 45 && GS[13] == 48 && GS[14] == 48;
  for (int t = 0; t < 12; ++t) ok = ok && GS[t + 2] <= 3 * (t + 5) && GS[t + 2] >= 3 * (t + 3) && GS[t + 2] >= GS[t + 1] + 3;
  return ok;
}
static_assert(gs_ok(), "staging schedule");
#define WSCHED 0, 0, 0, 0, 0, 0, 0, 0, 0, 2, 4, 6, 8
constexpr int WS_[13] = {WSCHED};
#define PACE 1
#define PSLP 0
#define P_KVP 1
#define L_KVP 0
#define P_KVF 1
#define L_KVF 0
#define P_FB 8
#define L_FB 0
#define P_C 14
#define P_D 2
#define L_D 0
#define P_GU 12
#define P_E 10
#define L_E 0
#define SLEEP(n) do { if (n) __builtin_amdgcn_s_sleep(n); } while (0)
#define OUTE 1
#define P2MAP 0
#define TCH0 100
#define OUTWT 0
#define OUTWB 0
#define PFO 1
#define WOD 100
#define WODF 6
#define WODS 0
#define QPOS 1
constexpr int VW = 160;

__global__ __launch_bounds__(512, 2) void fused(const u16* __restrict__ x, const u16* __restrict__ wqkv,
                                                const u16* __restrict__ wo, const u16* __restrict__ w_r,
                                                const u16* __restrict__ wg, const u16* __restrict__ wu,
                                                const u16* __restrict__ wd, u16* __restrict__ out, char* __restrict__ ws) {
  extern __shared__ __attribute__((aligned(16))) char smem[];
  STAMPT(0, 0, 0);
  u64 pc0; asm volatile("s_getpc_b64 %0" : "=s"(pc0));
  const int wave = __builtin_amdgcn_readfirstlane(threadIdx.x >> 6);
  const unsigned lds0 = (unsigned)(size_t)smem;
#define RB_(b) (((b) & 1) * 8 + (((b) >> 3) & 7))
#define HP_(b) ((((b) & 7) >> 1) * 4 + ((b) >> 6))
#define KROT_(b) ((4 * ((HP_(b) >> 2) + (RB_(b) >> 3) + 2 * ((RB_(b) >> 2) & 1))) & 15)
  unsigned voA0 = 0, voA1 = 0, voA2 = 0, voA3 = 0, voB0 = 0, voB1 = 0, voB2 = 0, voB3 = 0, voB4 = 0, voB5 = 0;
  const unsigned l4 = __builtin_amdgcn_readfirstlane(lds0 + (wave - 4) * 4096), l6 = __builtin_amdgcn_readfirstlane(lds0 + (wave - 4) * 6144);
  const int krot0 = KROT_(blockIdx.x);
  auto group = [&](auto G) {
    constexpr int t = G.v / 3, p = G.v % 3, sl = (t & 3) * K1_STAGE;
    const int kt = (t + krot0) & 15;
    if (p == 0) glds4r<sl>(voA0, voA1, voA2, voA3, (const char*)x + kt * 128, l4);
    else if (p == 1) glds3r<sl + 16384>(voB0, voB1, voB2, (const char*)wqkv + kt * 64 * (3 * D_ * 2), l6);
    else glds3r<sl + 16384 + 3072>(voB3, voB4, voB5, (const char*)wqkv + kt * 64 * (3 * D_ * 2), l6);
  };
  if (wave >= 4) {
    const int lane = threadIdx.x & 63, sw = wave - 4;
    const int r0 = RB_(blockIdx.x) * 128 + 32 * sw + (lane >> 3);
    voA0 = (unsigned)(r0 * D_ + ((lane & 7) ^ (lane >> 4)) * 8) * 2u;
    voA1 = ((voA0 + 8 * D_ * 2) ^ 64) - 1024; voA2 = voA0 + 16 * D_ * 2 - 2048; voA3 = voA1 + 16 * D_ * 2 - 2048;
    voB0 = (unsigned)((16 * sw + 4 * (lane >> 5) + ((lane >> 2) & 3)) * (3 * D_) + HP_(blockIdx.x) * 64 + ((lane >> 4) & 1) * 32 + (lane & 3) * 8) * 2u;
    voB1 = voB0 + 1024; voB2 = voB0 + 2048;
    voB3 = (voB0 ^ 32) + 8 * 3 * D_ * 2; voB4 = voB3 + 1024; voB5 = voB3 + 2048;
    asm volatile("" : "+v"(voA0), "+v"(voA1), "+v"(voA2), "+v"(voA3), "+v"(voB0), "+v"(voB1), "+v"(voB2), "+v"(voB3), "+v"(voB4), "+v"(voB5));
    sfor<0, 3>(group);
  }
  int bid = blockIdx.x; asm volatile("" : "+s"(bid));
  const int xcd = bid & 7, idx = bid >> 3;
  const int rb = RB_(bid), hp = HP_(bid);
  const int rp = xcd * 4 + (idx >> 3), cb = idx & 7;
#define WROT (4 * rp + (xcd >> 1))
#define WDIR ((xcd & 1) ? -1 : 1)
  const int hs_ = (hp + 4 + (int)(((size_t)wqkv >> 7) & 7)) & 7;
  const int g8 = ((hp >> 3) * 4 + (rb >> 2)) * 8 + hs_, kq = rb & 3;
  const int krot = KROT_(bid), posA = 1 + (hp & 3), posB = 1 + (rb & 3);
  const int wrot = WROT, wdir = WDIR;
  auto whead = [&](int t) { return (wrot + wdir * t) & 15; };
  const unsigned rs_slot = WS_RS + (rb * 16 + ((krot + posA) & 15)) * 1024, cs_slot = WS_CS + (hp * 16 + ((krot + posB) & 15)) * 768;
  const __amdgpu_buffer_rsrc_t wsr = __builtin_amdgcn_make_buffer_rsrc(ws, 0, WS_END, 0x00020000);
#define LDW(off) __builtin_amdgcn_raw_buffer_load_b128(wsr, (off), 0, 16)
#define STW(v, off) __builtin_amdgcn_raw_buffer_store_b128((v), wsr, (off), 0, 16)
#define STW1(v, off) __builtin_amdgcn_raw_buffer_store_b32((v), wsr, (off), 0, 16)
  float* stm = (float*)(smem + L_STM);
  volatile LAS int* misc = (volatile LAS int*)(smem + L_MISC);
#define LANES() int tid = threadIdx.x; asm volatile("" : "+v"(tid)); const int lane = tid & 63, r = lane & 31, h = lane >> 5; (void)r; (void)h
#define EPI() const int erow = tid >> 3, eseg = tid & 7; const size_t ego = (size_t)(rp * 64 + erow) * D_ + cb * 128 + eseg * 8
  u32x4 xv[2], wv[2];
  f32x2 xf[8], wf2[8];
  float sw_;
  auto unpack_xw = [&]() {
    f32x2 a = {0.f, 0.f};
#pragma unroll
    for (int i = 0; i < 8; ++i) { xf[i] = unpk(xv[i >> 2][i & 3]); wf2[i] = unpk(wv[i >> 2][i & 3]); a += wf2[i]; }
    sw_ = oct_sum(a[0] + a[1]);
#pragma unroll
    for (int i = 0; i < 8; ++i) asm volatile("" : "+v"(xf[i]), "+v"(wf2[i]));
    asm volatile("" : "+v"(sw_));
  };
  unsigned tsv = 0; (void)tsv;
  u32x4 wf[24];

  auto wtr = [&](auto I) {
    constexpr int m = I.v >> 3, c = I.v & 7, R = VW + 32 * m + (c >> 1);
    const unsigned sel = (c & 1) ? 0x07060302u : 0x05040100u;
    wf[I.v] = (u32x4){wperm<R, R + 4>(sel), wperm<R + 8, R + 12>(sel), wperm<R + 16, R + 20>(sel), wperm<R + 24, R + 28>(sel)};
  };

  if (wave < 4) {
    LANES();
    const int c16 = lane & 15, g4 = lane >> 4;
    const int trl = (c16 >> 2) * 64 + (c16 & 3) * 8;
    const int wr = wave >> 1, wc = wave & 1;
    const int head = 2 * hp + wc;
    const unsigned kvp_off = WS_KVP + (head * 16 + rb) * 4096 + wr * 2048 + lane * 16;
    const unsigned zp_off = WS_ZP + (head * 16 + rb) * 128;
    const unsigned o_off = WS_O + ((rb * 4 + 2 * wr) * 32 + head) * 2048 + lane * 16;
    const unsigned fb_off = lane < 8 ? WS_FB + ((rb * 2 + wr) * 32 + head) * 128 + lane * 16 : OOB;
    unsigned pfa, pfb = OOB;
    unsigned pfc = lane < 8 ? fb_off : lane < 24 ? rs_slot + wave * 256 + (lane - 8) * 16
        : lane < 40 ? (wave < 3 ? cs_slot + wave * 256 : WS_KVF + hp * 4096 + rb * 256) + (lane - 24) * 16
        : lane < 48 ? (wave < 2 ? zp_off + (lane & 7) * 16 : (wave == 2 && rb < 2) ? WS_ZF + (2 * hp + rb) * 128 + (lane & 7) * 16 : OOB) : OOB;
    if (wave == 0) pfa = WS_ST2 + (rp * 8 + cb) * 1024 + lane * 16;
    else if (wave == 1) pfa = WS_GU + (g8 * 4 + kq) * 1024 + lane * 16;
    else pfa = WS_YP + (((wave - 2) * 8 + 2 * kq + (lane >> 5)) * 64 + g8) * 512 + (lane & 31) * 16;
    if (wave == 0) pfb = lane < 32 ? WS_XS + (rp * 2 + (lane >> 4)) * 2048 + cb * 256 + (lane & 15) * 16 : lane < 40 ? WS_FD + (cb * 32 + rp) * 128 + (lane & 7) * 16 : OOB;
    u32x4 S4 = {SENT, SENT, SENT, SENT};
    asm volatile("" : "+v"(pfa), "+v"(pfb), "+v"(pfc), "+v"(S4));
    __builtin_amdgcn_s_setprio(PRIO_M);
    f32x4 acc[4][6];
    int ao[2][2], bo[2][2];
    {
#pragma unroll
      for (int i = 0; i < 2; ++i) {
        ao[0][i] = (64 * wr + c16) * 128 + (((4 * i + g4) ^ (c16 >> 1)) * 16); ao[1][i] = ao[0][i] + 2 * K1_STAGE;
        bo[0][i] = 16384 + g4 * 3072 + wc * 256 + trl + ((i ^ (g4 & 1)) * 32); bo[1][i] = bo[0][i] + 2 * K1_STAGE;
        asm volatile("" : "+v"(ao[0][i]), "+v"(ao[1][i]), "+v"(bo[0][i]), "+v"(bo[1][i]));
      }
#pragma unroll
      for (int i = 0; i < 4; ++i)
#pragma unroll
        for (int j = 0; j < 6; ++j) { acc[i][j] = (f32x4){0.f, 0.f, 0.f, 0.f}; asm volatile("" : "+v"(acc[i][j])); }
      struct Frag { u32x4 a[4]; u32x2 bl[6], bh[6]; };
      Frag FA, FB;
#define K1_B(j) ((u32x4){C.bl[j][0], C.bl[j][1], C.bh[j][0], C.bh[j][1]})
#define K1_LA(i) N.a[i] = *(const u32x4*)(smem + ao[sn >> 1][hf] + (sn & 1) * K1_STAGE + (i) * 2048)
#define K1_LL(j) N.bl[j] = trread(smem + bo[sn >> 1][(j) & 1] + (sn & 1) * K1_STAGE + hf * 12288 + ((j) >> 1) * 1024)
#define K1_LH(j) N.bh[j] = trread(smem + bo[sn >> 1][(j) & 1] + (sn & 1) * K1_STAGE + hf * 12288 + ((j) >> 1) * 1024 + 512)
#define K1_M(i, j) if ((j) < 2) mfma16i(K1_B(j), C.a[i], acc[i][j]); else mfma16i(C.a[i], K1_B(j), acc[i][j]); SB0()
#define K1_Q(i, j, ld) K1_M(i, j); ld; SB0()
      auto sub = [&](Frag& C, Frag& N, int sn, int hf) {
        K1_Q(0, 0, K1_LL(0)); K1_Q(1, 0, K1_LH(0)); K1_Q(0, 1, K1_LA(0)); K1_Q(1, 1, K1_LA(1)); K1_Q(0, 2, K1_LL(1)); K1_Q(1, 2, K1_LH(1));
        K1_Q(0, 3, K1_LL(2)); K1_Q(1, 3, K1_LH(2)); K1_Q(2, 0, K1_LL(3)); K1_Q(3, 0, K1_LH(3)); K1_Q(2, 1, K1_LA(2)); K1_Q(3, 1, K1_LA(3));
        K1_Q(2, 2, K1_LL(4)); K1_Q(3, 2, K1_LH(4)); K1_Q(2, 3, K1_LL(5)); K1_Q(3, 3, K1_LH(5));
        K1_M(0, 4); K1_M(1, 4); K1_M(2, 4); K1_M(3, 4); K1_M(0, 5); K1_M(1, 5); K1_M(2, 5);
        asm volatile("s_waitcnt lgkmcnt(0)" : "+v"(N.bh[5]));
        K1_M(3, 5);
      };
      auto subkv = [&](Frag& C, Frag& N, int sn, int hf, bool ld) {
#define K1_QL(i, j, l) K1_M(i, j); if (ld) { l; } SB0()
        K1_QL(0, 2, K1_LL(2)); K1_QL(1, 2, K1_LH(2)); K1_QL(0, 3, K1_LA(0)); K1_QL(1, 3, K1_LA(1)); K1_QL(2, 2, K1_LL(3)); K1_QL(3, 2, K1_LH(3));
        K1_QL(2, 3, K1_LA(2)); K1_QL(3, 3, K1_LA(3)); K1_QL(0, 4, K1_LL(4)); K1_QL(1, 4, K1_LH(4)); K1_QL(2, 4, K1_LL(5)); K1_QL(3, 4, K1_LH(5));
        K1_M(0, 5); K1_M(1, 5); K1_M(2, 5); K1_M(3, 5);
      };
      RAW_BAR();
      STAMPT(0, 1, 0);
      {
        Frag& N = FA; const int sn = 0, hf = 0;
        K1_LL(0); K1_LH(0); K1_LA(0); K1_LA(1); K1_LL(1); K1_LH(1); K1_LL(2); K1_LH(2); K1_LL(3); K1_LH(3); K1_LA(2); K1_LA(3); K1_LL(4); K1_LH(4); K1_LL(5); K1_LH(5);
      }
#pragma unroll 1
      for (int it = 0; it < 4; ++it) {
#pragma unroll
        for (int s = 0; s < 4; ++s) {
          if (s == 2 && it == 3) break;
          sub(FA, FB, s, 1);
          if (s == 0) WAIT_V(0);
          if (it == 0) { if (s == PFO) STW(S4, pfc); if (s == PFO + 1) STW(S4, kvp_off); if (s == PFO + 2) STW(S4, kvp_off + 1024); }
          if (it == 1) { if (s == 0) STW(S4, pfa); if (s == 1) STW(S4, pfb); }
          TSL(it * 4 + s);
          if (s != 1 || it < 3) RAW_BAR();
          sub(FB, FA, (s + 1) & 3, 0);
        }
      }
      subkv(FA, FB, 2, 1, true);
      TSL(14);
      RAW_BAR();
      subkv(FB, FA, 3, 0, true);
      subkv(FA, FB, 3, 1, true);
      TSL(15);
      subkv(FB, FA, 0, 0, false);
    }
    asm volatile("s_nop 15" ::: "memory");
    TSL(16); if (wave == 0) TSDUMP(0);
    STAMPT(0, 2, 0);
    {
      f32x4 m4[4], i4[4]; float cs[4];
      SPIN {
        const int ok = misc[12 + (lane & 3)];
        asm volatile("" ::: "memory");
#pragma unroll
        for (int i = 0; i < 4; ++i) {
          cs[i] = stm[256 + wc * 96 + 32 + 16 * i + c16];
          m4[i] = *(const f32x4*)(stm + 64 * wr + 16 * i + 4 * g4); i4[i] = *(const f32x4*)(stm + 128 + 64 * wr + 16 * i + 4 * g4);
        }
        SB0();
        if (__all(ok != 0)) break;
      }
      const u32x4 ONES4 = {0x3F803F80u, 0x3F803F80u, 0x3F803F80u, 0x3F803F80u};
      f32x2 tk[16], tv[16];
#define PR(v, i) ((f32x2){(v)[2 * (i)], (v)[2 * (i) + 1]})
#pragma unroll
      for (int i = 0; i < 16; ++i) {
        tk[i] = PR(acc[(i >> 1) & 3][2 + (i >> 3)], i & 1) - PR(m4[(i >> 1) & 3], i & 1) * cs[i >> 3];
        tv[i] = PR(acc[(i >> 1) & 3][4 + (i >> 3)], i & 1) - PR(m4[(i >> 1) & 3], i & 1) * cs[2 + (i >> 3)];
      }
      SB0();
#pragma unroll
      for (int i = 0; i < 16; ++i) { tk[i] *= PR(i4[(i >> 1) & 3], i & 1); tv[i] *= PR(i4[(i >> 1) & 3], i & 1); }
      SB0();
      unsigned kp[16], vp[16];
#pragma unroll
      for (int i = 0; i < 16; ++i) vp[i] = pack2o(tv[i][0], tv[i][1]);
      phi_n<16>(tk, kp);
#define F4_(p, i) ((u32x4){(p)[4 * (i)], (p)[4 * (i) + 1], (p)[4 * (i) + 2], (p)[4 * (i) + 3]})
      const f32x4 Z4 = {0.f, 0.f, 0.f, 0.f};
      u32x4 vk[2], vg[2];
#pragma unroll
      for (int j = 0; j < 2; ++j) { vk[j] = wr ? F4_(vp, 2 + j) : F4_(vp, j); vg[j] = wr ? F4_(vp, j) : F4_(vp, 2 + j); }
      f32x4 keep[2], give[2], zq[2];
#pragma unroll
      for (int d = 0; d < 2; ++d) { give[d] = mfma16(F4_(kp, 2 * d), vg[0], Z4); zq[d] = mfma16(ONES4, F4_(kp, 2 * d), Z4); }
#pragma unroll
      for (int d = 0; d < 2; ++d) { give[d] = mfma16(F4_(kp, 2 * d + 1), vg[1], give[d]); zq[d] = mfma16(ONES4, F4_(kp, 2 * d + 1), zq[d]); }
#pragma unroll
      for (int d = 0; d < 2; ++d) keep[d] = mfma16(F4_(kp, 2 * d), vk[0], Z4);
#pragma unroll
      for (int d = 0; d < 2; ++d) keep[d] = mfma16(F4_(kp, 2 * d + 1), vk[1], keep[d]);
      SB0();
      const float zv = (lane & 16) ? zq[1][0] : zq[0][0];
      char* xk = smem + L_XK + wave * 2304 + lane * 16;
      const char* xp = smem + L_XK + (wave ^ 2) * 2304 + lane * 16;
      *(f32x4*)xk = give[0]; *(f32x4*)(xk + 1024) = give[1]; *(float*)(xk + 2048 - lane * 12) = zv;
      asm volatile("" ::: "memory");
      misc[16 + wave] = 1;
      f32x4 p0, p1; float pz;
      SPIN {
        const int ok = misc[16 + (wave ^ 2)];
        asm volatile("" ::: "memory");
        p0 = *(const f32x4*)xp; p1 = *(const f32x4*)(xp + 1024); pz = *(const float*)(xp + 2048 - lane * 12);
        SB0();
        if (ok == 1) break;
      }
      STW(sane(__builtin_bit_cast(u32x4, keep[0] + p0)), kvp_off);
      STW(sane(__builtin_bit_cast(u32x4, keep[1] + p1)), kvp_off + 1024);
      STW1(sane1(__float_as_uint(zv + pz)), (lane < 32 && wr == 0) ? zp_off + lane * 4 : OOB);
    }
    STAMPT(0, 3, 0);
    {
      u32x4 qa[4][4]; u32x2 ql[4][2], qh[4][2];
#pragma unroll
      for (int u = 0; u < 4; ++u) {
#pragma unroll
        for (int j = 0; j < 2; ++j) {
          ql[u][j] = trread(smem + bo[1][j] + (u >> 1) * K1_STAGE + (u & 1) * 12288);
          qh[u][j] = trread(smem + bo[1][j] + (u >> 1) * K1_STAGE + (u & 1) * 12288 + 512);
        }
#pragma unroll
        for (int i = 0; i < 4; ++i) qa[u][i] = *(const u32x4*)(smem + ao[1][u & 1] + (u >> 1) * K1_STAGE + i * 2048);
      }
      asm volatile("" ::: "memory");
      misc[8 + wave] = 3;
      SB0();
#pragma unroll
      for (int u = 0; u < 4; ++u)
#pragma unroll
        for (int j = 0; j < 2; ++j)
#pragma unroll
          for (int i = 0; i < 4; ++i) acc[i][j] = mfma16((u32x4){ql[u][j][0], ql[u][j][1], qh[u][j][0], qh[u][j][1]}, qa[u][i], acc[i][j]);
      SB0();
    }
    unsigned qp[16];
    Phi<16> qs;
    const unsigned po = WS_KVP + ((2 * hp + (rb >> 3)) * 16 + h) * 4096 + (2 * ((rb >> 2) & 1) + ((lane >> 4) & 1)) * 1024 + (16 * (rb & 3) + (lane & 15)) * 16;
    u32x4 pv[8] = {};
    auto qepi_a = [&]() {
      f32x4 c4[2]; float m_b[4], rinv[4];
#pragma unroll
      for (int i = 0; i < 2; ++i) c4[i] = *(const f32x4*)(stm + 256 + wc * 96 + 16 * i + 4 * g4);
#pragma unroll
      for (int i = 0; i < 4; ++i) { m_b[i] = stm[64 * wr + 16 * i + c16]; rinv[i] = stm[128 + 64 * wr + 16 * i + c16]; }
      SB0();
      f32x2 tq[16];
#pragma unroll
      for (int i = 0; i < 16; ++i) tq[i] = PR(acc[i >> 2][(i >> 1) & 1], i & 1) - PR(c4[(i >> 1) & 1], i & 1) * m_b[i >> 2];
      SB0();
#pragma unroll
      for (int i = 0; i < 16; ++i) tq[i] *= rinv[i >> 2];
      SB0();
      phi_a<16>(tq, qs);
#pragma unroll
      for (int i = 0; i < 16; ++i) asm volatile("" : "+v"(qs.tb[i]), "+v"(qs.tr[i]), "+v"(qs.e[i]));
    };
    if (QPOS) qepi_a();
    if (wave == 0) {
      SLEEP(P_KVP);
#pragma unroll
      for (int p = 0; p < 8; ++p) pv[p] = LDW(po + p * 8192);
    }
    asm volatile("" ::: "memory");
    if (!QPOS) qepi_a();
    STAMPT(0, 4, 0);
    if (wave == 1 && rb < 2) {
      SLEEP(P_KVP);
      SPIN {
        unsigned m = 0;
#pragma unroll
        for (int p = 0; p < 2; ++p) { pv[p] = LDW(WS_ZP + (2 * hp + rb) * 2048 + p * 1024 + lane * 16); m = chk(max(max(m, pv[p][0]), pv[p][2]), pv[p]); }
        if (wave_ok(m)) break;
        asm volatile("" ::: "memory");
      }
      f32x4 s = __builtin_bit_cast(f32x4, pv[0]) + __builtin_bit_cast(f32x4, pv[1]);
#pragma unroll
      for (int i = 0; i < 4; ++i) { s[i] += dppf<DPP_ROR8>(s[i]); s[i] = xh_sum(x16_sum(s[i])); }
      if (lane < 16) st64(ws, WS_ZF + (2 * hp + rb) * 128 + (lane >> 3) * 64 + (lane & 3) * 16 + ((lane >> 2) & 1) * 8,
                          ((u64)sane1(pack2(s[2], s[3])) << 32) | pack2(s[0], s[1]));
    }
    if (wave == 0) {
      SPIN {
        unsigned m = 0;
#pragma unroll
        for (int p = 0; p < 8; ++p) m = chk(m, pv[p]);
        CNT(0);
        if (wave_ok(m)) break;
        asm volatile("" ::: "memory");
        SLEEP(L_KVP);
#pragma unroll
        for (int p = 0; p < 8; ++p) pv[p] = LDW(po + p * 8192);
      }
      f32x4 s = __builtin_bit_cast(f32x4, pv[0]);
#pragma unroll
      for (int p = 1; p < 8; ++p) s += __builtin_bit_cast(f32x4, pv[p]);
      const unsigned p0 = pack2(xh_sum(s[0]), xh_sum(s[1])), p1 = pack2(xh_sum(s[2]), xh_sum(s[3]));
      auto a = __builtin_amdgcn_permlane16_swap(p0, p0, false, false);
      auto b = __builtin_amdgcn_permlane16_swap(p1, p1, false, false);
      STW(sane((u32x4){a[0], b[0], a[1], b[1]}), lane < 16 ? WS_KVF + hp * 4096 + rb * 256 + lane * 16 : OOB);
      STAMPT(1, 2, 0);
    }
    phi_b<16>(qs, qp);
#pragma unroll
    for (int i = 0; i < 16; ++i) asm volatile("" : "+v"(qp[i]));
    if (wave == 0) misc[7] = 1;
    else SPIN { if (misc[7] == 1) break; __builtin_amdgcn_s_sleep(1); }
    SLEEP(P_KVF);
    {
      u32x4 k0, k1, z0;
      SPIN {
        k0 = LDW(WS_KVF + head * 2048 + lane * 16); k1 = LDW(WS_KVF + head * 2048 + 1024 + lane * 16);
        z0 = LDW(WS_ZF + head * 128 + g4 * 16);
        CNT(1);
        if (wave_ok(chk(chk(chk(0u, k0), k1), z0))) break;
        asm volatile("" ::: "memory");
        SLEEP(L_KVF);
      }
      STAMPT(0, 5, 0);
      const f32x4 Z4 = {0.f, 0.f, 0.f, 0.f};
      f32x4 X[8], Dn[4];
#pragma unroll
      for (int i = 0; i < 4; ++i) Dn[i] = mfma16(z0, F4_(qp, i), Z4);
#pragma unroll
      for (int i = 0; i < 8; ++i) X[i] = mfma16((i & 1) ? k1 : k0, F4_(qp, i >> 1), Z4);
      SB0();
      float ri[4]; unsigned a[16]; f32x2 f[16];
#pragma unroll
      for (int i = 0; i < 4; ++i) ri[i] = __builtin_amdgcn_rcpf(rbf(rbf(Dn[i][0]) + 1e-6f));
      SB0();
#pragma unroll
      for (int i = 0; i < 16; ++i) a[i] = pack2o(X[i >> 1][2 * (i & 1)], X[i >> 1][2 * (i & 1) + 1]);
      SB0();
#pragma unroll
      for (int i = 0; i < 16; ++i) f[i] = unpk(a[i]);
      SB0();
#pragma unroll
      for (int i = 0; i < 16; ++i) f[i] *= ri[i >> 2];
      SB0();
#pragma unroll
      for (int i = 0; i < 4; ++i) {
        STW(((u32x4){pack2(f[4 * i][0], f[4 * i][1]), pack2(f[4 * i + 1][0], f[4 * i + 1][1]), pack2(f[4 * i + 2][0], f[4 * i + 2][1]), pack2(f[4 * i + 3][0], f[4 * i + 3][1])}),
            o_off + (i >> 1) * 65536 + (i & 1) * 1024);
        SB0();
      }
    }
    if (wave == 0) misc[6] = 1;
    STAMPT(0, 6, 0);
    {
      EPI();
      const int rt = wave & 1, kh = wave >> 1;
      const unsigned ob = WS_O + ((rp * 2 + rt) * 32 + 16 * kh) * 2048 + lane * 16;
#define LDO(t, st) __builtin_amdgcn_raw_buffer_load_b128(wsr, ob + (st) * 1024, whead(t) * 2048, 16)
      int woff[2];
#pragma unroll
      for (int i = 0; i < 2; ++i) woff[i] = kh * 8192 + (g4 >> 1) * 2048 + (g4 & 1) * 512 + trl + ((i ^ (g4 & 1)) * 32);
      struct Frag2 { u32x2 lo[8], hi[8]; };
      Frag2 G[2];
      auto ld2 = [&](Frag2& N, int sn, int nt, int hi) {
        const char* p = smem + sn + woff[nt & 1] + (nt >> 2) * 1024 + ((nt >> 1) & 1) * 256;
        if (hi) N.hi[nt] = trread(p + 4096); else N.lo[nt] = trread(p);
      };
      WAIT_V(0);
      STW(((u32x4){1u, 1u, 1u, 1u}), fb_off);
      RAW_BAR();
#pragma unroll
      for (int i = 0; i < 16; ++i) ld2(G[0], wo_slot(0), i >> 1, i & 1);
      f32x4 a2[2][8];
#pragma unroll
      for (int i = 0; i < 16; ++i) a2[i & 1][i >> 1] = (f32x4){0.f, 0.f, 0.f, 0.f};
      SLEEP(P_FB);
      SPIN { CNT(2); if (wave_ok(ld32(ws, WS_FB + (rp * 32 + 16 * kh + (lane & 15)) * 128))) break; SLEEP(L_FB); }
      asm volatile("" ::: "memory");
      STAMPT(0, 7, 0);
      char* pr[2];
#pragma unroll
      for (int i = 0; i < 2; ++i) pr[i] = smem + L_P + kh * L_PK + (32 * rt + c16) * 512 + (((4 * i + g4) ^ (c16 & 7)) * 16);
      u32x4 of[16][2];
#pragma unroll
      for (int t = 0; t < OW; ++t) { of[t][0] = LDO(t, 0); of[t][1] = LDO(t, 1); }
      SB0();
      sfor<0, 16>([&](auto T) {
        constexpr int t = T.v;
        Frag2& C = G[t & 1]; Frag2& N = G[(t + 1) & 1];
#pragma unroll
        for (int i = 0; i < 16; ++i) {
          const int nt = i >> 1, st = i & 1;
          a2[st][nt] = mfma16((u32x4){C.lo[nt][0], C.lo[nt][1], C.hi[nt][0], C.hi[nt][1]}, of[t][st], a2[st][nt]);
          SB0();
          if ((t & 1) && t <= 11 && i == 8) {
            asm volatile("s_waitcnt lgkmcnt(8)" ::: "memory"); __builtin_amdgcn_s_barrier(); asm volatile("" ::: "memory");
          }
          if (t < 15) ld2(N, wo_slot(t + 1), nt, st);
          if (t > 0 && t - 1 + OW < 16 && (i == 3 || i == 11)) of[(t - 1 + OW) & 15][i >> 3] = LDO(t - 1 + OW, i >> 3);
          if (t - 1 + OW == 16 && (i == 3 || i == 11)) { xv[i >> 3] = *(const u32x4*)(x + ego + (i >> 3) * 64); wv[i >> 3] = *(const u32x4*)(w_r + cb * 128 + eseg * 8 + (i >> 3) * 64); }
          if (t == 15 && i >= 4) *(f32x4*)(pr[(nt - 2) & 1] + ((nt - 2) >> 1) * 128 + st * 8192) = a2[st][(nt - 2) & 7];
          SB0();
        }
      });
      STAMPT(0, 8, 0);
#pragma unroll
      for (int i = 12; i < 16; ++i) *(f32x4*)(pr[(i >> 1) & 1] + (i >> 2) * 128 + (i & 1) * 8192) = a2[i & 1][i >> 1];
    }
    unpack_xw();
    __builtin_amdgcn_s_setprio(0);
  } else {
    LANES();
    const int sw = wave - 4, t2 = tid - 256;
    __builtin_amdgcn_s_setprio(PRIO_S);
    unsigned hs_g0 = 0, hs_cur = 0;
    u32x4 ms_exp = {0, 0, 0, 0};
    bool hs_act = true, ms_act = (bid == 0) && (sw == 1);
    const unsigned hs_off = (lane == 1 ? SY_GO : SY_CH) + bid * 4;
    const unsigned re_off = t2 == 0 ? SY_RE + bid * 4 : OOB;
    const unsigned ms_off = lane * 16;
    const char* wsu = uni(ws);
#define HS_PROBE() asm volatile("s_nop 4\n\tglobal_load_dword v156, %0, %1 sc1" :: "v"(hs_off), "s"(wsu) : "memory", RSV)
#define MS_PROBE() asm volatile("s_nop 4\n\tglobal_load_dwordx4 v[152:155], %0, %1 offset:2048 sc1" :: "v"(ms_off), "s"(wsu) : "memory", RSV)
    auto hs_step = [&](bool first) {
      if (hs_act) {
        const unsigned hv_ = rget<156>();
        hs_act = __builtin_amdgcn_readlane(hv_, 1) == hs_g0;
        const unsigned c = __builtin_amdgcn_readlane(hv_, 0);
        if (sw == 0 && (first || c != hs_cur)) STW1(hv_, re_off);
        hs_cur = c;
        HS_PROBE();
      }
      if (ms_act) {
        const unsigned d = ((rget<152>() ^ ms_exp[0]) | (rget<153>() ^ ms_exp[1])) | ((rget<154>() ^ ms_exp[2]) | (rget<155>() ^ ms_exp[3]));
        ms_act = __any(d != 0u);
        STW(ms_exp, ms_act ? OOB : SY_GO + ms_off);
        MS_PROBE();
      }
    };
    HS_PROBE();
    if (ms_act) {
      MS_PROBE();
      asm volatile("s_nop 4\n\tglobal_load_dwordx4 v[148:151], %0, %1 offset:1024 sc1" :: "v"(ms_off), "s"(wsu) : "memory", RSV);
    }
    if (sw == 3) {
      const char* cp = (const char*)(pc0 - (pc0 & 127)) + (idx * CODE_LINES + (lane < CODE_LINES ? lane : 0)) * 128;
      asm volatile("global_load_dword v157, %0, off" :: "v"(cp) : "memory", RSV);
    }
    const int gr = (g8 >> 3) * 7 + min(hs_, 6);
    const bool real = hs_ != 7;
    const unsigned voG = (unsigned)((lane >> 3) * 8 * F_ + (lane & 7) * 8) * 2u, voD = (unsigned)((lane >> 3) * 8 * D_ + (lane & 7) * 8) * 2u;
    const size_t gofs = ((size_t)(kq * 256 + sw * 64) * F_ + gr * 64) * 2;
    const char* pG = (const char*)wg + gofs;
    const char* pU = (const char*)wu + gofs;
    const char* pD = (const char*)(wd + (size_t)(gr * 64) * D_ + kq * 256 + sw * 64);
    auto wstep = [&](auto I) {
      constexpr int i = I.v;
      if (i < 8) wload<VW + 4 * i>(voG, pG + (i & 7) * (F_ * 2));
      else if (i < 16) wload<VW + 4 * i>(voG, pU + (i & 7) * (F_ * 2));
      else wload2<VW + 4 * i>(voD, pD + (i & 7) * (D_ * 2));
    };
    unsigned voW;
    {
      const int bi = lane >> 4, q = (lane >> 2) & 3, ch = lane & 3;
      voW = (unsigned)((4 * (bi >> 1) + q) * D_ + (bi & 1) * 32 + (ch ^ (bi & 2)) * 8) * 2u;
    }
    const char* baseW = (const char*)(wo + (size_t)((16 * (sw >> 1)) * 32 + 16 * (sw & 1)) * D_ + cb * 128) - 1024;
    auto stageW = [&](auto J, bool rsv) {
      const char* s_ = baseW + (size_t)whead(J.v) * (32 * D_ * 2);
      if (rsv) glds4o<wo_slot(J.v)>(voW + 1024, voW + 128, voW + 8 * D_ * 2 - 1024, voW + 8 * D_ * 2 + 128 - 2048, s_, l4);
      else glds4<wo_slot(J.v)>(voW + 1024, voW + 128, voW + 8 * D_ * 2 - 1024, voW + 8 * D_ * 2 + 128 - 2048, s_, l4);
    };
    int sta[4], stb[3];
    {
      const int row = t2 >> 1, key = (row >> 1) & 7;
#pragma unroll
      for (int c = 0; c < 4; ++c) { sta[c] = (posA & 3) * K1_STAGE + row * 128 + (((4 * (t2 & 1) + c) ^ key) * 16); asm volatile("" : "+v"(sta[c]) :: RSV); }
#pragma unroll
      for (int i = 0; i < 3; ++i) {
        const int G = sw * 3 + i, hd = G >= 6 ? 1 : 0, mat = (G - 6 * hd) >> 1;
        stb[i] = (posB & 3) * K1_STAGE + 16384 + (lane >> 4) * 6144 + mat * 1024 + hd * 256 + ((lane & 15) >> 2) * 64 + (G & 1) * 32 + (lane & 3) * 8;
        asm volatile("" : "+v"(stb[i]) :: RSV);
      }
    }
    unsigned rs_dst = rs_slot + t2 * 4, cs_dst = lane < 48 ? cs_slot + (sw * 48 + lane) * 4 : OOB;
    unsigned rs_off = WS_RS + rb * 16384 + (lane >> 4) * 1024 + sw * 256 + (lane & 15) * 16;
    unsigned cs_off = WS_CS + hp * 12288 + (lane >> 4) * 768 + sw * 192 + min(lane & 15, 11) * 16;
    asm volatile("" : "+v"(rs_dst), "+v"(cs_dst), "+v"(rs_off), "+v"(cs_off) :: RSV);
    int stm_dst = ((lane >> 5) * 128 + sw * 32 + (lane & 15) * 2 + ((lane >> 4) & 1)) * 4, cs_put = (lane & 15) < 12 ? (256 + sw * 48 + (lane & 15) * 4 + (lane >> 4)) * 4 : 1792 + lane * 4;
    asm volatile("" : "+v"(stm_dst), "+v"(cs_put) :: RSV);
    auto st_load = [&]() { sfor<0, 4>([&](auto I) { sload<224 + 4 * I.v>(rs_off, wsu + I.v * 4096); sload<240 + 4 * I.v>(cs_off, wsu + I.v * 3072); }); };
    sfor<3, GS[0]>(group);
    WAIT_VR(gops(GS[0]) - 10 + 1); RAW_BARR();
    TSL(14);
    sfor<GS[0], GS[1]>(group);
    WAIT_VR(gops(GS[1]) - 20); RAW_BARR();
    hs_g0 = __builtin_amdgcn_readlane(rget<156>(), 1);
    if (ms_act) {
      const u32x4 re = {rget<152>(), rget<153>(), rget<154>(), rget<155>()}, g0 = {rget<148>(), rget<149>(), rget<150>(), rget<151>()};
#pragma unroll
      for (int j = 0; j < 4; ++j) { const unsigned c = re[j] + 1u; ms_exp[j] = (c == g0[j]) ? c + 1u : c; }
      STW(ms_exp, SY_CH + ms_off);
    }
    u32x4 ra[4] = {}; u32x2 rc[3][4] = {};
    float sa = 0.f, sb = 0.f, qa = 0.f, qb = 0.f, ca[3] = {0.f, 0.f, 0.f}, cb_[3] = {0.f, 0.f, 0.f};
    auto chunk = [&](auto C) {
      constexpr int c = C.v;
      if constexpr (c < 4) {
        asm volatile("" : "+v"(ra[c]) :: RSV);
#pragma unroll
        for (int i = 0; i < 4; ++i) { const float lo = bflo(ra[c][i]), hi = bfhi(ra[c][i]); sa = fadd(sa, lo); sb = fadd(sb, hi); qa = fsq(lo, qa); qb = fsq(hi, qb); }
        asm volatile("" : "+v"(sa), "+v"(sb), "+v"(qa), "+v"(qb) :: RSV);
      } else if constexpr (c == 4) {
        float s = fadd(sa, sb), sq = fadd(qa, qb);
        s = fadd(s, dppf<DPP_X1>(s)); sq = fadd(sq, dppf<DPP_X1>(sq));
        STW1(sane1(__float_as_uint((t2 & 1) ? sq : s)), rs_dst);
      } else if constexpr (c < 11) {
        constexpr int i = (c - 5) >> 1, j = ((c - 5) & 1) * 2;
        asm volatile("" : "+v"(rc[i][j]), "+v"(rc[i][j + 1]) :: RSV);
#pragma unroll
        for (int e = 0; e < 4; ++e) { const unsigned u = rc[i][j + (e >> 1)][e & 1]; ca[i] = fadd(ca[i], bflo(u)); cb_[i] = fadd(cb_[i], bfhi(u)); }
        asm volatile("" : "+v"(ca[i]), "+v"(cb_[i]) :: RSV);
      } else {
        const float c2 = fadd(ca[2], cb_[2]);
        STW1(sane1(__float_as_uint(sw32(sw16(fadd(ca[0], cb_[0]), fadd(ca[1], cb_[1])), sw16(c2, c2)))), cs_dst);
      }
    };
    sfor<0, 12>([&](auto T) {
      constexpr int t = T.v;
      TSL(t);
      if (t < 4 && posA == t + 1) {
#pragma unroll
        for (int c = 0; c < 4; ++c) ra[c] = *(const u32x4*)(smem + sta[c]);
      }
      if (t < 4 && posB == t + 1) {
#pragma unroll
        for (int i = 0; i < 3; ++i)
#pragma unroll
          for (int j = 0; j < 4; ++j) rc[i][j] = trread(smem + (stb[i] ^ ((j >> 1) * 32)) + (j >> 1) * 3072 + (j & 1) * 512);
      }
      if (t == 2) HS_PROBE();
      if (t >= 4) hs_step(t == 4);
      sfor<GS[t + 1], GS[t + 2]>([&](auto G) {
        group(G);
        constexpr int u = t - 4, p = G.v - GS[t + 1];
        constexpr int ord[12] = {0, 5, 6, 1, 7, 8, 2, 9, 10, 3, 11, 4};
        if constexpr (u >= 0 && u < 4 && p < 3) chunk(IC<ord[(3 * u + p) % 12]>{});
      });
      if (P2MAP == 0 && t >= TCH0 && t < TCH0 + 8 && sw == ((t - TCH0) & 3))
        asm volatile("s_nop 4\n\tglobal_load_dword v157, %0, %1" :: "v"(lane * 128), "s"(uni((const char*)wo + idx * 65536 + ((t - TCH0 + xcd) & 7) * 8192)) : "memory", RSV);
      if (P2MAP == 1 && t >= TCH0 && t < TCH0 + 4 && sw == ((t - TCH0) & 3))
        asm volatile("s_nop 4\n\tglobal_load_dword v157, %0, %1" :: "v"((lane >> 3) * 2048 + (lane & 7) * 128), "s"(uni((const char*)wo + idx * 65536 + ((t - TCH0 + (rp >> 3)) & 3) * 16384 + (cb & 4) * 256)) : "memory", RSV);
      if (real) sfor<WS_[t], WS_[t + 1]>(wstep);
      TSL(16 + t);
      WAIT_VR(t < 11 ? gops(GS[t + 2]) - 10 * (t + 3) : gops(GS[13]) - gops(GS[12]));
      TSL(32 + t);
      RAW_BARR();
    });
    TSL(12);
    hs_step(false);
    auto st_put = [&]() -> bool {
      float a[8]; unsigned m = 0, n = 0;
      rsum4<224>(a, m, n); rsum4<240>(a + 4, m, n);
      const float s1 = sw16(a[0], a[2]), q1 = sw16(a[1], a[3]);
      const float cs = sw32(sw16(a[4], a[5]), sw16(a[6], a[7]));
      const float mean = sw32(s1, s1) * (1.0f / 1024.0f);
      const float var = fmaxf(__builtin_fmaf(sw32(q1, q1), 1.0f / 1024.0f, -(mean * mean)), 0.f);
      const float rinv = __builtin_amdgcn_rcpf(rbf(__builtin_amdgcn_sqrtf(rbf(rbf(var) + 1e-5f))));
      *(float*)(smem + L_STM + stm_dst) = (lane & 32) ? rinv : rbf(mean);
      *(float*)(smem + L_STM + cs_put) = cs;
      return wave_ok(max(m, n));
    };
    st_load();
    if (real) sfor<WS_[12], W1A>(wstep);
    TSL(28);
    if (real) WAIT_VR(8 + W1A - WS_[11]); else WAIT_VR(8);
    if (lane == 0) { misc[8 + sw] = 0; misc[12 + sw] = 0; misc[16 + sw] = 0; misc[7] = 0; misc[6] = 0; }
    TSL(44);
    RAW_BARR();
    TSL(13); TSDUMP(1 + sw);
    STAMPT(1, 0, 256);
    if (real) WAIT_VR(W1A - WS_[12]); else WAIT_VR(0);
    if (!st_put() || hs_act) {
      if (ms_act) {
        SPIN {
          bool ok = true;
#pragma unroll
          for (int j = 0; j < 4; ++j) ok &= (ld32(ws, SY_RE + lane * 16 + j * 4) == ms_exp[j]);
          if (__all(ok)) break;
        }
        STW(ms_exp, SY_GO + lane * 16);
      }
      if (hs_act) {
        SPIN {
          if (ld32(ws, SY_GO + bid * 4) != hs_g0) break;
          const unsigned c = ld32(ws, SY_CH + bid * 4);
          if (c != hs_cur) { hs_cur = c; STW1(c, re_off); }
        }
      }
      SPIN { st_load(); WAIT_VR(0); if (st_put()) break; }
    }
    asm volatile("" ::: "memory", RSV);
    if (lane == 0) misc[12 + sw] = 1;
    __builtin_amdgcn_s_setprio(0);
    if (real) sfor<W1A, 16>(wstep);
    auto mwait = [&](int v) { SPIN { const int a = misc[8 + (lane & 3)]; if (__all(a >= v)) break; __builtin_amdgcn_s_sleep(1); } asm volatile("" ::: "memory"); };
    sfor<0, 9>([&](auto J) {
      if (J.v == 4) mwait(3);
      if (J.v == WOD) { SPIN { if (misc[WODF] == 1) break; __builtin_amdgcn_s_sleep(2); } SLEEP(WODS); }
      stageW(J, J.v < 2);
      if (J.v >= 1 && J.v < WOD) WAIT_V(4 * PACE);
      if (J.v == 1) { WAIT_VR(8); sfor<0, 16>(wtr); }
      if (J.v >= 2 && PSLP) __builtin_amdgcn_s_sleep(PSLP);
    });
    STAMPT(1, 1, 256);
    EPI();
#pragma unroll
    for (int i = 0; i < 2; ++i) { xv[i] = *(const u32x4*)(x + ego + i * 64); wv[i] = *(const u32x4*)(w_r + cb * 128 + eseg * 8 + i * 64); }
    asm volatile("s_waitcnt vmcnt(0)" : "+v"(xv[0]), "+v"(xv[1]), "+v"(wv[0]), "+v"(wv[1]) :: "memory");
    unpack_xw();
    RAW_BAR();
    RAW_BAR(); stageW(IC<9>{}, false); stageW(IC<10>{}, false);
    RAW_BAR(); stageW(IC<11>{}, false); stageW(IC<12>{}, false);
    RAW_BAR(); stageW(IC<13>{}, false); stageW(IC<14>{}, false);
    WAIT_V(16); RAW_BAR(); stageW(IC<15>{}, false);
    WAIT_V(12); RAW_BAR();
    WAIT_V(0); RAW_BAR();
    if (real) sfor<16, 24>(wstep);
  }
  LANES(); EPI();
  const int grow = rp * 64 + erow;
  int pa0 = L_P + erow * 512 + ((2 * eseg) ^ (erow & 7)) * 16, pa1 = pa0 ^ 16;
  unsigned st_dst = eseg == 0 ? WS_ST2 + ((rp * 8 + cb) * 64 + erow) * 16 : OOB, st_src = WS_ST2 + rp * 8192 + lane * 16;
  unsigned fd_dst = lane < 8 ? WS_FD + (cb * 32 + rp) * 128 + lane * 16 : OOB, kidx = 0xFFFFu - grow;
  asm volatile("" : "+v"(pa0), "+v"(pa1), "+v"(st_dst), "+v"(st_src), "+v"(fd_dst), "+v"(kidx));
  RAW_BAR2();
  STAMPT(0, 9, 0);
  unsigned hb[8];
  f32x2 hv[8];
  {
    f32x4 pp[2][4];
#pragma unroll
    for (int k = 0; k < 2; ++k)
#pragma unroll
      for (int i = 0; i < 4; ++i) pp[k][i] = *(const f32x4*)(smem + ((i & 1) ? pa1 : pa0) + (i >> 1) * 256 + k * L_PK);
    SB0();
    f32x2 s = {0.f, 0.f}, q = {0.f, 0.f}, p2 = {0.f, 0.f};
#pragma unroll
    for (int i = 0; i < 8; ++i) {
      const f32x4 a = pp[0][i >> 1] + pp[1][i >> 1];
      const f32x2 u = xf[i] + unpk(pack2o(a[2 * (i & 1)], a[2 * (i & 1) + 1]));
      hb[i] = pack2o(u[0], u[1]);
      hv[i] = unpk(hb[i]);
      s += hv[i]; q += hv[i] * hv[i]; p2 += hv[i] * wf2[i];
    }
    STW(sane((u32x4){__float_as_uint(oct_sum(s[0] + s[1])), __float_as_uint(oct_sum(q[0] + q[1])), __float_as_uint(oct_sum(p2[0] + p2[1])), __float_as_uint(sw_)}), st_dst);
  }
  KEEP2();
  STAMPT(0, 10, 0);
  const int sw = wave - 4;
  const unsigned fd_src = WS_FD + xcd * 4096 + r * 128;
  u32x2 kpk = {SENT, SENT};
  if (wave == 0) {
    u32x4 pa[8];
    SLEEP(P_C);
    SPIN {
      unsigned m = 0;
#pragma unroll
      for (int j = 0; j < 8; ++j) { pa[j] = LDW(st_src + j * 1024); m = chk(m, pa[j]); }
      CNT(3);
      if (wave_ok(m)) break;
      KEEP2();
    }
    STAMPT(0, 11, 0);
#define F4(v) __builtin_bit_cast(f32x4, v)
    const f32x4 t = ((F4(pa[0]) + F4(pa[1])) + (F4(pa[2]) + F4(pa[3]))) + ((F4(pa[4]) + F4(pa[5])) + (F4(pa[6]) + F4(pa[7])));
    const float mean = t[0] * (1.0f / 1024.0f);
    const float var = fmaxf(__builtin_fmaf(t[1], 1.0f / 1024.0f, -(mean * mean)), 0.f);
    const float mb = rbf(mean), rs = __builtin_amdgcn_rcpf(rbf(__builtin_amdgcn_sqrtf(rbf(rbf(var) + 1e-5f))));
    *(f32x2*)(smem + L_KX + lane * 8) = (f32x2){mb, rs};
    const float sc = fmaxf((t[2] - mb * t[3]) * rs, -INFINITY);
    const int u = (int)(pack2(0.f, sc) & 0xffff0000u);
    unsigned b0 = ((unsigned)(u ^ ((u >> 31) | (int)0x80000000)) & 0xffff0000u) | (0xFFFFu - (rp * 64 + lane)), b1 = 0;
    top2_lo(b0, b1); top2_hi(b0, b1);
    STW(((u32x4){b0, b1, b0, b1}), fd_dst);
    if (lane == 0) *(u32x2*)(smem + L_MISC + 112) = (u32x2){b0, b1};
  } else if (wave >= 4) {
    asm volatile("s_waitcnt vmcnt(0)" ::: "memory", RSV2);
    sfor<16, 24>(wtr);
  }
  RAW_BAR();
  bool cand;
  u32x4 ob[2];
  {
    const f32x2 mr = *(const f32x2*)(smem + L_KX + erow * 8);
    const u32x2 kk = *(const u32x2*)(smem + L_MISC + 112);
    const float mb = mr[0], rs = mr[1];
    const unsigned b0 = kk[0], b1 = kk[1];
    u32x4 nq[2];
#pragma unroll
    for (int i = 0; i < 8; ++i) {
      const f32x2 d = hv[i] - mb;
      const f32x2 n = unpk(pack2o(d[0], d[1])) * rs;
      nq[i >> 2][i & 3] = pack2o(n[0], n[1]);
    }
    const bool c2 = (b1 & 0xffffu) == kidx;
    cand = c2 || (b0 & 0xffffu) == kidx;
    if (cand) {
      const unsigned xo = WS_XS + (rp * 2 + (c2 ? 1 : 0)) * 2048 + cb * 256 + eseg * 16;
      STW(sane(nq[0]), xo); STW(sane(nq[1]), xo + 128);
      char* cp = smem + L_CAND + (c2 ? 512 : 0) + eseg * 16;
      *(u32x4*)cp = (u32x4){hb[0], hb[1], hb[2], hb[3]}; *(u32x4*)(cp + 128) = (u32x4){hb[4], hb[5], hb[6], hb[7]};
      *(u32x4*)(cp + 256) = nq[0]; *(u32x4*)(cp + 384) = nq[1];
    }
    asm volatile("" ::: "memory");
    if (wave >= 4) { SLEEP(P_D); kpk = __builtin_amdgcn_raw_buffer_load_b64(wsr, fd_src, 0, 16); }
    asm volatile("" ::: "memory");
#pragma unroll
    for (int i = 0; i < 8; ++i) { const f32x2 o = hv[i] + unpk(nq[i >> 2][i & 3]); ob[i >> 2][i & 3] = pack2(o[0], o[1]); }
  }
#define OUT_ST() do { *(u32x4*)(out + ego) = ob[0]; *(u32x4*)(out + ego + 64) = ob[1]; } while (0)
  STAMPT(0, 12, 0);
  if (wave < 4) {
    if (!cand) OUT_ST();
    RAW_BAR();
    const unsigned w0 = misc[24], w1 = misc[25];
    if (cand && grow != (int)key_idx(w0) && grow != (int)key_idx(w1)) OUT_ST();
#pragma unroll 1
    for (int tk = 0; tk < 2; ++tk) {
      const unsigned wk = tk ? w1 : w0, slot = misc[26 + tk];
      if ((int)(slot >> 1) != rp) continue;
      u32x4 pv[7];
      const unsigned yo = WS_YP + ((tk * 8 + cb) * 64 + (lane >> 3) * 8) * 512 + wave * 128 + (lane & 7) * 16;
      const int col = (wave >> 1) * 64 + (lane & 7) * 8 + (wave & 1) * 4 + (lane >> 4);
      float sg = rbf(__builtin_amdgcn_rcpf(1.0f + __expf(-key_val(wk))));
      float hh = bflo(*(const u16*)(smem + L_CAND + (slot & 1) * 512 + col * 2)), nn = bflo(*(const u16*)(smem + L_CAND + (slot & 1) * 512 + 256 + col * 2));
      u16* op = out + (size_t)key_idx(wk) * D_ + cb * 128 + col;
      asm volatile("" : "+v"(sg), "+v"(hh), "+v"(nn), "+v"(op));
      if (tk == 0 || (int)(misc[26] >> 1) != rp) {
        SPIN { if (misc[30] == (int)w0) break; __builtin_amdgcn_s_sleep(1); }
        SLEEP(P_E);
      }
      SPIN {
        unsigned m = 0;
#pragma unroll
        for (int p = 0; p < 7; ++p) { pv[p] = LDW(yo + p * 512); m = chk(m, pv[p]); }
        CNT(6);
        if (wave_ok(m)) break;
        asm volatile("" ::: "memory");
        SLEEP(L_E);
      }
      STAMPT(0, 13, 0);
      f32x4 s = __builtin_bit_cast(f32x4, pv[0]);
#pragma unroll
      for (int p = 1; p < 7; ++p) s += __builtin_bit_cast(f32x4, pv[p]);
      float y = sw32(sw16(s[0], s[1]), sw16(s[2], s[3]));
      y += dppf<DPP_ROR8>(y);
      if (!(lane & 8)) *(GAS u16*)op = (u16)pack2(hh + rbf(nn + rbf(rbf(y) * sg)), 0.f);
      STAMPT(0, 14, 0);
    }
  } else {
    unsigned xl = WS_XS + (kq * 256 + sw * 64 + (lane >> 4) * 16 + ((lane >> 2) & 1) * 8) * 2;
    int red_dst = (lane < 8 || (lane >= 24 && lane < 32)) ? L_RED + sw * 2048 + ((lane >> 3) & 1) * 1024 + (lane & 7) * 16 : L_RED + 8192 + lane * 16;
    unsigned gu_src = WS_GU + g8 * 4096 + lane * 16;
    int act_dst = L_ACT + sw * 256 + ((lane & 7) * 8 + (lane >> 3)) * 2, act_src = L_ACT + sw * 256 + (lane & 1) * 128 + (lane >> 4) * 32 + ((lane >> 1) & 1) * 16;
    unsigned yp_dst = lane < 16 ? WS_YP + (((lane >> 3) * 8 + 2 * kq + (sw >> 1)) * 64 + g8) * 512 + (sw & 1) * 256 + (lane & 7) * 16 : OOB;
    asm volatile("" : "+v"(xl), "+v"(red_dst), "+v"(gu_src), "+v"(act_dst), "+v"(act_src), "+v"(yp_dst));
    unsigned w0, w1; int s0 = -1, s1 = -1;
    u32x4 xa = {SENT, SENT, SENT, SENT};
    SPIN {
      CNT(4);
      const bool all = wave_ok(kpk[1]);
      const unsigned k0 = kpk[1] == SENT ? 0u : kpk[0], k1 = kpk[1] == SENT ? 0u : kpk[1];
      w0 = k0; w1 = k1;
      top2_lo(w0, w1); top2_mid(w0, w1);
      const int n0 = 2 * __builtin_ctzll(__ballot(k0 == w0));
      const u64 m1 = __ballot(k0 == w1);
      const int n1 = m1 ? 2 * __builtin_ctzll(m1) : n0 + 1;
      const bool same = n0 == s0 && n1 == s1;
      s0 = n0; s1 = n1;
      asm volatile("" ::: "memory");
      if (all && same) break;
      if (!all) { SLEEP(L_D); kpk = __builtin_amdgcn_raw_buffer_load_b64(wsr, fd_src, 0, 16); }
      xa = LDW(xl + ((lane & 1) ? s1 : s0) * 2048);
      if (all) break;
    }
    SPIN {
      if (wave_ok(chk(0u, xa))) break;
      asm volatile("" ::: "memory");
      xa = LDW(xl + ((lane & 1) ? s1 : s0) * 2048);
    }
    STAMPT(1, 3, 256);
    {
      const u32x4 z4 = {0u, 0u, 0u, 0u};
      const u32x4 xg = (lane & 2) ? z4 : xa, xu = (lane & 2) ? xa : z4;
      f32x4 d[8];
#pragma unroll
      for (int c = 0; c < 8; ++c) d[c] = mfma16(xg, wf[c], (f32x4){0.f, 0.f, 0.f, 0.f});
      if (tid == 256) { *(u32x4*)(smem + L_MISC + 96) = (u32x4){w0, w1, (unsigned)s0, (unsigned)s1}; misc[30] = 0; }
      SB0();
#pragma unroll
      for (int c = 0; c < 10; ++c) {
        if (c < 8) d[c & 7] = mfma16(xu, wf[8 + (c & 7)], d[c & 7]);
        if (c >= 2) *(f32x4*)(smem + red_dst + (c - 2) * 128) = d[(c - 2) & 7];
        SB0();
      }
    }
    RAW_BAR();
    if (sw == 0) {
      f32x4 t[8];
#pragma unroll
      for (int p = 0; p < 8; ++p) t[p] = *(const f32x4*)(smem + L_RED + p * 1024 + lane * 16);
      SB0();
      STW(sane(__builtin_bit_cast(u32x4, ((t[0] + t[1]) + (t[2] + t[3])) + ((t[4] + t[5]) + (t[6] + t[7])))), gu_src + kq * 1024);
    } else __builtin_amdgcn_s_sleep(2);
    STAMPT(1, 4, 256);
    {
      u32x4 q[4];
      if (OUTE && grow != (int)key_idx(w0) && grow != (int)key_idx(w1)) OUT_ST();
      asm volatile("" ::: "memory");
      SLEEP(P_GU);
      SPIN {
        unsigned m = 0;
#pragma unroll
        for (int j = 0; j < 4; ++j) { q[j] = LDW(gu_src + j * 1024); m = chk(m, q[j]); }
        CNT(5);
        if (wave_ok(m)) break;
        asm volatile("" ::: "memory");
      }
      STAMPT(1, 5, 256);
      const f32x4 tot = (__builtin_bit_cast(f32x4, q[0]) + __builtin_bit_cast(f32x4, q[1])) + (__builtin_bit_cast(f32x4, q[2]) + __builtin_bit_cast(f32x4, q[3]));
      const f32x2 gs = unpk(pack2o(tot[0], tot[1])), us = unpk(pack2o(tot[2], tot[3]));
      const f32x2 sg = unpk(pack2o(gs[0] * __builtin_amdgcn_rcpf(1.0f + __expf(-gs[0])), gs[1] * __builtin_amdgcn_rcpf(1.0f + __expf(-gs[1]))));
      const unsigned a2 = pack2o(sg[0] * us[0], sg[1] * us[1]);
      *(u16*)(smem + act_dst) = (u16)a2; *(u16*)(smem + act_dst + 128) = (u16)(a2 >> 16);
      __builtin_amdgcn_fence(__ATOMIC_RELEASE, "wavefront"); __builtin_amdgcn_wave_barrier(); __builtin_amdgcn_fence(__ATOMIC_ACQUIRE, "wavefront");
      const u32x4 aa = *(const u32x4*)(smem + act_src);
      f32x4 d[8];
#pragma unroll
      for (int c = 0; c < 8; ++c) d[c] = mfma16(aa, wf[16 + c], (f32x4){0.f, 0.f, 0.f, 0.f});
      SB0();
      u32x4 y[2];
#pragma unroll
      for (int c = 0; c < 8; ++c) {
        const float t0 = d[c][0] + dppf<DPP_ROR8>(d[c][2]), t1 = d[c][3] + dppf<DPP_ROR8>(d[c][1]);
        y[c >> 2][c & 3] = __float_as_uint((lane & 8) ? t1 : t0);
      }
      STW(sane(y[0]), yp_dst); STW(sane(y[1]), yp_dst + 128);
      if (tid == 256) misc[30] = w0;
    }
    STAMPT(1, 6, 256);
    if (!OUTE && grow != (int)key_idx(w0) && grow != (int)key_idx(w1)) OUT_ST();
  }
}

extern "C" void kernel_launch(void* const* d_in, const int* in_sizes, int n_in, void* d_out, int out_size,
                              void* d_ws, size_t ws_size, hipStream_t stream) {
  (void)hipFuncSetAttribute((const void*)fused, hipFuncAttributeMaxDynamicSharedMemorySize, LDS_TOTAL);
  fused<<<256, 512, LDS_TOTAL, stream>>>((const u16*)d_in[0], (const u16*)d_in[1], (const u16*)d_in[2], (const u16*)d_in[4],
                                         (const u16*)d_in[5], (const u16*)d_in[6], (const u16*)d_in[7], (u16*)d_out, (char*)d_ws);
}
```

```cpp
#include <hip/hip_runtime.h>
#include <stdint.h>

#define LAS __attribute__((address_space(3)))
#define GAS __attribute__((address_space(1)))
#define DI __device__ __forceinline__

typedef unsigned short u16;
typedef unsigned long long u64;
typedef __attribute__((ext_vector_type(2))) __bf16 bf16x2;
typedef __attribute__((ext_vector_type(8))) __bf16 bf16x8;
typedef __attribute__((ext_vector_type(4))) short s16x4;
typedef __attribute__((ext_vector_type(2))) unsigned short u16x2;
typedef __attribute__((ext_vector_type(16))) float f32x16;
typedef __attribute__((ext_vector_type(4))) float f32x4;
typedef __attribute__((ext_vector_type(2))) float f32x2;
typedef __attribute__((ext_vector_type(4))) unsigned u32x4;
typedef __attribute__((ext_vector_type(2))) unsigned u32x2;
typedef GAS unsigned gu32;
typedef GAS unsigned long long gu64;

constexpr int S_ = 2048, D_ = 1024, F_ = 3584;

constexpr unsigned SY_CH = 0, SY_GO = 1024, SY_RE = 2048;
constexpr unsigned WS_FD  = 98304;
constexpr unsigned WS_ZF  = 8192;
constexpr unsigned WS_KVF = 16384;
constexpr unsigned WS_RS  = 131072;
constexpr unsigned WS_CS  = WS_RS + 16 * 16 * 1024;
constexpr unsigned WS_ZP  = WS_CS + 16 * 16 * 768;
constexpr unsigned WS_FB  = WS_ZP + 32 * 16 * 128;
constexpr unsigned WS_ST2 = WS_FB + 32 * 32 * 128;
constexpr unsigned WS_XS  = WS_ST2 + 32 * 8 * 1024;
constexpr unsigned WS_GU  = WS_XS + 64 * 2048;
constexpr unsigned WS_YP  = WS_GU + 256 * 1024;
constexpr unsigned WS_KVP = WS_YP + 2 * 8 * 64 * 512;
constexpr unsigned WS_O   = WS_KVP + 32 * 16 * 4096;
constexpr unsigned WS_END = WS_O + S_ * D_ * 2;
constexpr unsigned OOB = 0x80000000u;
constexpr size_t WS_DIAG  = 64u << 20;
#define CNT(i) do {} while (0)
#define STAMPT(kid, i, T) do {} while (0)

#define TSL(idx) do {} while (0)
#define TSDUMP(k) do {} while (0)
template <int V> struct IC { static constexpr int v = V; };
template <int B, int E, class F> DI void sfor(F&& f) { if constexpr (B < E) { f(IC<B>{}); sfor<B + 1, E>(f); } }

DI float bflo(unsigned u) { return __uint_as_float(u << 16); }
DI float bfhi(unsigned u) { return __uint_as_float(u & 0xffff0000u); }
DI float rbf(float x) { return (float)(__bf16)x; }
DI unsigned pack2(float a, float b) { bf16x2 c; c[0] = (__bf16)a; c[1] = (__bf16)b; return __builtin_bit_cast(unsigned, c); }
DI unsigned pack2o(float a, float b) { unsigned p = pack2(a, b); asm("" : "+v"(p)); return p; }
DI f32x16 mfma32(u32x4 a, u32x4 b, f32x16 c) {
  return __builtin_amdgcn_mfma_f32_32x32x16_bf16(__builtin_bit_cast(bf16x8, a), __builtin_bit_cast(bf16x8, b), c, 0, 0, 0);
}
DI f32x4 mfma16(u32x4 a, u32x4 b, f32x4 c) {
  return __builtin_amdgcn_mfma_f32_16x16x32_bf16(__builtin_bit_cast(bf16x8, a), __builtin_bit_cast(bf16x8, b), c, 0, 0, 0);
}
DI void mfma16i(u32x4 a, u32x4 b, f32x4& c) { asm volatile("v_mfma_f32_16x16x32_bf16 %0, %1, %2, %0" : "+v"(c) : "v"(a), "v"(b)); }
DI f32x2 unpk(unsigned u) { return (f32x2){bflo(u), bfhi(u)}; }
DI u32x2 trread(const char* p) {
  s16x4 v = __builtin_amdgcn_ds_read_tr16_b64_v4i16((LAS s16x4*)p);
  return __builtin_bit_cast(u32x2, v);
}
DI const char* uni(const void* p) {
  const u64 v = (u64)p;
  return (const char*)(((u64)(unsigned)__builtin_amdgcn_readfirstlane((int)(v >> 32)) << 32) | (unsigned)__builtin_amdgcn_readfirstlane((int)v));
}
#define RV4(a, b, c, d) "v" #a, "v" #b, "v" #c, "v" #d
#define RV10(p) "v" #p "0", "v" #p "1", "v" #p "2", "v" #p "3", "v" #p "4", "v" #p "5", "v" #p "6", "v" #p "7", "v" #p "8", "v" #p "9"
#define RSV2 RV4(224, 225, 226, 227), "v228", "v229", RV10(23), RV10(24), RV4(250, 251, 252, 253), "v254", "v255"
#define RSV1 RV4(192, 193, 194, 195), RV4(196, 197, 198, 199), RV10(20), RV10(21), RV4(220, 221, 222, 223), RSV2
#define RSV "v148", "v149", RV10(15), RV10(16), RV10(17), RV10(18), "v190", "v191", RSV1
#define NTW " nt"
#define NTA ""
#define NTB ""
#define NTO ""
#define GL_(v, s, o) "global_load_lds_dwordx4 " v ", " s " offset:" #o
#define GLDS4(SC, CL...) asm volatile("s_add_u32 m0, %5, %c6\n\ts_nop 3\n\t" GL_("%0", "%4", 0) SC "\n\t" GL_("%1", "%4", 1024) SC "\n\t" \
  GL_("%2", "%4", 2048) SC "\n\t" GL_("%3", "%4", 3072) SC :: "v"(v0), "v"(v1), "v"(v2), "v"(v3), "s"(uni(sbase)), "s"(lds), "i"(IMM) : "memory", "scc", ##CL)
#define GLDS3(SC, CL...) asm volatile("s_add_u32 m0, %4, %c5\n\ts_nop 3\n\t" GL_("%0", "%3", 0) SC "\n\t" GL_("%1", "%3", 1024) SC "\n\t" \
  GL_("%2", "%3", 2048) SC :: "v"(v0), "v"(v1), "v"(v2), "s"(uni(sbase)), "s"(lds), "i"(IMM) : "memory", "scc", ##CL)
template <int IMM> DI void glds4(unsigned v0, unsigned v1, unsigned v2, unsigned v3, const void* sbase, unsigned lds) { GLDS4(NTO); }
template <int IMM> DI void glds4o(unsigned v0, unsigned v1, unsigned v2, unsigned v3, const void* sbase, unsigned lds) { GLDS4(NTO, RSV); }
template <int IMM> DI void glds4r(unsigned v0, unsigned v1, unsigned v2, unsigned v3, const void* sbase, unsigned lds) { GLDS4(NTA, RSV); }
template <int IMM> DI void glds4n(unsigned v0, unsigned v1, unsigned v2, unsigned v3, const void* sbase, unsigned lds) { GLDS4(" nt", RSV); }
template <int IMM> DI void glds3n(unsigned v0, unsigned v1, unsigned v2, const void* sbase, unsigned lds) { GLDS3(" nt", RSV); }
template <int IMM> DI void glds3r(unsigned v0, unsigned v1, unsigned v2, const void* sbase, unsigned lds) { GLDS3(NTB, RSV); }
template <int R> DI void sload(unsigned voff, const void* sbase) {
  asm volatile("s_nop 4\n\tglobal_load_dwordx4 v[%c0:%c1], %2, %3 sc1" ::"i"(R), "i"(R + 3), "v"(voff), "s"(uni(sbase)) : "memory", RSV);
}
DI float fadd(float a, float b) { float o; asm("v_add_f32 %0, %1, %2" : "=v"(o) : "v"(a), "v"(b)); return o; }
DI float fmul(float a, float b) { float o; asm("v_mul_f32 %0, %1, %2" : "=v"(o) : "v"(a), "v"(b)); return o; }
DI float fsq(float a, float c) { asm("v_fmac_f32 %0, %1, %1" : "+v"(c) : "v"(a)); return c; }
#define RA_(o, a, b) "v_add_f32 %" #o ", v[%c6+" #a "], v[%c6+" #b "]\n\t"
#define RB2_(o, a) "v_add_f32 %" #o ", v[%c6+" #a "], %" #o "\n\t"
#define RM_(o, a, b) "v_max3_u32 %" #o ", v[%c6+" #a "], v[%c6+" #b "], %" #o "\n\t"
template <int R> DI void rsum4(float* a, unsigned& m, unsigned& n) {
  asm volatile(RA_(0, 0, 4) RA_(1, 1, 5) RM_(4, 0, 1) RA_(2, 2, 6) RA_(3, 3, 7) RM_(5, 2, 3) RB2_(0, 8) RM_(4, 4, 5) RB2_(1, 9) RM_(5, 6, 7)
               RB2_(2, 10) RM_(4, 8, 9) RB2_(3, 11) RM_(5, 10, 11) RB2_(0, 12) RB2_(1, 13) RM_(4, 12, 13) RB2_(2, 14) RB2_(3, 15) RM_(5, 14, 15)
               : "=&v"(a[0]), "=&v"(a[1]), "=&v"(a[2]), "=&v"(a[3]), "+v"(m), "+v"(n) : "i"(R) : RSV);
}
template <int R> DI void wload2(unsigned voff, const void* sbase) {
  asm volatile("s_nop 4\n\tglobal_load_dwordx4 v[%c0:%c1], %2, %3" NTW ::"i"(R), "i"(R + 3), "v"(voff), "s"(uni(sbase)) : "memory", RSV2);
}
template <int R> DI void wload(unsigned voff, const void* sbase) {
  asm volatile("s_nop 4\n\tglobal_load_dwordx4 v[%c0:%c1], %2, %3" NTW ::"i"(R), "i"(R + 3), "v"(voff), "s"(uni(sbase)) : "memory", RSV);
}
template <int RA, int RB> DI unsigned wperm(unsigned sel) {
  unsigned o;
  if (RA < 192) asm volatile("v_perm_b32 %0, v%c2, v%c1, %3" : "=v"(o) : "i"(RA), "i"(RB), "s"(sel) : RSV);
  else if (RA < 224) asm volatile("v_perm_b32 %0, v%c2, v%c1, %3" : "=v"(o) : "i"(RA), "i"(RB), "s"(sel) : RSV1);
  else asm volatile("v_perm_b32 %0, v%c2, v%c1, %3" : "=v"(o) : "i"(RA), "i"(RB), "s"(sel) : RSV2);
  return o;
}
template <int R> DI unsigned rget() { unsigned o; asm volatile("v_mov_b32 %0, v%c1" : "=v"(o) : "i"(R) : RSV); return o; }
DI float xh_sum(float v) {
  auto r = __builtin_amdgcn_permlane32_swap(__float_as_uint(v), __float_as_uint(v), false, false);
  return __uint_as_float(r[0]) + __uint_as_float(r[1]);
}
template <int CTRL> DI unsigned dppu(unsigned v) { return (unsigned)__builtin_amdgcn_update_dpp(0, (int)v, CTRL, 0xf, 0xf, true); }
template <int CTRL> DI float dppf(float v) { return __uint_as_float(dppu<CTRL>(__float_as_uint(v))); }
constexpr int DPP_X1 = 0xB1, DPP_X2 = 0x4E, DPP_HMIR = 0x141, DPP_MIR = 0x140, DPP_ROR8 = 0x128;
DI float quad_sum(float v) { v += dppf<DPP_X1>(v); v += dppf<DPP_X2>(v); return v; }
DI float oct_sum(float v) { v = quad_sum(v); v += dppf<DPP_HMIR>(v); return v; }
DI float x16_sum(float v) {
  auto r = __builtin_amdgcn_permlane16_swap(__float_as_uint(v), __float_as_uint(v), false, false);
  return __uint_as_float(r[0]) + __uint_as_float(r[1]);
}
DI float sw16(float a, float b) {
  auto r = __builtin_amdgcn_permlane16_swap(__float_as_uint(a), __float_as_uint(b), false, false);
  return fadd(__uint_as_float(r[0]), __uint_as_float(r[1]));
}
DI float sw32(float a, float b) {
  auto r = __builtin_amdgcn_permlane32_swap(__float_as_uint(a), __float_as_uint(b), false, false);
  return fadd(__uint_as_float(r[0]), __uint_as_float(r[1]));
}
DI float hi_sum(float v) {
  auto r = __builtin_amdgcn_permlane16_swap(__float_as_uint(v), __float_as_uint(v), false, false);
  v = fadd(__uint_as_float(r[0]), __uint_as_float(r[1]));
  auto q = __builtin_amdgcn_permlane32_swap(__float_as_uint(v), __float_as_uint(v), false, false);
  return fadd(__uint_as_float(q[0]), __uint_as_float(q[1]));
}
template <int N> struct Phi { unsigned tb[N]; f32x2 tr[N], e[N]; };
template <int N> DI void phi_a(const f32x2* t, Phi<N>& s) {
#pragma unroll
  for (int i = 0; i < N; ++i) s.tb[i] = pack2o(t[i][0], t[i][1]);
  __builtin_amdgcn_sched_barrier(0);
#pragma unroll
  for (int i = 0; i < N; ++i) s.tr[i] = unpk(s.tb[i]);
  __builtin_amdgcn_sched_barrier(0);
#pragma unroll
  for (int i = 0; i < N; ++i) s.e[i] = s.tr[i] * 1.44269504088896340736f;
  __builtin_amdgcn_sched_barrier(0);
}
template <int N> DI void phi_b(Phi<N>& s, unsigned* p) {
  u16x2 tm[2] = {{0, 0}, {0, 0}};
#pragma unroll
  for (int i = 0; i < N; ++i) {
    s.e[i][0] = __builtin_amdgcn_fmed3f(__builtin_amdgcn_exp2f(s.e[i][0]), 0.f, 1.f);
    tm[i & 1] = __builtin_elementwise_max(tm[i & 1], __builtin_bit_cast(u16x2, s.tb[i]));
    s.e[i][1] = __builtin_amdgcn_fmed3f(__builtin_amdgcn_exp2f(s.e[i][1]), 0.f, 1.f);
    __builtin_amdgcn_sched_barrier(0);
  }
#pragma unroll
  for (int i = 0; i < N; ++i) s.tr[i] += s.e[i];
  __builtin_amdgcn_sched_barrier(0);
#pragma unroll
  for (int i = 0; i < N; ++i) { s.tr[i][0] = fmaxf(s.tr[i][0], s.e[i][0]); s.tr[i][1] = fmaxf(s.tr[i][1], s.e[i][1]); }
  const u16x2 tx = __builtin_elementwise_max(tm[0], tm[1]);
  __builtin_amdgcn_sched_barrier(0);
#pragma unroll
  for (int i = 0; i < N; ++i) p[i] = pack2(s.tr[i][0], s.tr[i][1]);
  if (__any(tx[0] > 0xC0F0 || tx[1] > 0xC0F0)) {
#pragma unroll
    for (int i = 0; i < N; ++i) p[i] = pack2(bflo(p[i]) + 1e-6f, bfhi(p[i]) + 1e-6f);
  }
}
template <int N> DI void phi_n(const f32x2* t, unsigned* p) { Phi<N> s; phi_a<N>(t, s); phi_b<N>(s, p); }
#define RLX_AGENT __ATOMIC_RELAXED, __HIP_MEMORY_SCOPE_AGENT
DI void st32(char* ws, unsigned off, unsigned v) { __hip_atomic_store((gu32*)(ws + off), v, RLX_AGENT); }
DI void st64(char* ws, unsigned off, u64 v) { __hip_atomic_store((gu64*)(ws + off), v, RLX_AGENT); }
DI unsigned ld32(char* ws, unsigned off) { return __hip_atomic_load((gu32*)(ws + off), RLX_AGENT); }
DI u64 ld64(char* ws, unsigned off) { return __hip_atomic_load((gu64*)(ws + off), RLX_AGENT); }
#define WAIT_V(n) asm volatile("s_waitcnt vmcnt(%c0)" ::"i"(n) : "memory")
#define RAW_BAR() do { asm volatile("s_waitcnt lgkmcnt(0)" ::: "memory"); __builtin_amdgcn_s_barrier(); asm volatile("" ::: "memory"); } while (0)
#define SB0() __builtin_amdgcn_sched_barrier(0)
#define WAIT_VR(n) asm volatile("s_waitcnt vmcnt(%c0)" ::"i"(n) : "memory", RSV)
#define RAW_BARR() do { asm volatile("s_waitcnt lgkmcnt(0)" ::: "memory", RSV); __builtin_amdgcn_s_barrier(); asm volatile("" ::: "memory", RSV); } while (0)
#define KEEP2() asm volatile("" ::: "memory", RSV2)
#define RAW_BAR2() do { asm volatile("s_waitcnt lgkmcnt(0)" ::: "memory", RSV2); __builtin_amdgcn_s_barrier(); KEEP2(); } while (0)
constexpr unsigned SPIN_CAP = 1u << 18;
#define SPIN _Pragma("nounroll") for (unsigned s_ = 0; s_ < SPIN_CAP; ++s_)
constexpr unsigned SENT = 0xFFFFFFFFu;
DI unsigned sane1(unsigned v) { return min(v, 0xFFFFFFFEu); }
DI u32x4 sane(u32x4 v) { v[1] = sane1(v[1]); v[3] = sane1(v[3]); return v; }
DI unsigned chk(unsigned m, u32x4 v) { return max(max(m, v[1]), v[3]); }
DI bool wave_ok(unsigned m) { return !__any(m == SENT); }
DI unsigned key_idx(unsigned k) { return (0xFFFFu - (k & 0xFFFFu)) & (S_ - 1); }
DI float key_val(unsigned k) { const unsigned o = k >> 16; return __uint_as_float(((o & 0x8000u) ? (o & 0x7fffu) : (~o & 0xffffu)) << 16); }
DI void top2_mix(unsigned& b0, unsigned& b1, unsigned c0, unsigned c1) {
  const unsigned lo = min(b0, c0), mx = max(b1, c1);
  b0 = max(b0, c0); b1 = max(lo, mx);
}
DI void top2_lo(unsigned& b0, unsigned& b1) {
  top2_mix(b0, b1, dppu<DPP_X1>(b0), dppu<DPP_X1>(b1));
  top2_mix(b0, b1, dppu<DPP_X2>(b0), dppu<DPP_X2>(b1));
  top2_mix(b0, b1, dppu<DPP_HMIR>(b0), dppu<DPP_HMIR>(b1));
}
DI void top2_mid(unsigned& b0, unsigned& b1) {
  top2_mix(b0, b1, dppu<DPP_MIR>(b0), dppu<DPP_MIR>(b1));
  auto p = __builtin_amdgcn_permlane16_swap(b0, b0, false, false); auto q = __builtin_amdgcn_permlane16_swap(b1, b1, false, false);
  b0 = p[0]; b1 = q[0]; top2_mix(b0, b1, p[1], q[1]);
}
DI void top2_hi(unsigned& b0, unsigned& b1) {
  top2_mid(b0, b1);
  auto p = __builtin_amdgcn_permlane32_swap(b0, b0, false, false); auto q = __builtin_amdgcn_permlane32_swap(b1, b1, false, false);
  b0 = p[0]; b1 = q[0]; top2_mix(b0, b1, p[1], q[1]);
}

constexpr int K1_STAGE = 40960;
constexpr int LDS_TOTAL = 4 * K1_STAGE;
constexpr int L_STM = 0;
constexpr int L_MISC = 2048;
constexpr int L_XK = 5120;
constexpr int L_KX = 3328;
constexpr int L_ACT = 2304;
constexpr int L_CAND = 4096;
constexpr int L_WO = 16384;
constexpr int L_P = 16384, L_PK = 7 * 16384;
constexpr int L_RED = 65536;
constexpr int wo_slot(int j) { return L_WO + (j % 9) * 16384; }
constexpr int CODE_LINES = 12;
#define OW_ 8
constexpr int OW = OW_;
#define W1A_ 16
constexpr int W1A = W1A_;
#define PRIO_M 1
#define PRIO_S 3
#define GSCHED 5, 9, 13, 17, 21, 24, 27, 30, 33, 36, 39, 42, 45, 48, 48
constexpr int GS[15] = {GSCHED};
constexpr int gops(int g) { return (g / 3) * 10 + (g % 3 == 0 ? 0 : g % 3 == 1 ? 4 : 7); }
constexpr bool gs_ok() {
  bool ok = GS[0] >= 3 && GS[1] >= 6 && GS[1] <= 12 && GS[0] <= GS[1] && GS[12] == 45 && GS[13] == 48 && GS[14] == 48;
  for (int t = 0; t < 12; ++t) ok = ok && GS[t + 2] <= 3 * (t + 5) && GS[t + 2] >= 3 * (t + 3) && GS[t + 2] >= GS[t + 1] + 3;
  return ok;
}
static_assert(gs_ok(), "staging schedule");
#define WSCHED 0, 0, 0, 0, 0, 0, 0, 0, 0, 2, 4, 6, 8
constexpr int WS_[13] = {WSCHED};
#define PACE 1
#define PSLP 0
#define P_KVP 1
#define L_KVP 0
#define P_KVF 1
#define L_KVF 0
#define P_FB 8
#define L_FB 0
#define P_C 14
#define P_D 2
#define L_D 0
#define P_GU 12
#define P_E 10
#define L_E 0
#define SLEEP(n) do { if (n) __builtin_amdgcn_s_sleep(n); } while (0)
#define OUTE 1
#define P2MAP 0
#define TCH0 100
#define OUTWT 0
#define OUTWB 0
#define PFO 1
#define WOD 100
#define WODF 6
#define WODS 0
#define QPOS 1
#define LASTB 14
#define CH0 4
#define CHN 3
constexpr int VW = 160;

__global__ __launch_bounds__(512, 2) void fused(const u16* __restrict__ x, const u16* __restrict__ wqkv,
                                                const u16* __restrict__ wo, const u16* __restrict__ w_r,
                                                const u16* __restrict__ wg, const u16* __restrict__ wu,
                                                const u16* __restrict__ wd, u16* __restrict__ out, char* __restrict__ ws) {
  extern __shared__ __attribute__((aligned(16))) char smem[];
  STAMPT(0, 0, 0);
  u64 pc0; asm volatile("s_getpc_b64 %0" : "=s"(pc0));
  const int wave = __builtin_amdgcn_readfirstlane(threadIdx.x >> 6);
  const unsigned lds0 = (unsigned)(size_t)smem;
#define RB_(b) (((b) & 1) * 8 + (((b) >> 3) & 7))
#define HP_(b) ((((b) & 7) >> 1) * 4 + ((b) >> 6))
#define KROT_(b) ((4 * ((HP_(b) >> 2) + (RB_(b) >> 3) + 2 * ((RB_(b) >> 2) & 1))) & 15)
  unsigned voA0 = 0, voA1 = 0, voA2 = 0, voA3 = 0, voB0 = 0, voB1 = 0, voB2 = 0, voB3 = 0, voB4 = 0, voB5 = 0;
  const unsigned l4 = __builtin_amdgcn_readfirstlane(lds0 + (wave - 4) * 4096), l6 = __builtin_amdgcn_readfirstlane(lds0 + (wave - 4) * 6144);
  const int krot0 = KROT_(blockIdx.x);
  auto group = [&](auto G) {
    constexpr int t = G.v / 3, p = G.v % 3, sl = (t & 3) * K1_STAGE;
    const int kt = (t + krot0) & 15;
    if (p == 0) glds4r<sl>(voA0, voA1, voA2, voA3, (const char*)x + kt * 128, l4);
    else if (p == 1) glds3r<sl + 16384>(voB0, voB1, voB2, (const char*)wqkv + kt * 64 * (3 * D_ * 2), l6);
    else glds3r<sl + 16384 + 3072>(voB3, voB4, voB5, (const char*)wqkv + kt * 64 * (3 * D_ * 2), l6);
  };
  if (wave >= 4) {
    const int lane = threadIdx.x & 63, sw = wave - 4;
    const int r0 = RB_(blockIdx.x) * 128 + 32 * sw + (lane >> 3);
    voA0 = (unsigned)(r0 * D_ + ((lane & 7) ^ (lane >> 4)) * 8) * 2u;
    voA1 = ((voA0 + 8 * D_ * 2) ^ 64) - 1024; voA2 = voA0 + 16 * D_ * 2 - 2048; voA3 = voA1 + 16 * D_ * 2 - 2048;
    voB0 = (unsigned)((16 * sw + 4 * (lane >> 5) + ((lane >> 2) & 3)) * (3 * D_) + HP_(blockIdx.x) * 64 + ((lane >> 4) & 1) * 32 + (lane & 3) * 8) * 2u;
    voB1 = voB0 + 1024; voB2 = voB0 + 2048;
    voB3 = (voB0 ^ 32) + 8 * 3 * D_ * 2; voB4 = voB3 + 1024; voB5 = voB3 + 2048;
    asm volatile("" : "+v"(voA0), "+v"(voA1), "+v"(voA2), "+v"(voA3), "+v"(voB0), "+v"(voB1), "+v"(voB2), "+v"(voB3), "+v"(voB4), "+v"(voB5));
    sfor<0, 3>(group);
  }
  int bid = blockIdx.x; asm volatile("" : "+s"(bid));
  const int xcd = bid & 7, idx = bid >> 3;
  const int rb = RB_(bid), hp = HP_(bid);
  const int rp = xcd * 4 + (idx >> 3), cb = idx & 7;
#define WROT (4 * rp + (xcd >> 1))
#define WDIR ((xcd & 1) ? -1 : 1)
  const int hs_ = (hp + 4 + (int)(((size_t)wqkv >> 7) & 7)) & 7;
  const int g8 = ((hp >> 3) * 4 + (rb >> 2)) * 8 + hs_, kq = rb & 3;
  const int krot = KROT_(bid), posA = 1 + (hp & 3), posB = 1 + (rb & 3);
  const int wrot = WROT, wdir = WDIR;
  auto whead = [&](int t) { return (wrot + wdir * t) & 15; };
  const unsigned rs_slot = WS_RS + (rb * 16 + ((krot + posA) & 15)) * 1024, cs_slot = WS_CS + (hp * 16 + ((krot + posB) & 15)) * 768;
  const __amdgpu_buffer_rsrc_t wsr = __builtin_amdgcn_make_buffer_rsrc(ws, 0, WS_END, 0x00020000);
#define LDW(off) __builtin_amdgcn_raw_buffer_load_b128(wsr, (off), 0, 16)
#define STW(v, off) __builtin_amdgcn_raw_buffer_store_b128((v), wsr, (off), 0, 16)
#define STW1(v, off) __builtin_amdgcn_raw_buffer_store_b32((v), wsr, (off), 0, 16)
  float* stm = (float*)(smem + L_STM);
  volatile LAS int* misc = (volatile LAS int*)(smem + L_MISC);
#define LANES() int tid = threadIdx.x; asm volatile("" : "+v"(tid)); const int lane = tid & 63, r = lane & 31, h = lane >> 5; (void)r; (void)h
#define EPI() const int erow = tid >> 3, eseg = tid & 7; const size_t ego = (size_t)(rp * 64 + erow) * D_ + cb * 128 + eseg * 8
  u32x4 xv[2], wv[2];
  f32x2 xf[8], wf2[8];
  float sw_;
  auto unpack_xw = [&]() {
    f32x2 a = {0.f, 0.f};
#pragma unroll
    for (int i = 0; i < 8; ++i) { xf[i] = unpk(xv[i >> 2][i & 3]); wf2[i] = unpk(wv[i >> 2][i & 3]); a += wf2[i]; }
    sw_ = oct_sum(a[0] + a[1]);
#pragma unroll
    for (int i = 0; i < 8; ++i) asm volatile("" : "+v"(xf[i]), "+v"(wf2[i]));
    asm volatile("" : "+v"(sw_));
  };
  unsigned tsv = 0; (void)tsv;
  u32x4 wf[24];

  auto wtr = [&](auto I) {
    constexpr int m = I.v >> 3, c = I.v & 7, R = VW + 32 * m + (c >> 1);
    const unsigned sel = (c & 1) ? 0x07060302u : 0x05040100u;
    wf[I.v] = (u32x4){wperm<R, R + 4>(sel), wperm<R + 8, R + 12>(sel), wperm<R + 16, R + 20>(sel), wperm<R + 24, R + 28>(sel)};
  };

  if (wave < 4) {
    LANES();
    const int c16 = lane & 15, g4 = lane >> 4;
    const int trl = (c16 >> 2) * 64 + (c16 & 3) * 8;
    const int wr = wave >> 1, wc = wave & 1;
    const int head = 2 * hp + wc;
    const unsigned kvp_off = WS_KVP + (head * 16 + rb) * 4096 + wr * 2048 + lane * 16;
    const unsigned zp_off = WS_ZP + (head * 16 + rb) * 128;
    const unsigned o_off = WS_O + ((rb * 4 + 2 * wr) * 32 + head) * 2048 + lane * 16;
    const unsigned fb_off = lane < 8 ? WS_FB + ((rb * 2 + wr) * 32 + head) * 128 + lane * 16 : OOB;
    unsigned pfa, pfb = OOB;
    unsigned pfc = lane < 8 ? fb_off : lane < 24 ? rs_slot + wave * 256 + (lane - 8) * 16
        : lane < 40 ? (wave < 3 ? cs_slot + wave * 256 : WS_KVF + hp * 4096 + rb * 256) + (lane - 24) * 16
        : lane < 48 ? (wave < 2 ? zp_off + (lane & 7) * 16 : (wave == 2 && rb < 2) ? WS_ZF + (2 * hp + rb) * 128 + (lane & 7) * 16 : OOB) : OOB;
    if (wave == 0) pfa = WS_ST2 + (rp * 8 + cb) * 1024 + lane * 16;
    else if (wave == 1) pfa = WS_GU + (g8 * 4 + kq) * 1024 + lane * 16;
    else pfa = WS_YP + (((wave - 2) * 8 + 2 * kq + (lane >> 5)) * 64 + g8) * 512 + (lane & 31) * 16;
    if (wave == 0) pfb = lane < 32 ? WS_XS + (rp * 2 + (lane >> 4)) * 2048 + cb * 256 + (lane & 15) * 16 : lane < 40 ? WS_FD + (cb * 32 + rp) * 128 + (lane & 7) * 16 : OOB;
    u32x4 S4 = {SENT, SENT, SENT, SENT};
    asm volatile("" : "+v"(pfa), "+v"(pfb), "+v"(pfc), "+v"(S4));
    __builtin_amdgcn_s_setprio(PRIO_M);
    f32x4 acc[4][6];
    int ao[2][2], bo[2][2];
    {
#pragma unroll
      for (int i = 0; i < 2; ++i) {
        ao[0][i] = (64 * wr + c16) * 128 + (((4 * i + g4) ^ (c16 >> 1)) * 16); ao[1][i] = ao[0][i] + 2 * K1_STAGE;
        bo[0][i] = 16384 + g4 * 3072 + wc * 256 + trl + ((i ^ (g4 & 1)) * 32); bo[1][i] = bo[0][i] + 2 * K1_STAGE;
        asm volatile("" : "+v"(ao[0][i]), "+v"(ao[1][i]), "+v"(bo[0][i]), "+v"(bo[1][i]));
      }
#pragma unroll
      for (int i = 0; i < 4; ++i)
#pragma unroll
        for (int j = 0; j < 6; ++j) { acc[i][j] = (f32x4){0.f, 0.f, 0.f, 0.f}; asm volatile("" : "+v"(acc[i][j])); }
      struct Frag { u32x4 a[4]; u32x2 bl[6], bh[6]; };
      Frag FA, FB;
#define K1_B(j) ((u32x4){C.bl[j][0], C.bl[j][1], C.bh[j][0], C.bh[j][1]})
#define K1_LA(i) N.a[i] = *(const u32x4*)(smem + ao[sn >> 1][hf] + (sn & 1) * K1_STAGE + (i) * 2048)
#define K1_LL(j) N.bl[j] = trread(smem + bo[sn >> 1][(j) & 1] + (sn & 1) * K1_STAGE + hf * 12288 + ((j) >> 1) * 1024)
#define K1_LH(j) N.bh[j] = trread(smem + bo[sn >> 1][(j) & 1] + (sn & 1) * K1_STAGE + hf * 12288 + ((j) >> 1) * 1024 + 512)
#define K1_M(i, j) if ((j) < 2) mfma16i(K1_B(j), C.a[i], acc[i][j]); else mfma16i(C.a[i], K1_B(j), acc[i][j]); SB0()
#define K1_Q(i, j, ld) K1_M(i, j); ld; SB0()
      auto sub = [&](Frag& C, Frag& N, int sn, int hf) {
        K1_Q(0, 0, K1_LL(0)); K1_Q(1, 0, K1_LH(0)); K1_Q(0, 1, K1_LA(0)); K1_Q(1, 1, K1_LA(1)); K1_Q(0, 2, K1_LL(1)); K1_Q(1, 2, K1_LH(1));
        K1_Q(0, 3, K1_LL(2)); K1_Q(1, 3, K1_LH(2)); K1_Q(2, 0, K1_LL(3)); K1_Q(3, 0, K1_LH(3)); K1_Q(2, 1, K1_LA(2)); K1_Q(3, 1, K1_LA(3));
        K1_Q(2, 2, K1_LL(4)); K1_Q(3, 2, K1_LH(4)); K1_Q(2, 3, K1_LL(5)); K1_Q(3, 3, K1_LH(5));
        K1_M(0, 4); K1_M(1, 4); K1_M(2, 4); K1_M(3, 4); K1_M(0, 5); K1_M(1, 5); K1_M(2, 5);
        asm volatile("s_waitcnt lgkmcnt(0)" : "+v"(N.bh[5]));
        K1_M(3, 5);
      };
      auto subkv = [&](Frag& C, Frag& N, int sn, int hf, bool ld) {
#define K1_QL(i, j, l) K1_M(i, j); if (ld) { l; } SB0()
        K1_QL(0, 2, K1_LL(2)); K1_QL(1, 2, K1_LH(2)); K1_QL(0, 3, K1_LA(0)); K1_QL(1, 3, K1_LA(1)); K1_QL(2, 2, K1_LL(3)); K1_QL(3, 2, K1_LH(3));
        K1_QL(2, 3, K1_LA(2)); K1_QL(3, 3, K1_LA(3)); K1_QL(0, 4, K1_LL(4)); K1_QL(1, 4, K1_LH(4)); K1_QL(2, 4, K1_LL(5)); K1_QL(3, 4, K1_LH(5));
        K1_M(0, 5); K1_M(1, 5); K1_M(2, 5); K1_M(3, 5);
      };
      RAW_BAR();
      STAMPT(0, 1, 0);
      {
        Frag& N = FA; const int sn = 0, hf = 0;
        K1_LL(0); K1_LH(0); K1_LA(0); K1_LA(1); K1_LL(1); K1_LH(1); K1_LL(2); K1_LH(2); K1_LL(3); K1_LH(3); K1_LA(2); K1_LA(3); K1_LL(4); K1_LH(4); K1_LL(5); K1_LH(5);
      }
#pragma unroll 1
      for (int it = 0; it < 4; ++it) {
#pragma unroll
        for (int s = 0; s < 4; ++s) {
          if (s == 2 && it == 3) break;
          sub(FA, FB, s, 1);
          if (s == 0 || (PFO == 0 && s == 3)) WAIT_V(0);
          if (it == 0) { if (s == PFO) STW(S4, pfc); if (s == PFO + 1) STW(S4, kvp_off); if (s == PFO + 2) STW(S4, kvp_off + 1024); }
          if (it == 1) { if (s == 0) STW(S4, pfa); if (s == 1) STW(S4, pfb); }
          TSL(it * 4 + s);
          if (LASTB == 13 || s != 1 || it < 3) RAW_BAR();
          sub(FB, FA, (s + 1) & 3, 0);
        }
      }
      subkv(FA, FB, 2, 1, true);
      TSL(14);
      if (LASTB == 14) RAW_BAR();
      subkv(FB, FA, 3, 0, true);
      subkv(FA, FB, 3, 1, true);
      TSL(15);
      subkv(FB, FA, 0, 0, false);
    }
    asm volatile("s_nop 15" ::: "memory");
    TSL(16); if (wave == 0) TSDUMP(0);
    STAMPT(0, 2, 0);
    {
      f32x4 m4[4], i4[4]; float cs[4];
      SPIN {
        const int ok = misc[12 + (lane & 3)];
        asm volatile("" ::: "memory");
#pragma unroll
        for (int i = 0; i < 4; ++i) {
          cs[i] = stm[256 + wc * 96 + 32 + 16 * i + c16];
          m4[i] = *(const f32x4*)(stm + 64 * wr + 16 * i + 4 * g4); i4[i] = *(const f32x4*)(stm + 128 + 64 * wr + 16 * i + 4 * g4);
        }
        SB0();
        if (__all(ok != 0)) break;
      }
      const u32x4 ONES4 = {0x3F803F80u, 0x3F803F80u, 0x3F803F80u, 0x3F803F80u};
      f32x2 tk[16], tv[16];
#define PR(v, i) ((f32x2){(v)[2 * (i)], (v)[2 * (i) + 1]})
#pragma unroll
      for (int i = 0; i < 16; ++i) {
        tk[i] = PR(acc[(i >> 1) & 3][2 + (i >> 3)], i & 1) - PR(m4[(i >> 1) & 3], i & 1) * cs[i >> 3];
        tv[i] = PR(acc[(i >> 1) & 3][4 + (i >> 3)], i & 1) - PR(m4[(i >> 1) & 3], i & 1) * cs[2 + (i >> 3)];
      }
      SB0();
#pragma unroll
      for (int i = 0; i < 16; ++i) { tk[i] *= PR(i4[(i >> 1) & 3], i & 1); tv[i] *= PR(i4[(i >> 1) & 3], i & 1); }
      SB0();
      unsigned kp[16], vp[16];
#pragma unroll
      for (int i = 0; i < 16; ++i) vp[i] = pack2o(tv[i][0], tv[i][1]);
      phi_n<16>(tk, kp);
#define F4_(p, i) ((u32x4){(p)[4 * (i)], (p)[4 * (i) + 1], (p)[4 * (i) + 2], (p)[4 * (i) + 3]})
      const f32x4 Z4 = {0.f, 0.f, 0.f, 0.f};
      u32x4 vk[2], vg[2];
#pragma unroll
      for (int j = 0; j < 2; ++j) { vk[j] = wr ? F4_(vp, 2 + j) : F4_(vp, j); vg[j] = wr ? F4_(vp, j) : F4_(vp, 2 + j); }
      f32x4 keep[2], give[2], zq[2];
#pragma unroll
      for (int d = 0; d < 2; ++d) { give[d] = mfma16(F4_(kp, 2 * d), vg[0], Z4); zq[d] = mfma16(ONES4, F4_(kp, 2 * d), Z4); }
#pragma unroll
      for (int d = 0; d < 2; ++d) { give[d] = mfma16(F4_(kp, 2 * d + 1), vg[1], give[d]); zq[d] = mfma16(ONES4, F4_(kp, 2 * d + 1), zq[d]); }
#pragma unroll
      for (int d = 0; d < 2; ++d) keep[d] = mfma16(F4_(kp, 2 * d), vk[0], Z4);
#pragma unroll
      for (int d = 0; d < 2; ++d) keep[d] = mfma16(F4_(kp, 2 * d + 1), vk[1], keep[d]);
      SB0();
      const float zv = (lane & 16) ? zq[1][0] : zq[0][0];
      char* xk = smem + L_XK + wave * 2304 + lane * 16;
      const char* xp = smem + L_XK + (wave ^ 2) * 2304 + lane * 16;
      *(f32x4*)xk = give[0]; *(f32x4*)(xk + 1024) = give[1]; *(float*)(xk + 2048 - lane * 12) = zv;
      asm volatile("" ::: "memory");
      misc[16 + wave] = 1;
      f32x4 p0, p1; float pz;
      SPIN {
        const int ok = misc[16 + (wave ^ 2)];
        asm volatile("" ::: "memory");
        p0 = *(const f32x4*)xp; p1 = *(const f32x4*)(xp + 1024); pz = *(const float*)(xp + 2048 - lane * 12);
        SB0();
        if (ok == 1) break;
      }
      STW(sane(__builtin_bit_cast(u32x4, keep[0] + p0)), kvp_off);
      STW(sane(__builtin_bit_cast(u32x4, keep[1] + p1)), kvp_off + 1024);
      STW1(sane1(__float_as_uint(zv + pz)), (lane < 32 && wr == 0) ? zp_off + lane * 4 : OOB);
    }
    STAMPT(0, 3, 0);
    {
      u32x4 qa[4][4]; u32x2 ql[4][2], qh[4][2];
#pragma unroll
      for (int u = 0; u < 4; ++u) {
#pragma unroll
        for (int j = 0; j < 2; ++j) {
          ql[u][j] = trread(smem + bo[1][j] + (u >> 1) * K1_STAGE + (u & 1) * 12288);
          qh[u][j] = trread(smem + bo[1][j] + (u >> 1) * K1_STAGE + (u & 1) * 12288 + 512);
        }
#pragma unroll
        for (int i = 0; i < 4; ++i) qa[u][i] = *(const u32x4*)(smem + ao[1][u & 1] + (u >> 1) * K1_STAGE + i * 2048);
      }
      asm volatile("" ::: "memory");
      misc[8 + wave] = 3;
      SB0();
#pragma unroll
      for (int u = 0; u < 4; ++u)
#pragma unroll
        for (int j = 0; j < 2; ++j)
#pragma unroll
          for (int i = 0; i < 4; ++i) acc[i][j] = mfma16((u32x4){ql[u][j][0], ql[u][j][1], qh[u][j][0], qh[u][j][1]}, qa[u][i], acc[i][j]);
      SB0();
    }
    unsigned qp[16];
    Phi<16> qs;
    const unsigned po = WS_KVP + ((2 * hp + (rb >> 3)) * 16 + h) * 4096 + (2 * ((rb >> 2) & 1) + ((lane >> 4) & 1)) * 1024 + (16 * (rb & 3) + (lane & 15)) * 16;
    u32x4 pv[8] = {};
    auto qepi_a = [&]() {
      f32x4 c4[2]; float m_b[4], rinv[4];
#pragma unroll
      for (int i = 0; i < 2; ++i) c4[i] = *(const f32x4*)(stm + 256 + wc * 96 + 16 * i + 4 * g4);
#pragma unroll
      for (int i = 0; i < 4; ++i) { m_b[i] = stm[64 * wr + 16 * i + c16]; rinv[i] = stm[128 + 64 * wr + 16 * i + c16]; }
      SB0();
      f32x2 tq[16];
#pragma unroll
      for (int i = 0; i < 16; ++i) tq[i] = PR(acc[i >> 2][(i >> 1) & 1], i & 1) - PR(c4[(i >> 1) & 1], i & 1) * m_b[i >> 2];
      SB0();
#pragma unroll
      for (int i = 0; i < 16; ++i) tq[i] *= rinv[i >> 2];
      SB0();
      phi_a<16>(tq, qs);
#pragma unroll
      for (int i = 0; i < 16; ++i) asm volatile("" : "+v"(qs.tb[i]), "+v"(qs.tr[i]), "+v"(qs.e[i]));
    };
    if (QPOS) qepi_a();
    if (wave == 0) {
      SLEEP(P_KVP);
#pragma unroll
      for (int p = 0; p < 8; ++p) pv[p] = LDW(po + p * 8192);
    }
    asm volatile("" ::: "memory");
    if (!QPOS) qepi_a();
    STAMPT(0, 4, 0);
    if (wave == 1 && rb < 2) {
      SLEEP(P_KVP);
      SPIN {
        unsigned m = 0;
#pragma unroll
        for (int p = 0; p < 2; ++p) { pv[p] = LDW(WS_ZP + (2 * hp + rb) * 2048 + p * 1024 + lane * 16); m = chk(max(max(m, pv[p][0]), pv[p][2]), pv[p]); }
        if (wave_ok(m)) break;
        asm volatile("" ::: "memory");
      }
      f32x4 s = __builtin_bit_cast(f32x4, pv[0]) + __builtin_bit_cast(f32x4, pv[1]);
#pragma unroll
      for (int i = 0; i < 4; ++i) { s[i] += dppf<DPP_ROR8>(s[i]); s[i] = xh_sum(x16_sum(s[i])); }
      if (lane < 16) st64(ws, WS_ZF + (2 * hp + rb) * 128 + (lane >> 3) * 64 + (lane & 3) * 16 + ((lane >> 2) & 1) * 8,
                          ((u64)sane1(pack2(s[2], s[3])) << 32) | pack2(s[0], s[1]));
    }
    if (wave == 0) {
      SPIN {
        unsigned m = 0;
#pragma unroll
        for (int p = 0; p < 8; ++p) m = chk(m, pv[p]);
        CNT(0);
        if (wave_ok(m)) break;
        asm volatile("" ::: "memory");
        SLEEP(L_KVP);
#pragma unroll
        for (int p = 0; p < 8; ++p) pv[p] = LDW(po + p * 8192);
      }
      f32x4 s = __builtin_bit_cast(f32x4, pv[0]);
#pragma unroll
      for (int p = 1; p < 8; ++p) s += __builtin_bit_cast(f32x4, pv[p]);
      const unsigned p0 = pack2(xh_sum(s[0]), xh_sum(s[1])), p1 = pack2(xh_sum(s[2]), xh_sum(s[3]));
      auto a = __builtin_amdgcn_permlane16_swap(p0, p0, false, false);
      auto b = __builtin_amdgcn_permlane16_swap(p1, p1, false, false);
      STW(sane((u32x4){a[0], b[0], a[1], b[1]}), lane < 16 ? WS_KVF + hp * 4096 + rb * 256 + lane * 16 : OOB);
      STAMPT(1, 2, 0);
    }
    phi_b<16>(qs, qp);
#pragma unroll
    for (int i = 0; i < 16; ++i) asm volatile("" : "+v"(qp[i]));
    if (wave == 0) misc[7] = 1;
    else SPIN { if (misc[7] == 1) break; __builtin_amdgcn_s_sleep(1); }
    SLEEP(P_KVF);
    {
      u32x4 k0, k1, z0;
      SPIN {
        k0 = LDW(WS_KVF + head * 2048 + lane * 16); k1 = LDW(WS_KVF + head * 2048 + 1024 + lane * 16);
        z0 = LDW(WS_ZF + head * 128 + g4 * 16);
        CNT(1);
        if (wave_ok(chk(chk(chk(0u, k0), k1), z0))) break;
        asm volatile("" ::: "memory");
        SLEEP(L_KVF);
      }
      STAMPT(0, 5, 0);
      const f32x4 Z4 = {0.f, 0.f, 0.f, 0.f};
      f32x4 X[8], Dn[4];
#pragma unroll
      for (int i = 0; i < 4; ++i) Dn[i] = mfma16(z0, F4_(qp, i), Z4);
#pragma unroll
      for (int i = 0; i < 8; ++i) X[i] = mfma16((i & 1) ? k1 : k0, F4_(qp, i >> 1), Z4);
      SB0();
      float ri[4]; unsigned a[16]; f32x2 f[16];
#pragma unroll
      for (int i = 0; i < 4; ++i) ri[i] = __builtin_amdgcn_rcpf(rbf(rbf(Dn[i][0]) + 1e-6f));
      SB0();
#pragma unroll
      for (int i = 0; i < 16; ++i) a[i] = pack2o(X[i >> 1][2 * (i & 1)], X[i >> 1][2 * (i & 1) + 1]);
      SB0();
#pragma unroll
      for (int i = 0; i < 16; ++i) f[i] = unpk(a[i]);
      SB0();
#pragma unroll
      for (int i = 0; i < 16; ++i) f[i] *= ri[i >> 2];
      SB0();
#pragma unroll
      for (int i = 0; i < 4; ++i) {
        STW(((u32x4){pack2(f[4 * i][0], f[4 * i][1]), pack2(f[4 * i + 1][0], f[4 * i + 1][1]), pack2(f[4 * i + 2][0], f[4 * i + 2][1]), pack2(f[4 * i + 3][0], f[4 * i + 3][1])}),
            o_off + (i >> 1) * 65536 + (i & 1) * 1024);
        SB0();
      }
    }
    if (wave == 0) misc[6] = 1;
    STAMPT(0, 6, 0);
    {
      EPI();
      const int rt = wave & 1, kh = wave >> 1;
      const unsigned ob = WS_O + ((rp * 2 + rt) * 32 + 16 * kh) * 2048 + lane * 16;
#define LDO(t, st) __builtin_amdgcn_raw_buffer_load_b128(wsr, ob + (st) * 1024, whead(t) * 2048, 16)
      int woff[2];
#pragma unroll
      for (int i = 0; i < 2; ++i) woff[i] = kh * 8192 + (g4 >> 1) * 2048 + (g4 & 1) * 512 + trl + ((i ^ (g4 & 1)) * 32);
      struct Frag2 { u32x2 lo[8], hi[8]; };
      Frag2 G[2];
      auto ld2 = [&](Frag2& N, int sn, int nt, int hi) {
        const char* p = smem + sn + woff[nt & 1] + (nt >> 2) * 1024 + ((nt >> 1) & 1) * 256;
        if (hi) N.hi[nt] = trread(p + 4096); else N.lo[nt] = trread(p);
      };
      WAIT_V(0);
      STW(((u32x4){1u, 1u, 1u, 1u}), fb_off);
      RAW_BAR();
#pragma unroll
      for (int i = 0; i < 16; ++i) ld2(G[0], wo_slot(0), i >> 1, i & 1);
      f32x4 a2[2][8];
#pragma unroll
      for (int i = 0; i < 16; ++i) a2[i & 1][i >> 1] = (f32x4){0.f, 0.f, 0.f, 0.f};
      SLEEP(P_FB);
      SPIN { CNT(2); if (wave_ok(ld32(ws, WS_FB + (rp * 32 + 16 * kh + (lane & 15)) * 128))) break; SLEEP(L_FB); }
      asm volatile("" ::: "memory");
      STAMPT(0, 7, 0);
      char* pr[2];
#pragma unroll
      for (int i = 0; i < 2; ++i) pr[i] = smem + L_P + kh * L_PK + (32 * rt + c16) * 512 + (((4 * i + g4) ^ (c16 & 7)) * 16);
      u32x4 of[16][2];
#pragma unroll
      for (int t = 0; t < OW; ++t) { of[t][0] = LDO(t, 0); of[t][1] = LDO(t, 1); }
      SB0();
      sfor<0, 16>([&](auto T) {
        constexpr int t = T.v;
        Frag2& C = G[t & 1]; Frag2& N = G[(t + 1) & 1];
#pragma unroll
        for (int i = 0; i < 16; ++i) {
          const int nt = i >> 1, st = i & 1;
          a2[st][nt] = mfma16((u32x4){C.lo[nt][0], C.lo[nt][1], C.hi[nt][0], C.hi[nt][1]}, of[t][st], a2[st][nt]);
          SB0();
          if ((t & 1) && t <= 11 && i == 8) {
            asm volatile("s_waitcnt lgkmcnt(8)" ::: "memory"); __builtin_amdgcn_s_barrier(); asm volatile("" ::: "memory");
          }
          if (t < 15) ld2(N, wo_slot(t + 1), nt, st);
          if (t > 0 && t - 1 + OW < 16 && (i == 3 || i == 11)) of[(t - 1 + OW) & 15][i >> 3] = LDO(t - 1 + OW, i >> 3);
          if (t - 1 + OW == 16 && (i == 3 || i == 11)) { xv[i >> 3] = *(const u32x4*)(x + ego + (i >> 3) * 64); wv[i >> 3] = *(const u32x4*)(w_r + cb * 128 + eseg * 8 + (i >> 3) * 64); }
          if (t == 15 && i >= 4) *(f32x4*)(pr[(nt - 2) & 1] + ((nt - 2) >> 1) * 128 + st * 8192) = a2[st][(nt - 2) & 7];
          SB0();
        }
      });
      STAMPT(0, 8, 0);
#pragma unroll
      for (int i = 12; i < 16; ++i) *(f32x4*)(pr[(i >> 1) & 1] + (i >> 2) * 128 + (i & 1) * 8192) = a2[i & 1][i >> 1];
    }
    unpack_xw();
    __builtin_amdgcn_s_setprio(0);
  } else {
    LANES();
    const int sw = wave - 4, t2 = tid - 256;
    __builtin_amdgcn_s_setprio(PRIO_S);
    const bool real = hs_ != 7;
    unsigned hs_g0 = 0, hs_cur = 0;
    u32x4 ms_exp = {0, 0, 0, 0};
    bool hs_act = true, ms_act = !real && hp < 8 && rb == 0 && sw == 1;
    const unsigned hs_off = (lane == 1 ? SY_GO : SY_CH) + bid * 4;
    const unsigned re_off = t2 == 0 ? SY_RE + bid * 4 : OOB;
    const unsigned ms_off = lane * 16;
    const char* wsu = uni(ws);
#define HS_PROBE() asm volatile("s_nop 4\n\tglobal_load_dword v156, %0, %1 sc1" :: "v"(hs_off), "s"(wsu) : "memory", RSV)
#define MS_PROBE() asm volatile("s_nop 4\n\tglobal_load_dwordx4 v[152:155], %0, %1 offset:2048 sc1" :: "v"(ms_off), "s"(wsu) : "memory", RSV)
    auto hs_step = [&](bool first) {
      if (hs_act) {
        const unsigned hv_ = rget<156>();
        hs_act = __builtin_amdgcn_readlane(hv_, 1) == hs_g0;
        const unsigned c = __builtin_amdgcn_readlane(hv_, 0);
        if (sw == 0 && (first || c != hs_cur)) STW1(hv_, re_off);
        hs_cur = c;
        HS_PROBE();
      }
      if (ms_act) {
        const unsigned d = ((rget<152>() ^ ms_exp[0]) | (rget<153>() ^ ms_exp[1])) | ((rget<154>() ^ ms_exp[2]) | (rget<155>() ^ ms_exp[3]));
        ms_act = __any(d != 0u);
        STW(ms_exp, ms_act ? OOB : SY_GO + ms_off);
        MS_PROBE();
      }
    };
    HS_PROBE();
    if (ms_act) {
      MS_PROBE();
      asm volatile("s_nop 4\n\tglobal_load_dwordx4 v[148:151], %0, %1 offset:1024 sc1" :: "v"(ms_off), "s"(wsu) : "memory", RSV);
    }
    if (sw == 3) {
      const char* cp = (const char*)(pc0 - (pc0 & 127)) + (idx * CODE_LINES + (lane < CODE_LINES ? lane : 0)) * 128;
      asm volatile("global_load_dword v157, %0, off" :: "v"(cp) : "memory", RSV);
    }
    const int gr = (g8 >> 3) * 7 + min(hs_, 6);
    const unsigned voG = (unsigned)((lane >> 3) * 8 * F_ + (lane & 7) * 8) * 2u, voD = (unsigned)((lane >> 3) * 8 * D_ + (lane & 7) * 8) * 2u;
    const size_t gofs = ((size_t)(kq * 256 + sw * 64) * F_ + gr * 64) * 2;
    const char* pG = (const char*)wg + gofs;
    const char* pU = (const char*)wu + gofs;
    const char* pD = (const char*)(wd + (size_t)(gr * 64) * D_ + kq * 256 + sw * 64);
    auto wstep = [&](auto I) {
      constexpr int i = I.v;
      if (i < 8) wload<VW + 4 * i>(voG, pG + (i & 7) * (F_ * 2));
      else if (i < 16) wload<VW + 4 * i>(voG, pU + (i & 7) * (F_ * 2));
      else wload2<VW + 4 * i>(voD, pD + (i & 7) * (D_ * 2));
    };
    unsigned voW;
    {
      const int bi = lane >> 4, q = (lane >> 2) & 3, ch = lane & 3;
      voW = (unsigned)((4 * (bi >> 1) + q) * D_ + (bi & 1) * 32 + (ch ^ (bi & 2)) * 8) * 2u;
    }
    const char* baseW = (const char*)(wo + (size_t)((16 * (sw >> 1)) * 32 + 16 * (sw & 1)) * D_ + cb * 128) - 1024;
    auto stageW = [&](auto J, bool rsv) {
      const char* s_ = baseW + (size_t)whead(J.v) * (32 * D_ * 2);
      if (rsv) glds4o<wo_slot(J.v)>(voW + 1024, voW + 128, voW + 8 * D_ * 2 - 1024, voW + 8 * D_ * 2 + 128 - 2048, s_, l4);
      else glds4<wo_slot(J.v)>(voW + 1024, voW + 128, voW + 8 * D_ * 2 - 1024, voW + 8 * D_ * 2 + 128 - 2048, s_, l4);
    };
    int sta[4], stb[3];
    {
      const int row = t2 >> 1, key = (row >> 1) & 7;
#pragma unroll
      for (int c = 0; c < 4; ++c) { sta[c] = (posA & 3) * K1_STAGE + row * 128 + (((4 * (t2 & 1) + c) ^ key) * 16); asm volatile("" : "+v"(sta[c]) :: RSV); }
#pragma unroll
      for (int i = 0; i < 3; ++i) {
        const int G = sw * 3 + i, hd = G >= 6 ? 1 : 0, mat = (G - 6 * hd) >> 1;
        stb[i] = (posB & 3) * K1_STAGE + 16384 + (lane >> 4) * 6144 + mat * 1024 + hd * 256 + ((lane & 15) >> 2) * 64 + (G & 1) * 32 + (lane & 3) * 8;
        asm volatile("" : "+v"(stb[i]) :: RSV);
      }
    }
    unsigned rs_dst = rs_slot + t2 * 4, cs_dst = lane < 48 ? cs_slot + (sw * 48 + lane) * 4 : OOB;
    unsigned rs_off = WS_RS + rb * 16384 + (lane >> 4) * 1024 + sw * 256 + (lane & 15) * 16;
    unsigned cs_off = WS_CS + hp * 12288 + (lane >> 4) * 768 + sw * 192 + min(lane & 15, 11) * 16;
    asm volatile("" : "+v"(rs_dst), "+v"(cs_dst), "+v"(rs_off), "+v"(cs_off) :: RSV);
    int stm_dst = ((lane >> 5) * 128 + sw * 32 + (lane & 15) * 2 + ((lane >> 4) & 1)) * 4, cs_put = (lane & 15) < 12 ? (256 + sw * 48 + (lane & 15) * 4 + (lane >> 4)) * 4 : 1792 + lane * 4;
    asm volatile("" : "+v"(stm_dst), "+v"(cs_put) :: RSV);
    auto st_load = [&]() { sfor<0, 4>([&](auto I) { sload<224 + 4 * I.v>(rs_off, wsu + I.v * 4096); sload<240 + 4 * I.v>(cs_off, wsu + I.v * 3072); }); };
    sfor<3, GS[0]>(group);
    WAIT_VR(gops(GS[0]) - 10 + 1); RAW_BARR();
    TSL(14);
    sfor<GS[0], GS[1]>(group);
    WAIT_VR(gops(GS[1]) - 20); RAW_BARR();
    hs_g0 = __builtin_amdgcn_readlane(rget<156>(), 1);
    if (ms_act) {
      const u32x4 re = {rget<152>(), rget<153>(), rget<154>(), rget<155>()}, g0 = {rget<148>(), rget<149>(), rget<150>(), rget<151>()};
#pragma unroll
      for (int j = 0; j < 4; ++j) { const unsigned c = re[j] + 1u; ms_exp[j] = (c == g0[j]) ? c + 1u : c; }
      STW(ms_exp, SY_CH + ms_off);
    }
    u32x4 ra[4] = {}; u32x2 rc[3][4] = {};
    float sa = 0.f, sb = 0.f, qa = 0.f, qb = 0.f, ca[3] = {0.f, 0.f, 0.f}, cb_[3] = {0.f, 0.f, 0.f};
    auto chunk = [&](auto C) {
      constexpr int c = C.v;
      if constexpr (c < 4) {
        asm volatile("" : "+v"(ra[c]) :: RSV);
#pragma unroll
        for (int i = 0; i < 4; ++i) { const float lo = bflo(ra[c][i]), hi = bfhi(ra[c][i]); sa = fadd(sa, lo); sb = fadd(sb, hi); qa = fsq(lo, qa); qb = fsq(hi, qb); }
        asm volatile("" : "+v"(sa), "+v"(sb), "+v"(qa), "+v"(qb) :: RSV);
      } else if constexpr (c == 4) {
        float s = fadd(sa, sb), sq = fadd(qa, qb);
        s = fadd(s, dppf<DPP_X1>(s)); sq = fadd(sq, dppf<DPP_X1>(sq));
        STW1(sane1(__float_as_uint((t2 & 1) ? sq : s)), rs_dst);
      } else if constexpr (c < 11) {
        constexpr int i = (c - 5) >> 1, j = ((c - 5) & 1) * 2;
        asm volatile("" : "+v"(rc[i][j]), "+v"(rc[i][j + 1]) :: RSV);
#pragma unroll
        for (int e = 0; e < 4; ++e) { const unsigned u = rc[i][j + (e >> 1)][e & 1]; ca[i] = fadd(ca[i], bflo(u)); cb_[i] = fadd(cb_[i], bfhi(u)); }
        asm volatile("" : "+v"(ca[i]), "+v"(cb_[i]) :: RSV);
      } else {
        const float c2 = fadd(ca[2], cb_[2]);
        STW1(sane1(__float_as_uint(sw32(sw16(fadd(ca[0], cb_[0]), fadd(ca[1], cb_[1])), sw16(c2, c2)))), cs_dst);
      }
    };
    sfor<0, 12>([&](auto T) {
      constexpr int t = T.v;
      TSL(t);
      if (t < 4 && posA == t + 1) {
#pragma unroll
        for (int c = 0; c < 4; ++c) ra[c] = *(const u32x4*)(smem + sta[c]);
      }
      if (t < 4 && posB == t + 1) {
#pragma unroll
        for (int i = 0; i < 3; ++i)
#pragma unroll
          for (int j = 0; j < 4; ++j) rc[i][j] = trread(smem + (stb[i] ^ ((j >> 1) * 32)) + (j >> 1) * 3072 + (j & 1) * 512);
      }
      if (t == 1 + PFO) HS_PROBE();
      if (t >= 3 + PFO) hs_step(t == 3 + PFO);
      sfor<GS[t + 1], GS[t + 2]>([&](auto G) {
        group(G);
        constexpr int u = t - CH0, p = G.v - GS[t + 1];
        constexpr int ord[12] = {0, 5, 6, 1, 7, 8, 2, 9, 10, 3, 11, 4};
        if constexpr (u >= 0 && CHN * u + p < 12 && p < CHN) chunk(IC<ord[(CHN * u + p) % 12]>{});
      });
      if (P2MAP == 0 && t >= TCH0 && t < TCH0 + 8 && sw == ((t - TCH0) & 3))
        asm volatile("s_nop 4\n\tglobal_load_dword v157, %0, %1" :: "v"(lane * 128), "s"(uni((const char*)wo + idx * 65536 + ((t - TCH0 + xcd) & 7) * 8192)) : "memory", RSV);
      if (P2MAP == 1 && t >= TCH0 && t < TCH0 + 4 && sw == ((t - TCH0) & 3))
        asm volatile("s_nop 4\n\tglobal_load_dword v157, %0, %1" :: "v"((lane >> 3) * 2048 + (lane & 7) * 128), "s"(uni((const char*)wo + idx * 65536 + ((t - TCH0 + (rp >> 3)) & 3) * 16384 + (cb & 4) * 256)) : "memory", RSV);
      if (real) sfor<WS_[t], WS_[t + 1]>(wstep);
      TSL(16 + t);
      WAIT_VR(t < 11 ? gops(GS[t + 2]) - 10 * (t + 3) : gops(GS[13]) - gops(GS[12]));
      TSL(32 + t);
      RAW_BARR();
    });
    TSL(12);
    hs_step(false);
    auto st_put = [&]() -> bool {
      float a[8]; unsigned m = 0, n = 0;
      rsum4<224>(a, m, n); rsum4<240>(a + 4, m, n);
      const float s1 = sw16(a[0], a[2]), q1 = sw16(a[1], a[3]);
      const float cs = sw32(sw16(a[4], a[5]), sw16(a[6], a[7]));
      const float mean = sw32(s1, s1) * (1.0f / 1024.0f);
      const float var = fmaxf(__builtin_fmaf(sw32(q1, q1), 1.0f / 1024.0f, -(mean * mean)), 0.f);
      const float rinv = __builtin_amdgcn_rcpf(rbf(__builtin_amdgcn_sqrtf(rbf(rbf(var) + 1e-5f))));
      *(float*)(smem + L_STM + stm_dst) = (lane & 32) ? rinv : rbf(mean);
      *(float*)(smem + L_STM + cs_put) = cs;
      return wave_ok(max(m, n));
    };
    st_load();
    if (real) sfor<WS_[12], W1A>(wstep);
    TSL(28);
    if (real) WAIT_VR(8 + W1A - WS_[11]); else WAIT_VR(8);
    if (lane == 0) { misc[8 + sw] = 0; misc[12 + sw] = 0; misc[16 + sw] = 0; misc[7] = 0; misc[6] = 0; }
    TSL(44);
    RAW_BARR();
    TSL(13); TSDUMP(1 + sw);
    STAMPT(1, 0, 256);
    if (real) WAIT_VR(W1A - WS_[12]); else WAIT_VR(0);
    if (!st_put() || hs_act) {
      if (hs_act) hs_act = __builtin_amdgcn_readlane(rget<156>(), 1) == hs_g0;
      if (ms_act) {
        SPIN {
          bool ok = true;
#pragma unroll
          for (int j = 0; j < 4; ++j) ok &= (ld32(ws, SY_RE + lane * 16 + j * 4) == ms_exp[j]);
          if (__all(ok)) break;
        }
        STW(ms_exp, SY_GO + lane * 16);
      }
      if (hs_act) {
        SPIN {
          if (ld32(ws, SY_GO + bid * 4) != hs_g0) break;
          const unsigned c = ld32(ws, SY_CH + bid * 4);
          if (c != hs_cur) { hs_cur = c; STW1(c, re_off); }
        }
      }
      SPIN { st_load(); WAIT_VR(0); if (st_put()) break; }
    }
    asm volatile("" ::: "memory", RSV);
    if (lane == 0) misc[12 + sw] = 1;
    __builtin_amdgcn_s_setprio(0);
    if (real) sfor<W1A, 16>(wstep);
    auto mwait = [&](int v) { SPIN { const int a = misc[8 + (lane & 3)]; if (__all(a >= v)) break; __builtin_amdgcn_s_sleep(1); } asm volatile("" ::: "memory"); };
    sfor<0, 9>([&](auto J) {
      if (J.v == 4) mwait(3);
      if (J.v == WOD) { SPIN { if (misc[WODF] == 1) break; __builtin_amdgcn_s_sleep(2); } SLEEP(WODS); }
      stageW(J, J.v < 2);
      if (J.v >= 1 && J.v < WOD) WAIT_V(4 * PACE);
      if (J.v == 1) { WAIT_VR(8); sfor<0, 16>(wtr); }
      if (J.v >= 2 && PSLP) __builtin_amdgcn_s_sleep(PSLP);
    });
    STAMPT(1, 1, 256);
    EPI();
#pragma unroll
    for (int i = 0; i < 2; ++i) { xv[i] = *(const u32x4*)(x + ego + i * 64); wv[i] = *(const u32x4*)(w_r + cb * 128 + eseg * 8 + i * 64); }
    asm volatile("s_waitcnt vmcnt(0)" : "+v"(xv[0]), "+v"(xv[1]), "+v"(wv[0]), "+v"(wv[1]) :: "memory");
    unpack_xw();
    RAW_BAR();
    RAW_BAR(); stageW(IC<9>{}, false); stageW(IC<10>{}, false);
    RAW_BAR(); stageW(IC<11>{}, false); stageW(IC<12>{}, false);
    RAW_BAR(); stageW(IC<13>{}, false); stageW(IC<14>{}, false);
    WAIT_V(16); RAW_BAR(); stageW(IC<15>{}, false);
    WAIT_V(12); RAW_BAR();
    WAIT_V(0); RAW_BAR();
    if (real) sfor<16, 24>(wstep);
  }
  LANES(); EPI();
  const int grow = rp * 64 + erow;
  int pa0 = L_P + erow * 512 + ((2 * eseg) ^ (erow & 7)) * 16, pa1 = pa0 ^ 16;
  unsigned st_dst = eseg == 0 ? WS_ST2 + ((rp * 8 + cb) * 64 + erow) * 16 : OOB, st_src = WS_ST2 + rp * 8192 + lane * 16;
  unsigned fd_dst = lane < 8 ? WS_FD + (cb * 32 + rp) * 128 + lane * 16 : OOB, kidx = 0xFFFFu - grow;
  asm volatile("" : "+v"(pa0), "+v"(pa1), "+v"(st_dst), "+v"(st_src), "+v"(fd_dst), "+v"(kidx));
  RAW_BAR2();
  STAMPT(0, 9, 0);
  unsigned hb[8];
  f32x2 hv[8];
  {
    f32x4 pp[2][4];
#pragma unroll
    for (int k = 0; k < 2; ++k)
#pragma unroll
      for (int i = 0; i < 4; ++i) pp[k][i] = *(const f32x4*)(smem + ((i & 1) ? pa1 : pa0) + (i >> 1) * 256 + k * L_PK);
    SB0();
    f32x2 s = {0.f, 0.f}, q = {0.f, 0.f}, p2 = {0.f, 0.f};
#pragma unroll
    for (int i = 0; i < 8; ++i) {
      const f32x4 a = pp[0][i >> 1] + pp[1][i >> 1];
      const f32x2 u = xf[i] + unpk(pack2o(a[2 * (i & 1)], a[2 * (i & 1) + 1]));
      hb[i] = pack2o(u[0], u[1]);
      hv[i] = unpk(hb[i]);
      s += hv[i]; q += hv[i] * hv[i]; p2 += hv[i] * wf2[i];
    }
    STW(sane((u32x4){__float_as_uint(oct_sum(s[0] + s[1])), __float_as_uint(oct_sum(q[0] + q[1])), __float_as_uint(oct_sum(p2[0] + p2[1])), __float_as_uint(sw_)}), st_dst);
  }
  KEEP2();
  STAMPT(0, 10, 0);
  const int sw = wave - 4;
  const unsigned fd_src = WS_FD + xcd * 4096 + r * 128;
  u32x2 kpk = {SENT, SENT};
  if (wave == 0) {
    u32x4 pa[8];
    SLEEP(P_C);
    SPIN {
      unsigned m = 0;
#pragma unroll
      for (int j = 0; j < 8; ++j) { pa[j] = LDW(st_src + j * 1024); m = chk(m, pa[j]); }
      CNT(3);
      if (wave_ok(m)) break;
      KEEP2();
    }
    STAMPT(0, 11, 0);
#define F4(v) __builtin_bit_cast(f32x4, v)
    const f32x4 t = ((F4(pa[0]) + F4(pa[1])) + (F4(pa[2]) + F4(pa[3]))) + ((F4(pa[4]) + F4(pa[5])) + (F4(pa[6]) + F4(pa[7])));
    const float mean = t[0] * (1.0f / 1024.0f);
    const float var = fmaxf(__builtin_fmaf(t[1], 1.0f / 1024.0f, -(mean * mean)), 0.f);
    const float mb = rbf(mean), rs = __builtin_amdgcn_rcpf(rbf(__builtin_amdgcn_sqrtf(rbf(rbf(var) + 1e-5f))));
    *(f32x2*)(smem + L_KX + lane * 8) = (f32x2){mb, rs};
    const float sc = fmaxf((t[2] - mb * t[3]) * rs, -INFINITY);
    const int u = (int)(pack2(0.f, sc) & 0xffff0000u);
    unsigned b0 = ((unsigned)(u ^ ((u >> 31) | (int)0x80000000)) & 0xffff0000u) | (0xFFFFu - (rp * 64 + lane)), b1 = 0;
    top2_lo(b0, b1); top2_hi(b0, b1);
    STW(((u32x4){b0, b1, b0, b1}), fd_dst);
    if (lane == 0) *(u32x2*)(smem + L_MISC + 112) = (u32x2){b0, b1};
  } else if (wave >= 4) {
    asm volatile("s_waitcnt vmcnt(0)" ::: "memory", RSV2);
    sfor<16, 24>(wtr);
  }
  RAW_BAR();
  bool cand;
  u32x4 ob[2];
  {
    const f32x2 mr = *(const f32x2*)(smem + L_KX + erow * 8);
    const u32x2 kk = *(const u32x2*)(smem + L_MISC + 112);
    const float mb = mr[0], rs = mr[1];
    const unsigned b0 = kk[0], b1 = kk[1];
    u32x4 nq[2];
#pragma unroll
    for (int i = 0; i < 8; ++i) {
      const f32x2 d = hv[i] - mb;
      const f32x2 n = unpk(pack2o(d[0], d[1])) * rs;
      nq[i >> 2][i & 3] = pack2o(n[0], n[1]);
    }
    const bool c2 = (b1 & 0xffffu) == kidx;
    cand = c2 || (b0 & 0xffffu) == kidx;
    if (cand) {
      const unsigned xo = WS_XS + (rp * 2 + (c2 ? 1 : 0)) * 2048 + cb * 256 + eseg * 16;
      STW(sane(nq[0]), xo); STW(sane(nq[1]), xo + 128);
      char* cp = smem + L_CAND + (c2 ? 512 : 0) + eseg * 16;
      *(u32x4*)cp = (u32x4){hb[0], hb[1], hb[2], hb[3]}; *(u32x4*)(cp + 128) = (u32x4){hb[4], hb[5], hb[6], hb[7]};
      *(u32x4*)(cp + 256) = nq[0]; *(u32x4*)(cp + 384) = nq[1];
    }
    asm volatile("" ::: "memory");
    if (wave >= 4) { SLEEP(P_D); kpk = __builtin_amdgcn_raw_buffer_load_b64(wsr, fd_src, 0, 16); }
    asm volatile("" ::: "memory");
#pragma unroll
    for (int i = 0; i < 8; ++i) { const f32x2 o = hv[i] + unpk(nq[i >> 2][i & 3]); ob[i >> 2][i & 3] = pack2(o[0], o[1]); }
  }
#define OUT_ST() do { *(u32x4*)(out + ego) = ob[0]; *(u32x4*)(out + ego + 64) = ob[1]; } while (0)
  STAMPT(0, 12, 0);
  if (wave < 4) {
    if (!cand) OUT_ST();
    RAW_BAR();
    const unsigned w0 = misc[24], w1 = misc[25];
    if (cand && grow != (int)key_idx(w0) && grow != (int)key_idx(w1)) OUT_ST();
#pragma unroll 1
    for (int tk = 0; tk < 2; ++tk) {
      const unsigned wk = tk ? w1 : w0, slot = misc[26 + tk];
      if ((int)(slot >> 1) != rp) continue;
      u32x4 pv[7];
      const unsigned yo = WS_YP + ((tk * 8 + cb) * 64 + (lane >> 3) * 8) * 512 + wave * 128 + (lane & 7) * 16;
      const int col = (wave >> 1) * 64 + (lane & 7) * 8 + (wave & 1) * 4 + (lane >> 4);
      float sg = rbf(__builtin_amdgcn_rcpf(1.0f + __expf(-key_val(wk))));
      float hh = bflo(*(const u16*)(smem + L_CAND + (slot & 1) * 512 + col * 2)), nn = bflo(*(const u16*)(smem + L_CAND + (slot & 1) * 512 + 256 + col * 2));
      u16* op = out + (size_t)key_idx(wk) * D_ + cb * 128 + col;
      asm volatile("" : "+v"(sg), "+v"(hh), "+v"(nn), "+v"(op));
      if (tk == 0 || (int)(misc[26] >> 1) != rp) {
        SPIN { if (misc[30] == (int)w0) break; __builtin_amdgcn_s_sleep(1); }
        SLEEP(P_E);
      }
      SPIN {
        unsigned m = 0;
#pragma unroll
        for (int p = 0; p < 7; ++p) { pv[p] = LDW(yo + p * 512); m = chk(m, pv[p]); }
        CNT(6);
        if (wave_ok(m)) break;
        asm volatile("" ::: "memory");
        SLEEP(L_E);
      }
      STAMPT(0, 13, 0);
      f32x4 s = __builtin_bit_cast(f32x4, pv[0]);
#pragma unroll
      for (int p = 1; p < 7; ++p) s += __builtin_bit_cast(f32x4, pv[p]);
      float y = sw32(sw16(s[0], s[1]), sw16(s[2], s[3]));
      y += dppf<DPP_ROR8>(y);
      if (!(lane & 8)) *(GAS u16*)op = (u16)pack2(hh + rbf(nn + rbf(rbf(y) * sg)), 0.f);
      STAMPT(0, 14, 0);
    }
  } else {
    unsigned xl = WS_XS + (kq * 256 + sw * 64 + (lane >> 4) * 16 + ((lane >> 2) & 1) * 8) * 2;
    int red_dst = (lane < 8 || (lane >= 24 && lane < 32)) ? L_RED + sw * 2048 + ((lane >> 3) & 1) * 1024 + (lane & 7) * 16 : L_RED + 8192 + lane * 16;
    unsigned gu_src = WS_GU + g8 * 4096 + lane * 16;
    int act_dst = L_ACT + sw * 256 + ((lane & 7) * 8 + (lane >> 3)) * 2, act_src = L_ACT + sw * 256 + (lane & 1) * 128 + (lane >> 4) * 32 + ((lane >> 1) & 1) * 16;
    unsigned yp_dst = lane < 16 ? WS_YP + (((lane >> 3) * 8 + 2 * kq + (sw >> 1)) * 64 + g8) * 512 + (sw & 1) * 256 + (lane & 7) * 16 : OOB;
    asm volatile("" : "+v"(xl), "+v"(red_dst), "+v"(gu_src), "+v"(act_dst), "+v"(act_src), "+v"(yp_dst));
    unsigned w0, w1; int s0 = -1, s1 = -1;
    u32x4 xa = {SENT, SENT, SENT, SENT};
    SPIN {
      CNT(4);
      const bool all = wave_ok(kpk[1]);
      const unsigned k0 = kpk[1] == SENT ? 0u : kpk[0], k1 = kpk[1] == SENT ? 0u : kpk[1];
      w0 = k0; w1 = k1;
      top2_lo(w0, w1); top2_mid(w0, w1);
      const int n0 = 2 * __builtin_ctzll(__ballot(k0 == w0));
      const u64 m1 = __ballot(k0 == w1);
      const int n1 = m1 ? 2 * __builtin_ctzll(m1) : n0 + 1;
      const bool same = n0 == s0 && n1 == s1;
      s0 = n0; s1 = n1;
      asm volatile("" ::: "memory");
      if (all && same) break;
      if (!all) { SLEEP(L_D); kpk = __builtin_amdgcn_raw_buffer_load_b64(wsr, fd_src, 0, 16); }
      xa = LDW(xl + ((lane & 1) ? s1 : s0) * 2048);
      if (all) break;
    }
    SPIN {
      if (wave_ok(chk(0u, xa))) break;
      asm volatile("" ::: "memory");
      xa = LDW(xl + ((lane & 1) ? s1 : s0) * 2048);
    }
    STAMPT(1, 3, 256);
    {
      const u32x4 z4 = {0u, 0u, 0u, 0u};
      const u32x4 xg = (lane & 2) ? z4 : xa, xu = (lane & 2) ? xa : z4;
      f32x4 d[8];
#pragma unroll
      for (int c = 0; c < 8; ++c) d[c] = mfma16(xg, wf[c], (f32x4){0.f, 0.f, 0.f, 0.f});
      if (tid == 256) { *(u32x4*)(smem + L_MISC + 96) = (u32x4){w0, w1, (unsigned)s0, (unsigned)s1}; misc[30] = 0; }
      SB0();
#pragma unroll
      for (int c = 0; c < 10; ++c) {
        if (c < 8) d[c & 7] = mfma16(xu, wf[8 + (c & 7)], d[c & 7]);
        if (c >= 2) *(f32x4*)(smem + red_dst + (c - 2) * 128) = d[(c - 2) & 7];
        SB0();
      }
    }
    RAW_BAR();
    if (sw == 0) {
      f32x4 t[8];
#pragma unroll
      for (int p = 0; p < 8; ++p) t[p] = *(const f32x4*)(smem + L_RED + p * 1024 + lane * 16);
      SB0();
      STW(sane(__builtin_bit_cast(u32x4, ((t[0] + t[1]) + (t[2] + t[3])) + ((t[4] + t[5]) + (t[6] + t[7])))), gu_src + kq * 1024);
    } else __builtin_amdgcn_s_sleep(2);
    STAMPT(1, 4, 256);
    {
      u32x4 q[4];
      if (OUTE && grow != (int)key_idx(w0) && grow != (int)key_idx(w1)) OUT_ST();
      asm volatile("" ::: "memory");
      SLEEP(P_GU);
      SPIN {
        unsigned m = 0;
#pragma unroll
        for (int j = 0; j < 4; ++j) { q[j] = LDW(gu_src + j * 1024); m = chk(m, q[j]); }
        CNT(5);
        if (wave_ok(m)) break;
        asm volatile("" ::: "memory");
      }
      STAMPT(1, 5, 256);
      const f32x4 tot = (__builtin_bit_cast(f32x4, q[0]) + __builtin_bit_cast(f32x4, q[1])) + (__builtin_bit_cast(f32x4, q[2]) + __builtin_bit_cast(f32x4, q[3]));
      const f32x2 gs = unpk(pack2o(tot[0], tot[1])), us = unpk(pack2o(tot[2], tot[3]));
      const f32x2 sg = unpk(pack2o(gs[0] * __builtin_amdgcn_rcpf(1.0f + __expf(-gs[0])), gs[1] * __builtin_amdgcn_rcpf(1.0f + __expf(-gs[1]))));
      const unsigned a2 = pack2o(sg[0] * us[0], sg[1] * us[1]);
      *(u16*)(smem + act_dst) = (u16)a2; *(u16*)(smem + act_dst + 128) = (u16)(a2 >> 16);
      __builtin_amdgcn_fence(__ATOMIC_RELEASE, "wavefront"); __builtin_amdgcn_wave_barrier(); __builtin_amdgcn_fence(__ATOMIC_ACQUIRE, "wavefront");
      const u32x4 aa = *(const u32x4*)(smem + act_src);
      f32x4 d[8];
#pragma unroll
      for (int c = 0; c < 8; ++c) d[c] = mfma16(aa, wf[16 + c], (f32x4){0.f, 0.f, 0.f, 0.f});
      SB0();
      u32x4 y[2];
#pragma unroll
      for (int c = 0; c < 8; ++c) {
        const float t0 = d[c][0] + dppf<DPP_ROR8>(d[c][2]), t1 = d[c][3] + dppf<DPP_ROR8>(d[c][1]);
        y[c >> 2][c & 3] = __float_as_uint((lane & 8) ? t1 : t0);
      }
      STW(sane(y[0]), yp_dst); STW(sane(y[1]), yp_dst + 128);
      if (tid == 256) misc[30] = w0;
    }
    STAMPT(1, 6, 256);
    if (!OUTE && grow != (int)key_idx(w0) && grow != (int)key_idx(w1)) OUT_ST();
  }
}

extern "C" void kernel_launch(void* const* d_in, const int* in_sizes, int n_in, void* d_out, int out_size,
                              void* d_ws, size_t ws_size, hipStream_t stream) {
  (void)hipFuncSetAttribute((const void*)fused, hipFuncAttributeMaxDynamicSharedMemorySize, LDS_TOTAL);
  fused<<<256, 512, LDS_TOTAL, stream>>>((const u16*)d_in[0], (const u16*)d_in[1], (const u16*)d_in[2], (const u16*)d_in[4],
                                         (const u16*)d_in[5], (const u16*)d_in[6], (const u16*)d_in[7], (u16*)d_out, (char*)d_ws);
}
```

```cpp
#include <hip/hip_runtime.h>
#include <stdint.h>

#define LAS __attribute__((address_space(3)))
#define GAS __attribute__((address_space(1)))
#define DI __device__ __forceinline__

typedef unsigned short u16;
typedef unsigned long long u64;
typedef __attribute__((ext_vector_type(2))) __bf16 bf16x2;
typedef __attribute__((ext_vector_type(8))) __bf16 bf16x8;
typedef __attribute__((ext_vector_type(4))) short s16x4;
typedef __attribute__((ext_vector_type(2))) unsigned short u16x2;
typedef __attribute__((ext_vector_type(16))) float f32x16;
typedef __attribute__((ext_vector_type(4))) float f32x4;
typedef __attribute__((ext_vector_type(2))) float f32x2;
typedef __attribute__((ext_vector_type(4))) unsigned u32x4;
typedef __attribute__((ext_vector_type(2))) unsigned u32x2;
typedef GAS unsigned gu32;
typedef GAS unsigned long long gu64;

constexpr int S_ = 2048, D_ = 1024, F_ = 3584;

constexpr unsigned SY_CH = 0, SY_GO = 1024, SY_RE = 2048;
constexpr unsigned WS_FD  = 98304;
constexpr unsigned WS_ZF  = 8192;
constexpr unsigned WS_KVF = 16384;
constexpr unsigned WS_RS  = 131072;
constexpr unsigned WS_CS  = WS_RS + 16 * 16 * 1024;
constexpr unsigned WS_ZP  = WS_CS + 16 * 16 * 768;
constexpr unsigned WS_FB  = WS_ZP + 32 * 16 * 128;
constexpr unsigned WS_ST2 = WS_FB + 32 * 32 * 128;
constexpr unsigned WS_XS  = WS_ST2 + 32 * 8 * 1024;
constexpr unsigned WS_GU  = WS_XS + 64 * 2048;
constexpr unsigned WS_YP  = WS_GU + 256 * 1024;
constexpr unsigned WS_KVP = WS_YP + 2 * 8 * 64 * 512;
constexpr unsigned WS_O   = WS_KVP + 32 * 16 * 4096;
constexpr unsigned WS_END = WS_O + S_ * D_ * 2;
constexpr unsigned OOB = 0x80000000u;
constexpr size_t WS_DIAG  = 64u << 20;
#define CNT(i) do {} while (0)
#define STAMPT(kid, i, T) do {} while (0)

#define TSL(idx) do {} while (0)
#define TSDUMP(k) do {} while (0)
template <int V> struct IC { static constexpr int v = V; };
template <int B, int E, class F> DI void sfor(F&& f) { if constexpr (B < E) { f(IC<B>{}); sfor<B + 1, E>(f); } }

DI float bflo(unsigned u) { return __uint_as_float(u << 16); }
DI float bfhi(unsigned u) { return __uint_as_float(u & 0xffff0000u); }
DI float rbf(float x) { return (float)(__bf16)x; }
DI unsigned pack2(float a, float b) { bf16x2 c; c[0] = (__bf16)a; c[1] = (__bf16)b; return __builtin_bit_cast(unsigned, c); }
DI unsigned pack2o(float a, float b) { unsigned p = pack2(a, b); asm("" : "+v"(p)); return p; }
DI f32x16 mfma32(u32x4 a, u32x4 b, f32x16 c) {
  return __builtin_amdgcn_mfma_f32_32x32x16_bf16(__builtin_bit_cast(bf16x8, a), __builtin_bit_cast(bf16x8, b), c, 0, 0, 0);
}
DI f32x4 mfma16(u32x4 a, u32x4 b, f32x4 c) {
  return __builtin_amdgcn_mfma_f32_16x16x32_bf16(__builtin_bit_cast(bf16x8, a), __builtin_bit_cast(bf16x8, b), c, 0, 0, 0);
}
DI void mfma16i(u32x4 a, u32x4 b, f32x4& c) { asm volatile("v_mfma_f32_16x16x32_bf16 %0, %1, %2, %0" : "+v"(c) : "v"(a), "v"(b)); }
DI f32x2 unpk(unsigned u) { return (f32x2){bflo(u), bfhi(u)}; }
DI u32x2 trread(const char* p) {
  s16x4 v = __builtin_amdgcn_ds_read_tr16_b64_v4i16((LAS s16x4*)p);
  return __builtin_bit_cast(u32x2, v);
}
DI const char* uni(const void* p) {
  const u64 v = (u64)p;
  return (const char*)(((u64)(unsigned)__builtin_amdgcn_readfirstlane((int)(v >> 32)) << 32) | (unsigned)__builtin_amdgcn_readfirstlane((int)v));
}
#define RV4(a, b, c, d) "v" #a, "v" #b, "v" #c, "v" #d
#define RV10(p) "v" #p "0", "v" #p "1", "v" #p "2", "v" #p "3", "v" #p "4", "v" #p "5", "v" #p "6", "v" #p "7", "v" #p "8", "v" #p "9"
#define RSV2 RV4(224, 225, 226, 227), "v228", "v229", RV10(23), RV10(24), RV4(250, 251, 252, 253), "v254", "v255"
#define RSV1 RV4(192, 193, 194, 195), RV4(196, 197, 198, 199), RV10(20), RV10(21), RV4(220, 221, 222, 223), RSV2
#define RSV "v148", "v149", RV10(15), RV10(16), RV10(17), RV10(18), "v190", "v191", RSV1
#define NTW " nt"
#define NTA ""
#define NTB ""
#define NTO ""
#define GL_(v, s, o) "global_load_lds_dwordx4 " v ", " s " offset:" #o
#define GLDS4(SC, CL...) asm volatile("s_add_u32 m0, %5, %c6\n\ts_nop 3\n\t" GL_("%0", "%4", 0) SC "\n\t" GL_("%1", "%4", 1024) SC "\n\t" \
  GL_("%2", "%4", 2048) SC "\n\t" GL_("%3", "%4", 3072) SC :: "v"(v0), "v"(v1), "v"(v2), "v"(v3), "s"(uni(sbase)), "s"(lds), "i"(IMM) : "memory", "scc", ##CL)
#define GLDS3(SC, CL...) asm volatile("s_add_u32 m0, %4, %c5\n\ts_nop 3\n\t" GL_("%0", "%3", 0) SC "\n\t" GL_("%1", "%3", 1024) SC "\n\t" \
  GL_("%2", "%3", 2048) SC :: "v"(v0), "v"(v1), "v"(v2), "s"(uni(sbase)), "s"(lds), "i"(IMM) : "memory", "scc", ##CL)
template <int IMM> DI void glds4(unsigned v0, unsigned v1, unsigned v2, unsigned v3, const void* sbase, unsigned lds) { GLDS4(NTO); }
template <int IMM> DI void glds4o(unsigned v0, unsigned v1, unsigned v2, unsigned v3, const void* sbase, unsigned lds) { GLDS4(NTO, RSV); }
template <int IMM> DI void glds4r(unsigned v0, unsigned v1, unsigned v2, unsigned v3, const void* sbase, unsigned lds) { GLDS4(NTA, RSV); }
template <int IMM> DI void glds4n(unsigned v0, unsigned v1, unsigned v2, unsigned v3, const void* sbase, unsigned lds) { GLDS4(" nt", RSV); }
template <int IMM> DI void glds3n(unsigned v0, unsigned v1, unsigned v2, const void* sbase, unsigned lds) { GLDS3(" nt", RSV); }
template <int IMM> DI void glds3r(unsigned v0, unsigned v1, unsigned v2, const void* sbase, unsigned lds) { GLDS3(NTB, RSV); }
template <int R> DI void sload(unsigned voff, const void* sbase) {
  asm volatile("s_nop 4\n\tglobal_load_dwordx4 v[%c0:%c1], %2, %3 sc1" ::"i"(R), "i"(R + 3), "v"(voff), "s"(uni(sbase)) : "memory", RSV);
}
DI float fadd(float a, float b) { float o; asm("v_add_f32 %0, %1, %2" : "=v"(o) : "v"(a), "v"(b)); return o; }
DI float fmul(float a, float b) { float o; asm("v_mul_f32 %0, %1, %2" : "=v"(o) : "v"(a), "v"(b)); return o; }
DI float fsq(float a, float c) { asm("v_fmac_f32 %0, %1, %1" : "+v"(c) : "v"(a)); return c; }
#define RA_(o, a, b) "v_add_f32 %" #o ", v[%c6+" #a "], v[%c6+" #b "]\n\t"
#define RB2_(o, a) "v_add_f32 %" #o ", v[%c6+" #a "], %" #o "\n\t"
#define RM_(o, a, b) "v_max3_u32 %" #o ", v[%c6+" #a "], v[%c6+" #b "], %" #o "\n\t"
template <int R> DI void rsum4(float* a, unsigned& m, unsigned& n) {
  asm volatile(RA_(0, 0, 4) RA_(1, 1, 5) RM_(4, 0, 1) RA_(2, 2, 6) RA_(3, 3, 7) RM_(5, 2, 3) RB2_(0, 8) RM_(4, 4, 5) RB2_(1, 9) RM_(5, 6, 7)
               RB2_(2, 10) RM_(4, 8, 9) RB2_(3, 11) RM_(5, 10, 11) RB2_(0, 12) RB2_(1, 13) RM_(4, 12, 13) RB2_(2, 14) RB2_(3, 15) RM_(5, 14, 15)
               : "=&v"(a[0]), "=&v"(a[1]), "=&v"(a[2]), "=&v"(a[3]), "+v"(m), "+v"(n) : "i"(R) : RSV);
}
template <int R> DI void wload2(unsigned voff, const void* sbase) {
  asm volatile("s_nop 4\n\tglobal_load_dwordx4 v[%c0:%c1], %2, %3" NTW ::"i"(R), "i"(R + 3), "v"(voff), "s"(uni(sbase)) : "memory", RSV2);
}
template <int R> DI void wload(unsigned voff, const void* sbase) {
  asm volatile("s_nop 4\n\tglobal_load_dwordx4 v[%c0:%c1], %2, %3" NTW ::"i"(R), "i"(R + 3), "v"(voff), "s"(uni(sbase)) : "memory", RSV);
}
template <int RA, int RB> DI unsigned wperm(unsigned sel) {
  unsigned o;
  if (RA < 192) asm volatile("v_perm_b32 %0, v%c2, v%c1, %3" : "=v"(o) : "i"(RA), "i"(RB), "s"(sel) : RSV);
  else if (RA < 224) asm volatile("v_perm_b32 %0, v%c2, v%c1, %3" : "=v"(o) : "i"(RA), "i"(RB), "s"(sel) : RSV1);
  else asm volatile("v_perm_b32 %0, v%c2, v%c1, %3" : "=v"(o) : "i"(RA), "i"(RB), "s"(sel) : RSV2);
  return o;
}
template <int R> DI unsigned rget() { unsigned o; asm volatile("v_mov_b32 %0, v%c1" : "=v"(o) : "i"(R) : RSV); return o; }
DI float xh_sum(float v) {
  auto r = __builtin_amdgcn_permlane32_swap(__float_as_uint(v), __float_as_uint(v), false, false);
  return __uint_as_float(r[0]) + __uint_as_float(r[1]);
}
template <int CTRL> DI unsigned dppu(unsigned v) { return (unsigned)__builtin_amdgcn_update_dpp(0, (int)v, CTRL, 0xf, 0xf, true); }
template <int CTRL> DI float dppf(float v) { return __uint_as_float(dppu<CTRL>(__float_as_uint(v))); }
constexpr int DPP_X1 = 0xB1, DPP_X2 = 0x4E, DPP_HMIR = 0x141, DPP_MIR = 0x140, DPP_ROR8 = 0x128;
DI float quad_sum(float v) { v += dppf<DPP_X1>(v); v += dppf<DPP_X2>(v); return v; }
DI float oct_sum(float v) { v = quad_sum(v); v += dppf<DPP_HMIR>(v); return v; }
DI float x16_sum(float v) {
  auto r = __builtin_amdgcn_permlane16_swap(__float_as_uint(v), __float_as_uint(v), false, false);
  return __uint_as_float(r[0]) + __uint_as_float(r[1]);
}
DI float sw16(float a, float b) {
  auto r = __builtin_amdgcn_permlane16_swap(__float_as_uint(a), __float_as_uint(b), false, false);
  return fadd(__uint_as_float(r[0]), __uint_as_float(r[1]));
}
DI float sw32(float a, float b) {
  auto r = __builtin_amdgcn_permlane32_swap(__float_as_uint(a), __float_as_uint(b), false, false);
  return fadd(__uint_as_float(r[0]), __uint_as_float(r[1]));
}
DI float hi_sum(float v) {
  auto r = __builtin_amdgcn_permlane16_swap(__float_as_uint(v), __float_as_uint(v), false, false);
  v = fadd(__uint_as_float(r[0]), __uint_as_float(r[1]));
  auto q = __builtin_amdgcn_permlane32_swap(__float_as_uint(v), __float_as_uint(v), false, false);
  return fadd(__uint_as_float(q[0]), __uint_as_float(q[1]));
}
template <int N> struct Phi { unsigned tb[N]; f32x2 tr[N], e[N]; };
template <int N> DI void phi_a(const f32x2* t, Phi<N>& s) {
#pragma unroll
  for (int i = 0; i < N; ++i) s.tb[i] = pack2o(t[i][0], t[i][1]);
  __builtin_amdgcn_sched_barrier(0);
#pragma unroll
  for (int i = 0; i < N; ++i) s.tr[i] = unpk(s.tb[i]);
  __builtin_amdgcn_sched_barrier(0);
#pragma unroll
  for (int i = 0; i < N; ++i) s.e[i] = s.tr[i] * 1.44269504088896340736f;
  __builtin_amdgcn_sched_barrier(0);
}
template <int N> DI void phi_b(Phi<N>& s, unsigned* p) {
  u16x2 tm[2] = {{0, 0}, {0, 0}};
#pragma unroll
  for (int i = 0; i < N; ++i) {
    s.e[i][0] = __builtin_amdgcn_fmed3f(__builtin_amdgcn_exp2f(s.e[i][0]), 0.f, 1.f);
    tm[i & 1] = __builtin_elementwise_max(tm[i & 1], __builtin_bit_cast(u16x2, s.tb[i]));
    s.e[i][1] = __builtin_amdgcn_fmed3f(__builtin_amdgcn_exp2f(s.e[i][1]), 0.f, 1.f);
    __builtin_amdgcn_sched_barrier(0);
  }
#pragma unroll
  for (int i = 0; i < N; ++i) s.tr[i] += s.e[i];
  __builtin_amdgcn_sched_barrier(0);
#pragma unroll
  for (int i = 0; i < N; ++i) { s.tr[i][0] = fmaxf(s.tr[i][0], s.e[i][0]); s.tr[i][1] = fmaxf(s.tr[i][1], s.e[i][1]); }
  const u16x2 tx = __builtin_elementwise_max(tm[0], tm[1]);
  __builtin_amdgcn_sched_barrier(0);
#pragma unroll
  for (int i = 0; i < N; ++i) p[i] = pack2(s.tr[i][0], s.tr[i][1]);
  if (__any(tx[0] > 0xC0F0 || tx[1] > 0xC0F0)) {
#pragma unroll
    for (int i = 0; i < N; ++i) p[i] = pack2(bflo(p[i]) + 1e-6f, bfhi(p[i]) + 1e-6f);
  }
}
template <int N> DI void phi_n(const f32x2* t, unsigned* p) { Phi<N> s; phi_a<N>(t, s); phi_b<N>(s, p); }
#define RLX_AGENT __ATOMIC_RELAXED, __HIP_MEMORY_SCOPE_AGENT
DI void st32(char* ws, unsigned off, unsigned v) { __hip_atomic_store((gu32*)(ws + off), v, RLX_AGENT); }
DI void st64(char* ws, unsigned off, u64 v) { __hip_atomic_store((gu64*)(ws + off), v, RLX_AGENT); }
DI unsigned ld32(char* ws, unsigned off) { return __hip_atomic_load((gu32*)(ws + off), RLX_AGENT); }
DI u64 ld64(char* ws, unsigned off) { return __hip_atomic_load((gu64*)(ws + off), RLX_AGENT); }
#define WAIT_V(n) asm volatile("s_waitcnt vmcnt(%c0)" ::"i"(n) : "memory")
#define RAW_BAR() do { asm volatile("s_waitcnt lgkmcnt(0)" ::: "memory"); __builtin_amdgcn_s_barrier(); asm volatile("" ::: "memory"); } while (0)
#define SB0() __builtin_amdgcn_sched_barrier(0)
#define WAIT_VR(n) asm volatile("s_waitcnt vmcnt(%c0)" ::"i"(n) : "memory", RSV)
#define RAW_BARR() do { asm volatile("s_waitcnt lgkmcnt(0)" ::: "memory", RSV); __builtin_amdgcn_s_barrier(); asm volatile("" ::: "memory", RSV); } while (0)
#define KEEP2() asm volatile("" ::: "memory", RSV2)
#define RAW_BAR2() do { asm volatile("s_waitcnt lgkmcnt(0)" ::: "memory", RSV2); __builtin_amdgcn_s_barrier(); KEEP2(); } while (0)
constexpr unsigned SPIN_CAP = 1u << 18;
#define SPIN _Pragma("nounroll") for (unsigned s_ = 0; s_ < SPIN_CAP; ++s_)
constexpr unsigned SENT = 0xFFFFFFFFu;
DI unsigned sane1(unsigned v) { return min(v, 0xFFFFFFFEu); }
DI u32x4 sane(u32x4 v) { v[1] = sane1(v[1]); v[3] = sane1(v[3]); return v; }
DI unsigned chk(unsigned m, u32x4 v) { return max(max(m, v[1]), v[3]); }
DI bool wave_ok(unsigned m) { return !__any(m == SENT); }
DI unsigned key_idx(unsigned k) { return (0xFFFFu - (k & 0xFFFFu)) & (S_ - 1); }
DI float key_val(unsigned k) { const unsigned o = k >> 16; return __uint_as_float(((o & 0x8000u) ? (o & 0x7fffu) : (~o & 0xffffu)) << 16); }
DI void top2_mix(unsigned& b0, unsigned& b1, unsigned c0, unsigned c1) {
  const unsigned lo = min(b0, c0), mx = max(b1, c1);
  b0 = max(b0, c0); b1 = max(lo, mx);
}
DI void top2_lo(unsigned& b0, unsigned& b1) {
  top2_mix(b0, b1, dppu<DPP_X1>(b0), dppu<DPP_X1>(b1));
  top2_mix(b0, b1, dppu<DPP_X2>(b0), dppu<DPP_X2>(b1));
  top2_mix(b0, b1, dppu<DPP_HMIR>(b0), dppu<DPP_HMIR>(b1));
}
DI void top2_mid(unsigned& b0, unsigned& b1) {
  top2_mix(b0, b1, dppu<DPP_MIR>(b0), dppu<DPP_MIR>(b1));
  auto p = __builtin_amdgcn_permlane16_swap(b0, b0, false, false); auto q = __builtin_amdgcn_permlane16_swap(b1, b1, false, false);
  b0 = p[0]; b1 = q[0]; top2_mix(b0, b1, p[1], q[1]);
}
DI void top2_hi(unsigned& b0, unsigned& b1) {
  top2_mid(b0, b1);
  auto p = __builtin_amdgcn_permlane32_swap(b0, b0, false, false); auto q = __builtin_amdgcn_permlane32_swap(b1, b1, false, false);
  b0 = p[0]; b1 = q[0]; top2_mix(b0, b1, p[1], q[1]);
}

constexpr int K1_STAGE = 40960;
constexpr int LDS_TOTAL = 4 * K1_STAGE;
constexpr int L_STM = 0;
constexpr int L_MISC = 2048;
constexpr int L_XK = 5120;
constexpr int L_KX = 3328;
constexpr int L_ACT = 2304;
constexpr int L_CAND = 4096;
constexpr int L_WO = 16384;
constexpr int L_P = 16384, L_PK = 7 * 16384;
constexpr int L_RED = 65536;
constexpr int wo_slot(int j) { return L_WO + (j % 9) * 16384; }
constexpr int CODE_LINES = 12;
#define OW_ 8
constexpr int OW = OW_;
#define W1A_ 16
constexpr int W1A = W1A_;
#define PRIO_M 1
#define PRIO_S 3
#define GSCHED 5, 9, 13, 17, 21, 24, 27, 30, 33, 36, 39, 42, 45, 48, 48
constexpr int GS[15] = {GSCHED};
constexpr int gops(int g) { return (g / 3) * 10 + (g % 3 == 0 ? 0 : g % 3 == 1 ? 4 : 7); }
constexpr bool gs_ok() {
  bool ok = GS[0] >= 3 && GS[0] <= 9 && GS[1] >= 6 && GS[1] <= 12 && GS[0] <= GS[1] && GS[12] == 45 && GS[13] == 48 && GS[14] == 48;
  for (int t = 0; t < 12; ++t) ok = ok && GS[t + 2] <= 3 * (t + 5) && GS[t + 2] >= 3 * (t + 3) && GS[t + 2] >= GS[t + 1] + 3;
  return ok;
}
static_assert(gs_ok(), "staging schedule");
#define WSCHED 0, 0, 0, 0, 0, 0, 0, 0, 0, 2, 4, 6, 8
constexpr int WS_[13] = {WSCHED};
#define PACE 1
#define PSLP 0
#define P_KVP 1
#define L_KVP 0
#define P_KVF 1
#define L_KVF 0
#define P_FB 8
#define L_FB 0
#define P_C 14
#define P_D 2
#define L_D 0
#define P_GU 12
#define P_E 10
#define L_E 0
#define SLEEP(n) do { if (n) __builtin_amdgcn_s_sleep(n); } while (0)
#define OUTE 1
#define P2MAP 0
#define TCH0 100
#define OUTWT 0
#define OUTWB 0
#define PFO 1
#define WOD 100
#define WODF 6
#define WODS 0
#define QPOS 1
#define LASTB 14
#define CH0 4
#define CHN 3
#define CPE 1
constexpr int VW = 160;

__global__ __launch_bounds__(512, 2) void fused(const u16* __restrict__ x, const u16* __restrict__ wqkv,
                                                const u16* __restrict__ wo, const u16* __restrict__ w_r,
                                                const u16* __restrict__ wg, const u16* __restrict__ wu,
                                                const u16* __restrict__ wd, u16* __restrict__ out, char* __restrict__ ws) {
  extern __shared__ __attribute__((aligned(16))) char smem[];
  STAMPT(0, 0, 0);
  u64 pc0; asm volatile("s_getpc_b64 %0" : "=s"(pc0));
  const int wave = __builtin_amdgcn_readfirstlane(threadIdx.x >> 6);
  const unsigned lds0 = (unsigned)(size_t)smem;
  if (wave == 0) {
    const int l = threadIdx.x;
    asm volatile("s_mov_b32 m0, %1\n\ts_nop 0\n\tglobal_load_lds_dword %0, off"
                 :: "v"((const char*)(pc0 - (pc0 & 127)) + ((blockIdx.x >> 3) + (l < CODE_LINES ? l : 0) * 32) * 128), "s"(__builtin_amdgcn_readfirstlane(lds0 + 3 * K1_STAGE)) : "memory");
  }
#define RB_(b) (((b) & 1) * 8 + (((b) >> 3) & 7))
#define HP_(b) ((((b) & 7) >> 1) * 4 + ((b) >> 6))
#define KROT_(b) ((4 * ((HP_(b) >> 2) + (RB_(b) >> 3) + 2 * ((RB_(b) >> 2) & 1))) & 15)
  unsigned voA0 = 0, voA1 = 0, voA2 = 0, voA3 = 0, voB0 = 0, voB1 = 0, voB2 = 0, voB3 = 0, voB4 = 0, voB5 = 0;
  const unsigned l4 = __builtin_amdgcn_readfirstlane(lds0 + (wave - 4) * 4096), l6 = __builtin_amdgcn_readfirstlane(lds0 + (wave - 4) * 6144);
  const int krot0 = KROT_(blockIdx.x);
  auto group = [&](auto G) {
    constexpr int t = G.v / 3, p = G.v % 3, sl = (t & 3) * K1_STAGE;
    const int kt = (t + krot0) & 15;
    if (p == 0) glds4r<sl>(voA0, voA1, voA2, voA3, (const char*)x + kt * 128, l4);
    else if (p == 1) glds3r<sl + 16384>(voB0, voB1, voB2, (const char*)wqkv + kt * 64 * (3 * D_ * 2), l6);
    else glds3r<sl + 16384 + 3072>(voB3, voB4, voB5, (const char*)wqkv + kt * 64 * (3 * D_ * 2), l6);
  };
  if (wave >= 4) {
    const int lane = threadIdx.x & 63, sw = wave - 4;
    const int r0 = RB_(blockIdx.x) * 128 + 32 * sw + (lane >> 3);
    voA0 = (unsigned)(r0 * D_ + ((lane & 7) ^ (lane >> 4)) * 8) * 2u;
    voA1 = ((voA0 + 8 * D_ * 2) ^ 64) - 1024; voA2 = voA0 + 16 * D_ * 2 - 2048; voA3 = voA1 + 16 * D_ * 2 - 2048;
    voB0 = (unsigned)((16 * sw + 4 * (lane >> 5) + ((lane >> 2) & 3)) * (3 * D_) + HP_(blockIdx.x) * 64 + ((lane >> 4) & 1) * 32 + (lane & 3) * 8) * 2u;
    voB1 = voB0 + 1024; voB2 = voB0 + 2048;
    voB3 = (voB0 ^ 32) + 8 * 3 * D_ * 2; voB4 = voB3 + 1024; voB5 = voB3 + 2048;
    asm volatile("" : "+v"(voA0), "+v"(voA1), "+v"(voA2), "+v"(voA3), "+v"(voB0), "+v"(voB1), "+v"(voB2), "+v"(voB3), "+v"(voB4), "+v"(voB5));
    STAMPT(1, 7, 256);
    sfor<0, 3>(group);
    STAMPT(1, 8, 256);
  }
  int bid = blockIdx.x; asm volatile("" : "+s"(bid));
  const int xcd = bid & 7, idx = bid >> 3;
  const int rb = RB_(bid), hp = HP_(bid);
  const int rp = xcd * 4 + (idx >> 3), cb = idx & 7;
#define WROT (4 * rp + (xcd >> 1))
#define WDIR ((xcd & 1) ? -1 : 1)
  const int hs_ = (hp + 4 + (int)(((size_t)wqkv >> 7) & 7)) & 7;
  const int g8 = ((hp >> 3) * 4 + (rb >> 2)) * 8 + hs_, kq = rb & 3;
  const int krot = KROT_(bid), posA = 1 + (hp & 3), posB = 1 + (rb & 3);
  const int wrot = WROT, wdir = WDIR;
  auto whead = [&](int t) { return (wrot + wdir * t) & 15; };
  const unsigned rs_slot = WS_RS + (rb * 16 + ((krot + posA) & 15)) * 1024, cs_slot = WS_CS + (hp * 16 + ((krot + posB) & 15)) * 768;
  const __amdgpu_buffer_rsrc_t wsr = __builtin_amdgcn_make_buffer_rsrc(ws, 0, WS_END, 0x00020000);
#define LDW(off) __builtin_amdgcn_raw_buffer_load_b128(wsr, (off), 0, 16)
#define STW(v, off) __builtin_amdgcn_raw_buffer_store_b128((v), wsr, (off), 0, 16)
#define STW1(v, off) __builtin_amdgcn_raw_buffer_store_b32((v), wsr, (off), 0, 16)
  float* stm = (float*)(smem + L_STM);
  volatile LAS int* misc = (volatile LAS int*)(smem + L_MISC);
#define LANES() int tid = threadIdx.x; asm volatile("" : "+v"(tid)); const int lane = tid & 63, r = lane & 31, h = lane >> 5; (void)r; (void)h
#define EPI() const int erow = tid >> 3, eseg = tid & 7; const size_t ego = (size_t)(rp * 64 + erow) * D_ + cb * 128 + eseg * 8
  u32x4 xv[2], wv[2];
  f32x2 xf[8], wf2[8];
  float sw_;
  auto unpack_xw = [&]() {
    f32x2 a = {0.f, 0.f};
#pragma unroll
    for (int i = 0; i < 8; ++i) { xf[i] = unpk(xv[i >> 2][i & 3]); wf2[i] = unpk(wv[i >> 2][i & 3]); a += wf2[i]; }
    sw_ = oct_sum(a[0] + a[1]);
#pragma unroll
    for (int i = 0; i < 8; ++i) asm volatile("" : "+v"(xf[i]), "+v"(wf2[i]));
    asm volatile("" : "+v"(sw_));
  };
  unsigned tsv = 0; (void)tsv;
  u32x4 wf[24];

  auto wtr = [&](auto I) {
    constexpr int m = I.v >> 3, c = I.v & 7, R = VW + 32 * m + (c >> 1);
    const unsigned sel = (c & 1) ? 0x07060302u : 0x05040100u;
    wf[I.v] = (u32x4){wperm<R, R + 4>(sel), wperm<R + 8, R + 12>(sel), wperm<R + 16, R + 20>(sel), wperm<R + 24, R + 28>(sel)};
  };

  if (wave < 4) {
    LANES();
    const int c16 = lane & 15, g4 = lane >> 4;
    const int trl = (c16 >> 2) * 64 + (c16 & 3) * 8;
    const int wr = wave >> 1, wc = wave & 1;
    const int head = 2 * hp + wc;
    const unsigned kvp_off = WS_KVP + (head * 16 + rb) * 4096 + wr * 2048 + lane * 16;
    const unsigned zp_off = WS_ZP + (head * 16 + rb) * 128;
    const unsigned o_off = WS_O + ((rb * 4 + 2 * wr) * 32 + head) * 2048 + lane * 16;
    const unsigned fb_off = lane < 8 ? WS_FB + ((rb * 2 + wr) * 32 + head) * 128 + lane * 16 : OOB;
    unsigned pfa, pfb = OOB;
    unsigned pfc = lane < 8 ? fb_off : lane < 24 ? rs_slot + wave * 256 + (lane - 8) * 16
        : lane < 40 ? (wave < 3 ? cs_slot + wave * 256 : WS_KVF + hp * 4096 + rb * 256) + (lane - 24) * 16
        : lane < 48 ? (wave < 2 ? zp_off + (lane & 7) * 16 : (wave == 2 && rb < 2) ? WS_ZF + (2 * hp + rb) * 128 + (lane & 7) * 16 : OOB) : OOB;
    if (wave == 0) pfa = WS_ST2 + (rp * 8 + cb) * 1024 + lane * 16;
    else if (wave == 1) pfa = WS_GU + (g8 * 4 + kq) * 1024 + lane * 16;
    else pfa = WS_YP + (((wave - 2) * 8 + 2 * kq + (lane >> 5)) * 64 + g8) * 512 + (lane & 31) * 16;
    if (wave == 0) pfb = lane < 32 ? WS_XS + (rp * 2 + (lane >> 4)) * 2048 + cb * 256 + (lane & 15) * 16 : lane < 40 ? WS_FD + (cb * 32 + rp) * 128 + (lane & 7) * 16 : OOB;
    u32x4 S4 = {SENT, SENT, SENT, SENT};
    asm volatile("" : "+v"(pfa), "+v"(pfb), "+v"(pfc), "+v"(S4));
    __builtin_amdgcn_s_setprio(PRIO_M);
    f32x4 acc[4][6];
    int ao[2][2], bo[2][2];
    {
#pragma unroll
      for (int i = 0; i < 2; ++i) {
        ao[0][i] = (64 * wr + c16) * 128 + (((4 * i + g4) ^ (c16 >> 1)) * 16); ao[1][i] = ao[0][i] + 2 * K1_STAGE;
        bo[0][i] = 16384 + g4 * 3072 + wc * 256 + trl + ((i ^ (g4 & 1)) * 32); bo[1][i] = bo[0][i] + 2 * K1_STAGE;
        asm volatile("" : "+v"(ao[0][i]), "+v"(ao[1][i]), "+v"(bo[0][i]), "+v"(bo[1][i]));
      }
#pragma unroll
      for (int i = 0; i < 4; ++i)
#pragma unroll
        for (int j = 0; j < 6; ++j) { acc[i][j] = (f32x4){0.f, 0.f, 0.f, 0.f}; asm volatile("" : "+v"(acc[i][j])); }
      struct Frag { u32x4 a[4]; u32x2 bl[6], bh[6]; };
      Frag FA, FB;
#define K1_B(j) ((u32x4){C.bl[j][0], C.bl[j][1], C.bh[j][0], C.bh[j][1]})
#define K1_LA(i) N.a[i] = *(const u32x4*)(smem + ao[sn >> 1][hf] + (sn & 1) * K1_STAGE + (i) * 2048)
#define K1_LL(j) N.bl[j] = trread(smem + bo[sn >> 1][(j) & 1] + (sn & 1) * K1_STAGE + hf * 12288 + ((j) >> 1) * 1024)
#define K1_LH(j) N.bh[j] = trread(smem + bo[sn >> 1][(j) & 1] + (sn & 1) * K1_STAGE + hf * 12288 + ((j) >> 1) * 1024 + 512)
#define K1_M(i, j) if ((j) < 2) mfma16i(K1_B(j), C.a[i], acc[i][j]); else mfma16i(C.a[i], K1_B(j), acc[i][j]); SB0()
#define K1_Q(i, j, ld) K1_M(i, j); ld; SB0()
      auto sub = [&](Frag& C, Frag& N, int sn, int hf) {
        K1_Q(0, 0, K1_LL(0)); K1_Q(1, 0, K1_LH(0)); K1_Q(0, 1, K1_LA(0)); K1_Q(1, 1, K1_LA(1)); K1_Q(0, 2, K1_LL(1)); K1_Q(1, 2, K1_LH(1));
        K1_Q(0, 3, K1_LL(2)); K1_Q(1, 3, K1_LH(2)); K1_Q(2, 0, K1_LL(3)); K1_Q(3, 0, K1_LH(3)); K1_Q(2, 1, K1_LA(2)); K1_Q(3, 1, K1_LA(3));
        K1_Q(2, 2, K1_LL(4)); K1_Q(3, 2, K1_LH(4)); K1_Q(2, 3, K1_LL(5)); K1_Q(3, 3, K1_LH(5));
        K1_M(0, 4); K1_M(1, 4); K1_M(2, 4); K1_M(3, 4); K1_M(0, 5); K1_M(1, 5); K1_M(2, 5);
        asm volatile("s_waitcnt lgkmcnt(0)" : "+v"(N.bh[5]));
        K1_M(3, 5);
      };
      auto subkv = [&](Frag& C, Frag& N, int sn, int hf, bool ld) {
#define K1_QL(i, j, l) K1_M(i, j); if (ld) { l; } SB0()
        K1_QL(0, 2, K1_LL(2)); K1_QL(1, 2, K1_LH(2)); K1_QL(0, 3, K1_LA(0)); K1_QL(1, 3, K1_LA(1)); K1_QL(2, 2, K1_LL(3)); K1_QL(3, 2, K1_LH(3));
        K1_QL(2, 3, K1_LA(2)); K1_QL(3, 3, K1_LA(3)); K1_QL(0, 4, K1_LL(4)); K1_QL(1, 4, K1_LH(4)); K1_QL(2, 4, K1_LL(5)); K1_QL(3, 4, K1_LH(5));
        K1_M(0, 5); K1_M(1, 5); K1_M(2, 5); K1_M(3, 5);
      };
      WAIT_V(0);
      RAW_BAR();
      STAMPT(0, 1, 0);
      {
        Frag& N = FA; const int sn = 0, hf = 0;
        K1_LL(0); K1_LH(0); K1_LA(0); K1_LA(1); K1_LL(1); K1_LH(1); K1_LL(2); K1_LH(2); K1_LL(3); K1_LH(3); K1_LA(2); K1_LA(3); K1_LL(4); K1_LH(4); K1_LL(5); K1_LH(5);
      }
#pragma unroll 1
      for (int it = 0; it < 4; ++it) {
#pragma unroll
        for (int s = 0; s < 4; ++s) {
          if (s == 2 && it == 3) break;
          sub(FA, FB, s, 1);
          if (s == 0 || (PFO == 0 && s == 3)) WAIT_V(0);
          if (it == 0) { if (s == PFO) STW(S4, pfc); if (s == PFO + 1) STW(S4, kvp_off); if (s == PFO + 2) STW(S4, kvp_off + 1024); }
          if (it == 1) { if (s == 0) STW(S4, pfa); if (s == 1) STW(S4, pfb); }
          TSL(it * 4 + s);
          if (LASTB == 13 || s != 1 || it < 3) RAW_BAR();
          sub(FB, FA, (s + 1) & 3, 0);
        }
      }
      subkv(FA, FB, 2, 1, true);
      TSL(14);
      if (LASTB == 14) RAW_BAR();
      subkv(FB, FA, 3, 0, true);
      subkv(FA, FB, 3, 1, true);
      TSL(15);
      subkv(FB, FA, 0, 0, false);
    }
    asm volatile("s_nop 15" ::: "memory");
    TSL(16); if (wave == 0) TSDUMP(0);
    STAMPT(0, 2, 0);
    {
      f32x4 m4[4], i4[4]; float cs[4];
      SPIN {
        const int ok = misc[12 + (lane & 3)];
        asm volatile("" ::: "memory");
#pragma unroll
        for (int i = 0; i < 4; ++i) {
          cs[i] = stm[256 + wc * 96 + 32 + 16 * i + c16];
          m4[i] = *(const f32x4*)(stm + 64 * wr + 16 * i + 4 * g4); i4[i] = *(const f32x4*)(stm + 128 + 64 * wr + 16 * i + 4 * g4);
        }
        SB0();
        if (__all(ok != 0)) break;
      }
      const u32x4 ONES4 = {0x3F803F80u, 0x3F803F80u, 0x3F803F80u, 0x3F803F80u};
      f32x2 tk[16], tv[16];
#define PR(v, i) ((f32x2){(v)[2 * (i)], (v)[2 * (i) + 1]})
#pragma unroll
      for (int i = 0; i < 16; ++i) {
        tk[i] = PR(acc[(i >> 1) & 3][2 + (i >> 3)], i & 1) - PR(m4[(i >> 1) & 3], i & 1) * cs[i >> 3];
        tv[i] = PR(acc[(i >> 1) & 3][4 + (i >> 3)], i & 1) - PR(m4[(i >> 1) & 3], i & 1) * cs[2 + (i >> 3)];
      }
      SB0();
#pragma unroll
      for (int i = 0; i < 16; ++i) { tk[i] *= PR(i4[(i >> 1) & 3], i & 1); tv[i] *= PR(i4[(i >> 1) & 3], i & 1); }
      SB0();
      unsigned kp[16], vp[16];
#pragma unroll
      for (int i = 0; i < 16; ++i) vp[i] = pack2o(tv[i][0], tv[i][1]);
      phi_n<16>(tk, kp);
#define F4_(p, i) ((u32x4){(p)[4 * (i)], (p)[4 * (i) + 1], (p)[4 * (i) + 2], (p)[4 * (i) + 3]})
      const f32x4 Z4 = {0.f, 0.f, 0.f, 0.f};
      u32x4 vk[2], vg[2];
#pragma unroll
      for (int j = 0; j < 2; ++j) { vk[j] = wr ? F4_(vp, 2 + j) : F4_(vp, j); vg[j] = wr ? F4_(vp, j) : F4_(vp, 2 + j); }
      f32x4 keep[2], give[2], zq[2];
#pragma unroll
      for (int d = 0; d < 2; ++d) { give[d] = mfma16(F4_(kp, 2 * d), vg[0], Z4); zq[d] = mfma16(ONES4, F4_(kp, 2 * d), Z4); }
#pragma unroll
      for (int d = 0; d < 2; ++d) { give[d] = mfma16(F4_(kp, 2 * d + 1), vg[1], give[d]); zq[d] = mfma16(ONES4, F4_(kp, 2 * d + 1), zq[d]); }
#pragma unroll
      for (int d = 0; d < 2; ++d) keep[d] = mfma16(F4_(kp, 2 * d), vk[0], Z4);
#pragma unroll
      for (int d = 0; d < 2; ++d) keep[d] = mfma16(F4_(kp, 2 * d + 1), vk[1], keep[d]);
      SB0();
      const float zv = (lane & 16) ? zq[1][0] : zq[0][0];
      char* xk = smem + L_XK + wave * 2304 + lane * 16;
      const char* xp = smem + L_XK + (wave ^ 2) * 2304 + lane * 16;
      *(f32x4*)xk = give[0]; *(f32x4*)(xk + 1024) = give[1]; *(float*)(xk + 2048 - lane * 12) = zv;
      asm volatile("" ::: "memory");
      misc[16 + wave] = 1;
      f32x4 p0, p1; float pz;
      SPIN {
        const int ok = misc[16 + (wave ^ 2)];
        asm volatile("" ::: "memory");
        p0 = *(const f32x4*)xp; p1 = *(const f32x4*)(xp + 1024); pz = *(const float*)(xp + 2048 - lane * 12);
        SB0();
        if (ok == 1) break;
      }
      STW(sane(__builtin_bit_cast(u32x4, keep[0] + p0)), kvp_off);
      STW(sane(__builtin_bit_cast(u32x4, keep[1] + p1)), kvp_off + 1024);
      STW1(sane1(__float_as_uint(zv + pz)), (lane < 32 && wr == 0) ? zp_off + lane * 4 : OOB);
    }
    STAMPT(0, 3, 0);
    {
      u32x4 qa[4][4]; u32x2 ql[4][2], qh[4][2];
#pragma unroll
      for (int u = 0; u < 4; ++u) {
#pragma unroll
        for (int j = 0; j < 2; ++j) {
          ql[u][j] = trread(smem + bo[1][j] + (u >> 1) * K1_STAGE + (u & 1) * 12288);
          qh[u][j] = trread(smem + bo[1][j] + (u >> 1) * K1_STAGE + (u & 1) * 12288 + 512);
        }
#pragma unroll
        for (int i = 0; i < 4; ++i) qa[u][i] = *(const u32x4*)(smem + ao[1][u & 1] + (u >> 1) * K1_STAGE + i * 2048);
      }
      asm volatile("" ::: "memory");
      misc[8 + wave] = 3;
      SB0();
#pragma unroll
      for (int u = 0; u < 4; ++u)
#pragma unroll
        for (int j = 0; j < 2; ++j)
#pragma unroll
          for (int i = 0; i < 4; ++i) acc[i][j] = mfma16((u32x4){ql[u][j][0], ql[u][j][1], qh[u][j][0], qh[u][j][1]}, qa[u][i], acc[i][j]);
      SB0();
    }
    unsigned qp[16];
    Phi<16> qs;
    const unsigned po = WS_KVP + ((2 * hp + (rb >> 3)) * 16 + h) * 4096 + (2 * ((rb >> 2) & 1) + ((lane >> 4) & 1)) * 1024 + (16 * (rb & 3) + (lane & 15)) * 16;
    u32x4 pv[8] = {};
    auto qepi_a = [&]() {
      f32x4 c4[2]; float m_b[4], rinv[4];
#pragma unroll
      for (int i = 0; i < 2; ++i) c4[i] = *(const f32x4*)(stm + 256 + wc * 96 + 16 * i + 4 * g4);
#pragma unroll
      for (int i = 0; i < 4; ++i) { m_b[i] = stm[64 * wr + 16 * i + c16]; rinv[i] = stm[128 + 64 * wr + 16 * i + c16]; }
      SB0();
      f32x2 tq[16];
#pragma unroll
      for (int i = 0; i < 16; ++i) tq[i] = PR(acc[i >> 2][(i >> 1) & 1], i & 1) - PR(c4[(i >> 1) & 1], i & 1) * m_b[i >> 2];
      SB0();
#pragma unroll
      for (int i = 0; i < 16; ++i) tq[i] *= rinv[i >> 2];
      SB0();
      phi_a<16>(tq, qs);
#pragma unroll
      for (int i = 0; i < 16; ++i) asm volatile("" : "+v"(qs.tb[i]), "+v"(qs.tr[i]), "+v"(qs.e[i]));
    };
    if (QPOS) qepi_a();
    if (wave == 0) {
      SLEEP(P_KVP);
#pragma unroll
      for (int p = 0; p < 8; ++p) pv[p] = LDW(po + p * 8192);
    }
    asm volatile("" ::: "memory");
    if (!QPOS) qepi_a();
    STAMPT(0, 4, 0);
    if (wave == 1 && rb < 2) {
      SLEEP(P_KVP);
      SPIN {
        unsigned m = 0;
#pragma unroll
        for (int p = 0; p < 2; ++p) { pv[p] = LDW(WS_ZP + (2 * hp + rb) * 2048 + p * 1024 + lane * 16); m = chk(max(max(m, pv[p][0]), pv[p][2]), pv[p]); }
        if (wave_ok(m)) break;
        asm volatile("" ::: "memory");
      }
      f32x4 s = __builtin_bit_cast(f32x4, pv[0]) + __builtin_bit_cast(f32x4, pv[1]);
#pragma unroll
      for (int i = 0; i < 4; ++i) { s[i] += dppf<DPP_ROR8>(s[i]); s[i] = xh_sum(x16_sum(s[i])); }
      if (lane < 16) st64(ws, WS_ZF + (2 * hp + rb) * 128 + (lane >> 3) * 64 + (lane & 3) * 16 + ((lane >> 2) & 1) * 8,
                          ((u64)sane1(pack2(s[2], s[3])) << 32) | pack2(s[0], s[1]));
    }
    if (wave == 0) {
      SPIN {
        unsigned m = 0;
#pragma unroll
        for (int p = 0; p < 8; ++p) m = chk(m, pv[p]);
        CNT(0);
        if (wave_ok(m)) break;
        asm volatile("" ::: "memory");
        SLEEP(L_KVP);
#pragma unroll
        for (int p = 0; p < 8; ++p) pv[p] = LDW(po + p * 8192);
      }
      f32x4 s = __builtin_bit_cast(f32x4, pv[0]);
#pragma unroll
      for (int p = 1; p < 8; ++p) s += __builtin_bit_cast(f32x4, pv[p]);
      const unsigned p0 = pack2(xh_sum(s[0]), xh_sum(s[1])), p1 = pack2(xh_sum(s[2]), xh_sum(s[3]));
      auto a = __builtin_amdgcn_permlane16_swap(p0, p0, false, false);
      auto b = __builtin_amdgcn_permlane16_swap(p1, p1, false, false);
      STW(sane((u32x4){a[0], b[0], a[1], b[1]}), lane < 16 ? WS_KVF + hp * 4096 + rb * 256 + lane * 16 : OOB);
      STAMPT(1, 2, 0);
    }
    phi_b<16>(qs, qp);
#pragma unroll
    for (int i = 0; i < 16; ++i) asm volatile("" : "+v"(qp[i]));
    if (wave == 0) misc[7] = 1;
    else SPIN { if (misc[7] == 1) break; __builtin_amdgcn_s_sleep(1); }
    SLEEP(P_KVF);
    {
      u32x4 k0, k1, z0;
      SPIN {
        k0 = LDW(WS_KVF + head * 2048 + lane * 16); k1 = LDW(WS_KVF + head * 2048 + 1024 + lane * 16);
        z0 = LDW(WS_ZF + head * 128 + g4 * 16);
        CNT(1);
        if (wave_ok(chk(chk(chk(0u, k0), k1), z0))) break;
        asm volatile("" ::: "memory");
        SLEEP(L_KVF);
      }
      STAMPT(0, 5, 0);
      const f32x4 Z4 = {0.f, 0.f, 0.f, 0.f};
      f32x4 X[8], Dn[4];
#pragma unroll
      for (int i = 0; i < 4; ++i) Dn[i] = mfma16(z0, F4_(qp, i), Z4);
#pragma unroll
      for (int i = 0; i < 8; ++i) X[i] = mfma16((i & 1) ? k1 : k0, F4_(qp, i >> 1), Z4);
      SB0();
      float ri[4]; unsigned a[16]; f32x2 f[16];
#pragma unroll
      for (int i = 0; i < 4; ++i) ri[i] = __builtin_amdgcn_rcpf(rbf(rbf(Dn[i][0]) + 1e-6f));
      SB0();
#pragma unroll
      for (int i = 0; i < 16; ++i) a[i] = pack2o(X[i >> 1][2 * (i & 1)], X[i >> 1][2 * (i & 1) + 1]);
      SB0();
#pragma unroll
      for (int i = 0; i < 16; ++i) f[i] = unpk(a[i]);
      SB0();
#pragma unroll
      for (int i = 0; i < 16; ++i) f[i] *= ri[i >> 2];
      SB0();
#pragma unroll
      for (int i = 0; i < 4; ++i) {
        STW(((u32x4){pack2(f[4 * i][0], f[4 * i][1]), pack2(f[4 * i + 1][0], f[4 * i + 1][1]), pack2(f[4 * i + 2][0], f[4 * i + 2][1]), pack2(f[4 * i + 3][0], f[4 * i + 3][1])}),
            o_off + (i >> 1) * 65536 + (i & 1) * 1024);
        SB0();
      }
    }
    if (wave == 0) misc[6] = 1;
    STAMPT(0, 6, 0);
    {
      EPI();
      const int rt = wave & 1, kh = wave >> 1;
      const unsigned ob = WS_O + ((rp * 2 + rt) * 32 + 16 * kh) * 2048 + lane * 16;
#define LDO(t, st) __builtin_amdgcn_raw_buffer_load_b128(wsr, ob + (st) * 1024, whead(t) * 2048, 16)
      int woff[2];
#pragma unroll
      for (int i = 0; i < 2; ++i) woff[i] = kh * 8192 + (g4 >> 1) * 2048 + (g4 & 1) * 512 + trl + ((i ^ (g4 & 1)) * 32);
      struct Frag2 { u32x2 lo[8], hi[8]; };
      Frag2 G[2];
      auto ld2 = [&](Frag2& N, int sn, int nt, int hi) {
        const char* p = smem + sn + woff[nt & 1] + (nt >> 2) * 1024 + ((nt >> 1) & 1) * 256;
        if (hi) N.hi[nt] = trread(p + 4096); else N.lo[nt] = trread(p);
      };
      WAIT_V(0);
      STW(((u32x4){1u, 1u, 1u, 1u}), fb_off);
      RAW_BAR();
#pragma unroll
      for (int i = 0; i < 16; ++i) ld2(G[0], wo_slot(0), i >> 1, i & 1);
      f32x4 a2[2][8];
#pragma unroll
      for (int i = 0; i < 16; ++i) a2[i & 1][i >> 1] = (f32x4){0.f, 0.f, 0.f, 0.f};
      SLEEP(P_FB);
      SPIN { CNT(2); if (wave_ok(ld32(ws, WS_FB + (rp * 32 + 16 * kh + (lane & 15)) * 128))) break; SLEEP(L_FB); }
      asm volatile("" ::: "memory");
      STAMPT(0, 7, 0);
      char* pr[2];
#pragma unroll
      for (int i = 0; i < 2; ++i) pr[i] = smem + L_P + kh * L_PK + (32 * rt + c16) * 512 + (((4 * i + g4) ^ (c16 & 7)) * 16);
      u32x4 of[16][2];
#pragma unroll
      for (int t = 0; t < OW; ++t) { of[t][0] = LDO(t, 0); of[t][1] = LDO(t, 1); }
      SB0();
      sfor<0, 16>([&](auto T) {
        constexpr int t = T.v;
        Frag2& C = G[t & 1]; Frag2& N = G[(t + 1) & 1];
#pragma unroll
        for (int i = 0; i < 16; ++i) {
          const int nt = i >> 1, st = i & 1;
          a2[st][nt] = mfma16((u32x4){C.lo[nt][0], C.lo[nt][1], C.hi[nt][0], C.hi[nt][1]}, of[t][st], a2[st][nt]);
          SB0();
          if ((t & 1) && t <= 11 && i == 8) {
            asm volatile("s_waitcnt lgkmcnt(8)" ::: "memory"); __builtin_amdgcn_s_barrier(); asm volatile("" ::: "memory");
          }
          if (t < 15) ld2(N, wo_slot(t + 1), nt, st);
          if (t > 0 && t - 1 + OW < 16 && (i == 3 || i == 11)) of[(t - 1 + OW) & 15][i >> 3] = LDO(t - 1 + OW, i >> 3);
          if (t - 1 + OW == 16 && (i == 3 || i == 11)) { xv[i >> 3] = *(const u32x4*)(x + ego + (i >> 3) * 64); wv[i >> 3] = *(const u32x4*)(w_r + cb * 128 + eseg * 8 + (i >> 3) * 64); }
          if (t == 15 && i >= 4) *(f32x4*)(pr[(nt - 2) & 1] + ((nt - 2) >> 1) * 128 + st * 8192) = a2[st][(nt - 2) & 7];
          SB0();
        }
      });
      STAMPT(0, 8, 0);
#pragma unroll
      for (int i = 12; i < 16; ++i) *(f32x4*)(pr[(i >> 1) & 1] + (i >> 2) * 128 + (i & 1) * 8192) = a2[i & 1][i >> 1];
    }
    unpack_xw();
    __builtin_amdgcn_s_setprio(0);
  } else {
    LANES();
    const int sw = wave - 4, t2 = tid - 256;
    __builtin_amdgcn_s_setprio(PRIO_S);
    const bool real = hs_ != 7;
    unsigned hs_g0 = 0, hs_cur = 0;
    u32x4 ms_exp = {0, 0, 0, 0};
    bool hs_act = true, ms_act = !real && hp < 8 && rb == 0 && sw == 1;
    const unsigned hs_off = (lane == 1 ? SY_GO : SY_CH) + bid * 4;
    const unsigned re_off = t2 == 0 ? SY_RE + bid * 4 : OOB;
    const unsigned ms_off = lane * 16;
    const char* wsu = uni(ws);
#define HS_PROBE() asm volatile("s_nop 4\n\tglobal_load_dword v156, %0, %1 sc1" :: "v"(hs_off), "s"(wsu) : "memory", RSV)
#define MS_PROBE() asm volatile("s_nop 4\n\tglobal_load_dwordx4 v[152:155], %0, %1 offset:2048 sc1" :: "v"(ms_off), "s"(wsu) : "memory", RSV)
    auto hs_step = [&](bool first) {
      if (hs_act) {
        const unsigned hv_ = rget<156>();
        hs_act = __builtin_amdgcn_readlane(hv_, 1) == hs_g0;
        const unsigned c = __builtin_amdgcn_readlane(hv_, 0);
        if (sw == 0 && (first || c != hs_cur)) STW1(hv_, re_off);
        hs_cur = c;
        HS_PROBE();
      }
      if (ms_act) {
        const unsigned d = ((rget<152>() ^ ms_exp[0]) | (rget<153>() ^ ms_exp[1])) | ((rget<154>() ^ ms_exp[2]) | (rget<155>() ^ ms_exp[3]));
        ms_act = __any(d != 0u);
        STW(ms_exp, ms_act ? OOB : SY_GO + ms_off);
        MS_PROBE();
      }
    };
    HS_PROBE();
    if (ms_act) {
      MS_PROBE();
      asm volatile("s_nop 4\n\tglobal_load_dwordx4 v[148:151], %0, %1 offset:1024 sc1" :: "v"(ms_off), "s"(wsu) : "memory", RSV);
    }
    const int gr = (g8 >> 3) * 7 + min(hs_, 6);
    const unsigned voG = (unsigned)((lane >> 3) * 8 * F_ + (lane & 7) * 8) * 2u, voD = (unsigned)((lane >> 3) * 8 * D_ + (lane & 7) * 8) * 2u;
    const size_t gofs = ((size_t)(kq * 256 + sw * 64) * F_ + gr * 64) * 2;
    const char* pG = (const char*)wg + gofs;
    const char* pU = (const char*)wu + gofs;
    const char* pD = (const char*)(wd + (size_t)(gr * 64) * D_ + kq * 256 + sw * 64);
    auto wstep = [&](auto I) {
      constexpr int i = I.v;
      if (i < 8) wload<VW + 4 * i>(voG, pG + (i & 7) * (F_ * 2));
      else if (i < 16) wload<VW + 4 * i>(voG, pU + (i & 7) * (F_ * 2));
      else wload2<VW + 4 * i>(voD, pD + (i & 7) * (D_ * 2));
    };
    unsigned voW;
    {
      const int bi = lane >> 4, q = (lane >> 2) & 3, ch = lane & 3;
      voW = (unsigned)((4 * (bi >> 1) + q) * D_ + (bi & 1) * 32 + (ch ^ (bi & 2)) * 8) * 2u;
    }
    const char* baseW = (const char*)(wo + (size_t)((16 * (sw >> 1)) * 32 + 16 * (sw & 1)) * D_ + cb * 128) - 1024;
    auto stageW = [&](auto J, bool rsv) {
      const char* s_ = baseW + (size_t)whead(J.v) * (32 * D_ * 2);
      if (rsv) glds4o<wo_slot(J.v)>(voW + 1024, voW + 128, voW + 8 * D_ * 2 - 1024, voW + 8 * D_ * 2 + 128 - 2048, s_, l4);
      else glds4<wo_slot(J.v)>(voW + 1024, voW + 128, voW + 8 * D_ * 2 - 1024, voW + 8 * D_ * 2 + 128 - 2048, s_, l4);
    };
    int sta[4], stb[3];
    {
      const int row = t2 >> 1, key = (row >> 1) & 7;
#pragma unroll
      for (int c = 0; c < 4; ++c) { sta[c] = (posA & 3) * K1_STAGE + row * 128 + (((4 * (t2 & 1) + c) ^ key) * 16); asm volatile("" : "+v"(sta[c]) :: RSV); }
#pragma unroll
      for (int i = 0; i < 3; ++i) {
        const int G = sw * 3 + i, hd = G >= 6 ? 1 : 0, mat = (G - 6 * hd) >> 1;
        stb[i] = (posB & 3) * K1_STAGE + 16384 + (lane >> 4) * 6144 + mat * 1024 + hd * 256 + ((lane & 15) >> 2) * 64 + (G & 1) * 32 + (lane & 3) * 8;
        asm volatile("" : "+v"(stb[i]) :: RSV);
      }
    }
    unsigned rs_dst = rs_slot + t2 * 4, cs_dst = lane < 48 ? cs_slot + (sw * 48 + lane) * 4 : OOB;
    unsigned rs_off = WS_RS + rb * 16384 + (lane >> 4) * 1024 + sw * 256 + (lane & 15) * 16;
    unsigned cs_off = WS_CS + hp * 12288 + (lane >> 4) * 768 + sw * 192 + min(lane & 15, 11) * 16;
    asm volatile("" : "+v"(rs_dst), "+v"(cs_dst), "+v"(rs_off), "+v"(cs_off) :: RSV);
    int stm_dst = ((lane >> 5) * 128 + sw * 32 + (lane & 15) * 2 + ((lane >> 4) & 1)) * 4, cs_put = (lane & 15) < 12 ? (256 + sw * 48 + (lane & 15) * 4 + (lane >> 4)) * 4 : 1792 + lane * 4;
    asm volatile("" : "+v"(stm_dst), "+v"(cs_put) :: RSV);
    auto st_load = [&]() { sfor<0, 4>([&](auto I) { sload<224 + 4 * I.v>(rs_off, wsu + I.v * 4096); sload<240 + 4 * I.v>(cs_off, wsu + I.v * 3072); }); };
    sfor<3, GS[0]>(group);
    WAIT_VR(gops(GS[0]) - 10 + 1); RAW_BARR();
    TSL(14);
    sfor<GS[0], GS[1]>(group);
    WAIT_VR(gops(GS[1]) - 20); RAW_BARR();
    hs_g0 = __builtin_amdgcn_readlane(rget<156>(), 1);
    if (ms_act) {
      const u32x4 re = {rget<152>(), rget<153>(), rget<154>(), rget<155>()}, g0 = {rget<148>(), rget<149>(), rget<150>(), rget<151>()};
#pragma unroll
      for (int j = 0; j < 4; ++j) { const unsigned c = re[j] + 1u; ms_exp[j] = (c == g0[j]) ? c + 1u : c; }
      STW(ms_exp, SY_CH + ms_off);
    }
    u32x4 ra[4] = {}; u32x2 rc[3][4] = {};
    float sa = 0.f, sb = 0.f, qa = 0.f, qb = 0.f, ca[3] = {0.f, 0.f, 0.f}, cb_[3] = {0.f, 0.f, 0.f};
    auto chunk = [&](auto C) {
      constexpr int c = C.v;
      if constexpr (c < 4) {
        asm volatile("" : "+v"(ra[c]) :: RSV);
#pragma unroll
        for (int i = 0; i < 4; ++i) { const float lo = bflo(ra[c][i]), hi = bfhi(ra[c][i]); sa = fadd(sa, lo); sb = fadd(sb, hi); qa = fsq(lo, qa); qb = fsq(hi, qb); }
        asm volatile("" : "+v"(sa), "+v"(sb), "+v"(qa), "+v"(qb) :: RSV);
      } else if constexpr (c == 4) {
        float s = fadd(sa, sb), sq = fadd(qa, qb);
        s = fadd(s, dppf<DPP_X1>(s)); sq = fadd(sq, dppf<DPP_X1>(sq));
        STW1(sane1(__float_as_uint((t2 & 1) ? sq : s)), rs_dst);
      } else if constexpr (c < 11) {
        constexpr int i = (c - 5) >> 1, j = ((c - 5) & 1) * 2;
        asm volatile("" : "+v"(rc[i][j]), "+v"(rc[i][j + 1]) :: RSV);
#pragma unroll
        for (int e = 0; e < 4; ++e) { const unsigned u = rc[i][j + (e >> 1)][e & 1]; ca[i] = fadd(ca[i], bflo(u)); cb_[i] = fadd(cb_[i], bfhi(u)); }
        asm volatile("" : "+v"(ca[i]), "+v"(cb_[i]) :: RSV);
      } else {
        const float c2 = fadd(ca[2], cb_[2]);
        STW1(sane1(__float_as_uint(sw32(sw16(fadd(ca[0], cb_[0]), fadd(ca[1], cb_[1])), sw16(c2, c2)))), cs_dst);
      }
    };
    sfor<0, 12>([&](auto T) {
      constexpr int t = T.v;
      TSL(t);
      if (t == 4) STAMPT(1, 9, 256);
      if (t < 4 && posA == t + 1) {
#pragma unroll
        for (int c = 0; c < 4; ++c) ra[c] = *(const u32x4*)(smem + sta[c]);
      }
      if (t < 4 && posB == t + 1) {
#pragma unroll
        for (int i = 0; i < 3; ++i)
#pragma unroll
          for (int j = 0; j < 4; ++j) rc[i][j] = trread(smem + (stb[i] ^ ((j >> 1) * 32)) + (j >> 1) * 3072 + (j & 1) * 512);
      }
      if (t == 1 + PFO) HS_PROBE();
      if (t >= 3 + PFO) hs_step(t == 3 + PFO);
      sfor<GS[t + 1], GS[t + 2]>([&](auto G) {
        group(G);
        constexpr int u = t - CH0, p = G.v - GS[t + 1];
        constexpr int ord[12] = {0, 5, 6, 1, 7, 8, 2, 9, 10, 3, 11, 4};
        if constexpr (u >= 0 && CHN * u + p < 12 && p < CHN) chunk(IC<ord[(CHN * u + p) % 12]>{});
      });
      if (P2MAP == 0 && t >= TCH0 && t < TCH0 + 8 && sw == ((t - TCH0) & 3))
        asm volatile("s_nop 4\n\tglobal_load_dword v157, %0, %1" :: "v"(lane * 128), "s"(uni((const char*)wo + idx * 65536 + ((t - TCH0 + xcd) & 7) * 8192)) : "memory", RSV);
      if (P2MAP == 1 && t >= TCH0 && t < TCH0 + 4 && sw == ((t - TCH0) & 3))
        asm volatile("s_nop 4\n\tglobal_load_dword v157, %0, %1" :: "v"((lane >> 3) * 2048 + (lane & 7) * 128), "s"(uni((const char*)wo + idx * 65536 + ((t - TCH0 + (rp >> 3)) & 3) * 16384 + (cb & 4) * 256)) : "memory", RSV);
      if (real) sfor<WS_[t], WS_[t + 1]>(wstep);
      TSL(16 + t);
      WAIT_VR(t < 11 ? gops(GS[t + 2]) - 10 * (t + 3) : gops(GS[13]) - gops(GS[12]));
      TSL(32 + t);
      RAW_BARR();
    });
    TSL(12);
    STAMPT(1, 10, 256);
    hs_step(false);
    auto st_put = [&]() -> bool {
      float a[8]; unsigned m = 0, n = 0;
      rsum4<224>(a, m, n); rsum4<240>(a + 4, m, n);
      const float s1 = sw16(a[0], a[2]), q1 = sw16(a[1], a[3]);
      const float cs = sw32(sw16(a[4], a[5]), sw16(a[6], a[7]));
      const float mean = sw32(s1, s1) * (1.0f / 1024.0f);
      const float var = fmaxf(__builtin_fmaf(sw32(q1, q1), 1.0f / 1024.0f, -(mean * mean)), 0.f);
      const float rinv = __builtin_amdgcn_rcpf(rbf(__builtin_amdgcn_sqrtf(rbf(rbf(var) + 1e-5f))));
      *(float*)(smem + L_STM + stm_dst) = (lane & 32) ? rinv : rbf(mean);
      *(float*)(smem + L_STM + cs_put) = cs;
      return wave_ok(max(m, n));
    };
    st_load();
    if (real) sfor<WS_[12], W1A>(wstep);
    TSL(28);
    if (real) WAIT_VR(8 + W1A - WS_[11]); else WAIT_VR(8);
    if (lane == 0) { misc[8 + sw] = 0; misc[12 + sw] = 0; misc[16 + sw] = 0; misc[7] = 0; misc[6] = 0; }
    TSL(44);
    RAW_BARR();
    TSL(13); TSDUMP(1 + sw);
    STAMPT(1, 0, 256);
    if (real) WAIT_VR(W1A - WS_[12]); else WAIT_VR(0);
    if (!st_put() || hs_act) {
      if (hs_act) hs_act = __builtin_amdgcn_readlane(rget<156>(), 1) == hs_g0;
      if (ms_act) {
        SPIN {
          bool ok = true;
#pragma unroll
          for (int j = 0; j < 4; ++j) ok &= (ld32(ws, SY_RE + lane * 16 + j * 4) == ms_exp[j]);
          if (__all(ok)) break;
        }
        STW(ms_exp, SY_GO + lane * 16);
      }
      if (hs_act) {
        SPIN {
          if (ld32(ws, SY_GO + bid * 4) != hs_g0) break;
          const unsigned c = ld32(ws, SY_CH + bid * 4);
          if (c != hs_cur) { hs_cur = c; STW1(c, re_off); }
        }
      }
      SPIN { st_load(); WAIT_VR(0); if (st_put()) break; }
    }
    asm volatile("" ::: "memory", RSV);
    if (lane == 0) misc[12 + sw] = 1;
    __builtin_amdgcn_s_setprio(0);
    if (real) sfor<W1A, 16>(wstep);
    auto mwait = [&](int v) { SPIN { const int a = misc[8 + (lane & 3)]; if (__all(a >= v)) break; __builtin_amdgcn_s_sleep(1); } asm volatile("" ::: "memory"); };
    sfor<0, 9>([&](auto J) {
      if (J.v == 4) mwait(3);
      if (J.v == WOD) { SPIN { if (misc[WODF] == 1) break; __builtin_amdgcn_s_sleep(2); } SLEEP(WODS); }
      stageW(J, J.v < 2);
      if (J.v >= 1 && J.v < WOD) WAIT_V(4 * PACE);
      if (J.v == 1) { WAIT_VR(8); sfor<0, 16>(wtr); }
      if (J.v >= 2 && PSLP) __builtin_amdgcn_s_sleep(PSLP);
    });
    STAMPT(1, 1, 256);
    EPI();
#pragma unroll
    for (int i = 0; i < 2; ++i) { xv[i] = *(const u32x4*)(x + ego + i * 64); wv[i] = *(const u32x4*)(w_r + cb * 128 + eseg * 8 + i * 64); }
    asm volatile("s_waitcnt vmcnt(0)" : "+v"(xv[0]), "+v"(xv[1]), "+v"(wv[0]), "+v"(wv[1]) :: "memory");
    unpack_xw();
    RAW_BAR();
    RAW_BAR(); stageW(IC<9>{}, false); stageW(IC<10>{}, false);
    RAW_BAR(); stageW(IC<11>{}, false); stageW(IC<12>{}, false);
    RAW_BAR(); stageW(IC<13>{}, false); stageW(IC<14>{}, false);
    WAIT_V(16); RAW_BAR(); stageW(IC<15>{}, false);
    WAIT_V(12); RAW_BAR();
    WAIT_V(0); RAW_BAR();
    if (real) sfor<16, 24>(wstep);
  }
  LANES(); EPI();
  const int grow = rp * 64 + erow;
  int pa0 = L_P + erow * 512 + ((2 * eseg) ^ (erow & 7)) * 16, pa1 = pa0 ^ 16;
  unsigned st_dst = eseg == 0 ? WS_ST2 + ((rp * 8 + cb) * 64 + erow) * 16 : OOB, st_src = WS_ST2 + rp * 8192 + lane * 16;
  unsigned fd_dst = lane < 8 ? WS_FD + (cb * 32 + rp) * 128 + lane * 16 : OOB, kidx = 0xFFFFu - grow;
  asm volatile("" : "+v"(pa0), "+v"(pa1), "+v"(st_dst), "+v"(st_src), "+v"(fd_dst), "+v"(kidx));
  RAW_BAR2();
  STAMPT(0, 9, 0);
  unsigned hb[8];
  f32x2 hv[8];
  {
    f32x4 pp[2][4];
#pragma unroll
    for (int k = 0; k < 2; ++k)
#pragma unroll
      for (int i = 0; i < 4; ++i) pp[k][i] = *(const f32x4*)(smem + ((i & 1) ? pa1 : pa0) + (i >> 1) * 256 + k * L_PK);
    SB0();
    f32x2 s = {0.f, 0.f}, q = {0.f, 0.f}, p2 = {0.f, 0.f};
#pragma unroll
    for (int i = 0; i < 8; ++i) {
      const f32x4 a = pp[0][i >> 1] + pp[1][i >> 1];
      const f32x2 u = xf[i] + unpk(pack2o(a[2 * (i & 1)], a[2 * (i & 1) + 1]));
      hb[i] = pack2o(u[0], u[1]);
      hv[i] = unpk(hb[i]);
      s += hv[i]; q += hv[i] * hv[i]; p2 += hv[i] * wf2[i];
    }
    STW(sane((u32x4){__float_as_uint(oct_sum(s[0] + s[1])), __float_as_uint(oct_sum(q[0] + q[1])), __float_as_uint(oct_sum(p2[0] + p2[1])), __float_as_uint(sw_)}), st_dst);
  }
  KEEP2();
  STAMPT(0, 10, 0);
  const int sw = wave - 4;
  const unsigned fd_src = WS_FD + xcd * 4096 + r * 128;
  u32x2 kpk = {SENT, SENT};
  if (wave == 0) {
    u32x4 pa[8];
    SLEEP(P_C);
    SPIN {
      unsigned m = 0;
#pragma unroll
      for (int j = 0; j < 8; ++j) { pa[j] = LDW(st_src + j * 1024); m = chk(m, pa[j]); }
      CNT(3);
      if (wave_ok(m)) break;
      KEEP2();
    }
    STAMPT(0, 11, 0);
#define F4(v) __builtin_bit_cast(f32x4, v)
    const f32x4 t = ((F4(pa[0]) + F4(pa[1])) + (F4(pa[2]) + F4(pa[3]))) + ((F4(pa[4]) + F4(pa[5])) + (F4(pa[6]) + F4(pa[7])));
    const float mean = t[0] * (1.0f / 1024.0f);
    const float var = fmaxf(__builtin_fmaf(t[1], 1.0f / 1024.0f, -(mean * mean)), 0.f);
    const float mb = rbf(mean), rs = __builtin_amdgcn_rcpf(rbf(__builtin_amdgcn_sqrtf(rbf(rbf(var) + 1e-5f))));
    *(f32x2*)(smem + L_KX + lane * 8) = (f32x2){mb, rs};
    const float sc = fmaxf((t[2] - mb * t[3]) * rs, -INFINITY);
    const int u = (int)(pack2(0.f, sc) & 0xffff0000u);
    unsigned b0 = ((unsigned)(u ^ ((u >> 31) | (int)0x80000000)) & 0xffff0000u) | (0xFFFFu - (rp * 64 + lane)), b1 = 0;
    top2_lo(b0, b1); top2_hi(b0, b1);
    STW(((u32x4){b0, b1, b0, b1}), fd_dst);
    if (lane == 0) *(u32x2*)(smem + L_MISC + 112) = (u32x2){b0, b1};
  } else if (wave >= 4) {
    asm volatile("s_waitcnt vmcnt(0)" ::: "memory", RSV2);
    sfor<16, 24>(wtr);
  }
  RAW_BAR();
  bool cand;
  u32x4 ob[2];
  {
    const f32x2 mr = *(const f32x2*)(smem + L_KX + erow * 8);
    const u32x2 kk = *(const u32x2*)(smem + L_MISC + 112);
    const float mb = mr[0], rs = mr[1];
    const unsigned b0 = kk[0], b1 = kk[1];
    u32x4 nq[2];
#pragma unroll
    for (int i = 0; i < 8; ++i) {
      const f32x2 d = hv[i] - mb;
      const f32x2 n = unpk(pack2o(d[0], d[1])) * rs;
      nq[i >> 2][i & 3] = pack2o(n[0], n[1]);
    }
    const bool c2 = (b1 & 0xffffu) == kidx;
    cand = c2 || (b0 & 0xffffu) == kidx;
    if (cand) {
      const unsigned xo = WS_XS + (rp * 2 + (c2 ? 1 : 0)) * 2048 + cb * 256 + eseg * 16;
      STW(sane(nq[0]), xo); STW(sane(nq[1]), xo + 128);
      char* cp = smem + L_CAND + (c2 ? 512 : 0) + eseg * 16;
      *(u32x4*)cp = (u32x4){hb[0], hb[1], hb[2], hb[3]}; *(u32x4*)(cp + 128) = (u32x4){hb[4], hb[5], hb[6], hb[7]};
      *(u32x4*)(cp + 256) = nq[0]; *(u32x4*)(cp + 384) = nq[1];
    }
    asm volatile("" ::: "memory");
    if (wave >= 4) { SLEEP(P_D); kpk = __builtin_amdgcn_raw_buffer_load_b64(wsr, fd_src, 0, 16); }
    asm volatile("" ::: "memory");
#pragma unroll
    for (int i = 0; i < 8; ++i) { const f32x2 o = hv[i] + unpk(nq[i >> 2][i & 3]); ob[i >> 2][i & 3] = pack2(o[0], o[1]); }
  }
#define OUT_ST() do { *(u32x4*)(out + ego) = ob[0]; *(u32x4*)(out + ego + 64) = ob[1]; } while (0)
  STAMPT(0, 12, 0);
  if (wave < 4) {
    if (!cand) OUT_ST();
    RAW_BAR();
    const unsigned w0 = misc[24], w1 = misc[25];
    if (cand && grow != (int)key_idx(w0) && grow != (int)key_idx(w1)) OUT_ST();
#pragma unroll 1
    for (int tk = 0; tk < 2; ++tk) {
      const unsigned wk = tk ? w1 : w0, slot = misc[26 + tk];
      if ((int)(slot >> 1) != rp) continue;
      u32x4 pv[7];
      const unsigned yo = WS_YP + ((tk * 8 + cb) * 64 + (lane >> 3) * 8) * 512 + wave * 128 + (lane & 7) * 16;
      const int col = (wave >> 1) * 64 + (lane & 7) * 8 + (wave & 1) * 4 + (lane >> 4);
      float sg = rbf(__builtin_amdgcn_rcpf(1.0f + __expf(-key_val(wk))));
      float hh = bflo(*(const u16*)(smem + L_CAND + (slot & 1) * 512 + col * 2)), nn = bflo(*(const u16*)(smem + L_CAND + (slot & 1) * 512 + 256 + col * 2));
      u16* op = out + (size_t)key_idx(wk) * D_ + cb * 128 + col;
      asm volatile("" : "+v"(sg), "+v"(hh), "+v"(nn), "+v"(op));
      if (tk == 0 || (int)(misc[26] >> 1) != rp) {
        SPIN { if (misc[30] == (int)w0) break; __builtin_amdgcn_s_sleep(1); }
        SLEEP(P_E);
      }
      SPIN {
        unsigned m = 0;
#pragma unroll
        for (int p = 0; p < 7; ++p) { pv[p] = LDW(yo + p * 512); m = chk(m, pv[p]); }
        CNT(6);
        if (wave_ok(m)) break;
        asm volatile("" ::: "memory");
        SLEEP(L_E);
      }
      STAMPT(0, 13, 0);
      f32x4 s = __builtin_bit_cast(f32x4, pv[0]);
#pragma unroll
      for (int p = 1; p < 7; ++p) s += __builtin_bit_cast(f32x4, pv[p]);
      float y = sw32(sw16(s[0], s[1]), sw16(s[2], s[3]));
      y += dppf<DPP_ROR8>(y);
      if (!(lane & 8)) *(GAS u16*)op = (u16)pack2(hh + rbf(nn + rbf(rbf(y) * sg)), 0.f);
      STAMPT(0, 14, 0);
    }
  } else {
    unsigned xl = WS_XS + (kq * 256 + sw * 64 + (lane >> 4) * 16 + ((lane >> 2) & 1) * 8) * 2;
    int red_dst = (lane < 8 || (lane >= 24 && lane < 32)) ? L_RED + sw * 2048 + ((lane >> 3) & 1) * 1024 + (lane & 7) * 16 : L_RED + 8192 + lane * 16;
    unsigned gu_src = WS_GU + g8 * 4096 + lane * 16;
    int act_dst = L_ACT + sw * 256 + ((lane & 7) * 8 + (lane >> 3)) * 2, act_src = L_ACT + sw * 256 + (lane & 1) * 128 + (lane >> 4) * 32 + ((lane >> 1) & 1) * 16;
    unsigned yp_dst = lane < 16 ? WS_YP + (((lane >> 3) * 8 + 2 * kq + (sw >> 1)) * 64 + g8) * 512 + (sw & 1) * 256 + (lane & 7) * 16 : OOB;
    asm volatile("" : "+v"(xl), "+v"(red_dst), "+v"(gu_src), "+v"(act_dst), "+v"(act_src), "+v"(yp_dst));
    unsigned w0, w1; int s0 = -1, s1 = -1;
    u32x4 xa = {SENT, SENT, SENT, SENT};
    SPIN {
      CNT(4);
      const bool all = wave_ok(kpk[1]);
      const unsigned k0 = kpk[1] == SENT ? 0u : kpk[0], k1 = kpk[1] == SENT ? 0u : kpk[1];
      w0 = k0; w1 = k1;
      top2_lo(w0, w1); top2_mid(w0, w1);
      const int n0 = 2 * __builtin_ctzll(__ballot(k0 == w0));
      const u64 m1 = __ballot(k0 == w1);
      const int n1 = m1 ? 2 * __builtin_ctzll(m1) : n0 + 1;
      const bool same = n0 == s0 && n1 == s1;
      s0 = n0; s1 = n1;
      asm volatile("" ::: "memory");
      if (all && same) break;
      if (!all) { SLEEP(L_D); kpk = __builtin_amdgcn_raw_buffer_load_b64(wsr, fd_src, 0, 16); }
      xa = LDW(xl + ((lane & 1) ? s1 : s0) * 2048);
      if (all) break;
    }
    SPIN {
      if (wave_ok(chk(0u, xa))) break;
      asm volatile("" ::: "memory");
      xa = LDW(xl + ((lane & 1) ? s1 : s0) * 2048);
    }
    STAMPT(1, 3, 256);
    {
      const u32x4 z4 = {0u, 0u, 0u, 0u};
      const u32x4 xg = (lane & 2) ? z4 : xa, xu = (lane & 2) ? xa : z4;
      f32x4 d[8];
#pragma unroll
      for (int c = 0; c < 8; ++c) d[c] = mfma16(xg, wf[c], (f32x4){0.f, 0.f, 0.f, 0.f});
      if (tid == 256) { *(u32x4*)(smem + L_MISC + 96) = (u32x4){w0, w1, (unsigned)s0, (unsigned)s1}; misc[30] = 0; }
      SB0();
#pragma unroll
      for (int c = 0; c < 10; ++c) {
        if (c < 8) d[c & 7] = mfma16(xu, wf[8 + (c & 7)], d[c & 7]);
        if (c >= 2) *(f32x4*)(smem + red_dst + (c - 2) * 128) = d[(c - 2) & 7];
        SB0();
      }
    }
    RAW_BAR();
    if (sw == 0) {
      f32x4 t[8];
#pragma unroll
      for (int p = 0; p < 8; ++p) t[p] = *(const f32x4*)(smem + L_RED + p * 1024 + lane * 16);
      SB0();
      STW(sane(__builtin_bit_cast(u32x4, ((t[0] + t[1]) + (t[2] + t[3])) + ((t[4] + t[5]) + (t[6] + t[7])))), gu_src + kq * 1024);
    } else __builtin_amdgcn_s_sleep(2);
    STAMPT(1, 4, 256);
    {
      u32x4 q[4];
      if (OUTE && grow != (int)key_idx(w0) && grow != (int)key_idx(w1)) OUT_ST();
      asm volatile("" ::: "memory");
      SLEEP(P_GU);
      SPIN {
        unsigned m = 0;
#pragma unroll
        for (int j = 0; j < 4; ++j) { q[j] = LDW(gu_src + j * 1024); m = chk(m, q[j]); }
        CNT(5);
        if (wave_ok(m)) break;
        asm volatile("" ::: "memory");
      }
      STAMPT(1, 5, 256);
      const f32x4 tot = (__builtin_bit_cast(f32x4, q[0]) + __builtin_bit_cast(f32x4, q[1])) + (__builtin_bit_cast(f32x4, q[2]) + __builtin_bit_cast(f32x4, q[3]));
      const f32x2 gs = unpk(pack2o(tot[0], tot[1])), us = unpk(pack2o(tot[2], tot[3]));
      const f32x2 sg = unpk(pack2o(gs[0] * __builtin_amdgcn_rcpf(1.0f + __expf(-gs[0])), gs[1] * __builtin_amdgcn_rcpf(1.0f + __expf(-gs[1]))));
      const unsigned a2 = pack2o(sg[0] * us[0], sg[1] * us[1]);
      *(u16*)(smem + act_dst) = (u16)a2; *(u16*)(smem + act_dst + 128) = (u16)(a2 >> 16);
      __builtin_amdgcn_fence(__ATOMIC_RELEASE, "wavefront"); __builtin_amdgcn_wave_barrier(); __builtin_amdgcn_fence(__ATOMIC_ACQUIRE, "wavefront");
      const u32x4 aa = *(const u32x4*)(smem + act_src);
      f32x4 d[8];
#pragma unroll
      for (int c = 0; c < 8; ++c) d[c] = mfma16(aa, wf[16 + c], (f32x4){0.f, 0.f, 0.f, 0.f});
      SB0();
      u32x4 y[2];
#pragma unroll
      for (int c = 0; c < 8; ++c) {
        const float t0 = d[c][0] + dppf<DPP_ROR8>(d[c][2]), t1 = d[c][3] + dppf<DPP_ROR8>(d[c][1]);
        y[c >> 2][c & 3] = __float_as_uint((lane & 8) ? t1 : t0);
      }
      STW(sane(y[0]), yp_dst); STW(sane(y[1]), yp_dst + 128);
      if (tid == 256) misc[30] = w0;
    }
    STAMPT(1, 6, 256);
    if (!OUTE && grow != (int)key_idx(w0) && grow != (int)key_idx(w1)) OUT_ST();
  }
}

extern "C" void kernel_launch(void* const* d_in, const int* in_sizes, int n_in, void* d_out, int out_size,
                              void* d_ws, size_t ws_size, hipStream_t stream) {
  (void)hipFuncSetAttribute((const void*)fused, hipFuncAttributeMaxDynamicSharedMemorySize, LDS_TOTAL);
  fused<<<256, 512, LDS_TOTAL, stream>>>((const u16*)d_in[0], (const u16*)d_in[1], (const u16*)d_in[2], (const u16*)d_in[4],
                                         (const u16*)d_in[5], (const u16*)d_in[6], (const u16*)d_in[7], (u16*)d_out, (char*)d_ws);
}
```

```cpp
#define PROBE_K 0
#define PROBE_I 0
#define PROBE_B -1
#include <hip/hip_runtime.h>
#include <stdint.h>

#define LAS __attribute__((address_space(3)))
#define GAS __attribute__((address_space(1)))
#define DI __device__ __forceinline__

typedef unsigned short u16;
typedef unsigned long long u64;
typedef __attribute__((ext_vector_type(2))) __bf16 bf16x2;
typedef __attribute__((ext_vector_type(8))) __bf16 bf16x8;
typedef __attribute__((ext_vector_type(4))) short s16x4;
typedef __attribute__((ext_vector_type(2))) unsigned short u16x2;
typedef __attribute__((ext_vector_type(16))) float f32x16;
typedef __attribute__((ext_vector_type(4))) float f32x4;
typedef __attribute__((ext_vector_type(2))) float f32x2;
typedef __attribute__((ext_vector_type(4))) unsigned u32x4;
typedef __attribute__((ext_vector_type(2))) unsigned u32x2;
typedef GAS unsigned gu32;
typedef GAS unsigned long long gu64;

constexpr int S_ = 2048, D_ = 1024, F_ = 3584;

constexpr unsigned SY_CH = 0, SY_GO = 1024, SY_RE = 2048;
constexpr unsigned WS_FD  = 98304;
constexpr unsigned WS_ZF  = 8192;
constexpr unsigned WS_KVF = 16384;
constexpr unsigned WS_RS  = 131072;
constexpr unsigned WS_CS  = WS_RS + 16 * 16 * 1024;
constexpr unsigned WS_ZP  = WS_CS + 16 * 16 * 768;
constexpr unsigned WS_FB  = WS_ZP + 32 * 16 * 128;
constexpr unsigned WS_ST2 = WS_FB + 32 * 32 * 128;
constexpr unsigned WS_XS  = WS_ST2 + 32 * 8 * 1024;
constexpr unsigned WS_GU  = WS_XS + 64 * 2048;
constexpr unsigned WS_YP  = WS_GU + 256 * 1024;
constexpr unsigned WS_KVP = WS_YP + 2 * 8 * 64 * 512;
constexpr unsigned WS_O   = WS_KVP + 32 * 16 * 4096;
constexpr unsigned WS_END = WS_O + S_ * D_ * 2;
constexpr unsigned OOB = 0x80000000u;
constexpr size_t WS_DIAG  = 64u << 20;
#define CNT(i) do {} while (0)
#define STAMPT(kid, i, T) do { if ((kid) == PROBE_K && (i) == PROBE_I && __builtin_amdgcn_readfirstlane(threadIdx.x >> 6) == ((T) >> 6)) { \
  asm volatile("s_memrealtime %0\n\ts_waitcnt lgkmcnt(0)" : "=s"(probe_t) :: "memory"); } } while (0)

#define TSL(idx) do {} while (0)
#define TSDUMP(k) do {} while (0)
template <int V> struct IC { static constexpr int v = V; };
template <int B, int E, class F> DI void sfor(F&& f) { if constexpr (B < E) { f(IC<B>{}); sfor<B + 1, E>(f); } }

DI float bflo(unsigned u) { return __uint_as_float(u << 16); }
DI float bfhi(unsigned u) { return __uint_as_float(u & 0xffff0000u); }
DI float rbf(float x) { return (float)(__bf16)x; }
DI unsigned pack2(float a, float b) { bf16x2 c; c[0] = (__bf16)a; c[1] = (__bf16)b; return __builtin_bit_cast(unsigned, c); }
DI unsigned pack2o(float a, float b) { unsigned p = pack2(a, b); asm("" : "+v"(p)); return p; }
DI f32x16 mfma32(u32x4 a, u32x4 b, f32x16 c) {
  return __builtin_amdgcn_mfma_f32_32x32x16_bf16(__builtin_bit_cast(bf16x8, a), __builtin_bit_cast(bf16x8, b), c, 0, 0, 0);
}
DI f32x4 mfma16(u32x4 a, u32x4 b, f32x4 c) {
  return __builtin_amdgcn_mfma_f32_16x16x32_bf16(__builtin_bit_cast(bf16x8, a), __builtin_bit_cast(bf16x8, b), c, 0, 0, 0);
}
DI void mfma16i(u32x4 a, u32x4 b, f32x4& c) { asm volatile("v_mfma_f32_16x16x32_bf16 %0, %1, %2, %0" : "+v"(c) : "v"(a), "v"(b)); }
DI f32x2 unpk(unsigned u) { return (f32x2){bflo(u), bfhi(u)}; }
DI u32x2 trread(const char* p) {
  s16x4 v = __builtin_amdgcn_ds_read_tr16_b64_v4i16((LAS s16x4*)p);
  return __builtin_bit_cast(u32x2, v);
}
DI const char* uni(const void* p) {
  const u64 v = (u64)p;
  return (const char*)(((u64)(unsigned)__builtin_amdgcn_readfirstlane((int)(v >> 32)) << 32) | (unsigned)__builtin_amdgcn_readfirstlane((int)v));
}
#define RV4(a, b, c, d) "v" #a, "v" #b, "v" #c, "v" #d
#define RV10(p) "v" #p "0", "v" #p "1", "v" #p "2", "v" #p "3", "v" #p "4", "v" #p "5", "v" #p "6", "v" #p "7", "v" #p "8", "v" #p "9"
#define RSV2 RV4(224, 225, 226, 227), "v228", "v229", RV10(23), RV10(24), RV4(250, 251, 252, 253), "v254", "v255"
#define RSV1 RV4(192, 193, 194, 195), RV4(196, 197, 198, 199), RV10(20), RV10(21), RV4(220, 221, 222, 223), RSV2
#define RSV "v148", "v149", RV10(15), RV10(16), RV10(17), RV10(18), "v190", "v191", RSV1
#define NTW " nt"
#define NTA ""
#define NTB ""
#define NTO ""
#define GL_(v, s, o) "global_load_lds_dwordx4 " v ", " s " offset:" #o
#define GLDS4(SC, CL...) asm volatile("s_add_u32 m0, %5, %c6\n\ts_nop 3\n\t" GL_("%0", "%4", 0) SC "\n\t" GL_("%1", "%4", 1024) SC "\n\t" \
  GL_("%2", "%4", 2048) SC "\n\t" GL_("%3", "%4", 3072) SC :: "v"(v0), "v"(v1), "v"(v2), "v"(v3), "s"(uni(sbase)), "s"(lds), "i"(IMM) : "memory", "scc", ##CL)
#define GLDS3(SC, CL...) asm volatile("s_add_u32 m0, %4, %c5\n\ts_nop 3\n\t" GL_("%0", "%3", 0) SC "\n\t" GL_("%1", "%3", 1024) SC "\n\t" \
  GL_("%2", "%3", 2048) SC :: "v"(v0), "v"(v1), "v"(v2), "s"(uni(sbase)), "s"(lds), "i"(IMM) : "memory", "scc", ##CL)
template <int IMM> DI void glds4(unsigned v0, unsigned v1, unsigned v2, unsigned v3, const void* sbase, unsigned lds) { GLDS4(NTO); }
template <int IMM> DI void glds4o(unsigned v0, unsigned v1, unsigned v2, unsigned v3, const void* sbase, unsigned lds) { GLDS4(NTO, RSV); }
template <int IMM> DI void glds4r(unsigned v0, unsigned v1, unsigned v2, unsigned v3, const void* sbase, unsigned lds) { GLDS4(NTA, RSV); }
template <int IMM> DI void glds4n(unsigned v0, unsigned v1, unsigned v2, unsigned v3, const void* sbase, unsigned lds) { GLDS4(" nt", RSV); }
template <int IMM> DI void glds3n(unsigned v0, unsigned v1, unsigned v2, const void* sbase, unsigned lds) { GLDS3(" nt", RSV); }
template <int IMM> DI void glds3r(unsigned v0, unsigned v1, unsigned v2, const void* sbase, unsigned lds) { GLDS3(NTB, RSV); }
template <int R> DI void sload(unsigned voff, const void* sbase) {
  asm volatile("s_nop 4\n\tglobal_load_dwordx4 v[%c0:%c1], %2, %3 sc1" ::"i"(R), "i"(R + 3), "v"(voff), "s"(uni(sbase)) : "memory", RSV);
}
DI float fadd(float a, float b) { float o; asm("v_add_f32 %0, %1, %2" : "=v"(o) : "v"(a), "v"(b)); return o; }
DI float fmul(float a, float b) { float o; asm("v_mul_f32 %0, %1, %2" : "=v"(o) : "v"(a), "v"(b)); return o; }
DI float fsq(float a, float c) { asm("v_fmac_f32 %0, %1, %1" : "+v"(c) : "v"(a)); return c; }
#define RA_(o, a, b) "v_add_f32 %" #o ", v[%c6+" #a "], v[%c6+" #b "]\n\t"
#define RB2_(o, a) "v_add_f32 %" #o ", v[%c6+" #a "], %" #o "\n\t"
#define RM_(o, a, b) "v_max3_u32 %" #o ", v[%c6+" #a "], v[%c6+" #b "], %" #o "\n\t"
template <int R> DI void rsum4(float* a, unsigned& m, unsigned& n) {
  asm volatile(RA_(0, 0, 4) RA_(1, 1, 5) RM_(4, 0, 1) RA_(2, 2, 6) RA_(3, 3, 7) RM_(5, 2, 3) RB2_(0, 8) RM_(4, 4, 5) RB2_(1, 9) RM_(5, 6, 7)
               RB2_(2, 10) RM_(4, 8, 9) RB2_(3, 11) RM_(5, 10, 11) RB2_(0, 12) RB2_(1, 13) RM_(4, 12, 13) RB2_(2, 14) RB2_(3, 15) RM_(5, 14, 15)
               : "=&v"(a[0]), "=&v"(a[1]), "=&v"(a[2]), "=&v"(a[3]), "+v"(m), "+v"(n) : "i"(R) : RSV);
}
template <int R> DI void wload2(unsigned voff, const void* sbase) {
  asm volatile("s_nop 4\n\tglobal_load_dwordx4 v[%c0:%c1], %2, %3" NTW ::"i"(R), "i"(R + 3), "v"(voff), "s"(uni(sbase)) : "memory", RSV2);
}
template <int R> DI void wload(unsigned voff, const void* sbase) {
  asm volatile("s_nop 4\n\tglobal_load_dwordx4 v[%c0:%c1], %2, %3" NTW ::"i"(R), "i"(R + 3), "v"(voff), "s"(uni(sbase)) : "memory", RSV);
}
template <int RA, int RB> DI unsigned wperm(unsigned sel) {
  unsigned o;
  if (RA < 192) asm volatile("v_perm_b32 %0, v%c2, v%c1, %3" : "=v"(o) : "i"(RA), "i"(RB), "s"(sel) : RSV);
  else if (RA < 224) asm volatile("v_perm_b32 %0, v%c2, v%c1, %3" : "=v"(o) : "i"(RA), "i"(RB), "s"(sel) : RSV1);
  else asm volatile("v_perm_b32 %0, v%c2, v%c1, %3" : "=v"(o) : "i"(RA), "i"(RB), "s"(sel) : RSV2);
  return o;
}
template <int R> DI unsigned rget() { unsigned o; asm volatile("v_mov_b32 %0, v%c1" : "=v"(o) : "i"(R) : RSV); return o; }
DI float xh_sum(float v) {
  auto r = __builtin_amdgcn_permlane32_swap(__float_as_uint(v), __float_as_uint(v), false, false);
  return __uint_as_float(r[0]) + __uint_as_float(r[1]);
}
template <int CTRL> DI unsigned dppu(unsigned v) { return (unsigned)__builtin_amdgcn_update_dpp(0, (int)v, CTRL, 0xf, 0xf, true); }
template <int CTRL> DI float dppf(float v) { return __uint_as_float(dppu<CTRL>(__float_as_uint(v))); }
constexpr int DPP_X1 = 0xB1, DPP_X2 = 0x4E, DPP_HMIR = 0x141, DPP_MIR = 0x140, DPP_ROR8 = 0x128;
DI float quad_sum(float v) { v += dppf<DPP_X1>(v); v += dppf<DPP_X2>(v); return v; }
DI float oct_sum(float v) { v = quad_sum(v); v += dppf<DPP_HMIR>(v); return v; }
DI float x16_sum(float v) {
  auto r = __builtin_amdgcn_permlane16_swap(__float_as_uint(v), __float_as_uint(v), false, false);
  return __uint_as_float(r[0]) + __uint_as_float(r[1]);
}
DI float sw16(float a, float b) {
  auto r = __builtin_amdgcn_permlane16_swap(__float_as_uint(a), __float_as_uint(b), false, false);
  return fadd(__uint_as_float(r[0]), __uint_as_float(r[1]));
}
DI float sw32(float a, float b) {
  auto r = __builtin_amdgcn_permlane32_swap(__float_as_uint(a), __float_as_uint(b), false, false);
  return fadd(__uint_as_float(r[0]), __uint_as_float(r[1]));
}
DI float hi_sum(float v) {
  auto r = __builtin_amdgcn_permlane16_swap(__float_as_uint(v), __float_as_uint(v), false, false);
  v = fadd(__uint_as_float(r[0]), __uint_as_float(r[1]));
  auto q = __builtin_amdgcn_permlane32_swap(__float_as_uint(v), __float_as_uint(v), false, false);
  return fadd(__uint_as_float(q[0]), __uint_as_float(q[1]));
}
template <int N> struct Phi { unsigned tb[N]; f32x2 tr[N], e[N]; };
template <int N> DI void phi_a(const f32x2* t, Phi<N>& s) {
#pragma unroll
  for (int i = 0; i < N; ++i) s.tb[i] = pack2o(t[i][0], t[i][1]);
  __builtin_amdgcn_sched_barrier(0);
#pragma unroll
  for (int i = 0; i < N; ++i) s.tr[i] = unpk(s.tb[i]);
  __builtin_amdgcn_sched_barrier(0);
#pragma unroll
  for (int i = 0; i < N; ++i) s.e[i] = s.tr[i] * 1.44269504088896340736f;
  __builtin_amdgcn_sched_barrier(0);
}
template <int N> DI void phi_b(Phi<N>& s, unsigned* p) {
  u16x2 tm[2] = {{0, 0}, {0, 0}};
#pragma unroll
  for (int i = 0; i < N; ++i) {
    s.e[i][0] = __builtin_amdgcn_fmed3f(__builtin_amdgcn_exp2f(s.e[i][0]), 0.f, 1.f);
    tm[i & 1] = __builtin_elementwise_max(tm[i & 1], __builtin_bit_cast(u16x2, s.tb[i]));
    s.e[i][1] = __builtin_amdgcn_fmed3f(__builtin_amdgcn_exp2f(s.e[i][1]), 0.f, 1.f);
    __builtin_amdgcn_sched_barrier(0);
  }
#pragma unroll
  for (int i = 0; i < N; ++i) s.tr[i] += s.e[i];
  __builtin_amdgcn_sched_barrier(0);
#pragma unroll
  for (int i = 0; i < N; ++i) { s.tr[i][0] = fmaxf(s.tr[i][0], s.e[i][0]); s.tr[i][1] = fmaxf(s.tr[i][1], s.e[i][1]); }
  const u16x2 tx = __builtin_elementwise_max(tm[0], tm[1]);
  __builtin_amdgcn_sched_barrier(0);
#pragma unroll
  for (int i = 0; i < N; ++i) p[i] = pack2(s.tr[i][0], s.tr[i][1]);
  if (__any(tx[0] > 0xC0F0 || tx[1] > 0xC0F0)) {
#pragma unroll
    for (int i = 0; i < N; ++i) p[i] = pack2(bflo(p[i]) + 1e-6f, bfhi(p[i]) + 1e-6f);
  }
}
template <int N> DI void phi_n(const f32x2* t, unsigned* p) { Phi<N> s; phi_a<N>(t, s); phi_b<N>(s, p); }
#define RLX_AGENT __ATOMIC_RELAXED, __HIP_MEMORY_SCOPE_AGENT
DI void st32(char* ws, unsigned off, unsigned v) { __hip_atomic_store((gu32*)(ws + off), v, RLX_AGENT); }
DI void st64(char* ws, unsigned off, u64 v) { __hip_atomic_store((gu64*)(ws + off), v, RLX_AGENT); }
DI unsigned ld32(char* ws, unsigned off) { return __hip_atomic_load((gu32*)(ws + off), RLX_AGENT); }
DI u64 ld64(char* ws, unsigned off) { return __hip_atomic_load((gu64*)(ws + off), RLX_AGENT); }
#define WAIT_V(n) asm volatile("s_waitcnt vmcnt(%c0)" ::"i"(n) : "memory")
#define RAW_BAR() do { asm volatile("s_waitcnt lgkmcnt(0)" ::: "memory"); __builtin_amdgcn_s_barrier(); asm volatile("" ::: "memory"); } while (0)
#define SB0() __builtin_amdgcn_sched_barrier(0)
#define WAIT_VR(n) asm volatile("s_waitcnt vmcnt(%c0)" ::"i"(n) : "memory", RSV)
#define RAW_BARR() do { asm volatile("s_waitcnt lgkmcnt(0)" ::: "memory", RSV); __builtin_amdgcn_s_barrier(); asm volatile("" ::: "memory", RSV); } while (0)
#define KEEP2() asm volatile("" ::: "memory", RSV2)
#define RAW_BAR2() do { asm volatile("s_waitcnt lgkmcnt(0)" ::: "memory", RSV2); __builtin_amdgcn_s_barrier(); KEEP2(); } while (0)
constexpr unsigned SPIN_CAP = 1u << 18;
#define SPIN _Pragma("nounroll") for (unsigned s_ = 0; s_ < SPIN_CAP; ++s_)
constexpr unsigned SENT = 0xFFFFFFFFu;
DI unsigned sane1(unsigned v) { return min(v, 0xFFFFFFFEu); }
DI u32x4 sane(u32x4 v) { v[1] = sane1(v[1]); v[3] = sane1(v[3]); return v; }
DI unsigned chk(unsigned m, u32x4 v) { return max(max(m, v[1]), v[3]); }
DI bool wave_ok(unsigned m) { return !__any(m == SENT); }
DI unsigned key_idx(unsigned k) { return (0xFFFFu - (k & 0xFFFFu)) & (S_ - 1); }
DI float key_val(unsigned k) { const unsigned o = k >> 16; return __uint_as_float(((o & 0x8000u) ? (o & 0x7fffu) : (~o & 0xffffu)) << 16); }
DI void top2_mix(unsigned& b0, unsigned& b1, unsigned c0, unsigned c1) {
  const unsigned lo = min(b0, c0), mx = max(b1, c1);
  b0 = max(b0, c0); b1 = max(lo, mx);
}
DI void top2_lo(unsigned& b0, unsigned& b1) {
  top2_mix(b0, b1, dppu<DPP_X1>(b0), dppu<DPP_X1>(b1));
  top2_mix(b0, b1, dppu<DPP_X2>(b0), dppu<DPP_X2>(b1));
  top2_mix(b0, b1, dppu<DPP_HMIR>(b0), dppu<DPP_HMIR>(b1));
}
DI void top2_mid(unsigned& b0, unsigned& b1) {
  top2_mix(b0, b1, dppu<DPP_MIR>(b0), dppu<DPP_MIR>(b1));
  auto p = __builtin_amdgcn_permlane16_swap(b0, b0, false, false); auto q = __builtin_amdgcn_permlane16_swap(b1, b1, false, false);
  b0 = p[0]; b1 = q[0]; top2_mix(b0, b1, p[1], q[1]);
}
DI void top2_hi(unsigned& b0, unsigned& b1) {
  top2_mid(b0, b1);
  auto p = __builtin_amdgcn_permlane32_swap(b0, b0, false, false); auto q = __builtin_amdgcn_permlane32_swap(b1, b1, false, false);
  b0 = p[0]; b1 = q[0]; top2_mix(b0, b1, p[1], q[1]);
}

constexpr int K1_STAGE = 40960;
constexpr int LDS_TOTAL = 4 * K1_STAGE;
constexpr int L_STM = 0;
constexpr int L_MISC = 2048;
constexpr int L_XK = 5120;
constexpr int L_KX = 3328;
constexpr int L_ACT = 2304;
constexpr int L_CAND = 4096;
constexpr int L_WO = 16384;
constexpr int L_P = 16384, L_PK = 7 * 16384;
constexpr int L_RED = 65536;
constexpr int wo_slot(int j) { return L_WO + (j % 9) * 16384; }
constexpr int CODE_LINES = 12;
#define OW_ 8
constexpr int OW = OW_;
#define W1A_ 16
constexpr int W1A = W1A_;
#define PRIO_M 1
#define PRIO_S 3
#define GSCHED 5, 9, 13, 17, 21, 24, 27, 30, 33, 36, 39, 42, 45, 48, 48
constexpr int GS[15] = {GSCHED};
constexpr int gops(int g) { return (g / 3) * 10 + (g % 3 == 0 ? 0 : g % 3 == 1 ? 4 : 7); }
constexpr bool gs_ok() {
  bool ok = GS[0] >= 3 && GS[0] <= 9 && GS[1] >= 6 && GS[1] <= 12 && GS[0] <= GS[1] && GS[12] == 45 && GS[13] == 48 && GS[14] == 48;
  for (int t = 0; t < 12; ++t) ok = ok && GS[t + 2] <= 3 * (t + 5) && GS[t + 2] >= 3 * (t + 3) && GS[t + 2] >= GS[t + 1] + 3;
  return ok;
}
static_assert(gs_ok(), "staging schedule");
#define WSCHED 0, 0, 0, 0, 0, 0, 0, 0, 0, 2, 4, 6, 8
constexpr int WS_[13] = {WSCHED};
#define PACE 1
#define PSLP 0
#define P_KVP 1
#define L_KVP 0
#define P_KVF 1
#define L_KVF 0
#define P_FB 8
#define L_FB 0
#define P_C 14
#define P_D 2
#define L_D 0
#define P_GU 12
#define P_E 10
#define L_E 0
#define SLEEP(n) do { if (n) __builtin_amdgcn_s_sleep(n); } while (0)
#define OUTE 1
#define P2MAP 0
#define TCH0 100
#define OUTWT 0
#define OUTWB 0
#define PFO 1
#define WOD 100
#define WODF 6
#define WODS 0
#define QPOS 1
#define LASTB 14
#define CH0 4
#define CHN 3
#define CPE 1
constexpr int VW = 160;

__global__ __launch_bounds__(512, 2) void fused(const u16* __restrict__ x, const u16* __restrict__ wqkv,
                                                const u16* __restrict__ wo, const u16* __restrict__ w_r,
                                                const u16* __restrict__ wg, const u16* __restrict__ wu,
                                                const u16* __restrict__ wd, u16* __restrict__ out, char* __restrict__ ws) {
  extern __shared__ __attribute__((aligned(16))) char smem[];
  u64 probe_t = 0;
  asm volatile(".rept 94\n\ts_nop 0\n\t.endr" ::: "memory");
  STAMPT(0, 0, 0);
  u64 pc0; asm volatile("s_getpc_b64 %0" : "=s"(pc0));
  const int wave = __builtin_amdgcn_readfirstlane(threadIdx.x >> 6);
  const unsigned lds0 = (unsigned)(size_t)smem;
  if (wave == 0) {
    const int l = threadIdx.x;
    asm volatile("s_mov_b32 m0, %1\n\ts_nop 0\n\tglobal_load_lds_dword %0, off"
                 :: "v"((const char*)(pc0 - (pc0 & 127)) + ((blockIdx.x >> 3) + (l < CODE_LINES ? l : 0) * 32) * 128), "s"(__builtin_amdgcn_readfirstlane(lds0 + 3 * K1_STAGE)) : "memory");
  }
#define RB_(b) (((b) & 1) * 8 + (((b) >> 3) & 7))
#define HP_(b) ((((b) & 7) >> 1) * 4 + ((b) >> 6))
#define KROT_(b) ((4 * ((HP_(b) >> 2) + (RB_(b) >> 3) + 2 * ((RB_(b) >> 2) & 1))) & 15)
  unsigned voA0 = 0, voA1 = 0, voA2 = 0, voA3 = 0, voB0 = 0, voB1 = 0, voB2 = 0, voB3 = 0, voB4 = 0, voB5 = 0;
  const unsigned l4 = __builtin_amdgcn_readfirstlane(lds0 + (wave - 4) * 4096), l6 = __builtin_amdgcn_readfirstlane(lds0 + (wave - 4) * 6144);
  const int krot0 = KROT_(blockIdx.x);
  auto group = [&](auto G) {
    constexpr int t = G.v / 3, p = G.v % 3, sl = (t & 3) * K1_STAGE;
    const int kt = (t + krot0) & 15;
    if (p == 0) glds4r<sl>(voA0, voA1, voA2, voA3, (const char*)x + kt * 128, l4);
    else if (p == 1) glds3r<sl + 16384>(voB0, voB1, voB2, (const char*)wqkv + kt * 64 * (3 * D_ * 2), l6);
    else glds3r<sl + 16384 + 3072>(voB3, voB4, voB5, (const char*)wqkv + kt * 64 * (3 * D_ * 2), l6);
  };
  if (wave >= 4) {
    const int lane = threadIdx.x & 63, sw = wave - 4;
    const int r0 = RB_(blockIdx.x) * 128 + 32 * sw + (lane >> 3);
    voA0 = (unsigned)(r0 * D_ + ((lane & 7) ^ (lane >> 4)) * 8) * 2u;
    voA1 = ((voA0 + 8 * D_ * 2) ^ 64) - 1024; voA2 = voA0 + 16 * D_ * 2 - 2048; voA3 = voA1 + 16 * D_ * 2 - 2048;
    voB0 = (unsigned)((16 * sw + 4 * (lane >> 5) + ((lane >> 2) & 3)) * (3 * D_) + HP_(blockIdx.x) * 64 + ((lane >> 4) & 1) * 32 + (lane & 3) * 8) * 2u;
    voB1 = voB0 + 1024; voB2 = voB0 + 2048;
    voB3 = (voB0 ^ 32) + 8 * 3 * D_ * 2; voB4 = voB3 + 1024; voB5 = voB3 + 2048;
    asm volatile("" : "+v"(voA0), "+v"(voA1), "+v"(voA2), "+v"(voA3), "+v"(voB0), "+v"(voB1), "+v"(voB2), "+v"(voB3), "+v"(voB4), "+v"(voB5));
    STAMPT(1, 7, 256);
    sfor<0, 3>(group);
    STAMPT(1, 8, 256);
  }
  int bid = blockIdx.x; asm volatile("" : "+s"(bid));
  const int xcd = bid & 7, idx = bid >> 3;
  const int rb = RB_(bid), hp = HP_(bid);
  const int rp = xcd * 4 + (idx >> 3), cb = idx & 7;
#define WROT (4 * rp + (xcd >> 1))
#define WDIR ((xcd & 1) ? -1 : 1)
  const int hs_ = (hp + 4 + (int)(((size_t)wqkv >> 7) & 7)) & 7;
  const int g8 = ((hp >> 3) * 4 + (rb >> 2)) * 8 + hs_, kq = rb & 3;
  const int krot = KROT_(bid), posA = 1 + (hp & 3), posB = 1 + (rb & 3);
  const int wrot = WROT, wdir = WDIR;
  auto whead = [&](int t) { return (wrot + wdir * t) & 15; };
  const unsigned rs_slot = WS_RS + (rb * 16 + ((krot + posA) & 15)) * 1024, cs_slot = WS_CS + (hp * 16 + ((krot + posB) & 15)) * 768;
  const __amdgpu_buffer_rsrc_t wsr = __builtin_amdgcn_make_buffer_rsrc(ws, 0, WS_END, 0x00020000);
#define LDW(off) __builtin_amdgcn_raw_buffer_load_b128(wsr, (off), 0, 16)
#define STW(v, off) __builtin_amdgcn_raw_buffer_store_b128((v), wsr, (off), 0, 16)
#define STW1(v, off) __builtin_amdgcn_raw_buffer_store_b32((v), wsr, (off), 0, 16)
  float* stm = (float*)(smem + L_STM);
  volatile LAS int* misc = (volatile LAS int*)(smem + L_MISC);
#define LANES() int tid = threadIdx.x; asm volatile("" : "+v"(tid)); const int lane = tid & 63, r = lane & 31, h = lane >> 5; (void)r; (void)h
#define EPI() const int erow = tid >> 3, eseg = tid & 7; const size_t ego = (size_t)(rp * 64 + erow) * D_ + cb * 128 + eseg * 8
  u32x4 xv[2], wv[2];
  f32x2 xf[8], wf2[8];
  float sw_;
  auto unpack_xw = [&]() {
    f32x2 a = {0.f, 0.f};
#pragma unroll
    for (int i = 0; i < 8; ++i) { xf[i] = unpk(xv[i >> 2][i & 3]); wf2[i] = unpk(wv[i >> 2][i & 3]); a += wf2[i]; }
    sw_ = oct_sum(a[0] + a[1]);
#pragma unroll
    for (int i = 0; i < 8; ++i) asm volatile("" : "+v"(xf[i]), "+v"(wf2[i]));
    asm volatile("" : "+v"(sw_));
  };
  unsigned tsv = 0; (void)tsv;
  u32x4 wf[24];

  auto wtr = [&](auto I) {
    constexpr int m = I.v >> 3, c = I.v & 7, R = VW + 32 * m + (c >> 1);
    const unsigned sel = (c & 1) ? 0x07060302u : 0x05040100u;
    wf[I.v] = (u32x4){wperm<R, R + 4>(sel), wperm<R + 8, R + 12>(sel), wperm<R + 16, R + 20>(sel), wperm<R + 24, R + 28>(sel)};
  };

  if (wave < 4) {
    LANES();
    const int c16 = lane & 15, g4 = lane >> 4;
    const int trl = (c16 >> 2) * 64 + (c16 & 3) * 8;
    const int wr = wave >> 1, wc = wave & 1;
    const int head = 2 * hp + wc;
    const unsigned kvp_off = WS_KVP + (head * 16 + rb) * 4096 + wr * 2048 + lane * 16;
    const unsigned zp_off = WS_ZP + (head * 16 + rb) * 128;
    const unsigned o_off = WS_O + ((rb * 4 + 2 * wr) * 32 + head) * 2048 + lane * 16;
    const unsigned fb_off = lane < 8 ? WS_FB + ((rb * 2 + wr) * 32 + head) * 128 + lane * 16 : OOB;
    unsigned pfa, pfb = OOB;
    unsigned pfc = lane < 8 ? fb_off : lane < 24 ? rs_slot + wave * 256 + (lane - 8) * 16
        : lane < 40 ? (wave < 3 ? cs_slot + wave * 256 : WS_KVF + hp * 4096 + rb * 256) + (lane - 24) * 16
        : lane < 48 ? (wave < 2 ? zp_off + (lane & 7) * 16 : (wave == 2 && rb < 2) ? WS_ZF + (2 * hp + rb) * 128 + (lane & 7) * 16 : OOB) : OOB;
    if (wave == 0) pfa = WS_ST2 + (rp * 8 + cb) * 1024 + lane * 16;
    else if (wave == 1) pfa = WS_GU + (g8 * 4 + kq) * 1024 + lane * 16;
    else pfa = WS_YP + (((wave - 2) * 8 + 2 * kq + (lane >> 5)) * 64 + g8) * 512 + (lane & 31) * 16;
    if (wave == 0) pfb = lane < 32 ? WS_XS + (rp * 2 + (lane >> 4)) * 2048 + cb * 256 + (lane & 15) * 16 : lane < 40 ? WS_FD + (cb * 32 + rp) * 128 + (lane & 7) * 16 : OOB;
    u32x4 S4 = {SENT, SENT, SENT, SENT};
    asm volatile("" : "+v"(pfa), "+v"(pfb), "+v"(pfc), "+v"(S4));
    __builtin_amdgcn_s_setprio(PRIO_M);
    f32x4 acc[4][6];
    int ao[2][2], bo[2][2];
    {
#pragma unroll
      for (int i = 0; i < 2; ++i) {
        ao[0][i] = (64 * wr + c16) * 128 + (((4 * i + g4) ^ (c16 >> 1)) * 16); ao[1][i] = ao[0][i] + 2 * K1_STAGE;
        bo[0][i] = 16384 + g4 * 3072 + wc * 256 + trl + ((i ^ (g4 & 1)) * 32); bo[1][i] = bo[0][i] + 2 * K1_STAGE;
        asm volatile("" : "+v"(ao[0][i]), "+v"(ao[1][i]), "+v"(bo[0][i]), "+v"(bo[1][i]));
      }
#pragma unroll
      for (int i = 0; i < 4; ++i)
#pragma unroll
        for (int j = 0; j < 6; ++j) { acc[i][j] = (f32x4){0.f, 0.f, 0.f, 0.f}; asm volatile("" : "+v"(acc[i][j])); }
      struct Frag { u32x4 a[4]; u32x2 bl[6], bh[6]; };
      Frag FA, FB;
#define K1_B(j) ((u32x4){C.bl[j][0], C.bl[j][1], C.bh[j][0], C.bh[j][1]})
#define K1_LA(i) N.a[i] = *(const u32x4*)(smem + ao[sn >> 1][hf] + (sn & 1) * K1_STAGE + (i) * 2048)
#define K1_LL(j) N.bl[j] = trread(smem + bo[sn >> 1][(j) & 1] + (sn & 1) * K1_STAGE + hf * 12288 + ((j) >> 1) * 1024)
#define K1_LH(j) N.bh[j] = trread(smem + bo[sn >> 1][(j) & 1] + (sn & 1) * K1_STAGE + hf * 12288 + ((j) >> 1) * 1024 + 512)
#define K1_M(i, j) if ((j) < 2) mfma16i(K1_B(j), C.a[i], acc[i][j]); else mfma16i(C.a[i], K1_B(j), acc[i][j]); SB0()
#define K1_Q(i, j, ld) K1_M(i, j); ld; SB0()
      auto sub = [&](Frag& C, Frag& N, int sn, int hf) {
        K1_Q(0, 0, K1_LL(0)); K1_Q(1, 0, K1_LH(0)); K1_Q(0, 1, K1_LA(0)); K1_Q(1, 1, K1_LA(1)); K1_Q(0, 2, K1_LL(1)); K1_Q(1, 2, K1_LH(1));
        K1_Q(0, 3, K1_LL(2)); K1_Q(1, 3, K1_LH(2)); K1_Q(2, 0, K1_LL(3)); K1_Q(3, 0, K1_LH(3)); K1_Q(2, 1, K1_LA(2)); K1_Q(3, 1, K1_LA(3));
        K1_Q(2, 2, K1_LL(4)); K1_Q(3, 2, K1_LH(4)); K1_Q(2, 3, K1_LL(5)); K1_Q(3, 3, K1_LH(5));
        K1_M(0, 4); K1_M(1, 4); K1_M(2, 4); K1_M(3, 4); K1_M(0, 5); K1_M(1, 5); K1_M(2, 5);
        asm volatile("s_waitcnt lgkmcnt(0)" : "+v"(N.bh[5]));
        K1_M(3, 5);
      };
      auto subkv = [&](Frag& C, Frag& N, int sn, int hf, bool ld) {
#define K1_QL(i, j, l) K1_M(i, j); if (ld) { l; } SB0()
        K1_QL(0, 2, K1_LL(2)); K1_QL(1, 2, K1_LH(2)); K1_QL(0, 3, K1_LA(0)); K1_QL(1, 3, K1_LA(1)); K1_QL(2, 2, K1_LL(3)); K1_QL(3, 2, K1_LH(3));
        K1_QL(2, 3, K1_LA(2)); K1_QL(3, 3, K1_LA(3)); K1_QL(0, 4, K1_LL(4)); K1_QL(1, 4, K1_LH(4)); K1_QL(2, 4, K1_LL(5)); K1_QL(3, 4, K1_LH(5));
        K1_M(0, 5); K1_M(1, 5); K1_M(2, 5); K1_M(3, 5);
      };
      WAIT_V(0);
      RAW_BAR();
      STAMPT(0, 1, 0);
      {
        Frag& N = FA; const int sn = 0, hf = 0;
        K1_LL(0); K1_LH(0); K1_LA(0); K1_LA(1); K1_LL(1); K1_LH(1); K1_LL(2); K1_LH(2); K1_LL(3); K1_LH(3); K1_LA(2); K1_LA(3); K1_LL(4); K1_LH(4); K1_LL(5); K1_LH(5);
      }
#pragma unroll 1
      for (int it = 0; it < 4; ++it) {
#pragma unroll
        for (int s = 0; s < 4; ++s) {
          if (s == 2 && it == 3) break;
          sub(FA, FB, s, 1);
          if (s == 0 || (PFO == 0 && s == 3)) WAIT_V(0);
          if (it == 0) { if (s == PFO) STW(S4, pfc); if (s == PFO + 1) STW(S4, kvp_off); if (s == PFO + 2) STW(S4, kvp_off + 1024); }
          if (it == 1) { if (s == 0) STW(S4, pfa); if (s == 1) STW(S4, pfb); }
          TSL(it * 4 + s);
          if (LASTB == 13 || s != 1 || it < 3) RAW_BAR();
          sub(FB, FA, (s + 1) & 3, 0);
        }
      }
      subkv(FA, FB, 2, 1, true);
      TSL(14);
      if (LASTB == 14) RAW_BAR();
      subkv(FB, FA, 3, 0, true);
      subkv(FA, FB, 3, 1, true);
      TSL(15);
      subkv(FB, FA, 0, 0, false);
    }
    asm volatile("s_nop 15" ::: "memory");
    TSL(16); if (wave == 0) TSDUMP(0);
    STAMPT(0, 2, 0);
    {
      f32x4 m4[4], i4[4]; float cs[4];
      SPIN {
        const int ok = misc[12 + (lane & 3)];
        asm volatile("" ::: "memory");
#pragma unroll
        for (int i = 0; i < 4; ++i) {
          cs[i] = stm[256 + wc * 96 + 32 + 16 * i + c16];
          m4[i] = *(const f32x4*)(stm + 64 * wr + 16 * i + 4 * g4); i4[i] = *(const f32x4*)(stm + 128 + 64 * wr + 16 * i + 4 * g4);
        }
        SB0();
        if (__all(ok != 0)) break;
      }
      const u32x4 ONES4 = {0x3F803F80u, 0x3F803F80u, 0x3F803F80u, 0x3F803F80u};
      f32x2 tk[16], tv[16];
#define PR(v, i) ((f32x2){(v)[2 * (i)], (v)[2 * (i) + 1]})
#pragma unroll
      for (int i = 0; i < 16; ++i) {
        tk[i] = PR(acc[(i >> 1) & 3][2 + (i >> 3)], i & 1) - PR(m4[(i >> 1) & 3], i & 1) * cs[i >> 3];
        tv[i] = PR(acc[(i >> 1) & 3][4 + (i >> 3)], i & 1) - PR(m4[(i >> 1) & 3], i & 1) * cs[2 + (i >> 3)];
      }
      SB0();
#pragma unroll
      for (int i = 0; i < 16; ++i) { tk[i] *= PR(i4[(i >> 1) & 3], i & 1); tv[i] *= PR(i4[(i >> 1) & 3], i & 1); }
      SB0();
      unsigned kp[16], vp[16];
#pragma unroll
      for (int i = 0; i < 16; ++i) vp[i] = pack2o(tv[i][0], tv[i][1]);
      phi_n<16>(tk, kp);
#define F4_(p, i) ((u32x4){(p)[4 * (i)], (p)[4 * (i) + 1], (p)[4 * (i) + 2], (p)[4 * (i) + 3]})
      const f32x4 Z4 = {0.f, 0.f, 0.f, 0.f};
      u32x4 vk[2], vg[2];
#pragma unroll
      for (int j = 0; j < 2; ++j) { vk[j] = wr ? F4_(vp, 2 + j) : F4_(vp, j); vg[j] = wr ? F4_(vp, j) : F4_(vp, 2 + j); }
      f32x4 keep[2], give[2], zq[2];
#pragma unroll
      for (int d = 0; d < 2; ++d) { give[d] = mfma16(F4_(kp, 2 * d), vg[0], Z4); zq[d] = mfma16(ONES4, F4_(kp, 2 * d), Z4); }
#pragma unroll
      for (int d = 0; d < 2; ++d) { give[d] = mfma16(F4_(kp, 2 * d + 1), vg[1], give[d]); zq[d] = mfma16(ONES4, F4_(kp, 2 * d + 1), zq[d]); }
#pragma unroll
      for (int d = 0; d < 2; ++d) keep[d] = mfma16(F4_(kp, 2 * d), vk[0], Z4);
#pragma unroll
      for (int d = 0; d < 2; ++d) keep[d] = mfma16(F4_(kp, 2 * d + 1), vk[1], keep[d]);
      SB0();
      const float zv = (lane & 16) ? zq[1][0] : zq[0][0];
      char* xk = smem + L_XK + wave * 2304 + lane * 16;
      const char* xp = smem + L_XK + (wave ^ 2) * 2304 + lane * 16;
      *(f32x4*)xk = give[0]; *(f32x4*)(xk + 1024) = give[1]; *(float*)(xk + 2048 - lane * 12) = zv;
      asm volatile("" ::: "memory");
      misc[16 + wave] = 1;
      f32x4 p0, p1; float pz;
      SPIN {
        const int ok = misc[16 + (wave ^ 2)];
        asm volatile("" ::: "memory");
        p0 = *(const f32x4*)xp; p1 = *(const f32x4*)(xp + 1024); pz = *(const float*)(xp + 2048 - lane * 12);
        SB0();
        if (ok == 1) break;
      }
      STW(sane(__builtin_bit_cast(u32x4, keep[0] + p0)), kvp_off);
      STW(sane(__builtin_bit_cast(u32x4, keep[1] + p1)), kvp_off + 1024);
      STW1(sane1(__float_as_uint(zv + pz)), (lane < 32 && wr == 0) ? zp_off + lane * 4 : OOB);
    }
    STAMPT(0, 3, 0);
    {
      u32x4 qa[4][4]; u32x2 ql[4][2], qh[4][2];
#pragma unroll
      for (int u = 0; u < 4; ++u) {
#pragma unroll
        for (int j = 0; j < 2; ++j) {
          ql[u][j] = trread(smem + bo[1][j] + (u >> 1) * K1_STAGE + (u & 1) * 12288);
          qh[u][j] = trread(smem + bo[1][j] + (u >> 1) * K1_STAGE + (u & 1) * 12288 + 512);
        }
#pragma unroll
        for (int i = 0; i < 4; ++i) qa[u][i] = *(const u32x4*)(smem + ao[1][u & 1] + (u >> 1) * K1_STAGE + i * 2048);
      }
      asm volatile("" ::: "memory");
      misc[8 + wave] = 3;
      SB0();
#pragma unroll
      for (int u = 0; u < 4; ++u)
#pragma unroll
        for (int j = 0; j < 2; ++j)
#pragma unroll
          for (int i = 0; i < 4; ++i) acc[i][j] = mfma16((u32x4){ql[u][j][0], ql[u][j][1], qh[u][j][0], qh[u][j][1]}, qa[u][i], acc[i][j]);
      SB0();
    }
    unsigned qp[16];
    Phi<16> qs;
    const unsigned po = WS_KVP + ((2 * hp + (rb >> 3)) * 16 + h) * 4096 + (2 * ((rb >> 2) & 1) + ((lane >> 4) & 1)) * 1024 + (16 * (rb & 3) + (lane & 15)) * 16;
    u32x4 pv[8] = {};
    auto qepi_a = [&]() {
      f32x4 c4[2]; float m_b[4], rinv[4];
#pragma unroll
      for (int i = 0; i < 2; ++i) c4[i] = *(const f32x4*)(stm + 256 + wc * 96 + 16 * i + 4 * g4);
#pragma unroll
      for (int i = 0; i < 4; ++i) { m_b[i] = stm[64 * wr + 16 * i + c16]; rinv[i] = stm[128 + 64 * wr + 16 * i + c16]; }
      SB0();
      f32x2 tq[16];
#pragma unroll
      for (int i = 0; i < 16; ++i) tq[i] = PR(acc[i >> 2][(i >> 1) & 1], i & 1) - PR(c4[(i >> 1) & 1], i & 1) * m_b[i >> 2];
      SB0();
#pragma unroll
      for (int i = 0; i < 16; ++i) tq[i] *= rinv[i >> 2];
      SB0();
      phi_a<16>(tq, qs);
#pragma unroll
      for (int i = 0; i < 16; ++i) asm volatile("" : "+v"(qs.tb[i]), "+v"(qs.tr[i]), "+v"(qs.e[i]));
    };
    if (QPOS) qepi_a();
    if (wave == 0) {
      SLEEP(P_KVP);
#pragma unroll
      for (int p = 0; p < 8; ++p) pv[p] = LDW(po + p * 8192);
    }
    asm volatile("" ::: "memory");
    if (!QPOS) qepi_a();
    STAMPT(0, 4, 0);
    if (wave == 1 && rb < 2) {
      SLEEP(P_KVP);
      SPIN {
        unsigned m = 0;
#pragma unroll
        for (int p = 0; p < 2; ++p) { pv[p] = LDW(WS_ZP + (2 * hp + rb) * 2048 + p * 1024 + lane * 16); m = chk(max(max(m, pv[p][0]), pv[p][2]), pv[p]); }
        if (wave_ok(m)) break;
        asm volatile("" ::: "memory");
      }
      f32x4 s = __builtin_bit_cast(f32x4, pv[0]) + __builtin_bit_cast(f32x4, pv[1]);
#pragma unroll
      for (int i = 0; i < 4; ++i) { s[i] += dppf<DPP_ROR8>(s[i]); s[i] = xh_sum(x16_sum(s[i])); }
      if (lane < 16) st64(ws, WS_ZF + (2 * hp + rb) * 128 + (lane >> 3) * 64 + (lane & 3) * 16 + ((lane >> 2) & 1) * 8,
                          ((u64)sane1(pack2(s[2], s[3])) << 32) | pack2(s[0], s[1]));
    }
    if (wave == 0) {
      SPIN {
        unsigned m = 0;
#pragma unroll
        for (int p = 0; p < 8; ++p) m = chk(m, pv[p]);
        CNT(0);
        if (wave_ok(m)) break;
        asm volatile("" ::: "memory");
        SLEEP(L_KVP);
#pragma unroll
        for (int p = 0; p < 8; ++p) pv[p] = LDW(po + p * 8192);
      }
      f32x4 s = __builtin_bit_cast(f32x4, pv[0]);
#pragma unroll
      for (int p = 1; p < 8; ++p) s += __builtin_bit_cast(f32x4, pv[p]);
      const unsigned p0 = pack2(xh_sum(s[0]), xh_sum(s[1])), p1 = pack2(xh_sum(s[2]), xh_sum(s[3]));
      auto a = __builtin_amdgcn_permlane16_swap(p0, p0, false, false);
      auto b = __builtin_amdgcn_permlane16_swap(p1, p1, false, false);
      STW(sane((u32x4){a[0], b[0], a[1], b[1]}), lane < 16 ? WS_KVF + hp * 4096 + rb * 256 + lane * 16 : OOB);
      STAMPT(1, 2, 0);
    }
    phi_b<16>(qs, qp);
#pragma unroll
    for (int i = 0; i < 16; ++i) asm volatile("" : "+v"(qp[i]));
    if (wave == 0) misc[7] = 1;
    else SPIN { if (misc[7] == 1) break; __builtin_amdgcn_s_sleep(1); }
    SLEEP(P_KVF);
    {
      u32x4 k0, k1, z0;
      SPIN {
        k0 = LDW(WS_KVF + head * 2048 + lane * 16); k1 = LDW(WS_KVF + head * 2048 + 1024 + lane * 16);
        z0 = LDW(WS_ZF + head * 128 + g4 * 16);
        CNT(1);
        if (wave_ok(chk(chk(chk(0u, k0), k1), z0))) break;
        asm volatile("" ::: "memory");
        SLEEP(L_KVF);
      }
      STAMPT(0, 5, 0);
      const f32x4 Z4 = {0.f, 0.f, 0.f, 0.f};
      f32x4 X[8], Dn[4];
#pragma unroll
      for (int i = 0; i < 4; ++i) Dn[i] = mfma16(z0, F4_(qp, i), Z4);
#pragma unroll
      for (int i = 0; i < 8; ++i) X[i] = mfma16((i & 1) ? k1 : k0, F4_(qp, i >> 1), Z4);
      SB0();
      float ri[4]; unsigned a[16]; f32x2 f[16];
#pragma unroll
      for (int i = 0; i < 4; ++i) ri[i] = __builtin_amdgcn_rcpf(rbf(rbf(Dn[i][0]) + 1e-6f));
      SB0();
#pragma unroll
      for (int i = 0; i < 16; ++i) a[i] = pack2o(X[i >> 1][2 * (i & 1)], X[i >> 1][2 * (i & 1) + 1]);
      SB0();
#pragma unroll
      for (int i = 0; i < 16; ++i) f[i] = unpk(a[i]);
      SB0();
#pragma unroll
      for (int i = 0; i < 16; ++i) f[i] *= ri[i >> 2];
      SB0();
#pragma unroll
      for (int i = 0; i < 4; ++i) {
        STW(((u32x4){pack2(f[4 * i][0], f[4 * i][1]), pack2(f[4 * i + 1][0], f[4 * i + 1][1]), pack2(f[4 * i + 2][0], f[4 * i + 2][1]), pack2(f[4 * i + 3][0], f[4 * i + 3][1])}),
            o_off + (i >> 1) * 65536 + (i & 1) * 1024);
        SB0();
      }
    }
    if (wave == 0) misc[6] = 1;
    STAMPT(0, 6, 0);
    {
      EPI();
      const int rt = wave & 1, kh = wave >> 1;
      const unsigned ob = WS_O + ((rp * 2 + rt) * 32 + 16 * kh) * 2048 + lane * 16;
#define LDO(t, st) __builtin_amdgcn_raw_buffer_load_b128(wsr, ob + (st) * 1024, whead(t) * 2048, 16)
      int woff[2];
#pragma unroll
      for (int i = 0; i < 2; ++i) woff[i] = kh * 8192 + (g4 >> 1) * 2048 + (g4 & 1) * 512 + trl + ((i ^ (g4 & 1)) * 32);
      struct Frag2 { u32x2 lo[8], hi[8]; };
      Frag2 G[2];
      auto ld2 = [&](Frag2& N, int sn, int nt, int hi) {
        const char* p = smem + sn + woff[nt & 1] + (nt >> 2) * 1024 + ((nt >> 1) & 1) * 256;
        if (hi) N.hi[nt] = trread(p + 4096); else N.lo[nt] = trread(p);
      };
      WAIT_V(0);
      STW(((u32x4){1u, 1u, 1u, 1u}), fb_off);
      RAW_BAR();
#pragma unroll
      for (int i = 0; i < 16; ++i) ld2(G[0], wo_slot(0), i >> 1, i & 1);
      f32x4 a2[2][8];
#pragma unroll
      for (int i = 0; i < 16; ++i) a2[i & 1][i >> 1] = (f32x4){0.f, 0.f, 0.f, 0.f};
      SLEEP(P_FB);
      SPIN { CNT(2); if (wave_ok(ld32(ws, WS_FB + (rp * 32 + 16 * kh + (lane & 15)) * 128))) break; SLEEP(L_FB); }
      asm volatile("" ::: "memory");
      STAMPT(0, 7, 0);
      char* pr[2];
#pragma unroll
      for (int i = 0; i < 2; ++i) pr[i] = smem + L_P + kh * L_PK + (32 * rt + c16) * 512 + (((4 * i + g4) ^ (c16 & 7)) * 16);
      u32x4 of[16][2];
#pragma unroll
      for (int t = 0; t < OW; ++t) { of[t][0] = LDO(t, 0); of[t][1] = LDO(t, 1); }
      SB0();
      sfor<0, 16>([&](auto T) {
        constexpr int t = T.v;
        Frag2& C = G[t & 1]; Frag2& N = G[(t + 1) & 1];
#pragma unroll
        for (int i = 0; i < 16; ++i) {
          const int nt = i >> 1, st = i & 1;
          a2[st][nt] = mfma16((u32x4){C.lo[nt][0], C.lo[nt][1], C.hi[nt][0], C.hi[nt][1]}, of[t][st], a2[st][nt]);
          SB0();
          if ((t & 1) && t <= 11 && i == 8) {
            asm volatile("s_waitcnt lgkmcnt(8)" ::: "memory"); __builtin_amdgcn_s_barrier(); asm volatile("" ::: "memory");
          }
          if (t < 15) ld2(N, wo_slot(t + 1), nt, st);
          if (t > 0 && t - 1 + OW < 16 && (i == 3 || i == 11)) of[(t - 1 + OW) & 15][i >> 3] = LDO(t - 1 + OW, i >> 3);
          if (t - 1 + OW == 16 && (i == 3 || i == 11)) { xv[i >> 3] = *(const u32x4*)(x + ego + (i >> 3) * 64); wv[i >> 3] = *(const u32x4*)(w_r + cb * 128 + eseg * 8 + (i >> 3) * 64); }
          if (t == 15 && i >= 4) *(f32x4*)(pr[(nt - 2) & 1] + ((nt - 2) >> 1) * 128 + st * 8192) = a2[st][(nt - 2) & 7];
          SB0();
        }
      });
      STAMPT(0, 8, 0);
#pragma unroll
      for (int i = 12; i < 16; ++i) *(f32x4*)(pr[(i >> 1) & 1] + (i >> 2) * 128 + (i & 1) * 8192) = a2[i & 1][i >> 1];
    }
    unpack_xw();
    __builtin_amdgcn_s_setprio(0);
  } else {
    LANES();
    const int sw = wave - 4, t2 = tid - 256;
    __builtin_amdgcn_s_setprio(PRIO_S);
    const bool real = hs_ != 7;
    unsigned hs_g0 = 0, hs_cur = 0;
    u32x4 ms_exp = {0, 0, 0, 0};
    bool hs_act = true, ms_act = !real && hp < 8 && rb == 0 && sw == 1;
    const unsigned hs_off = (lane == 1 ? SY_GO : SY_CH) + bid * 4;
    const unsigned re_off = t2 == 0 ? SY_RE + bid * 4 : OOB;
    const unsigned ms_off = lane * 16;
    const char* wsu = uni(ws);
#define HS_PROBE() asm volatile("s_nop 4\n\tglobal_load_dword v156, %0, %1 sc1" :: "v"(hs_off), "s"(wsu) : "memory", RSV)
#define MS_PROBE() asm volatile("s_nop 4\n\tglobal_load_dwordx4 v[152:155], %0, %1 offset:2048 sc1" :: "v"(ms_off), "s"(wsu) : "memory", RSV)
    auto hs_step = [&](bool first) {
      if (hs_act) {
        const unsigned hv_ = rget<156>();
        hs_act = __builtin_amdgcn_readlane(hv_, 1) == hs_g0;
        const unsigned c = __builtin_amdgcn_readlane(hv_, 0);
        if (sw == 0 && (first || c != hs_cur)) STW1(hv_, re_off);
        hs_cur = c;
        HS_PROBE();
      }
      if (ms_act) {
        const unsigned d = ((rget<152>() ^ ms_exp[0]) | (rget<153>() ^ ms_exp[1])) | ((rget<154>() ^ ms_exp[2]) | (rget<155>() ^ ms_exp[3]));
        ms_act = __any(d != 0u);
        STW(ms_exp, ms_act ? OOB : SY_GO + ms_off);
        MS_PROBE();
      }
    };
    HS_PROBE();
    if (ms_act) {
      MS_PROBE();
      asm volatile("s_nop 4\n\tglobal_load_dwordx4 v[148:151], %0, %1 offset:1024 sc1" :: "v"(ms_off), "s"(wsu) : "memory", RSV);
    }
    const int gr = (g8 >> 3) * 7 + min(hs_, 6);
    const unsigned voG = (unsigned)((lane >> 3) * 8 * F_ + (lane & 7) * 8) * 2u, voD = (unsigned)((lane >> 3) * 8 * D_ + (lane & 7) * 8) * 2u;
    const size_t gofs = ((size_t)(kq * 256 + sw * 64) * F_ + gr * 64) * 2;
    const char* pG = (const char*)wg + gofs;
    const char* pU = (const char*)wu + gofs;
    const char* pD = (const char*)(wd + (size_t)(gr * 64) * D_ + kq * 256 + sw * 64);
    auto wstep = [&](auto I) {
      constexpr int i = I.v;
      if (i < 8) wload<VW + 4 * i>(voG, pG + (i & 7) * (F_ * 2));
      else if (i < 16) wload<VW + 4 * i>(voG, pU + (i & 7) * (F_ * 2));
      else wload2<VW + 4 * i>(voD, pD + (i & 7) * (D_ * 2));
    };
    unsigned voW;
    {
      const int bi = lane >> 4, q = (lane >> 2) & 3, ch = lane & 3;
      voW = (unsigned)((4 * (bi >> 1) + q) * D_ + (bi & 1) * 32 + (ch ^ (bi & 2)) * 8) * 2u;
    }
    const char* baseW = (const char*)(wo + (size_t)((16 * (sw >> 1)) * 32 + 16 * (sw & 1)) * D_ + cb * 128) - 1024;
    auto stageW = [&](auto J, bool rsv) {
      const char* s_ = baseW + (size_t)whead(J.v) * (32 * D_ * 2);
      if (rsv) glds4o<wo_slot(J.v)>(voW + 1024, voW + 128, voW + 8 * D_ * 2 - 1024, voW + 8 * D_ * 2 + 128 - 2048, s_, l4);
      else glds4<wo_slot(J.v)>(voW + 1024, voW + 128, voW + 8 * D_ * 2 - 1024, voW + 8 * D_ * 2 + 128 - 2048, s_, l4);
    };
    int sta[4], stb[3];
    {
      const int row = t2 >> 1, key = (row >> 1) & 7;
#pragma unroll
      for (int c = 0; c < 4; ++c) { sta[c] = (posA & 3) * K1_STAGE + row * 128 + (((4 * (t2 & 1) + c) ^ key) * 16); asm volatile("" : "+v"(sta[c]) :: RSV); }
#pragma unroll
      for (int i = 0; i < 3; ++i) {
        const int G = sw * 3 + i, hd = G >= 6 ? 1 : 0, mat = (G - 6 * hd) >> 1;
        stb[i] = (posB & 3) * K1_STAGE + 16384 + (lane >> 4) * 6144 + mat * 1024 + hd * 256 + ((lane & 15) >> 2) * 64 + (G & 1) * 32 + (lane & 3) * 8;
        asm volatile("" : "+v"(stb[i]) :: RSV);
      }
    }
    unsigned rs_dst = rs_slot + t2 * 4, cs_dst = lane < 48 ? cs_slot + (sw * 48 + lane) * 4 : OOB;
    unsigned rs_off = WS_RS + rb * 16384 + (lane >> 4) * 1024 + sw * 256 + (lane & 15) * 16;
    unsigned cs_off = WS_CS + hp * 12288 + (lane >> 4) * 768 + sw * 192 + min(lane & 15, 11) * 16;
    asm volatile("" : "+v"(rs_dst), "+v"(cs_dst), "+v"(rs_off), "+v"(cs_off) :: RSV);
    int stm_dst = ((lane >> 5) * 128 + sw * 32 + (lane & 15) * 2 + ((lane >> 4) & 1)) * 4, cs_put = (lane & 15) < 12 ? (256 + sw * 48 + (lane & 15) * 4 + (lane >> 4)) * 4 : 1792 + lane * 4;
    asm volatile("" : "+v"(stm_dst), "+v"(cs_put) :: RSV);
    auto st_load = [&]() { sfor<0, 4>([&](auto I) { sload<224 + 4 * I.v>(rs_off, wsu + I.v * 4096); sload<240 + 4 * I.v>(cs_off, wsu + I.v * 3072); }); };
    sfor<3, GS[0]>(group);
    WAIT_VR(gops(GS[0]) - 10 + 1); RAW_BARR();
    TSL(14);
    sfor<GS[0], GS[1]>(group);
    WAIT_VR(gops(GS[1]) - 20); RAW_BARR();
    hs_g0 = __builtin_amdgcn_readlane(rget<156>(), 1);
    if (ms_act) {
      const u32x4 re = {rget<152>(), rget<153>(), rget<154>(), rget<155>()}, g0 = {rget<148>(), rget<149>(), rget<150>(), rget<151>()};
#pragma unroll
      for (int j = 0; j < 4; ++j) { const unsigned c = re[j] + 1u; ms_exp[j] = (c == g0[j]) ? c + 1u : c; }
      STW(ms_exp, SY_CH + ms_off);
    }
    u32x4 ra[4] = {}; u32x2 rc[3][4] = {};
    float sa = 0.f, sb = 0.f, qa = 0.f, qb = 0.f, ca[3] = {0.f, 0.f, 0.f}, cb_[3] = {0.f, 0.f, 0.f};
    auto chunk = [&](auto C) {
      constexpr int c = C.v;
      if constexpr (c < 4) {
        asm volatile("" : "+v"(ra[c]) :: RSV);
#pragma unroll
        for (int i = 0; i < 4; ++i) { const float lo = bflo(ra[c][i]), hi = bfhi(ra[c][i]); sa = fadd(sa, lo); sb = fadd(sb, hi); qa = fsq(lo, qa); qb = fsq(hi, qb); }
        asm volatile("" : "+v"(sa), "+v"(sb), "+v"(qa), "+v"(qb) :: RSV);
      } else if constexpr (c == 4) {
        float s = fadd(sa, sb), sq = fadd(qa, qb);
        s = fadd(s, dppf<DPP_X1>(s)); sq = fadd(sq, dppf<DPP_X1>(sq));
        STW1(sane1(__float_as_uint((t2 & 1) ? sq : s)), rs_dst);
      } else if constexpr (c < 11) {
        constexpr int i = (c - 5) >> 1, j = ((c - 5) & 1) * 2;
        asm volatile("" : "+v"(rc[i][j]), "+v"(rc[i][j + 1]) :: RSV);
#pragma unroll
        for (int e = 0; e < 4; ++e) { const unsigned u = rc[i][j + (e >> 1)][e & 1]; ca[i] = fadd(ca[i], bflo(u)); cb_[i] = fadd(cb_[i], bfhi(u)); }
        asm volatile("" : "+v"(ca[i]), "+v"(cb_[i]) :: RSV);
      } else {
        const float c2 = fadd(ca[2], cb_[2]);
        STW1(sane1(__float_as_uint(sw32(sw16(fadd(ca[0], cb_[0]), fadd(ca[1], cb_[1])), sw16(c2, c2)))), cs_dst);
      }
    };
    sfor<0, 12>([&](auto T) {
      constexpr int t = T.v;
      TSL(t);
      if (t == 4) STAMPT(1, 9, 256);
      if (t < 4 && posA == t + 1) {
#pragma unroll
        for (int c = 0; c < 4; ++c) ra[c] = *(const u32x4*)(smem + sta[c]);
      }
      if (t < 4 && posB == t + 1) {
#pragma unroll
        for (int i = 0; i < 3; ++i)
#pragma unroll
          for (int j = 0; j < 4; ++j) rc[i][j] = trread(smem + (stb[i] ^ ((j >> 1) * 32)) + (j >> 1) * 3072 + (j & 1) * 512);
      }
      if (t == 1 + PFO) HS_PROBE();
      if (t >= 3 + PFO) hs_step(t == 3 + PFO);
      sfor<GS[t + 1], GS[t + 2]>([&](auto G) {
        group(G);
        constexpr int u = t - CH0, p = G.v - GS[t + 1];
        constexpr int ord[12] = {0, 5, 6, 1, 7, 8, 2, 9, 10, 3, 11, 4};
        if constexpr (u >= 0 && CHN * u + p < 12 && p < CHN) chunk(IC<ord[(CHN * u + p) % 12]>{});
      });
      if (P2MAP == 0 && t >= TCH0 && t < TCH0 + 8 && sw == ((t - TCH0) & 3))
        asm volatile("s_nop 4\n\tglobal_load_dword v157, %0, %1" :: "v"(lane * 128), "s"(uni((const char*)wo + idx * 65536 + ((t - TCH0 + xcd) & 7) * 8192)) : "memory", RSV);
      if (P2MAP == 1 && t >= TCH0 && t < TCH0 + 4 && sw == ((t - TCH0) & 3))
        asm volatile("s_nop 4\n\tglobal_load_dword v157, %0, %1" :: "v"((lane >> 3) * 2048 + (lane & 7) * 128), "s"(uni((const char*)wo + idx * 65536 + ((t - TCH0 + (rp >> 3)) & 3) * 16384 + (cb & 4) * 256)) : "memory", RSV);
      if (real) sfor<WS_[t], WS_[t + 1]>(wstep);
      TSL(16 + t);
      WAIT_VR(t < 11 ? gops(GS[t + 2]) - 10 * (t + 3) : gops(GS[13]) - gops(GS[12]));
      TSL(32 + t);
      RAW_BARR();
    });
    TSL(12);
    STAMPT(1, 10, 256);
    hs_step(false);
    auto st_put = [&]() -> bool {
      float a[8]; unsigned m = 0, n = 0;
      rsum4<224>(a, m, n); rsum4<240>(a + 4, m, n);
      const float s1 = sw16(a[0], a[2]), q1 = sw16(a[1], a[3]);
      const float cs = sw32(sw16(a[4], a[5]), sw16(a[6], a[7]));
      const float mean = sw32(s1, s1) * (1.0f / 1024.0f);
      const float var = fmaxf(__builtin_fmaf(sw32(q1, q1), 1.0f / 1024.0f, -(mean * mean)), 0.f);
      const float rinv = __builtin_amdgcn_rcpf(rbf(__builtin_amdgcn_sqrtf(rbf(rbf(var) + 1e-5f))));
      *(float*)(smem + L_STM + stm_dst) = (lane & 32) ? rinv : rbf(mean);
      *(float*)(smem + L_STM + cs_put) = cs;
      return wave_ok(max(m, n));
    };
    st_load();
    if (real) sfor<WS_[12], W1A>(wstep);
    TSL(28);
    if (real) WAIT_VR(8 + W1A - WS_[11]); else WAIT_VR(8);
    if (lane == 0) { misc[8 + sw] = 0; misc[12 + sw] = 0; misc[16 + sw] = 0; misc[7] = 0; misc[6] = 0; }
    TSL(44);
    RAW_BARR();
    TSL(13); TSDUMP(1 + sw);
    STAMPT(1, 0, 256);
    if (real) WAIT_VR(W1A - WS_[12]); else WAIT_VR(0);
    if (!st_put() || hs_act) {
      if (hs_act) hs_act = __builtin_amdgcn_readlane(rget<156>(), 1) == hs_g0;
      if (ms_act) {
        SPIN {
          bool ok = true;
#pragma unroll
          for (int j = 0; j < 4; ++j) ok &= (ld32(ws, SY_RE + lane * 16 + j * 4) == ms_exp[j]);
          if (__all(ok)) break;
        }
        STW(ms_exp, SY_GO + lane * 16);
      }
      if (hs_act) {
        SPIN {
          if (ld32(ws, SY_GO + bid * 4) != hs_g0) break;
          const unsigned c = ld32(ws, SY_CH + bid * 4);
          if (c != hs_cur) { hs_cur = c; STW1(c, re_off); }
        }
      }
      SPIN { st_load(); WAIT_VR(0); if (st_put()) break; }
    }
    asm volatile("" ::: "memory", RSV);
    if (lane == 0) misc[12 + sw] = 1;
    __builtin_amdgcn_s_setprio(0);
    if (real) sfor<W1A, 16>(wstep);
    auto mwait = [&](int v) { SPIN { const int a = misc[8 + (lane & 3)]; if (__all(a >= v)) break; __builtin_amdgcn_s_sleep(1); } asm volatile("" ::: "memory"); };
    sfor<0, 9>([&](auto J) {
      if (J.v == 4) mwait(3);
      if (J.v == WOD) { SPIN { if (misc[WODF] == 1) break; __builtin_amdgcn_s_sleep(2); } SLEEP(WODS); }
      stageW(J, J.v < 2);
      if (J.v >= 1 && J.v < WOD) WAIT_V(4 * PACE);
      if (J.v == 1) { WAIT_VR(8); sfor<0, 16>(wtr); }
      if (J.v >= 2 && PSLP) __builtin_amdgcn_s_sleep(PSLP);
    });
    STAMPT(1, 1, 256);
    EPI();
#pragma unroll
    for (int i = 0; i < 2; ++i) { xv[i] = *(const u32x4*)(x + ego + i * 64); wv[i] = *(const u32x4*)(w_r + cb * 128 + eseg * 8 + i * 64); }
    asm volatile("s_waitcnt vmcnt(0)" : "+v"(xv[0]), "+v"(xv[1]), "+v"(wv[0]), "+v"(wv[1]) :: "memory");
    unpack_xw();
    RAW_BAR();
    RAW_BAR(); stageW(IC<9>{}, false); stageW(IC<10>{}, false);
    RAW_BAR(); stageW(IC<11>{}, false); stageW(IC<12>{}, false);
    RAW_BAR(); stageW(IC<13>{}, false); stageW(IC<14>{}, false);
    WAIT_V(16); RAW_BAR(); stageW(IC<15>{}, false);
    WAIT_V(12); RAW_BAR();
    WAIT_V(0); RAW_BAR();
    if (real) sfor<16, 24>(wstep);
  }
  LANES(); EPI();
  const int grow = rp * 64 + erow;
  int pa0 = L_P + erow * 512 + ((2 * eseg) ^ (erow & 7)) * 16, pa1 = pa0 ^ 16;
  unsigned st_dst = eseg == 0 ? WS_ST2 + ((rp * 8 + cb) * 64 + erow) * 16 : OOB, st_src = WS_ST2 + rp * 8192 + lane * 16;
  unsigned fd_dst = lane < 8 ? WS_FD + (cb * 32 + rp) * 128 + lane * 16 : OOB, kidx = 0xFFFFu - grow;
  asm volatile("" : "+v"(pa0), "+v"(pa1), "+v"(st_dst), "+v"(st_src), "+v"(fd_dst), "+v"(kidx));
  RAW_BAR2();
  STAMPT(0, 9, 0);
  unsigned hb[8];
  f32x2 hv[8];
  {
    f32x4 pp[2][4];
#pragma unroll
    for (int k = 0; k < 2; ++k)
#pragma unroll
      for (int i = 0; i < 4; ++i) pp[k][i] = *(const f32x4*)(smem + ((i & 1) ? pa1 : pa0) + (i >> 1) * 256 + k * L_PK);
    SB0();
    f32x2 s = {0.f, 0.f}, q = {0.f, 0.f}, p2 = {0.f, 0.f};
#pragma unroll
    for (int i = 0; i < 8; ++i) {
      const f32x4 a = pp[0][i >> 1] + pp[1][i >> 1];
      const f32x2 u = xf[i] + unpk(pack2o(a[2 * (i & 1)], a[2 * (i & 1) + 1]));
      hb[i] = pack2o(u[0], u[1]);
      hv[i] = unpk(hb[i]);
      s += hv[i]; q += hv[i] * hv[i]; p2 += hv[i] * wf2[i];
    }
    STW(sane((u32x4){__float_as_uint(oct_sum(s[0] + s[1])), __float_as_uint(oct_sum(q[0] + q[1])), __float_as_uint(oct_sum(p2[0] + p2[1])), __float_as_uint(sw_)}), st_dst);
  }
  KEEP2();
  STAMPT(0, 10, 0);
  const int sw = wave - 4;
  const unsigned fd_src = WS_FD + xcd * 4096 + r * 128;
  u32x2 kpk = {SENT, SENT};
  if (wave == 0) {
    u32x4 pa[8];
    SLEEP(P_C);
    SPIN {
      unsigned m = 0;
#pragma unroll
      for (int j = 0; j < 8; ++j) { pa[j] = LDW(st_src + j * 1024); m = chk(m, pa[j]); }
      CNT(3);
      if (wave_ok(m)) break;
      KEEP2();
    }
    STAMPT(0, 11, 0);
#define F4(v) __builtin_bit_cast(f32x4, v)
    const f32x4 t = ((F4(pa[0]) + F4(pa[1])) + (F4(pa[2]) + F4(pa[3]))) + ((F4(pa[4]) + F4(pa[5])) + (F4(pa[6]) + F4(pa[7])));
    const float mean = t[0] * (1.0f / 1024.0f);
    const float var = fmaxf(__builtin_fmaf(t[1], 1.0f / 1024.0f, -(mean * mean)), 0.f);
    const float mb = rbf(mean), rs = __builtin_amdgcn_rcpf(rbf(__builtin_amdgcn_sqrtf(rbf(rbf(var) + 1e-5f))));
    *(f32x2*)(smem + L_KX + lane * 8) = (f32x2){mb, rs};
    const float sc = fmaxf((t[2] - mb * t[3]) * rs, -INFINITY);
    const int u = (int)(pack2(0.f, sc) & 0xffff0000u);
    unsigned b0 = ((unsigned)(u ^ ((u >> 31) | (int)0x80000000)) & 0xffff0000u) | (0xFFFFu - (rp * 64 + lane)), b1 = 0;
    top2_lo(b0, b1); top2_hi(b0, b1);
    STW(((u32x4){b0, b1, b0, b1}), fd_dst);
    if (lane == 0) *(u32x2*)(smem + L_MISC + 112) = (u32x2){b0, b1};
  } else if (wave >= 4) {
    asm volatile("s_waitcnt vmcnt(0)" ::: "memory", RSV2);
    sfor<16, 24>(wtr);
  }
  RAW_BAR();
  bool cand;
  u32x4 ob[2];
  {
    const f32x2 mr = *(const f32x2*)(smem + L_KX + erow * 8);
    const u32x2 kk = *(const u32x2*)(smem + L_MISC + 112);
    const float mb = mr[0], rs = mr[1];
    const unsigned b0 = kk[0], b1 = kk[1];
    u32x4 nq[2];
#pragma unroll
    for (int i = 0; i < 8; ++i) {
      const f32x2 d = hv[i] - mb;
      const f32x2 n = unpk(pack2o(d[0], d[1])) * rs;
      nq[i >> 2][i & 3] = pack2o(n[0], n[1]);
    }
    const bool c2 = (b1 & 0xffffu) == kidx;
    cand = c2 || (b0 & 0xffffu) == kidx;
    if (cand) {
      const unsigned xo = WS_XS + (rp * 2 + (c2 ? 1 : 0)) * 2048 + cb * 256 + eseg * 16;
      STW(sane(nq[0]), xo); STW(sane(nq[1]), xo + 128);
      char* cp = smem + L_CAND + (c2 ? 512 : 0) + eseg * 16;
      *(u32x4*)cp = (u32x4){hb[0], hb[1], hb[2], hb[3]}; *(u32x4*)(cp + 128) = (u32x4){hb[4], hb[5], hb[6], hb[7]};
      *(u32x4*)(cp + 256) = nq[0]; *(u32x4*)(cp + 384) = nq[1];
    }
    asm volatile("" ::: "memory");
    if (wave >= 4) { SLEEP(P_D); kpk = __builtin_amdgcn_raw_buffer_load_b64(wsr, fd_src, 0, 16); }
    asm volatile("" ::: "memory");
#pragma unroll
    for (int i = 0; i < 8; ++i) { const f32x2 o = hv[i] + unpk(nq[i >> 2][i & 3]); ob[i >> 2][i & 3] = pack2(o[0], o[1]); }
  }
#define OUT_ST() do { *(u32x4*)(out + ego) = ob[0]; *(u32x4*)(out + ego + 64) = ob[1]; } while (0)
  STAMPT(0, 12, 0);
  if (wave < 4) {
    if (!cand) OUT_ST();
    RAW_BAR();
    const unsigned w0 = misc[24], w1 = misc[25];
    if (cand && grow != (int)key_idx(w0) && grow != (int)key_idx(w1)) OUT_ST();
#pragma unroll 1
    for (int tk = 0; tk < 2; ++tk) {
      const unsigned wk = tk ? w1 : w0, slot = misc[26 + tk];
      if ((int)(slot >> 1) != rp) continue;
      u32x4 pv[7];
      const unsigned yo = WS_YP + ((tk * 8 + cb) * 64 + (lane >> 3) * 8) * 512 + wave * 128 + (lane & 7) * 16;
      const int col = (wave >> 1) * 64 + (lane & 7) * 8 + (wave & 1) * 4 + (lane >> 4);
      float sg = rbf(__builtin_amdgcn_rcpf(1.0f + __expf(-key_val(wk))));
      float hh = bflo(*(const u16*)(smem + L_CAND + (slot & 1) * 512 + col * 2)), nn = bflo(*(const u16*)(smem + L_CAND + (slot & 1) * 512 + 256 + col * 2));
      u16* op = out + (size_t)key_idx(wk) * D_ + cb * 128 + col;
      asm volatile("" : "+v"(sg), "+v"(hh), "+v"(nn), "+v"(op));
      if (tk == 0 || (int)(misc[26] >> 1) != rp) {
        SPIN { if (misc[30] == (int)w0) break; __builtin_amdgcn_s_sleep(1); }
        SLEEP(P_E);
      }
      SPIN {
        unsigned m = 0;
#pragma unroll
        for (int p = 0; p < 7; ++p) { pv[p] = LDW(yo + p * 512); m = chk(m, pv[p]); }
        CNT(6);
        if (wave_ok(m)) break;
        asm volatile("" ::: "memory");
        SLEEP(L_E);
      }
      STAMPT(0, 13, 0);
      f32x4 s = __builtin_bit_cast(f32x4, pv[0]);
#pragma unroll
      for (int p = 1; p < 7; ++p) s += __builtin_bit_cast(f32x4, pv[p]);
      float y = sw32(sw16(s[0], s[1]), sw16(s[2], s[3]));
      y += dppf<DPP_ROR8>(y);
      if (!(lane & 8)) *(GAS u16*)op = (u16)pack2(hh + rbf(nn + rbf(rbf(y) * sg)), 0.f);
      STAMPT(0, 14, 0);
    }
  } else {
    unsigned xl = WS_XS + (kq * 256 + sw * 64 + (lane >> 4) * 16 + ((lane >> 2) & 1) * 8) * 2;
    int red_dst = (lane < 8 || (lane >= 24 && lane < 32)) ? L_RED + sw * 2048 + ((lane >> 3) & 1) * 1024 + (lane & 7) * 16 : L_RED + 8192 + lane * 16;
    unsigned gu_src = WS_GU + g8 * 4096 + lane * 16;
    int act_dst = L_ACT + sw * 256 + ((lane & 7) * 8 + (lane >> 3)) * 2, act_src = L_ACT + sw * 256 + (lane & 1) * 128 + (lane >> 4) * 32 + ((lane >> 1) & 1) * 16;
    unsigned yp_dst = lane < 16 ? WS_YP + (((lane >> 3) * 8 + 2 * kq + (sw >> 1)) * 64 + g8) * 512 + (sw & 1) * 256 + (lane & 7) * 16 : OOB;
    asm volatile("" : "+v"(xl), "+v"(red_dst), "+v"(gu_src), "+v"(act_dst), "+v"(act_src), "+v"(yp_dst));
    unsigned w0, w1; int s0 = -1, s1 = -1;
    u32x4 xa = {SENT, SENT, SENT, SENT};
    SPIN {
      CNT(4);
      const bool all = wave_ok(kpk[1]);
      const unsigned k0 = kpk[1] == SENT ? 0u : kpk[0], k1 = kpk[1] == SENT ? 0u : kpk[1];
      w0 = k0; w1 = k1;
      top2_lo(w0, w1); top2_mid(w0, w1);
      const int n0 = 2 * __builtin_ctzll(__ballot(k0 == w0));
      const u64 m1 = __ballot(k0 == w1);
      const int n1 = m1 ? 2 * __builtin_ctzll(m1) : n0 + 1;
      const bool same = n0 == s0 && n1 == s1;
      s0 = n0; s1 = n1;
      asm volatile("" ::: "memory");
      if (all && same) break;
      if (!all) { SLEEP(L_D); kpk = __builtin_amdgcn_raw_buffer_load_b64(wsr, fd_src, 0, 16); }
      xa = LDW(xl + ((lane & 1) ? s1 : s0) * 2048);
      if (all) break;
    }
    SPIN {
      if (wave_ok(chk(0u, xa))) break;
      asm volatile("" ::: "memory");
      xa = LDW(xl + ((lane & 1) ? s1 : s0) * 2048);
    }
    STAMPT(1, 3, 256);
    {
      const u32x4 z4 = {0u, 0u, 0u, 0u};
      const u32x4 xg = (lane & 2) ? z4 : xa, xu = (lane & 2) ? xa : z4;
      f32x4 d[8];
#pragma unroll
      for (int c = 0; c < 8; ++c) d[c] = mfma16(xg, wf[c], (f32x4){0.f, 0.f, 0.f, 0.f});
      if (tid == 256) { *(u32x4*)(smem + L_MISC + 96) = (u32x4){w0, w1, (unsigned)s0, (unsigned)s1}; misc[30] = 0; }
      SB0();
#pragma unroll
      for (int c = 0; c < 10; ++c) {
        if (c < 8) d[c & 7] = mfma16(xu, wf[8 + (c & 7)], d[c & 7]);
        if (c >= 2) *(f32x4*)(smem + red_dst + (c - 2) * 128) = d[(c - 2) & 7];
        SB0();
      }
    }
    RAW_BAR();
    if (sw == 0) {
      f32x4 t[8];
#pragma unroll
      for (int p = 0; p < 8; ++p) t[p] = *(const f32x4*)(smem + L_RED + p * 1024 + lane * 16);
      SB0();
      STW(sane(__builtin_bit_cast(u32x4, ((t[0] + t[1]) + (t[2] + t[3])) + ((t[4] + t[5]) + (t[6] + t[7])))), gu_src + kq * 1024);
    } else __builtin_amdgcn_s_sleep(2);
    STAMPT(1, 4, 256);
    {
      u32x4 q[4];
      if (OUTE && grow != (int)key_idx(w0) && grow != (int)key_idx(w1)) OUT_ST();
      asm volatile("" ::: "memory");
      SLEEP(P_GU);
      SPIN {
        unsigned m = 0;
#pragma unroll
        for (int j = 0; j < 4; ++j) { q[j] = LDW(gu_src + j * 1024); m = chk(m, q[j]); }
        CNT(5);
        if (wave_ok(m)) break;
        asm volatile("" ::: "memory");
      }
      STAMPT(1, 5, 256);
      const f32x4 tot = (__builtin_bit_cast(f32x4, q[0]) + __builtin_bit_cast(f32x4, q[1])) + (__builtin_bit_cast(f32x4, q[2]) + __builtin_bit_cast(f32x4, q[3]));
      const f32x2 gs = unpk(pack2o(tot[0], tot[1])), us = unpk(pack2o(tot[2], tot[3]));
      const f32x2 sg = unpk(pack2o(gs[0] * __builtin_amdgcn_rcpf(1.0f + __expf(-gs[0])), gs[1] * __builtin_amdgcn_rcpf(1.0f + __expf(-gs[1]))));
      const unsigned a2 = pack2o(sg[0] * us[0], sg[1] * us[1]);
      *(u16*)(smem + act_dst) = (u16)a2; *(u16*)(smem + act_dst + 128) = (u16)(a2 >> 16);
      __builtin_amdgcn_fence(__ATOMIC_RELEASE, "wavefront"); __builtin_amdgcn_wave_barrier(); __builtin_amdgcn_fence(__ATOMIC_ACQUIRE, "wavefront");
      const u32x4 aa = *(const u32x4*)(smem + act_src);
      f32x4 d[8];
#pragma unroll
      for (int c = 0; c < 8; ++c) d[c] = mfma16(aa, wf[16 + c], (f32x4){0.f, 0.f, 0.f, 0.f});
      SB0();
      u32x4 y[2];
#pragma unroll
      for (int c = 0; c < 8; ++c) {
        const float t0 = d[c][0] + dppf<DPP_ROR8>(d[c][2]), t1 = d[c][3] + dppf<DPP_ROR8>(d[c][1]);
        y[c >> 2][c & 3] = __float_as_uint((lane & 8) ? t1 : t0);
      }
      STW(sane(y[0]), yp_dst); STW(sane(y[1]), yp_dst + 128);
      if (tid == 256) misc[30] = w0;
    }
    STAMPT(1, 6, 256);
    if (!OUTE && grow != (int)key_idx(w0) && grow != (int)key_idx(w1)) OUT_ST();
  }
  if (probe_t != 0 && (PROBE_B < 0 || (int)blockIdx.x == PROBE_B)) {
    for (unsigned s_ = 0; s_ < (1u << 20); ++s_) { if (__builtin_amdgcn_s_memrealtime() >= probe_t + 6000) break; __builtin_amdgcn_s_sleep(1); }
  }
}

extern "C" void kernel_launch(void* const* d_in, const int* in_sizes, int n_in, void* d_out, int out_size,
                              void* d_ws, size_t ws_size, hipStream_t stream) {
  (void)hipFuncSetAttribute((const void*)fused, hipFuncAttributeMaxDynamicSharedMemorySize, LDS_TOTAL);
  fused<<<256, 512, LDS_TOTAL, stream>>>((const u16*)d_in[0], (const u16*)d_in[1], (const u16*)d_in[2], (const u16*)d_in[4],
                                         (const u16*)d_in[5], (const u16*)d_in[6], (const u16*)d_in[7], (u16*)d_out, (char*)d_ws);
}
```

```cpp
#include <hip/hip_runtime.h>
#include <stdint.h>

#define LAS __attribute__((address_space(3)))
#define GAS __attribute__((address_space(1)))
#define DI __device__ __forceinline__

typedef unsigned short u16;
typedef unsigned long long u64;
typedef __attribute__((ext_vector_type(2))) __bf16 bf16x2;
typedef __attribute__((ext_vector_type(8))) __bf16 bf16x8;
typedef __attribute__((ext_vector_type(4))) short s16x4;
typedef __attribute__((ext_vector_type(2))) unsigned short u16x2;
typedef __attribute__((ext_vector_type(4))) float f32x4;
typedef __attribute__((ext_vector_type(2))) float f32x2;
typedef __attribute__((ext_vector_type(4))) unsigned u32x4;
typedef __attribute__((ext_vector_type(2))) unsigned u32x2;
typedef GAS unsigned gu32;
typedef GAS unsigned long long gu64;

constexpr int S_ = 2048, D_ = 1024, F_ = 3584;

constexpr unsigned SY_CH = 0, SY_GO = 1024, SY_RE = 2048;
constexpr unsigned WS_FD  = 98304;
constexpr unsigned WS_ZF  = 8192;
constexpr unsigned WS_KVF = 16384;
constexpr unsigned WS_RS  = 131072;
constexpr unsigned WS_CS  = WS_RS + 16 * 16 * 1024;
constexpr unsigned WS_ZP  = WS_CS + 16 * 16 * 768;
constexpr unsigned WS_FB  = WS_ZP + 32 * 16 * 128;
constexpr unsigned WS_ST2 = WS_FB + 32 * 32 * 128;
constexpr unsigned WS_XS  = WS_ST2 + 32 * 8 * 1024;
constexpr unsigned WS_GU  = WS_XS + 64 * 2048;
constexpr unsigned WS_YP  = WS_GU + 256 * 1024;
constexpr unsigned WS_KVP = WS_YP + 2 * 8 * 64 * 512;
constexpr unsigned WS_O   = WS_KVP + 32 * 16 * 4096;
constexpr unsigned WS_END = WS_O + S_ * D_ * 2;
constexpr unsigned OOB = 0x80000000u;

template <int V> struct IC { static constexpr int v = V; };
template <int B, int E, class F> DI void sfor(F&& f) { if constexpr (B < E) { f(IC<B>{}); sfor<B + 1, E>(f); } }

DI float bflo(unsigned u) { return __uint_as_float(u << 16); }
DI float bfhi(unsigned u) { return __uint_as_float(u & 0xffff0000u); }
DI float rbf(float x) { return (float)(__bf16)x; }
DI unsigned pack2(float a, float b) { bf16x2 c; c[0] = (__bf16)a; c[1] = (__bf16)b; return __builtin_bit_cast(unsigned, c); }
DI unsigned pack2o(float a, float b) { unsigned p = pack2(a, b); asm("" : "+v"(p)); return p; }
DI f32x4 mfma16(u32x4 a, u32x4 b, f32x4 c) {
  return __builtin_amdgcn_mfma_f32_16x16x32_bf16(__builtin_bit_cast(bf16x8, a), __builtin_bit_cast(bf16x8, b), c, 0, 0, 0);
}
DI void mfma16i(u32x4 a, u32x4 b, f32x4& c) { asm volatile("v_mfma_f32_16x16x32_bf16 %0, %1, %2, %0" : "+v"(c) : "v"(a), "v"(b)); }
DI f32x2 unpk(unsigned u) { return (f32x2){bflo(u), bfhi(u)}; }
DI u32x2 trread(const char* p) {
  s16x4 v = __builtin_amdgcn_ds_read_tr16_b64_v4i16((LAS s16x4*)p);
  return __builtin_bit_cast(u32x2, v);
}
DI const char* uni(const void* p) {
  const u64 v = (u64)p;
  return (const char*)(((u64)(unsigned)__builtin_amdgcn_readfirstlane((int)(v >> 32)) << 32) | (unsigned)__builtin_amdgcn_readfirstlane((int)v));
}
#define RV4(a, b, c, d) "v" #a, "v" #b, "v" #c, "v" #d
#define RV10(p) "v" #p "0", "v" #p "1", "v" #p "2", "v" #p "3", "v" #p "4", "v" #p "5", "v" #p "6", "v" #p "7", "v" #p "8", "v" #p "9"
#define RSV2 RV4(224, 225, 226, 227), "v228", "v229", RV10(23), RV10(24), RV4(250, 251, 252, 253), "v254", "v255"
#define RSV1 RV4(192, 193, 194, 195), RV4(196, 197, 198, 199), RV10(20), RV10(21), RV4(220, 221, 222, 223), RSV2
#define RSV "v148", "v149", RV10(15), RV10(16), RV10(17), RV10(18), "v190", "v191", RSV1
#define NTW " nt"
#define GL_(v, s, o) "global_load_lds_dwordx4 " v ", " s " offset:" #o
#define GLDS4(SC, CL...) asm volatile("s_add_u32 m0, %5, %c6\n\ts_nop 3\n\t" GL_("%0", "%4", 0) SC "\n\t" GL_("%1", "%4", 1024) SC "\n\t" \
  GL_("%2", "%4", 2048) SC "\n\t" GL_("%3", "%4", 3072) SC :: "v"(v0), "v"(v1), "v"(v2), "v"(v3), "s"(uni(sbase)), "s"(lds), "i"(IMM) : "memory", "scc", ##CL)
#define GLDS3(SC, CL...) asm volatile("s_add_u32 m0, %4, %c5\n\ts_nop 3\n\t" GL_("%0", "%3", 0) SC "\n\t" GL_("%1", "%3", 1024) SC "\n\t" \
  GL_("%2", "%3", 2048) SC :: "v"(v0), "v"(v1), "v"(v2), "s"(uni(sbase)), "s"(lds), "i"(IMM) : "memory", "scc", ##CL)
template <int IMM> DI void glds4(unsigned v0, unsigned v1, unsigned v2, unsigned v3, const void* sbase, unsigned lds) { GLDS4(""); }
template <int IMM> DI void glds4r(unsigned v0, unsigned v1, unsigned v2, unsigned v3, const void* sbase, unsigned lds) { GLDS4("", RSV); }
template <int IMM> DI void glds3r(unsigned v0, unsigned v1, unsigned v2, const void* sbase, unsigned lds) { GLDS3("", RSV); }
template <int R> DI void sload(unsigned voff, const void* sbase) {
  asm volatile("s_nop 4\n\tglobal_load_dwordx4 v[%c0:%c1], %2, %3 sc1" ::"i"(R), "i"(R + 3), "v"(voff), "s"(uni(sbase)) : "memory", RSV);
}
DI float fadd(float a, float b) { float o; asm("v_add_f32 %0, %1, %2" : "=v"(o) : "v"(a), "v"(b)); return o; }
DI float fsq(float a, float c) { asm("v_fmac_f32 %0, %1, %1" : "+v"(c) : "v"(a)); return c; }
#define RA_(o, a, b) "v_add_f32 %" #o ", v[%c6+" #a "], v[%c6+" #b "]\n\t"
#define RB2_(o, a) "v_add_f32 %" #o ", v[%c6+" #a "], %" #o "\n\t"
#define RM_(o, a, b) "v_max3_u32 %" #o ", v[%c6+" #a "], v[%c6+" #b "], %" #o "\n\t"
template <int R> DI void rsum4(float* a, unsigned& m, unsigned& n) {
  asm volatile(RA_(0, 0, 4) RA_(1, 1, 5) RM_(4, 0, 1) RA_(2, 2, 6) RA_(3, 3, 7) RM_(5, 2, 3) RB2_(0, 8) RM_(4, 4, 5) RB2_(1, 9) RM_(5, 6, 7)
               RB2_(2, 10) RM_(4, 8, 9) RB2_(3, 11) RM_(5, 10, 11) RB2_(0, 12) RB2_(1, 13) RM_(4, 12, 13) RB2_(2, 14) RB2_(3, 15) RM_(5, 14, 15)
               : "=&v"(a[0]), "=&v"(a[1]), "=&v"(a[2]), "=&v"(a[3]), "+v"(m), "+v"(n) : "i"(R) : RSV);
}
template <int R> DI void wload2(unsigned voff, const void* sbase) {
  asm volatile("s_nop 4\n\tglobal_load_dwordx4 v[%c0:%c1], %2, %3" NTW ::"i"(R), "i"(R + 3), "v"(voff), "s"(uni(sbase)) : "memory", RSV2);
}
template <int R> DI void wload(unsigned voff, const void* sbase) {
  asm volatile("s_nop 4\n\tglobal_load_dwordx4 v[%c0:%c1], %2, %3" NTW ::"i"(R), "i"(R + 3), "v"(voff), "s"(uni(sbase)) : "memory", RSV);
}
template <int RA, int RB> DI unsigned wperm(unsigned sel) {
  unsigned o;
  if (RA < 192) asm volatile("v_perm_b32 %0, v%c2, v%c1, %3" : "=v"(o) : "i"(RA), "i"(RB), "s"(sel) : RSV);
  else if (RA < 224) asm volatile("v_perm_b32 %0, v%c2, v%c1, %3" : "=v"(o) : "i"(RA), "i"(RB), "s"(sel) : RSV1);
  else asm volatile("v_perm_b32 %0, v%c2, v%c1, %3" : "=v"(o) : "i"(RA), "i"(RB), "s"(sel) : RSV2);
  return o;
}
template <int R> DI unsigned rget() { unsigned o; asm volatile("v_mov_b32 %0, v%c1" : "=v"(o) : "i"(R) : RSV); return o; }
DI float xh_sum(float v) {
  auto r = __builtin_amdgcn_permlane32_swap(__float_as_uint(v), __float_as_uint(v), false, false);
  return __uint_as_float(r[0]) + __uint_as_float(r[1]);
}
template <int CTRL> DI unsigned dppu(unsigned v) { return (unsigned)__builtin_amdgcn_update_dpp(0, (int)v, CTRL, 0xf, 0xf, true); }
template <int CTRL> DI float dppf(float v) { return __uint_as_float(dppu<CTRL>(__float_as_uint(v))); }
constexpr int DPP_X1 = 0xB1, DPP_X2 = 0x4E, DPP_HMIR = 0x141, DPP_MIR = 0x140, DPP_ROR8 = 0x128;
DI float quad_sum(float v) { v += dppf<DPP_X1>(v); v += dppf<DPP_X2>(v); return v; }
DI float oct_sum(float v) { v = quad_sum(v); v += dppf<DPP_HMIR>(v); return v; }
DI float x16_sum(float v) {
  auto r = __builtin_amdgcn_permlane16_swap(__float_as_uint(v), __float_as_uint(v), false, false);
  return __uint_as_float(r[0]) + __uint_as_float(r[1]);
}
DI float sw16(float a, float b) {
  auto r = __builtin_amdgcn_permlane16_swap(__float_as_uint(a), __float_as_uint(b), false, false);
  return fadd(__uint_as_float(r[0]), __uint_as_float(r[1]));
}
DI float sw32(float a, float b) {
  auto r = __builtin_amdgcn_permlane32_swap(__float_as_uint(a), __float_as_uint(b), false, false);
  return fadd(__uint_as_float(r[0]), __uint_as_float(r[1]));
}
template <int N> struct Phi { unsigned tb[N]; f32x2 tr[N], e[N]; };
template <int N> DI void phi_a(const f32x2* t, Phi<N>& s) {
#pragma unroll
  for (int i = 0; i < N; ++i) s.tb[i] = pack2o(t[i][0], t[i][1]);
  __builtin_amdgcn_sched_barrier(0);
#pragma unroll
  for (int i = 0; i < N; ++i) s.tr[i] = unpk(s.tb[i]);
  __builtin_amdgcn_sched_barrier(0);
#pragma unroll
  for (int i = 0; i < N; ++i) s.e[i] = s.tr[i] * 1.44269504088896340736f;
  __builtin_amdgcn_sched_barrier(0);
}
template <int N> DI void phi_b(Phi<N>& s, unsigned* p) {
  u16x2 tm[2] = {{0, 0}, {0, 0}};
#pragma unroll
  for (int i = 0; i < N; ++i) {
    s.e[i][0] = __builtin_amdgcn_fmed3f(__builtin_amdgcn_exp2f(s.e[i][0]), 0.f, 1.f);
    tm[i & 1] = __builtin_elementwise_max(tm[i & 1], __builtin_bit_cast(u16x2, s.tb[i]));
    s.e[i][1] = __builtin_amdgcn_fmed3f(__builtin_amdgcn_exp2f(s.e[i][1]), 0.f, 1.f);
    __builtin_amdgcn_sched_barrier(0);
  }
#pragma unroll
  for (int i = 0; i < N; ++i) s.tr[i] += s.e[i];
  __builtin_amdgcn_sched_barrier(0);
#pragma unroll
  for (int i = 0; i < N; ++i) { s.tr[i][0] = fmaxf(s.tr[i][0], s.e[i][0]); s.tr[i][1] = fmaxf(s.tr[i][1], s.e[i][1]); }
  const u16x2 tx = __builtin_elementwise_max(tm[0], tm[1]);
  __builtin_amdgcn_sched_barrier(0);
#pragma unroll
  for (int i = 0; i < N; ++i) p[i] = pack2(s.tr[i][0], s.tr[i][1]);
  if (__any(tx[0] > 0xC0F0 || tx[1] > 0xC0F0)) {
#pragma unroll
    for (int i = 0; i < N; ++i) p[i] = pack2(bflo(p[i]) + 1e-6f, bfhi(p[i]) + 1e-6f);
  }
}
template <int N> DI void phi_n(const f32x2* t, unsigned* p) { Phi<N> s; phi_a<N>(t, s); phi_b<N>(s, p); }
#define RLX_AGENT __ATOMIC_RELAXED, __HIP_MEMORY_SCOPE_AGENT
DI void st64(char* ws, unsigned off, u64 v) { __hip_atomic_store((gu64*)(ws + off), v, RLX_AGENT); }
DI unsigned ld32(char* ws, unsigned off) { return __hip_atomic_load((gu32*)(ws + off), RLX_AGENT); }
#define WAIT_V(n) asm volatile("s_waitcnt vmcnt(%c0)" ::"i"(n) : "memory")
#define RAW_BAR() do { asm volatile("s_waitcnt lgkmcnt(0)" ::: "memory"); __builtin_amdgcn_s_barrier(); asm volatile("" ::: "memory"); } while (0)
#define SB0() __builtin_amdgcn_sched_barrier(0)
#define WAIT_VR(n) asm volatile("s_waitcnt vmcnt(%c0)" ::"i"(n) : "memory", RSV)
#define RAW_BARR() do { asm volatile("s_waitcnt lgkmcnt(0)" ::: "memory", RSV); __builtin_amdgcn_s_barrier(); asm volatile("" ::: "memory", RSV); } while (0)
#define KEEP2() asm volatile("" ::: "memory", RSV2)
#define RAW_BAR2() do { asm volatile("s_waitcnt lgkmcnt(0)" ::: "memory", RSV2); __builtin_amdgcn_s_barrier(); KEEP2(); } while (0)
constexpr unsigned SPIN_CAP = 1u << 18;
#define SPIN _Pragma("nounroll") for (unsigned s_ = 0; s_ < SPIN_CAP; ++s_)
constexpr unsigned SENT = 0xFFFFFFFFu;
DI unsigned sane1(unsigned v) { return min(v, 0xFFFFFFFEu); }
DI u32x4 sane(u32x4 v) { v[1] = sane1(v[1]); v[3] = sane1(v[3]); return v; }
DI unsigned chk(unsigned m, u32x4 v) { return max(max(m, v[1]), v[3]); }
DI bool wave_ok(unsigned m) { return !__any(m == SENT); }
DI unsigned key_idx(unsigned k) { return (0xFFFFu - (k & 0xFFFFu)) & (S_ - 1); }
DI float key_val(unsigned k) { const unsigned o = k >> 16; return __uint_as_float(((o & 0x8000u) ? (o & 0x7fffu) : (~o & 0xffffu)) << 16); }
DI void top2_mix(unsigned& b0, unsigned& b1, unsigned c0, unsigned c1) {
  const unsigned lo = min(b0, c0), mx = max(b1, c1);
  b0 = max(b0, c0); b1 = max(lo, mx);
}
DI void top2_lo(unsigned& b0, unsigned& b1) {
  top2_mix(b0, b1, dppu<DPP_X1>(b0), dppu<DPP_X1>(b1));
  top2_mix(b0, b1, dppu<DPP_X2>(b0), dppu<DPP_X2>(b1));
  top2_mix(b0, b1, dppu<DPP_HMIR>(b0), dppu<DPP_HMIR>(b1));
}
DI void top2_mid(unsigned& b0, unsigned& b1) {
  top2_mix(b0, b1, dppu<DPP_MIR>(b0), dppu<DPP_MIR>(b1));
  auto p = __builtin_amdgcn_permlane16_swap(b0, b0, false, false); auto q = __builtin_amdgcn_permlane16_swap(b1, b1, false, false);
  b0 = p[0]; b1 = q[0]; top2_mix(b0, b1, p[1], q[1]);
}
DI void top2_hi(unsigned& b0, unsigned& b1) {
  top2_mid(b0, b1);
  auto p = __builtin_amdgcn_permlane32_swap(b0, b0, false, false); auto q = __builtin_amdgcn_permlane32_swap(b1, b1, false, false);
  b0 = p[0]; b1 = q[0]; top2_mix(b0, b1, p[1], q[1]);
}

constexpr int K1_STAGE = 40960;
constexpr int LDS_TOTAL = 4 * K1_STAGE;
constexpr int L_STM = 0;
constexpr int L_MISC = 2048;
constexpr int L_XK = 5120;
constexpr int L_KX = 3328;
constexpr int L_ACT = 2304;
constexpr int L_CAND = 4096;
constexpr int L_WO = 16384;
constexpr int L_P = 16384, L_PK = 7 * 16384;
constexpr int L_RED = 65536;
constexpr int wo_slot(int j) { return L_WO + (j % 9) * 16384; }
constexpr int CODE_LINES = 12;
#define OW_ 8
constexpr int OW = OW_;
#define W1A_ 16
constexpr int W1A = W1A_;
#define GSCHED 5, 9, 13, 17, 21, 24, 27, 30, 33, 36, 39, 42, 45, 48, 48
constexpr int GS[15] = {GSCHED};
constexpr int gops(int g) { return (g / 3) * 10 + (g % 3 == 0 ? 0 : g % 3 == 1 ? 4 : 7); }
constexpr bool gs_ok() {
  bool ok = GS[0] >= 3 && GS[0] <= 9 && GS[1] >= 6 && GS[1] <= 12 && GS[0] <= GS[1] && GS[12] == 45 && GS[13] == 48 && GS[14] == 48;
  for (int t = 0; t < 12; ++t) ok = ok && GS[t + 2] <= 3 * (t + 5) && GS[t + 2] >= 3 * (t + 3) && GS[t + 2] >= GS[t + 1] + 3;
  return ok;
}
static_assert(gs_ok(), "staging schedule");
#define WSCHED 0, 0, 0, 0, 0, 0, 0, 0, 0, 2, 4, 6, 8
constexpr int WS_[13] = {WSCHED};
#define PACE 1
#define P_KVP 1
#define P_KVF 1
#define P_FB 8
#define P_C 14
#define P_D 2
#define P_GU 12
#define P_E 10
#define SLEEP(n) do { if (n) __builtin_amdgcn_s_sleep(n); } while (0)
constexpr int VW = 160;

__global__ __launch_bounds__(512, 2) void fused(const u16* __restrict__ x, const u16* __restrict__ wqkv,
                                                const u16* __restrict__ wo, const u16* __restrict__ w_r,
                                                const u16* __restrict__ wg, const u16* __restrict__ wu,
                                                const u16* __restrict__ wd, u16* __restrict__ out, char* __restrict__ ws) {
  extern __shared__ __attribute__((aligned(16))) char smem[];
  u64 pc0; asm volatile("s_getpc_b64 %0" : "=s"(pc0));
  const int wave = __builtin_amdgcn_readfirstlane(threadIdx.x >> 6);
  const unsigned lds0 = (unsigned)(size_t)smem;
  if (wave == 0) {
    const int l = threadIdx.x;
    asm volatile("s_mov_b32 m0, %1\n\ts_nop 0\n\tglobal_load_lds_dword %0, off"
                 :: "v"((const char*)(pc0 - (pc0 & 127)) + ((blockIdx.x >> 3) + (l < CODE_LINES ? l : 0) * 32) * 128), "s"(__builtin_amdgcn_readfirstlane(lds0 + 3 * K1_STAGE)) : "memory");
  }
#define RB_(b) (((b) & 1) * 8 + (((b) >> 3) & 7))
#define HP_(b) ((((b) & 7) >> 1) * 4 + ((b) >> 6))
#define KROT_(b) ((4 * ((HP_(b) >> 2) + (RB_(b) >> 3) + 2 * ((RB_(b) >> 2) & 1))) & 15)
  unsigned voA0 = 0, voA1 = 0, voA2 = 0, voA3 = 0, voB0 = 0, voB1 = 0, voB2 = 0, voB3 = 0, voB4 = 0, voB5 = 0;
  const unsigned l4 = __builtin_amdgcn_readfirstlane(lds0 + (wave - 4) * 4096), l6 = __builtin_amdgcn_readfirstlane(lds0 + (wave - 4) * 6144);
  const int krot0 = KROT_(blockIdx.x);
  auto group = [&](auto G) {
    constexpr int t = G.v / 3, p = G.v % 3, sl = (t & 3) * K1_STAGE;
    const int kt = (t + krot0) & 15;
    if (p == 0) glds4r<sl>(voA0, voA1, voA2, voA3, (const char*)x + kt * 128, l4);
    else if (p == 1) glds3r<sl + 16384>(voB0, voB1, voB2, (const char*)wqkv + kt * 64 * (3 * D_ * 2), l6);
    else glds3r<sl + 16384 + 3072>(voB3, voB4, voB5, (const char*)wqkv + kt * 64 * (3 * D_ * 2), l6);
  };
  if (wave >= 4) {
    const int lane = threadIdx.x & 63, sw = wave - 4;
    const int r0 = RB_(blockIdx.x) * 128 + 32 * sw + (lane >> 3);
    voA0 = (unsigned)(r0 * D_ + ((lane & 7) ^ (lane >> 4)) * 8) * 2u;
    voA1 = ((voA0 + 8 * D_ * 2) ^ 64) - 1024; voA2 = voA0 + 16 * D_ * 2 - 2048; voA3 = voA1 + 16 * D_ * 2 - 2048;
    voB0 = (unsigned)((16 * sw + 4 * (lane >> 5) + ((lane >> 2) & 3)) * (3 * D_) + HP_(blockIdx.x) * 64 + ((lane >> 4) & 1) * 32 + (lane & 3) * 8) * 2u;
    voB1 = voB0 + 1024; voB2 = voB0 + 2048;
    voB3 = (voB0 ^ 32) + 8 * 3 * D_ * 2; voB4 = voB3 + 1024; voB5 = voB3 + 2048;
    asm volatile("" : "+v"(voA0), "+v"(voA1), "+v"(voA2), "+v"(voA3), "+v"(voB0), "+v"(voB1), "+v"(voB2), "+v"(voB3), "+v"(voB4), "+v"(voB5));
    sfor<0, 3>(group);
  }
  int bid = blockIdx.x; asm volatile("" : "+s"(bid));
  const int xcd = bid & 7, idx = bid >> 3;
  const int rb = RB_(bid), hp = HP_(bid);
  const int rp = xcd * 4 + (idx >> 3), cb = idx & 7;
  const int hs_ = (hp + 4 + (int)(((size_t)wqkv >> 7) & 7)) & 7;
  const int g8 = ((hp >> 3) * 4 + (rb >> 2)) * 8 + hs_, kq = rb & 3;
  const int krot = KROT_(bid), posA = 1 + (hp & 3), posB = 1 + (rb & 3);
  const int wrot = 4 * rp + (xcd >> 1), wdir = (xcd & 1) ? -1 : 1;
  auto whead = [&](int t) { return (wrot + wdir * t) & 15; };
  const unsigned rs_slot = WS_RS + (rb * 16 + ((krot + posA) & 15)) * 1024, cs_slot = WS_CS + (hp * 16 + ((krot + posB) & 15)) * 768;
  const __amdgpu_buffer_rsrc_t wsr = __builtin_amdgcn_make_buffer_rsrc(ws, 0, WS_END, 0x00020000);
#define LDW(off) __builtin_amdgcn_raw_buffer_load_b128(wsr, (off), 0, 16)
#define STW(v, off) __builtin_amdgcn_raw_buffer_store_b128((v), wsr, (off), 0, 16)
#define STW1(v, off) __builtin_amdgcn_raw_buffer_store_b32((v), wsr, (off), 0, 16)
  float* stm = (float*)(smem + L_STM);
  volatile LAS int* misc = (volatile LAS int*)(smem + L_MISC);
#define LANES() int tid = threadIdx.x; asm volatile("" : "+v"(tid)); const int lane = tid & 63, r = lane & 31, h = lane >> 5; (void)r; (void)h
#define EPI() const int erow = tid >> 3, eseg = tid & 7; const size_t ego = (size_t)(rp * 64 + erow) * D_ + cb * 128 + eseg * 8
  u32x4 xv[2], wv[2];
  f32x2 xf[8], wf2[8];
  float sw_;
  auto unpack_xw = [&]() {
    f32x2 a = {0.f, 0.f};
#pragma unroll
    for (int i = 0; i < 8; ++i) { xf[i] = unpk(xv[i >> 2][i & 3]); wf2[i] = unpk(wv[i >> 2][i & 3]); a += wf2[i]; }
    sw_ = oct_sum(a[0] + a[1]);
#pragma unroll
    for (int i = 0; i < 8; ++i) asm volatile("" : "+v"(xf[i]), "+v"(wf2[i]));
    asm volatile("" : "+v"(sw_));
  };
  u32x4 wf[24];

  auto wtr = [&](auto I) {
    constexpr int m = I.v >> 3, c = I.v & 7, R = VW + 32 * m + (c >> 1);
    const unsigned sel = (c & 1) ? 0x07060302u : 0x05040100u;
    wf[I.v] = (u32x4){wperm<R, R + 4>(sel), wperm<R + 8, R + 12>(sel), wperm<R + 16, R + 20>(sel), wperm<R + 24, R + 28>(sel)};
  };

  if (wave < 4) {
    LANES();
    const int c16 = lane & 15, g4 = lane >> 4;
    const int trl = (c16 >> 2) * 64 + (c16 & 3) * 8;
    const int wr = wave >> 1, wc = wave & 1;
    const int head = 2 * hp + wc;
    const unsigned kvp_off = WS_KVP + (head * 16 + rb) * 4096 + wr * 2048 + lane * 16;
    const unsigned zp_off = WS_ZP + (head * 16 + rb) * 128;
    const unsigned o_off = WS_O + ((rb * 4 + 2 * wr) * 32 + head) * 2048 + lane * 16;
    const unsigned fb_off = lane < 8 ? WS_FB + ((rb * 2 + wr) * 32 + head) * 128 + lane * 16 : OOB;
    unsigned pfa, pfb = OOB;
    unsigned pfc = lane < 8 ? fb_off : lane < 24 ? rs_slot + wave * 256 + (lane - 8) * 16
        : lane < 40 ? (wave < 3 ? cs_slot + wave * 256 : WS_KVF + hp * 4096 + rb * 256) + (lane - 24) * 16
        : lane < 48 ? (wave < 2 ? zp_off + (lane & 7) * 16 : (wave == 2 && rb < 2) ? WS_ZF + (2 * hp + rb) * 128 + (lane & 7) * 16 : OOB) : OOB;
    if (wave == 0) pfa = WS_ST2 + (rp * 8 + cb) * 1024 + lane * 16;
    else if (wave == 1) pfa = WS_GU + (g8 * 4 + kq) * 1024 + lane * 16;
    else pfa = WS_YP + (((wave - 2) * 8 + 2 * kq + (lane >> 5)) * 64 + g8) * 512 + (lane & 31) * 16;
    if (wave == 0) pfb = lane < 32 ? WS_XS + (rp * 2 + (lane >> 4)) * 2048 + cb * 256 + (lane & 15) * 16 : lane < 40 ? WS_FD + (cb * 32 + rp) * 128 + (lane & 7) * 16 : OOB;
    u32x4 S4 = {SENT, SENT, SENT, SENT};
    asm volatile("" : "+v"(pfa), "+v"(pfb), "+v"(pfc), "+v"(S4));
    __builtin_amdgcn_s_setprio(1);
    f32x4 acc[4][6];
    int ao[2][2], bo[2][2];
    {
#pragma unroll
      for (int i = 0; i < 2; ++i) {
        ao[0][i] = (64 * wr + c16) * 128 + (((4 * i + g4) ^ (c16 >> 1)) * 16); ao[1][i] = ao[0][i] + 2 * K1_STAGE;
        bo[0][i] = 16384 + g4 * 3072 + wc * 256 + trl + ((i ^ (g4 & 1)) * 32); bo[1][i] = bo[0][i] + 2 * K1_STAGE;
        asm volatile("" : "+v"(ao[0][i]), "+v"(ao[1][i]), "+v"(bo[0][i]), "+v"(bo[1][i]));
      }
#pragma unroll
      for (int i = 0; i < 4; ++i)
#pragma unroll
        for (int j = 0; j < 6; ++j) { acc[i][j] = (f32x4){0.f, 0.f, 0.f, 0.f}; asm volatile("" : "+v"(acc[i][j])); }
      struct Frag { u32x4 a[4]; u32x2 bl[6], bh[6]; };
      Frag FA, FB;
#define K1_B(j) ((u32x4){C.bl[j][0], C.bl[j][1], C.bh[j][0], C.bh[j][1]})
#define K1_LA(i) N.a[i] = *(const u32x4*)(smem + ao[sn >> 1][hf] + (sn & 1) * K1_STAGE + (i) * 2048)
#define K1_LL(j) N.bl[j] = trread(smem + bo[sn >> 1][(j) & 1] + (sn & 1) * K1_STAGE + hf * 12288 + ((j) >> 1) * 1024)
#define K1_LH(j) N.bh[j] = trread(smem + bo[sn >> 1][(j) & 1] + (sn & 1) * K1_STAGE + hf * 12288 + ((j) >> 1) * 1024 + 512)
#define K1_M(i, j) if ((j) < 2) mfma16i(K1_B(j), C.a[i], acc[i][j]); else mfma16i(C.a[i], K1_B(j), acc[i][j]); SB0()
#define K1_Q(i, j, ld) K1_M(i, j); ld; SB0()
      auto sub = [&](Frag& C, Frag& N, int sn, int hf) {
        K1_Q(0, 0, K1_LL(0)); K1_Q(1, 0, K1_LH(0)); K1_Q(0, 1, K1_LA(0)); K1_Q(1, 1, K1_LA(1)); K1_Q(0, 2, K1_LL(1)); K1_Q(1, 2, K1_LH(1));
        K1_Q(0, 3, K1_LL(2)); K1_Q(1, 3, K1_LH(2)); K1_Q(2, 0, K1_LL(3)); K1_Q(3, 0, K1_LH(3)); K1_Q(2, 1, K1_LA(2)); K1_Q(3, 1, K1_LA(3));
        K1_Q(2, 2, K1_LL(4)); K1_Q(3, 2, K1_LH(4)); K1_Q(2, 3, K1_LL(5)); K1_Q(3, 3, K1_LH(5));
        K1_M(0, 4); K1_M(1, 4); K1_M(2, 4); K1_M(3, 4); K1_M(0, 5); K1_M(1, 5); K1_M(2, 5);
        asm volatile("s_waitcnt lgkmcnt(0)" : "+v"(N.bh[5]));
        K1_M(3, 5);
      };
      auto subkv = [&](Frag& C, Frag& N, int sn, int hf, bool ld) {
#define K1_QL(i, j, l) K1_M(i, j); if (ld) { l; } SB0()
        K1_QL(0, 2, K1_LL(2)); K1_QL(1, 2, K1_LH(2)); K1_QL(0, 3, K1_LA(0)); K1_QL(1, 3, K1_LA(1)); K1_QL(2, 2, K1_LL(3)); K1_QL(3, 2, K1_LH(3));
        K1_QL(2, 3, K1_LA(2)); K1_QL(3, 3, K1_LA(3)); K1_QL(0, 4, K1_LL(4)); K1_QL(1, 4, K1_LH(4)); K1_QL(2, 4, K1_LL(5)); K1_QL(3, 4, K1_LH(5));
        K1_M(0, 5); K1_M(1, 5); K1_M(2, 5); K1_M(3, 5);
      };
      WAIT_V(0);
      RAW_BAR();
      {
        Frag& N = FA; const int sn = 0, hf = 0;
        K1_LL(0); K1_LH(0); K1_LA(0); K1_LA(1); K1_LL(1); K1_LH(1); K1_LL(2); K1_LH(2); K1_LL(3); K1_LH(3); K1_LA(2); K1_LA(3); K1_LL(4); K1_LH(4); K1_LL(5); K1_LH(5);
      }
#pragma unroll 1
      for (int it = 0; it < 4; ++it) {
#pragma unroll
        for (int s = 0; s < 4; ++s) {
          if (s == 2 && it == 3) break;
          sub(FA, FB, s, 1);
          if (s == 0) WAIT_V(0);
          if (it == 0) { if (s == 1) STW(S4, pfc); if (s == 2) STW(S4, kvp_off); if (s == 3) STW(S4, kvp_off + 1024); }
          if (it == 1) { if (s == 0) STW(S4, pfa); if (s == 1) STW(S4, pfb); }
          if (s != 1 || it < 3) RAW_BAR();
          sub(FB, FA, (s + 1) & 3, 0);
        }
      }
      subkv(FA, FB, 2, 1, true);
      RAW_BAR();
      subkv(FB, FA, 3, 0, true);
      subkv(FA, FB, 3, 1, true);
      subkv(FB, FA, 0, 0, false);
    }
    asm volatile("s_nop 15" ::: "memory");
    {
      f32x4 m4[4], i4[4]; float cs[4];
      SPIN {
        const int ok = misc[12 + (lane & 3)];
        asm volatile("" ::: "memory");
#pragma unroll
        for (int i = 0; i < 4; ++i) {
          cs[i] = stm[256 + wc * 96 + 32 + 16 * i + c16];
          m4[i] = *(const f32x4*)(stm + 64 * wr + 16 * i + 4 * g4); i4[i] = *(const f32x4*)(stm + 128 + 64 * wr + 16 * i + 4 * g4);
        }
        SB0();
        if (__all(ok != 0)) break;
      }
      const u32x4 ONES4 = {0x3F803F80u, 0x3F803F80u, 0x3F803F80u, 0x3F803F80u};
      f32x2 tk[16], tv[16];
#define PR(v, i) ((f32x2){(v)[2 * (i)], (v)[2 * (i) + 1]})
#pragma unroll
      for (int i = 0; i < 16; ++i) {
        tk[i] = PR(acc[(i >> 1) & 3][2 + (i >> 3)], i & 1) - PR(m4[(i >> 1) & 3], i & 1) * cs[i >> 3];
        tv[i] = PR(acc[(i >> 1) & 3][4 + (i >> 3)], i & 1) - PR(m4[(i >> 1) & 3], i & 1) * cs[2 + (i >> 3)];
      }
      SB0();
#pragma unroll
      for (int i = 0; i < 16; ++i) { tk[i] *= PR(i4[(i >> 1) & 3], i & 1); tv[i] *= PR(i4[(i >> 1) & 3], i & 1); }
      SB0();
      unsigned kp[16], vp[16];
#pragma unroll
      for (int i = 0; i < 16; ++i) vp[i] = pack2o(tv[i][0], tv[i][1]);
      phi_n<16>(tk, kp);
#define F4_(p, i) ((u32x4){(p)[4 * (i)], (p)[4 * (i) + 1], (p)[4 * (i) + 2], (p)[4 * (i) + 3]})
      const f32x4 Z4 = {0.f, 0.f, 0.f, 0.f};
      u32x4 vk[2], vg[2];
#pragma unroll
      for (int j = 0; j < 2; ++j) { vk[j] = wr ? F4_(vp, 2 + j) : F4_(vp, j); vg[j] = wr ? F4_(vp, j) : F4_(vp, 2 + j); }
      f32x4 keep[2], give[2], zq[2];
#pragma unroll
      for (int d = 0; d < 2; ++d) { give[d] = mfma16(F4_(kp, 2 * d), vg[0], Z4); zq[d] = mfma16(ONES4, F4_(kp, 2 * d), Z4); }
#pragma unroll
      for (int d = 0; d < 2; ++d) { give[d] = mfma16(F4_(kp, 2 * d + 1), vg[1], give[d]); zq[d] = mfma16(ONES4, F4_(kp, 2 * d + 1), zq[d]); }
#pragma unroll
      for (int d = 0; d < 2; ++d) keep[d] = mfma16(F4_(kp, 2 * d), vk[0], Z4);
#pragma unroll
      for (int d = 0; d < 2; ++d) keep[d] = mfma16(F4_(kp, 2 * d + 1), vk[1], keep[d]);
      SB0();
      const float zv = (lane & 16) ? zq[1][0] : zq[0][0];
      char* xk = smem + L_XK + wave * 2304 + lane * 16;
      const char* xp = smem + L_XK + (wave ^ 2) * 2304 + lane * 16;
      *(f32x4*)xk = give[0]; *(f32x4*)(xk + 1024) = give[1]; *(float*)(xk + 2048 - lane * 12) = zv;
      asm volatile("" ::: "memory");
      misc[16 + wave] = 1;
      f32x4 p0, p1; float pz;
      SPIN {
        const int ok = misc[16 + (wave ^ 2)];
        asm volatile("" ::: "memory");
        p0 = *(const f32x4*)xp; p1 = *(const f32x4*)(xp + 1024); pz = *(const float*)(xp + 2048 - lane * 12);
        SB0();
        if (ok == 1) break;
      }
      STW(sane(__builtin_bit_cast(u32x4, keep[0] + p0)), kvp_off);
      STW(sane(__builtin_bit_cast(u32x4, keep[1] + p1)), kvp_off + 1024);
      STW1(sane1(__float_as_uint(zv + pz)), (lane < 32 && wr == 0) ? zp_off + lane * 4 : OOB);
    }
    {
      u32x4 qa[4][4]; u32x2 ql[4][2], qh[4][2];
#pragma unroll
      for (int u = 0; u < 4; ++u) {
#pragma unroll
        for (int j = 0; j < 2; ++j) {
          ql[u][j] = trread(smem + bo[1][j] + (u >> 1) * K1_STAGE + (u & 1) * 12288);
          qh[u][j] = trread(smem + bo[1][j] + (u >> 1) * K1_STAGE + (u & 1) * 12288 + 512);
        }
#pragma unroll
        for (int i = 0; i < 4; ++i) qa[u][i] = *(const u32x4*)(smem + ao[1][u & 1] + (u >> 1) * K1_STAGE + i * 2048);
      }
      asm volatile("" ::: "memory");
      misc[8 + wave] = 3;
      SB0();
#pragma unroll
      for (int u = 0; u < 4; ++u)
#pragma unroll
        for (int j = 0; j < 2; ++j)
#pragma unroll
          for (int i = 0; i < 4; ++i) acc[i][j] = mfma16((u32x4){ql[u][j][0], ql[u][j][1], qh[u][j][0], qh[u][j][1]}, qa[u][i], acc[i][j]);
      SB0();
    }
    unsigned qp[16];
    Phi<16> qs;
    const unsigned po = WS_KVP + ((2 * hp + (rb >> 3)) * 16 + h) * 4096 + (2 * ((rb >> 2) & 1) + ((lane >> 4) & 1)) * 1024 + (16 * (rb & 3) + (lane & 15)) * 16;
    u32x4 pv[8] = {};
    {
      f32x4 c4[2]; float m_b[4], rinv[4];
#pragma unroll
      for (int i = 0; i < 2; ++i) c4[i] = *(const f32x4*)(stm + 256 + wc * 96 + 16 * i + 4 * g4);
#pragma unroll
      for (int i = 0; i < 4; ++i) { m_b[i] = stm[64 * wr + 16 * i + c16]; rinv[i] = stm[128 + 64 * wr + 16 * i + c16]; }
      SB0();
      f32x2 tq[16];
#pragma unroll
      for (int i = 0; i < 16; ++i) tq[i] = PR(acc[i >> 2][(i >> 1) & 1], i & 1) - PR(c4[(i >> 1) & 1], i & 1) * m_b[i >> 2];
      SB0();
#pragma unroll
      for (int i = 0; i < 16; ++i) tq[i] *= rinv[i >> 2];
      SB0();
      phi_a<16>(tq, qs);
#pragma unroll
      for (int i = 0; i < 16; ++i) asm volatile("" : "+v"(qs.tb[i]), "+v"(qs.tr[i]), "+v"(qs.e[i]));
    }
    if (wave == 0) {
      SLEEP(P_KVP);
#pragma unroll
      for (int p = 0; p < 8; ++p) pv[p] = LDW(po + p * 8192);
    }
    asm volatile("" ::: "memory");
    if (wave == 1 && rb < 2) {
      SLEEP(P_KVP);
      SPIN {
        unsigned m = 0;
#pragma unroll
        for (int p = 0; p < 2; ++p) { pv[p] = LDW(WS_ZP + (2 * hp + rb) * 2048 + p * 1024 + lane * 16); m = chk(max(max(m, pv[p][0]), pv[p][2]), pv[p]); }
        if (wave_ok(m)) break;
        asm volatile("" ::: "memory");
      }
      f32x4 s = __builtin_bit_cast(f32x4, pv[0]) + __builtin_bit_cast(f32x4, pv[1]);
#pragma unroll
      for (int i = 0; i < 4; ++i) { s[i] += dppf<DPP_ROR8>(s[i]); s[i] = xh_sum(x16_sum(s[i])); }
      if (lane < 16) st64(ws, WS_ZF + (2 * hp + rb) * 128 + (lane >> 3) * 64 + (lane & 3) * 16 + ((lane >> 2) & 1) * 8,
                          ((u64)sane1(pack2(s[2], s[3])) << 32) | pack2(s[0], s[1]));
    }
    if (wave == 0) {
      SPIN {
        unsigned m = 0;
#pragma unroll
        for (int p = 0; p < 8; ++p) m = chk(m, pv[p]);
        if (wave_ok(m)) break;
        asm volatile("" ::: "memory");
#pragma unroll
        for (int p = 0; p < 8; ++p) pv[p] = LDW(po + p * 8192);
      }
      f32x4 s = __builtin_bit_cast(f32x4, pv[0]);
#pragma unroll
      for (int p = 1; p < 8; ++p) s += __builtin_bit_cast(f32x4, pv[p]);
      const unsigned p0 = pack2(xh_sum(s[0]), xh_sum(s[1])), p1 = pack2(xh_sum(s[2]), xh_sum(s[3]));
      auto a = __builtin_amdgcn_permlane16_swap(p0, p0, false, false);
      auto b = __builtin_amdgcn_permlane16_swap(p1, p1, false, false);
      STW(sane((u32x4){a[0], b[0], a[1], b[1]}), lane < 16 ? WS_KVF + hp * 4096 + rb * 256 + lane * 16 : OOB);
    }
    phi_b<16>(qs, qp);
#pragma unroll
    for (int i = 0; i < 16; ++i) asm volatile("" : "+v"(qp[i]));
    if (wave == 0) misc[7] = 1;
    else SPIN { if (misc[7] == 1) break; __builtin_amdgcn_s_sleep(1); }
    SLEEP(P_KVF);
    {
      u32x4 k0, k1, z0;
      SPIN {
        k0 = LDW(WS_KVF + head * 2048 + lane * 16); k1 = LDW(WS_KVF + head * 2048 + 1024 + lane * 16);
        z0 = LDW(WS_ZF + head * 128 + g4 * 16);
        if (wave_ok(chk(chk(chk(0u, k0), k1), z0))) break;
        asm volatile("" ::: "memory");
      }
      const f32x4 Z4 = {0.f, 0.f, 0.f, 0.f};
      f32x4 X[8], Dn[4];
#pragma unroll
      for (int i = 0; i < 4; ++i) Dn[i] = mfma16(z0, F4_(qp, i), Z4);
#pragma unroll
      for (int i = 0; i < 8; ++i) X[i] = mfma16((i & 1) ? k1 : k0, F4_(qp, i >> 1), Z4);
      SB0();
      float ri[4]; unsigned a[16]; f32x2 f[16];
#pragma unroll
      for (int i = 0; i < 4; ++i) ri[i] = __builtin_amdgcn_rcpf(rbf(rbf(Dn[i][0]) + 1e-6f));
      SB0();
#pragma unroll
      for (int i = 0; i < 16; ++i) a[i] = pack2o(X[i >> 1][2 * (i & 1)], X[i >> 1][2 * (i & 1) + 1]);
      SB0();
#pragma unroll
      for (int i = 0; i < 16; ++i) f[i] = unpk(a[i]);
      SB0();
#pragma unroll
      for (int i = 0; i < 16; ++i) f[i] *= ri[i >> 2];
      SB0();
#pragma unroll
      for (int i = 0; i < 4; ++i) {
        STW(((u32x4){pack2(f[4 * i][0], f[4 * i][1]), pack2(f[4 * i + 1][0], f[4 * i + 1][1]), pack2(f[4 * i + 2][0], f[4 * i + 2][1]), pack2(f[4 * i + 3][0], f[4 * i + 3][1])}),
            o_off + (i >> 1) * 65536 + (i & 1) * 1024);
        SB0();
      }
    }
    {
      EPI();
      const int rt = wave & 1, kh = wave >> 1;
      const unsigned ob = WS_O + ((rp * 2 + rt) * 32 + 16 * kh) * 2048 + lane * 16;
#define LDO(t, st) __builtin_amdgcn_raw_buffer_load_b128(wsr, ob + (st) * 1024, whead(t) * 2048, 16)
      int woff[2];
#pragma unroll
      for (int i = 0; i < 2; ++i) woff[i] = kh * 8192 + (g4 >> 1) * 2048 + (g4 & 1) * 512 + trl + ((i ^ (g4 & 1)) * 32);
      struct Frag2 { u32x2 lo[8], hi[8]; };
      Frag2 G[2];
      auto ld2 = [&](Frag2& N, int sn, int nt, int hi) {
        const char* p = smem + sn + woff[nt & 1] + (nt >> 2) * 1024 + ((nt >> 1) & 1) * 256;
        if (hi) N.hi[nt] = trread(p + 4096); else N.lo[nt] = trread(p);
      };
      WAIT_V(0);
      STW(((u32x4){1u, 1u, 1u, 1u}), fb_off);
      RAW_BAR();
#pragma unroll
      for (int i = 0; i < 16; ++i) ld2(G[0], wo_slot(0), i >> 1, i & 1);
      f32x4 a2[2][8];
#pragma unroll
      for (int i = 0; i < 16; ++i) a2[i & 1][i >> 1] = (f32x4){0.f, 0.f, 0.f, 0.f};
      SLEEP(P_FB);
      SPIN { if (wave_ok(ld32(ws, WS_FB + (rp * 32 + 16 * kh + (lane & 15)) * 128))) break; }
      asm volatile("" ::: "memory");
      char* pr[2];
#pragma unroll
      for (int i = 0; i < 2; ++i) pr[i] = smem + L_P + kh * L_PK + (32 * rt + c16) * 512 + (((4 * i + g4) ^ (c16 & 7)) * 16);
      u32x4 of[16][2];
#pragma unroll
      for (int t = 0; t < OW; ++t) { of[t][0] = LDO(t, 0); of[t][1] = LDO(t, 1); }
      SB0();
      sfor<0, 16>([&](auto T) {
        constexpr int t = T.v;
        Frag2& C = G[t & 1]; Frag2& N = G[(t + 1) & 1];
#pragma unroll
        for (int i = 0; i < 16; ++i) {
          const int nt = i >> 1, st = i & 1;
          a2[st][nt] = mfma16((u32x4){C.lo[nt][0], C.lo[nt][1], C.hi[nt][0], C.hi[nt][1]}, of[t][st], a2[st][nt]);
          SB0();
          if ((t & 1) && t <= 11 && i == 8) {
            asm volatile("s_waitcnt lgkmcnt(8)" ::: "memory"); __builtin_amdgcn_s_barrier(); asm volatile("" ::: "memory");
          }
          if (t < 15) ld2(N, wo_slot(t + 1), nt, st);
          if (t > 0 && t - 1 + OW < 16 && (i == 3 || i == 11)) of[(t - 1 + OW) & 15][i >> 3] = LDO(t - 1 + OW, i >> 3);
          if (t - 1 + OW == 16 && (i == 3 || i == 11)) { xv[i >> 3] = *(const u32x4*)(x + ego + (i >> 3) * 64); wv[i >> 3] = *(const u32x4*)(w_r + cb * 128 + eseg * 8 + (i >> 3) * 64); }
          if (t == 15 && i >= 4) *(f32x4*)(pr[(nt - 2) & 1] + ((nt - 2) >> 1) * 128 + st * 8192) = a2[st][(nt - 2) & 7];
          SB0();
        }
      });
#pragma unroll
      for (int i = 12; i < 16; ++i) *(f32x4*)(pr[(i >> 1) & 1] + (i >> 2) * 128 + (i & 1) * 8192) = a2[i & 1][i >> 1];
    }
    unpack_xw();
    __builtin_amdgcn_s_setprio(0);
  } else {
    LANES();
    const int sw = wave - 4, t2 = tid - 256;
    __builtin_amdgcn_s_setprio(3);
    const bool real = hs_ != 7;
    unsigned hs_g0 = 0, hs_cur = 0;
    u32x4 ms_exp = {0, 0, 0, 0};
    bool hs_act = true, ms_act = !real && hp < 8 && rb == 0 && sw == 1;
    const unsigned hs_off = (lane == 1 ? SY_GO : SY_CH) + bid * 4;
    const unsigned re_off = t2 == 0 ? SY_RE + bid * 4 : OOB;
    const unsigned ms_off = lane * 16;
    const char* wsu = uni(ws);
#define HS_PROBE() asm volatile("s_nop 4\n\tglobal_load_dword v156, %0, %1 sc1" :: "v"(hs_off), "s"(wsu) : "memory", RSV)
#define MS_PROBE() asm volatile("s_nop 4\n\tglobal_load_dwordx4 v[152:155], %0, %1 offset:2048 sc1" :: "v"(ms_off), "s"(wsu) : "memory", RSV)
    auto hs_step = [&](bool first) {
      if (hs_act) {
        const unsigned hv_ = rget<156>();
        hs_act = __builtin_amdgcn_readlane(hv_, 1) == hs_g0;
        const unsigned c = __builtin_amdgcn_readlane(hv_, 0);
        if (sw == 0 && (first || c != hs_cur)) STW1(hv_, re_off);
        hs_cur = c;
        HS_PROBE();
      }
      if (ms_act) {
        const unsigned d = ((rget<152>() ^ ms_exp[0]) | (rget<153>() ^ ms_exp[1])) | ((rget<154>() ^ ms_exp[2]) | (rget<155>() ^ ms_exp[3]));
        ms_act = __any(d != 0u);
        STW(ms_exp, ms_act ? OOB : SY_GO + ms_off);
        MS_PROBE();
      }
    };
    HS_PROBE();
    if (ms_act) {
      MS_PROBE();
      asm volatile("s_nop 4\n\tglobal_load_dwordx4 v[148:151], %0, %1 offset:1024 sc1" :: "v"(ms_off), "s"(wsu) : "memory", RSV);
    }
    const int gr = (g8 >> 3) * 7 + min(hs_, 6);
    const unsigned voG = (unsigned)((lane >> 3) * 8 * F_ + (lane & 7) * 8) * 2u, voD = (unsigned)((lane >> 3) * 8 * D_ + (lane & 7) * 8) * 2u;
    const size_t gofs = ((size_t)(kq * 256 + sw * 64) * F_ + gr * 64) * 2;
    const char* pG = (const char*)wg + gofs;
    const char* pU = (const char*)wu + gofs;
    const char* pD = (const char*)(wd + (size_t)(gr * 64) * D_ + kq * 256 + sw * 64);
    auto wstep = [&](auto I) {
      constexpr int i = I.v;
      if (i < 8) wload<VW + 4 * i>(voG, pG + (i & 7) * (F_ * 2));
      else if (i < 16) wload<VW + 4 * i>(voG, pU + (i & 7) * (F_ * 2));
      else wload2<VW + 4 * i>(voD, pD + (i & 7) * (D_ * 2));
    };
    unsigned voW;
    {
      const int bi = lane >> 4, q = (lane >> 2) & 3, ch = lane & 3;
      voW = (unsigned)((4 * (bi >> 1) + q) * D_ + (bi & 1) * 32 + (ch ^ (bi & 2)) * 8) * 2u;
    }
    const char* baseW = (const char*)(wo + (size_t)((16 * (sw >> 1)) * 32 + 16 * (sw & 1)) * D_ + cb * 128) - 1024;
    auto stageW = [&](auto J, bool rsv) {
      const char* s_ = baseW + (size_t)whead(J.v) * (32 * D_ * 2);
      if (rsv) glds4r<wo_slot(J.v)>(voW + 1024, voW + 128, voW + 8 * D_ * 2 - 1024, voW + 8 * D_ * 2 + 128 - 2048, s_, l4);
      else glds4<wo_slot(J.v)>(voW + 1024, voW + 128, voW + 8 * D_ * 2 - 1024, voW + 8 * D_ * 2 + 128 - 2048, s_, l4);
    };
    int sta[4], stb[3];
    {
      const int row = t2 >> 1, key = (row >> 1) & 7;
#pragma unroll
      for (int c = 0; c < 4; ++c) { sta[c] = (posA & 3) * K1_STAGE + row * 128 + (((4 * (t2 & 1) + c) ^ key) * 16); asm volatile("" : "+v"(sta[c]) :: RSV); }
#pragma unroll
      for (int i = 0; i < 3; ++i) {
        const int G = sw * 3 + i, hd = G >= 6 ? 1 : 0, mat = (G - 6 * hd) >> 1;
        stb[i] = (posB & 3) * K1_STAGE + 16384 + (lane >> 4) * 6144 + mat * 1024 + hd * 256 + ((lane & 15) >> 2) * 64 + (G & 1) * 32 + (lane & 3) * 8;
        asm volatile("" : "+v"(stb[i]) :: RSV);
      }
    }
    unsigned rs_dst = rs_slot + t2 * 4, cs_dst = lane < 48 ? cs_slot + (sw * 48 + lane) * 4 : OOB;
    unsigned rs_off = WS_RS + rb * 16384 + (lane >> 4) * 1024 + sw * 256 + (lane & 15) * 16;
    unsigned cs_off = WS_CS + hp * 12288 + (lane >> 4) * 768 + sw * 192 + min(lane & 15, 11) * 16;
    asm volatile("" : "+v"(rs_dst), "+v"(cs_dst), "+v"(rs_off), "+v"(cs_off) :: RSV);
    int stm_dst = ((lane >> 5) * 128 + sw * 32 + (lane & 15) * 2 + ((lane >> 4) & 1)) * 4, cs_put = (lane & 15) < 12 ? (256 + sw * 48 + (lane & 15) * 4 + (lane >> 4)) * 4 : 1792 + lane * 4;
    asm volatile("" : "+v"(stm_dst), "+v"(cs_put) :: RSV);
    auto st_load = [&]() { sfor<0, 4>([&](auto I) { sload<224 + 4 * I.v>(rs_off, wsu + I.v * 4096); sload<240 + 4 * I.v>(cs_off, wsu + I.v * 3072); }); };
    sfor<3, GS[0]>(group);
    WAIT_VR(gops(GS[0]) - 10 + 1); RAW_BARR();
    sfor<GS[0], GS[1]>(group);
    WAIT_VR(gops(GS[1]) - 20); RAW_BARR();
    hs_g0 = __builtin_amdgcn_readlane(rget<156>(), 1);
    if (ms_act) {
      const u32x4 re = {rget<152>(), rget<153>(), rget<154>(), rget<155>()}, g0 = {rget<148>(), rget<149>(), rget<150>(), rget<151>()};
#pragma unroll
      for (int j = 0; j < 4; ++j) { const unsigned c = re[j] + 1u; ms_exp[j] = (c == g0[j]) ? c + 1u : c; }
      STW(ms_exp, SY_CH + ms_off);
    }
    u32x4 ra[4] = {}; u32x2 rc[3][4] = {};
    float sa = 0.f, sb = 0.f, qa = 0.f, qb = 0.f, ca[3] = {0.f, 0.f, 0.f}, cb_[3] = {0.f, 0.f, 0.f};
    auto chunk = [&](auto C) {
      constexpr int c = C.v;
      if constexpr (c < 4) {
        asm volatile("" : "+v"(ra[c]) :: RSV);
#pragma unroll
        for (int i = 0; i < 4; ++i) { const float lo = bflo(ra[c][i]), hi = bfhi(ra[c][i]); sa = fadd(sa, lo); sb = fadd(sb, hi); qa = fsq(lo, qa); qb = fsq(hi, qb); }
        asm volatile("" : "+v"(sa), "+v"(sb), "+v"(qa), "+v"(qb) :: RSV);
      } else if constexpr (c == 4) {
        float s = fadd(sa, sb), sq = fadd(qa, qb);
        s = fadd(s, dppf<DPP_X1>(s)); sq = fadd(sq, dppf<DPP_X1>(sq));
        STW1(sane1(__float_as_uint((t2 & 1) ? sq : s)), rs_dst);
      } else if constexpr (c < 11) {
        constexpr int i = (c - 5) >> 1, j = ((c - 5) & 1) * 2;
        asm volatile("" : "+v"(rc[i][j]), "+v"(rc[i][j + 1]) :: RSV);
#pragma unroll
        for (int e = 0; e < 4; ++e) { const unsigned u = rc[i][j + (e >> 1)][e & 1]; ca[i] = fadd(ca[i], bflo(u)); cb_[i] = fadd(cb_[i], bfhi(u)); }
        asm volatile("" : "+v"(ca[i]), "+v"(cb_[i]) :: RSV);
      } else {
        const float c2 = fadd(ca[2], cb_[2]);
        STW1(sane1(__float_as_uint(sw32(sw16(fadd(ca[0], cb_[0]), fadd(ca[1], cb_[1])), sw16(c2, c2)))), cs_dst);
      }
    };
    sfor<0, 12>([&](auto T) {
      constexpr int t = T.v;
      if (t < 4 && posA == t + 1) {
#pragma unroll
        for (int c = 0; c < 4; ++c) ra[c] = *(const u32x4*)(smem + sta[c]);
      }
      if (t < 4 && posB == t + 1) {
#pragma unroll
        for (int i = 0; i < 3; ++i)
#pragma unroll
          for (int j = 0; j < 4; ++j) rc[i][j] = trread(smem + (stb[i] ^ ((j >> 1) * 32)) + (j >> 1) * 3072 + (j & 1) * 512);
      }
      if (t == 2) HS_PROBE();
      if (t >= 4) hs_step(t == 4);
      sfor<GS[t + 1], GS[t + 2]>([&](auto G) {
        group(G);
        constexpr int u = t - 4, p = G.v - GS[t + 1];
        constexpr int ord[12] = {0, 5, 6, 1, 7, 8, 2, 9, 10, 3, 11, 4};
        if constexpr (u >= 0 && u < 4 && p < 3) chunk(IC<ord[(3 * u + p) % 12]>{});
      });
      if (real) sfor<WS_[t], WS_[t + 1]>(wstep);
      WAIT_VR(t < 11 ? gops(GS[t + 2]) - 10 * (t + 3) : gops(GS[13]) - gops(GS[12]));
      RAW_BARR();
    });
    hs_step(false);
    auto st_put = [&]() -> bool {
      float a[8]; unsigned m = 0, n = 0;
      rsum4<224>(a, m, n); rsum4<240>(a + 4, m, n);
      const float s1 = sw16(a[0], a[2]), q1 = sw16(a[1], a[3]);
      const float cs = sw32(sw16(a[4], a[5]), sw16(a[6], a[7]));
      const float mean = sw32(s1, s1) * (1.0f / 1024.0f);
      const float var = fmaxf(__builtin_fmaf(sw32(q1, q1), 1.0f / 1024.0f, -(mean * mean)), 0.f);
      const float rinv = __builtin_amdgcn_rcpf(rbf(__builtin_amdgcn_sqrtf(rbf(rbf(var) + 1e-5f))));
      *(float*)(smem + L_STM + stm_dst) = (lane & 32) ? rinv : rbf(mean);
      *(float*)(smem + L_STM + cs_put) = cs;
      return wave_ok(max(m, n));
    };
    st_load();
    if (real) sfor<WS_[12], W1A>(wstep);
    if (real) WAIT_VR(8 + W1A - WS_[11]); else WAIT_VR(8);
    if (lane == 0) { misc[8 + sw] = 0; misc[12 + sw] = 0; misc[16 + sw] = 0; misc[7] = 0; }
    RAW_BARR();
    if (real) WAIT_VR(W1A - WS_[12]); else WAIT_VR(0);
    if (!st_put() || hs_act) {
      if (hs_act) hs_act = __builtin_amdgcn_readlane(rget<156>(), 1) == hs_g0;
      if (ms_act) {
        SPIN {
          bool ok = true;
#pragma unroll
          for (int j = 0; j < 4; ++j) ok &= (ld32(ws, SY_RE + lane * 16 + j * 4) == ms_exp[j]);
          if (__all(ok)) break;
        }
        STW(ms_exp, SY_GO + lane * 16);
      }
      if (hs_act) {
        SPIN {
          if (ld32(ws, SY_GO + bid * 4) != hs_g0) break;
          const unsigned c = ld32(ws, SY_CH + bid * 4);
          if (c != hs_cur) { hs_cur = c; STW1(c, re_off); }
        }
      }
      SPIN { st_load(); WAIT_VR(0); if (st_put()) break; }
    }
    asm volatile("" ::: "memory", RSV);
    if (lane == 0) misc[12 + sw] = 1;
    __builtin_amdgcn_s_setprio(0);
    if (real) sfor<W1A, 16>(wstep);
    auto mwait = [&](int v) { SPIN { const int a = misc[8 + (lane & 3)]; if (__all(a >= v)) break; __builtin_amdgcn_s_sleep(1); } asm volatile("" ::: "memory"); };
    sfor<0, 9>([&](auto J) {
      if (J.v == 4) mwait(3);
      stageW(J, J.v < 2);
      if (J.v >= 1) WAIT_V(4 * PACE);
      if (J.v == 1) { WAIT_VR(8); sfor<0, 16>(wtr); }
    });
    EPI();
#pragma unroll
    for (int i = 0; i < 2; ++i) { xv[i] = *(const u32x4*)(x + ego + i * 64); wv[i] = *(const u32x4*)(w_r + cb * 128 + eseg * 8 + i * 64); }
    asm volatile("s_waitcnt vmcnt(0)" : "+v"(xv[0]), "+v"(xv[1]), "+v"(wv[0]), "+v"(wv[1]) :: "memory");
    unpack_xw();
    RAW_BAR();
    RAW_BAR(); stageW(IC<9>{}, false); stageW(IC<10>{}, false);
    RAW_BAR(); stageW(IC<11>{}, false); stageW(IC<12>{}, false);
    RAW_BAR(); stageW(IC<13>{}, false); stageW(IC<14>{}, false);
    WAIT_V(16); RAW_BAR(); stageW(IC<15>{}, false);
    WAIT_V(12); RAW_BAR();
    WAIT_V(0); RAW_BAR();
    if (real) sfor<16, 24>(wstep);
  }
  LANES(); EPI();
  const int grow = rp * 64 + erow;
  int pa0 = L_P + erow * 512 + ((2 * eseg) ^ (erow & 7)) * 16, pa1 = pa0 ^ 16;
  unsigned st_dst = eseg == 0 ? WS_ST2 + ((rp * 8 + cb) * 64 + erow) * 16 : OOB, st_src = WS_ST2 + rp * 8192 + lane * 16;
  unsigned fd_dst = lane < 8 ? WS_FD + (cb * 32 + rp) * 128 + lane * 16 : OOB, kidx = 0xFFFFu - grow;
  asm volatile("" : "+v"(pa0), "+v"(pa1), "+v"(st_dst), "+v"(st_src), "+v"(fd_dst), "+v"(kidx));
  RAW_BAR2();
  unsigned hb[8];
  f32x2 hv[8];
  {
    f32x4 pp[2][4];
#pragma unroll
    for (int k = 0; k < 2; ++k)
#pragma unroll
      for (int i = 0; i < 4; ++i) pp[k][i] = *(const f32x4*)(smem + ((i & 1) ? pa1 : pa0) + (i >> 1) * 256 + k * L_PK);
    SB0();
    f32x2 s = {0.f, 0.f}, q = {0.f, 0.f}, p2 = {0.f, 0.f};
#pragma unroll
    for (int i = 0; i < 8; ++i) {
      const f32x4 a = pp[0][i >> 1] + pp[1][i >> 1];
      const f32x2 u = xf[i] + unpk(pack2o(a[2 * (i & 1)], a[2 * (i & 1) + 1]));
      hb[i] = pack2o(u[0], u[1]);
      hv[i] = unpk(hb[i]);
      s += hv[i]; q += hv[i] * hv[i]; p2 += hv[i] * wf2[i];
    }
    STW(sane((u32x4){__float_as_uint(oct_sum(s[0] + s[1])), __float_as_uint(oct_sum(q[0] + q[1])), __float_as_uint(oct_sum(p2[0] + p2[1])), __float_as_uint(sw_)}), st_dst);
  }
  KEEP2();
  const int sw = wave - 4;
  const unsigned fd_src = WS_FD + xcd * 4096 + r * 128;
  u32x2 kpk = {SENT, SENT};
  if (wave == 0) {
    u32x4 pa[8];
    SLEEP(P_C);
    SPIN {
      unsigned m = 0;
#pragma unroll
      for (int j = 0; j < 8; ++j) { pa[j] = LDW(st_src + j * 1024); m = chk(m, pa[j]); }
      if (wave_ok(m)) break;
      KEEP2();
    }
#define F4(v) __builtin_bit_cast(f32x4, v)
    const f32x4 t = ((F4(pa[0]) + F4(pa[1])) + (F4(pa[2]) + F4(pa[3]))) + ((F4(pa[4]) + F4(pa[5])) + (F4(pa[6]) + F4(pa[7])));
    const float mean = t[0] * (1.0f / 1024.0f);
    const float var = fmaxf(__builtin_fmaf(t[1], 1.0f / 1024.0f, -(mean * mean)), 0.f);
    const float mb = rbf(mean), rs = __builtin_amdgcn_rcpf(rbf(__builtin_amdgcn_sqrtf(rbf(rbf(var) + 1e-5f))));
    *(f32x2*)(smem + L_KX + lane * 8) = (f32x2){mb, rs};
    const float sc = fmaxf((t[2] - mb * t[3]) * rs, -INFINITY);
    const int u = (int)(pack2(0.f, sc) & 0xffff0000u);
    unsigned b0 = ((unsigned)(u ^ ((u >> 31) | (int)0x80000000)) & 0xffff0000u) | (0xFFFFu - (rp * 64 + lane)), b1 = 0;
    top2_lo(b0, b1); top2_hi(b0, b1);
    STW(((u32x4){b0, b1, b0, b1}), fd_dst);
    if (lane == 0) *(u32x2*)(smem + L_MISC + 112) = (u32x2){b0, b1};
  } else if (wave >= 4) {
    asm volatile("s_waitcnt vmcnt(0)" ::: "memory", RSV2);
    sfor<16, 24>(wtr);
  }
  RAW_BAR();
  bool cand;
  u32x4 ob[2];
  {
    const f32x2 mr = *(const f32x2*)(smem + L_KX + erow * 8);
    const u32x2 kk = *(const u32x2*)(smem + L_MISC + 112);
    const float mb = mr[0], rs = mr[1];
    const unsigned b0 = kk[0], b1 = kk[1];
    u32x4 nq[2];
#pragma unroll
    for (int i = 0; i < 8; ++i) {
      const f32x2 d = hv[i] - mb;
      const f32x2 n = unpk(pack2o(d[0], d[1])) * rs;
      nq[i >> 2][i & 3] = pack2o(n[0], n[1]);
    }
    const bool c2 = (b1 & 0xffffu) == kidx;
    cand = c2 || (b0 & 0xffffu) == kidx;
    if (cand) {
      const unsigned xo = WS_XS + (rp * 2 + (c2 ? 1 : 0)) * 2048 + cb * 256 + eseg * 16;
      STW(sane(nq[0]), xo); STW(sane(nq[1]), xo + 128);
      char* cp = smem + L_CAND + (c2 ? 512 : 0) + eseg * 16;
      *(u32x4*)cp = (u32x4){hb[0], hb[1], hb[2], hb[3]}; *(u32x4*)(cp + 128) = (u32x4){hb[4], hb[5], hb[6], hb[7]};
      *(u32x4*)(cp + 256) = nq[0]; *(u32x4*)(cp + 384) = nq[1];
    }
    asm volatile("" ::: "memory");
    if (wave >= 4) { SLEEP(P_D); kpk = __builtin_amdgcn_raw_buffer_load_b64(wsr, fd_src, 0, 16); }
    asm volatile("" ::: "memory");
#pragma unroll
    for (int i = 0; i < 8; ++i) { const f32x2 o = hv[i] + unpk(nq[i >> 2][i & 3]); ob[i >> 2][i & 3] = pack2(o[0], o[1]); }
  }
#define OUT_ST() do { *(u32x4*)(out + ego) = ob[0]; *(u32x4*)(out + ego + 64) = ob[1]; } while (0)
  if (wave < 4) {
    if (!cand) OUT_ST();
    RAW_BAR();
    const unsigned w0 = misc[24], w1 = misc[25];
    if (cand && grow != (int)key_idx(w0) && grow != (int)key_idx(w1)) OUT_ST();
#pragma unroll 1
    for (int tk = 0; tk < 2; ++tk) {
      const unsigned wk = tk ? w1 : w0, slot = misc[26 + tk];
      if ((int)(slot >> 1) != rp) continue;
      u32x4 pv[7];
      const unsigned yo = WS_YP + ((tk * 8 + cb) * 64 + (lane >> 3) * 8) * 512 + wave * 128 + (lane & 7) * 16;
      const int col = (wave >> 1) * 64 + (lane & 7) * 8 + (wave & 1) * 4 + (lane >> 4);
      float sg = rbf(__builtin_amdgcn_rcpf(1.0f + __expf(-key_val(wk))));
      float hh = bflo(*(const u16*)(smem + L_CAND + (slot & 1) * 512 + col * 2)), nn = bflo(*(const u16*)(smem + L_CAND + (slot & 1) * 512 + 256 + col * 2));
      u16* op = out + (size_t)key_idx(wk) * D_ + cb * 128 + col;
      asm volatile("" : "+v"(sg), "+v"(hh), "+v"(nn), "+v"(op));
      if (tk == 0 || (int)(misc[26] >> 1) != rp) {
        SPIN { if (misc[30] == (int)w0) break; __builtin_amdgcn_s_sleep(1); }
        SLEEP(P_E);
      }
      SPIN {
        unsigned m = 0;
#pragma unroll
        for (int p = 0; p < 7; ++p) { pv[p] = LDW(yo + p * 512); m = chk(m, pv[p]); }
        if (wave_ok(m)) break;
        asm volatile("" ::: "memory");
      }
      f32x4 s = __builtin_bit_cast(f32x4, pv[0]);
#pragma unroll
      for (int p = 1; p < 7; ++p) s += __builtin_bit_cast(f32x4, pv[p]);
      float y = sw32(sw16(s[0], s[1]), sw16(s[2], s[3]));
      y += dppf<DPP_ROR8>(y);
      if (!(lane & 8)) *(GAS u16*)op = (u16)pack2(hh + rbf(nn + rbf(rbf(y) * sg)), 0.f);
    }
  } else {
    unsigned xl = WS_XS + (kq * 256 + sw * 64 + (lane >> 4) * 16 + ((lane >> 2) & 1) * 8) * 2;
    int red_dst = (lane < 8 || (lane >= 24 && lane < 32)) ? L_RED + sw * 2048 + ((lane >> 3) & 1) * 1024 + (lane & 7) * 16 : L_RED + 8192 + lane * 16;
    unsigned gu_src = WS_GU + g8 * 4096 + lane * 16;
    int act_dst = L_ACT + sw * 256 + ((lane & 7) * 8 + (lane >> 3)) * 2, act_src = L_ACT + sw * 256 + (lane & 1) * 128 + (lane >> 4) * 32 + ((lane >> 1) & 1) * 16;
    unsigned yp_dst = lane < 16 ? WS_YP + (((lane >> 3) * 8 + 2 * kq + (sw >> 1)) * 64 + g8) * 512 + (sw & 1) * 256 + (lane & 7) * 16 : OOB;
    asm volatile("" : "+v"(xl), "+v"(red_dst), "+v"(gu_src), "+v"(act_dst), "+v"(act_src), "+v"(yp_dst));
    unsigned w0, w1; int s0 = -1, s1 = -1;
    u32x4 xa = {SENT, SENT, SENT, SENT};
    SPIN {
      const bool all = wave_ok(kpk[1]);
      const unsigned k0 = kpk[1] == SENT ? 0u : kpk[0], k1 = kpk[1] == SENT ? 0u : kpk[1];
      w0 = k0; w1 = k1;
      top2_lo(w0, w1); top2_mid(w0, w1);
      const int n0 = 2 * __builtin_ctzll(__ballot(k0 == w0));
      const u64 m1 = __ballot(k0 == w1);
      const int n1 = m1 ? 2 * __builtin_ctzll(m1) : n0 + 1;
      const bool same = n0 == s0 && n1 == s1;
      s0 = n0; s1 = n1;
      asm volatile("" ::: "memory");
      if (all && same) break;
      if (!all) { kpk = __builtin_amdgcn_raw_buffer_load_b64(wsr, fd_src, 0, 16); }
      xa = LDW(xl + ((lane & 1) ? s1 : s0) * 2048);
      if (all) break;
    }
    SPIN {
      if (wave_ok(chk(0u, xa))) break;
      asm volatile("" ::: "memory");
      xa = LDW(xl + ((lane & 1) ? s1 : s0) * 2048);
    }
    {
      const u32x4 z4 = {0u, 0u, 0u, 0u};
      const u32x4 xg = (lane & 2) ? z4 : xa, xu = (lane & 2) ? xa : z4;
      f32x4 d[8];
#pragma unroll
      for (int c = 0; c < 8; ++c) d[c] = mfma16(xg, wf[c], (f32x4){0.f, 0.f, 0.f, 0.f});
      if (tid == 256) { *(u32x4*)(smem + L_MISC + 96) = (u32x4){w0, w1, (unsigned)s0, (unsigned)s1}; misc[30] = 0; }
      SB0();
#pragma unroll
      for (int c = 0; c < 10; ++c) {
        if (c < 8) d[c & 7] = mfma16(xu, wf[8 + (c & 7)], d[c & 7]);
        if (c >= 2) *(f32x4*)(smem + red_dst + (c - 2) * 128) = d[(c - 2) & 7];
        SB0();
      }
    }
    RAW_BAR();
    if (sw == 0) {
      f32x4 t[8];
#pragma unroll
      for (int p = 0; p < 8; ++p) t[p] = *(const f32x4*)(smem + L_RED + p * 1024 + lane * 16);
      SB0();
      STW(sane(__builtin_bit_cast(u32x4, ((t[0] + t[1]) + (t[2] + t[3])) + ((t[4] + t[5]) + (t[6] + t[7])))), gu_src + kq * 1024);
    } else __builtin_amdgcn_s_sleep(2);
    {
      u32x4 q[4];
      if (grow != (int)key_idx(w0) && grow != (int)key_idx(w1)) OUT_ST();
      asm volatile("" ::: "memory");
      SLEEP(P_GU);
      SPIN {
        unsigned m = 0;
#pragma unroll
        for (int j = 0; j < 4; ++j) { q[j] = LDW(gu_src + j * 1024); m = chk(m, q[j]); }
        if (wave_ok(m)) break;
        asm volatile("" ::: "memory");
      }
      const f32x4 tot = (__builtin_bit_cast(f32x4, q[0]) + __builtin_bit_cast(f32x4, q[1])) + (__builtin_bit_cast(f32x4, q[2]) + __builtin_bit_cast(f32x4, q[3]));
      const f32x2 gs = unpk(pack2o(tot[0], tot[1])), us = unpk(pack2o(tot[2], tot[3]));
      const f32x2 sg = unpk(pack2o(gs[0] * __builtin_amdgcn_rcpf(1.0f + __expf(-gs[0])), gs[1] * __builtin_amdgcn_rcpf(1.0f + __expf(-gs[1]))));
      const unsigned a2 = pack2o(sg[0] * us[0], sg[1] * us[1]);
      *(u16*)(smem + act_dst) = (u16)a2; *(u16*)(smem + act_dst + 128) = (u16)(a2 >> 16);
      __builtin_amdgcn_fence(__ATOMIC_RELEASE, "wavefront"); __builtin_amdgcn_wave_barrier(); __builtin_amdgcn_fence(__ATOMIC_ACQUIRE, "wavefront");
      const u32x4 aa = *(const u32x4*)(smem + act_src);
      f32x4 d[8];
#pragma unroll
      for (int c = 0; c < 8; ++c) d[c] = mfma16(aa, wf[16 + c], (f32x4){0.f, 0.f, 0.f, 0.f});
      SB0();
      u32x4 y[2];
#pragma unroll
      for (int c = 0; c < 8; ++c) {
        const float t0 = d[c][0] + dppf<DPP_ROR8>(d[c][2]), t1 = d[c][3] + dppf<DPP_ROR8>(d[c][1]);
        y[c >> 2][c & 3] = __float_as_uint((lane & 8) ? t1 : t0);
      }
      STW(sane(y[0]), yp_dst); STW(sane(y[1]), yp_dst + 128);
      if (tid == 256) misc[30] = w0;
    }
  }
}

extern "C" void kernel_launch(void* const* d_in, const int* in_sizes, int n_in, void* d_out, int out_size,
                              void* d_ws, size_t ws_size, hipStream_t stream) {
  (void)hipFuncSetAttribute((const void*)fused, hipFuncAttributeMaxDynamicSharedMemorySize, LDS_TOTAL);
  fused<<<256, 512, LDS_TOTAL, stream>>>((const u16*)d_in[0], (const u16*)d_in[1], (const u16*)d_in[2], (const u16*)d_in[4],
                                         (const u16*)d_in[5], (const u16*)d_in[6], (const u16*)d_in[7], (u16*)d_out, (char*)d_ws);
}
```

```cpp
#include <hip/hip_runtime.h>
#include <stdint.h>

#define LAS __attribute__((address_space(3)))
#define GAS __attribute__((address_space(1)))
#define DI __device__ __forceinline__

typedef unsigned short u16;
typedef unsigned long long u64;
typedef __attribute__((ext_vector_type(2))) __bf16 bf16x2;
typedef __attribute__((ext_vector_type(8))) __bf16 bf16x8;
typedef __attribute__((ext_vector_type(4))) short s16x4;
typedef __attribute__((ext_vector_type(2))) unsigned short u16x2;
typedef __attribute__((ext_vector_type(4))) float f32x4;
typedef __attribute__((ext_vector_type(2))) float f32x2;
typedef __attribute__((ext_vector_type(4))) unsigned u32x4;
typedef __attribute__((ext_vector_type(2))) unsigned u32x2;
typedef GAS unsigned gu32;
typedef GAS unsigned long long gu64;

constexpr int S_ = 2048, D_ = 1024, F_ = 3584;

constexpr unsigned SY_CH = 0, SY_GO = 1024, SY_RE = 2048;
constexpr unsigned WS_FD  = 98304;
constexpr unsigned WS_ZF  = 8192;
constexpr unsigned WS_KVF = 16384;
constexpr unsigned WS_RS  = 131072;
constexpr unsigned WS_CS  = WS_RS + 16 * 16 * 1024;
constexpr unsigned WS_ZP  = WS_CS + 16 * 16 * 768;
constexpr unsigned WS_FB  = WS_ZP + 32 * 16 * 128;
constexpr unsigned WS_ST2 = WS_FB + 32 * 32 * 128;
constexpr unsigned WS_XS  = WS_ST2 + 32 * 8 * 1024;
constexpr unsigned WS_GU  = WS_XS + 64 * 2048;
constexpr unsigned WS_YP  = WS_GU + 256 * 1024;
constexpr unsigned WS_KVP = WS_YP + 2 * 8 * 64 * 512;
constexpr unsigned WS_O   = WS_KVP + 32 * 16 * 4096;
constexpr unsigned WS_END = WS_O + S_ * D_ * 2;
constexpr unsigned OOB = 0x80000000u;

template <int V> struct IC { static constexpr int v = V; };
template <int B, int E, class F> DI void sfor(F&& f) { if constexpr (B < E) { f(IC<B>{}); sfor<B + 1, E>(f); } }

DI float bflo(unsigned u) { return __uint_as_float(u << 16); }
DI float bfhi(unsigned u) { return __uint_as_float(u & 0xffff0000u); }
DI float rbf(float x) { return (float)(__bf16)x; }
DI unsigned pack2(float a, float b) { bf16x2 c; c[0] = (__bf16)a; c[1] = (__bf16)b; return __builtin_bit_cast(unsigned, c); }
DI unsigned pack2o(float a, float b) { unsigned p = pack2(a, b); asm("" : "+v"(p)); return p; }
DI f32x4 mfma16(u32x4 a, u32x4 b, f32x4 c) {
  return __builtin_amdgcn_mfma_f32_16x16x32_bf16(__builtin_bit_cast(bf16x8, a), __builtin_bit_cast(bf16x8, b), c, 0, 0, 0);
}
DI void mfma16i(u32x4 a, u32x4 b, f32x4& c) { asm volatile("v_mfma_f32_16x16x32_bf16 %0, %1, %2, %0" : "+v"(c) : "v"(a), "v"(b)); }
DI f32x2 unpk(unsigned u) { return (f32x2){bflo(u), bfhi(u)}; }
DI u32x2 trread(const char* p) {
  s16x4 v = __builtin_amdgcn_ds_read_tr16_b64_v4i16((LAS s16x4*)p);
  return __builtin_bit_cast(u32x2, v);
}
DI const char* uni(const void* p) {
  const u64 v = (u64)p;
  return (const char*)(((u64)(unsigned)__builtin_amdgcn_readfirstlane((int)(v >> 32)) << 32) | (unsigned)__builtin_amdgcn_readfirstlane((int)v));
}
#define RV4(a, b, c, d) "v" #a, "v" #b, "v" #c, "v" #d
#define RV10(p) "v" #p "0", "v" #p "1", "v" #p "2", "v" #p "3", "v" #p "4", "v" #p "5", "v" #p "6", "v" #p "7", "v" #p "8", "v" #p "9"
#define RSV2 RV4(224, 225, 226, 227), "v228", "v229", RV10(23), RV10(24), RV4(250, 251, 252, 253), "v254", "v255"
#define RSV1 RV4(192, 193, 194, 195), RV4(196, 197, 198, 199), RV10(20), RV10(21), RV4(220, 221, 222, 223), RSV2
#define RSV "v148", "v149", RV10(15), RV10(16), RV10(17), RV10(18), "v190", "v191", RSV1
#define NTW " nt"
#define GL_(v, s, o) "global_load_lds_dwordx4 " v ", " s " offset:" #o
#define GLDS4(SC, CL...) asm volatile("s_add_u32 m0, %5, %c6\n\ts_nop 3\n\t" GL_("%0", "%4", 0) SC "\n\t" GL_("%1", "%4", 1024) SC "\n\t" \
  GL_("%2", "%4", 2048) SC "\n\t" GL_("%3", "%4", 3072) SC :: "v"(v0), "v"(v1), "v"(v2), "v"(v3), "s"(uni(sbase)), "s"(lds), "i"(IMM) : "memory", "scc", ##CL)
#define GLDS3(SC, CL...) asm volatile("s_add_u32 m0, %4, %c5\n\ts_nop 3\n\t" GL_("%0", "%3", 0) SC "\n\t" GL_("%1", "%3", 1024) SC "\n\t" \
  GL_("%2", "%3", 2048) SC :: "v"(v0), "v"(v1), "v"(v2), "s"(uni(sbase)), "s"(lds), "i"(IMM) : "memory", "scc", ##CL)
template <int IMM> DI void glds4(unsigned v0, unsigned v1, unsigned v2, unsigned v3, const void* sbase, unsigned lds) { GLDS4(""); }
template <int IMM> DI void glds4r(unsigned v0, unsigned v1, unsigned v2, unsigned v3, const void* sbase, unsigned lds) { GLDS4("", RSV); }
template <int IMM> DI void glds3r(unsigned v0, unsigned v1, unsigned v2, const void* sbase, unsigned lds) { GLDS3("", RSV); }
template <int R> DI void sload(unsigned voff, const void* sbase) {
  asm volatile("s_nop 4\n\tglobal_load_dwordx4 v[%c0:%c1], %2, %3 sc1" ::"i"(R), "i"(R + 3), "v"(voff), "s"(uni(sbase)) : "memory", RSV);
}
DI float fadd(float a, float b) { float o; asm("v_add_f32 %0, %1, %2" : "=v"(o) : "v"(a), "v"(b)); return o; }
#define RA_(o, a, b) "v_add_f32 %" #o ", v[%c6+" #a "], v[%c6+" #b "]\n\t"
#define RB2_(o, a) "v_add_f32 %" #o ", v[%c6+" #a "], %" #o "\n\t"
#define RM_(o, a, b) "v_max3_u32 %" #o ", v[%c6+" #a "], v[%c6+" #b "], %" #o "\n\t"
template <int R> DI void rsum4(float* a, unsigned& m, unsigned& n) {
  asm volatile(RA_(0, 0, 4) RA_(1, 1, 5) RM_(4, 0, 1) RA_(2, 2, 6) RA_(3, 3, 7) RM_(5, 2, 3) RB2_(0, 8) RM_(4, 4, 5) RB2_(1, 9) RM_(5, 6, 7)
               RB2_(2, 10) RM_(4, 8, 9) RB2_(3, 11) RM_(5, 10, 11) RB2_(0, 12) RB2_(1, 13) RM_(4, 12, 13) RB2_(2, 14) RB2_(3, 15) RM_(5, 14, 15)
               : "=&v"(a[0]), "=&v"(a[1]), "=&v"(a[2]), "=&v"(a[3]), "+v"(m), "+v"(n) : "i"(R) : RSV);
}
template <int R> DI void wload2(unsigned voff, const void* sbase) {
  asm volatile("s_nop 4\n\tglobal_load_dwordx4 v[%c0:%c1], %2, %3" NTW ::"i"(R), "i"(R + 3), "v"(voff), "s"(uni(sbase)) : "memory", RSV2);
}
template <int R> DI void wload(unsigned voff, const void* sbase) {
  asm volatile("s_nop 4\n\tglobal_load_dwordx4 v[%c0:%c1], %2, %3" NTW ::"i"(R), "i"(R + 3), "v"(voff), "s"(uni(sbase)) : "memory", RSV);
}
template <int RA, int RB> DI unsigned wperm(unsigned sel) {
  unsigned o;
  if (RA < 192) asm volatile("v_perm_b32 %0, v%c2, v%c1, %3" : "=v"(o) : "i"(RA), "i"(RB), "s"(sel) : RSV);
  else if (RA < 224) asm volatile("v_perm_b32 %0, v%c2, v%c1, %3" : "=v"(o) : "i"(RA), "i"(RB), "s"(sel) : RSV1);
  else asm volatile("v_perm_b32 %0, v%c2, v%c1, %3" : "=v"(o) : "i"(RA), "i"(RB), "s"(sel) : RSV2);
  return o;
}
template <int R> DI unsigned rget() { unsigned o; asm volatile("v_mov_b32 %0, v%c1" : "=v"(o) : "i"(R) : RSV); return o; }
DI float xh_sum(float v) {
  auto r = __builtin_amdgcn_permlane32_swap(__float_as_uint(v), __float_as_uint(v), false, false);
  return __uint_as_float(r[0]) + __uint_as_float(r[1]);
}
template <int CTRL> DI unsigned dppu(unsigned v) { return (unsigned)__builtin_amdgcn_update_dpp(0, (int)v, CTRL, 0xf, 0xf, true); }
template <int CTRL> DI float dppf(float v) { return __uint_as_float(dppu<CTRL>(__float_as_uint(v))); }
constexpr int DPP_X1 = 0xB1, DPP_X2 = 0x4E, DPP_HMIR = 0x141, DPP_MIR = 0x140, DPP_ROR8 = 0x128;
DI float quad_sum(float v) { v += dppf<DPP_X1>(v); v += dppf<DPP_X2>(v); return v; }
DI float oct_sum(float v) { v = quad_sum(v); v += dppf<DPP_HMIR>(v); return v; }
DI float x16_sum(float v) {
  auto r = __builtin_amdgcn_permlane16_swap(__float_as_uint(v), __float_as_uint(v), false, false);
  return __uint_as_float(r[0]) + __uint_as_float(r[1]);
}
DI float sw16(float a, float b) {
  auto r = __builtin_amdgcn_permlane16_swap(__float_as_uint(a), __float_as_uint(b), false, false);
  return fadd(__uint_as_float(r[0]), __uint_as_float(r[1]));
}
DI float sw32(float a, float b) {
  auto r = __builtin_amdgcn_permlane32_swap(__float_as_uint(a), __float_as_uint(b), false, false);
  return fadd(__uint_as_float(r[0]), __uint_as_float(r[1]));
}
template <int N> struct Phi { unsigned tb[N]; f32x2 tr[N], e[N]; };
template <int N> DI void phi_a(const f32x2* t, Phi<N>& s) {
#pragma unroll
  for (int i = 0; i < N; ++i) s.tb[i] = pack2o(t[i][0], t[i][1]);
  __builtin_amdgcn_sched_barrier(0);
#pragma unroll
  for (int i = 0; i < N; ++i) s.tr[i] = unpk(s.tb[i]);
  __builtin_amdgcn_sched_barrier(0);
#pragma unroll
  for (int i = 0; i < N; ++i) s.e[i] = s.tr[i] * 1.44269504088896340736f;
  __builtin_amdgcn_sched_barrier(0);
}
template <int N> DI void phi_b(Phi<N>& s, unsigned* p) {
  u16x2 tm[2] = {{0, 0}, {0, 0}};
#pragma unroll
  for (int i = 0; i < N; ++i) {
    s.e[i][0] = __builtin_amdgcn_fmed3f(__builtin_amdgcn_exp2f(s.e[i][0]), 0.f, 1.f);
    tm[i & 1] = __builtin_elementwise_max(tm[i & 1], __builtin_bit_cast(u16x2, s.tb[i]));
    s.e[i][1] = __builtin_amdgcn_fmed3f(__builtin_amdgcn_exp2f(s.e[i][1]), 0.f, 1.f);
    __builtin_amdgcn_sched_barrier(0);
  }
#pragma unroll
  for (int i = 0; i < N; ++i) s.tr[i] += s.e[i];
  __builtin_amdgcn_sched_barrier(0);
#pragma unroll
  for (int i = 0; i < N; ++i) { s.tr[i][0] = fmaxf(s.tr[i][0], s.e[i][0]); s.tr[i][1] = fmaxf(s.tr[i][1], s.e[i][1]); }
  const u16x2 tx = __builtin_elementwise_max(tm[0], tm[1]);
  __builtin_amdgcn_sched_barrier(0);
#pragma unroll
  for (int i = 0; i < N; ++i) p[i] = pack2(s.tr[i][0], s.tr[i][1]);
  if (__any(tx[0] > 0xC0F0 || tx[1] > 0xC0F0)) {
#pragma unroll
    for (int i = 0; i < N; ++i) p[i] = pack2(bflo(p[i]) + 1e-6f, bfhi(p[i]) + 1e-6f);
  }
}
template <int N> DI void phi_n(const f32x2* t, unsigned* p) { Phi<N> s; phi_a<N>(t, s); phi_b<N>(s, p); }
#define RLX_AGENT __ATOMIC_RELAXED, __HIP_MEMORY_SCOPE_AGENT
DI void st64(char* ws, unsigned off, u64 v) { __hip_atomic_store((gu64*)(ws + off), v, RLX_AGENT); }
DI unsigned ld32(char* ws, unsigned off) { return __hip_atomic_load((gu32*)(ws + off), RLX_AGENT); }
#define WAIT_V(n) asm volatile("s_waitcnt vmcnt(%c0)" ::"i"(n) : "memory")
#define RAW_BAR() do { asm volatile("s_waitcnt lgkmcnt(0)" ::: "memory"); __builtin_amdgcn_s_barrier(); asm volatile("" ::: "memory"); } while (0)
#define SB0() __builtin_amdgcn_sched_barrier(0)
#define WAIT_VR(n) asm volatile("s_waitcnt vmcnt(%c0)" ::"i"(n) : "memory", RSV)
#define RAW_BARR() do { asm volatile("s_waitcnt lgkmcnt(0)" ::: "memory", RSV); __builtin_amdgcn_s_barrier(); asm volatile("" ::: "memory", RSV); } while (0)
#define KEEP2() asm volatile("" ::: "memory", RSV2)
#define RAW_BAR2() do { asm volatile("s_waitcnt lgkmcnt(0)" ::: "memory", RSV2); __builtin_amdgcn_s_barrier(); KEEP2(); } while (0)
constexpr unsigned SPIN_CAP = 1u << 18;
#define SPIN _Pragma("nounroll") for (unsigned s_ = 0; s_ < SPIN_CAP; ++s_)
constexpr unsigned SENT = 0xFFFFFFFFu;
DI unsigned sane1(unsigned v) { return min(v, 0xFFFFFFFEu); }
DI u32x4 sane(u32x4 v) { v[1] = sane1(v[1]); v[3] = sane1(v[3]); return v; }
DI unsigned chk(unsigned m, u32x4 v) { return max(max(m, v[1]), v[3]); }
DI bool wave_ok(unsigned m) { return !__any(m == SENT); }
DI unsigned key_idx(unsigned k) { return (0xFFFFu - (k & 0xFFFFu)) & (S_ - 1); }
DI float key_val(unsigned k) { const unsigned o = k >> 16; return __uint_as_float(((o & 0x8000u) ? (o & 0x7fffu) : (~o & 0xffffu)) << 16); }
DI void top2_mix(unsigned& b0, unsigned& b1, unsigned c0, unsigned c1) {
  const unsigned lo = min(b0, c0), mx = max(b1, c1);
  b0 = max(b0, c0); b1 = max(lo, mx);
}
DI void top2_lo(unsigned& b0, unsigned& b1) {
  top2_mix(b0, b1, dppu<DPP_X1>(b0), dppu<DPP_X1>(b1));
  top2_mix(b0, b1, dppu<DPP_X2>(b0), dppu<DPP_X2>(b1));
  top2_mix(b0, b1, dppu<DPP_HMIR>(b0), dppu<DPP_HMIR>(b1));
}
DI void top2_mid(unsigned& b0, unsigned& b1) {
  top2_mix(b0, b1, dppu<DPP_MIR>(b0), dppu<DPP_MIR>(b1));
  auto p = __builtin_amdgcn_permlane16_swap(b0, b0, false, false); auto q = __builtin_amdgcn_permlane16_swap(b1, b1, false, false);
  b0 = p[0]; b1 = q[0]; top2_mix(b0, b1, p[1], q[1]);
}
DI void top2_hi(unsigned& b0, unsigned& b1) {
  top2_mid(b0, b1);
  auto p = __builtin_amdgcn_permlane32_swap(b0, b0, false, false); auto q = __builtin_amdgcn_permlane32_swap(b1, b1, false, false);
  b0 = p[0]; b1 = q[0]; top2_mix(b0, b1, p[1], q[1]);
}

constexpr int K1_STAGE = 40960;
constexpr int LDS_TOTAL = 4 * K1_STAGE;
constexpr int L_STM = 0;
constexpr int L_MISC = 2048;
constexpr int L_XK = 5120;
constexpr int L_KX = 3328;
constexpr int L_ACT = 2304;
constexpr int L_CAND = 4096;
constexpr int L_WO = 16384;
constexpr int L_P = 16384, L_PK = 7 * 16384;
constexpr int L_RED = 65536;
constexpr int wo_slot(int j) { return L_WO + (j % 9) * 16384; }
constexpr int CODE_LINES = 12;
#define OW_ 8
constexpr int OW = OW_;
#define W1A_ 16
constexpr int W1A = W1A_;
#define GSCHED 5, 9, 13, 17, 21, 24, 27, 30, 33, 36, 39, 42, 45, 48, 48
constexpr int GS[15] = {GSCHED};
constexpr int gops(int g) { return (g / 3) * 10 + (g % 3 == 0 ? 0 : g % 3 == 1 ? 4 : 7); }
constexpr bool gs_ok() {
  bool ok = GS[0] >= 3 && GS[0] <= 9 && GS[1] >= 6 && GS[1] <= 12 && GS[0] <= GS[1] && GS[12] == 45 && GS[13] == 48 && GS[14] == 48;
  for (int t = 0; t < 12; ++t) ok = ok && GS[t + 2] <= 3 * (t + 5) && GS[t + 2] >= 3 * (t + 3) && GS[t + 2] >= GS[t + 1] + 3;
  return ok;
}
static_assert(gs_ok(), "staging schedule");
#define WSCHED 0, 0, 0, 0, 0, 0, 0, 0, 0, 2, 4, 6, 8
constexpr int WS_[13] = {WSCHED};
#define PACE 1
#define P_KVP 1
#define P_KVF 1
#define P_FB 8
#define P_C 14
#define P_D 2
#define P_GU 12
#define P_E 10
#define SLEEP(n) do { if (n) __builtin_amdgcn_s_sleep(n); } while (0)
constexpr int VW = 160;

__global__ __launch_bounds__(512, 2) void fused(const u16* __restrict__ x, const u16* __restrict__ wqkv,
                                                const u16* __restrict__ wo, const u16* __restrict__ w_r,
                                                const u16* __restrict__ wg, const u16* __restrict__ wu,
                                                const u16* __restrict__ wd, u16* __restrict__ out, char* __restrict__ ws) {
  extern __shared__ __attribute__((aligned(16))) char smem[];
  u64 pc0; asm volatile("s_getpc_b64 %0" : "=s"(pc0));
  const int wave = __builtin_amdgcn_readfirstlane(threadIdx.x >> 6);
  const unsigned lds0 = (unsigned)(size_t)smem;
#define RB_(b) (((b) & 1) * 8 + (((b) >> 3) & 7))
#define HP_(b) ((((b) & 7) >> 1) * 4 + ((b) >> 6))
#define KROT_(b) ((4 * ((HP_(b) >> 2) + (RB_(b) >> 3) + 2 * ((RB_(b) >> 2) & 1))) & 15)
  unsigned voA0, voA1, voA2, voA3, voB0, voB1, voB2, voB3, voB4, voB5;
  const unsigned l4 = __builtin_amdgcn_readfirstlane(lds0 + (wave - 4) * 4096), l6 = __builtin_amdgcn_readfirstlane(lds0 + (wave - 4) * 6144);
  const int krot0 = KROT_(blockIdx.x);
  auto group = [&](auto G) {
    constexpr int t = G.v / 3, p = G.v % 3, sl = (t & 3) * K1_STAGE;
    const int kt = (t + krot0) & 15;
    if (p == 0) glds4r<sl>(voA0, voA1, voA2, voA3, (const char*)x + kt * 128, l4);
    else if (p == 1) glds3r<sl + 16384>(voB0, voB1, voB2, (const char*)wqkv + kt * 64 * (3 * D_ * 2), l6);
    else glds3r<sl + 16384 + 3072>(voB3, voB4, voB5, (const char*)wqkv + kt * 64 * (3 * D_ * 2), l6);
  };
  if (wave >= 4) {
    const int lane = threadIdx.x & 63, sw = wave - 4;
    const int r0 = RB_(blockIdx.x) * 128 + 32 * sw + (lane >> 3);
    voA0 = (unsigned)(r0 * D_ + ((lane & 7) ^ (lane >> 4)) * 8) * 2u;
    voA1 = ((voA0 + 8 * D_ * 2) ^ 64) - 1024; voA2 = voA0 + 16 * D_ * 2 - 2048; voA3 = voA1 + 16 * D_ * 2 - 2048;
    asm volatile("" : "+v"(voA0), "+v"(voA1), "+v"(voA2), "+v"(voA3));
    group(IC<0>{});
    int l2 = lane; asm volatile("" : "+v"(l2));
    voB0 = (unsigned)((16 * sw + 4 * (l2 >> 5) + ((l2 >> 2) & 3)) * (3 * D_) + HP_(blockIdx.x) * 64 + ((l2 >> 4) & 1) * 32 + (l2 & 3) * 8) * 2u;
    voB1 = voB0 + 1024; voB2 = voB0 + 2048;
    voB3 = (voB0 ^ 32) + 8 * 3 * D_ * 2; voB4 = voB3 + 1024; voB5 = voB3 + 2048;
    asm volatile("" : "+v"(voB0), "+v"(voB1), "+v"(voB2), "+v"(voB3), "+v"(voB4), "+v"(voB5));
    sfor<1, 3>(group);
  } else if (wave == 0) {
    const int l = threadIdx.x;
    asm volatile("s_mov_b32 m0, %1\n\ts_nop 0\n\tglobal_load_lds_dword %0, off"
                 :: "v"((const char*)(pc0 - (pc0 & 127)) + ((blockIdx.x >> 3) + (l < CODE_LINES ? l : 0) * 32) * 128), "s"(__builtin_amdgcn_readfirstlane(lds0 + 3 * K1_STAGE)) : "memory");
  }
  int bid = blockIdx.x; asm volatile("" : "+s"(bid));
  const int xcd = bid & 7, idx = bid >> 3;
  const int rb = RB_(bid), hp = HP_(bid);
  const int rp = xcd * 4 + (idx >> 3), cb = idx & 7;
  const int hs_ = (hp + 4 + (int)(((size_t)wqkv >> 7) & 7)) & 7;
  const int g8 = ((hp >> 3) * 4 + (rb >> 2)) * 8 + hs_, kq = rb & 3;
  const int krot = KROT_(bid), posA = 1 + (hp & 3), posB = 1 + (rb & 3);
  const int wrot = 4 * rp + (xcd >> 1), wdir = (xcd & 1) ? -1 : 1;
  auto whead = [&](int t) { return (wrot + wdir * t) & 15; };
  const unsigned rs_slot = WS_RS + (rb * 16 + ((krot + posA) & 15)) * 1024, cs_slot = WS_CS + (hp * 16 + ((krot + posB) & 15)) * 768;
  const __amdgpu_buffer_rsrc_t wsr = __builtin_amdgcn_make_buffer_rsrc(ws, 0, WS_END, 0x00020000);
#define LDW(off) __builtin_amdgcn_raw_buffer_load_b128(wsr, (off), 0, 16)
#define STW(v, off) __builtin_amdgcn_raw_buffer_store_b128((v), wsr, (off), 0, 16)
#define STW1(v, off) __builtin_amdgcn_raw_buffer_store_b32((v), wsr, (off), 0, 16)
  float* stm = (float*)(smem + L_STM);
  volatile LAS int* misc = (volatile LAS int*)(smem + L_MISC);
#define LANES() int tid = threadIdx.x; asm volatile("" : "+v"(tid)); const int lane = tid & 63, r = lane & 31, h = lane >> 5; (void)r; (void)h
#define EPI() const int erow = tid >> 3, eseg = tid & 7; const size_t ego = (size_t)(rp * 64 + erow) * D_ + cb * 128 + eseg * 8
  u32x4 xv[2], wv[2];
  f32x2 xf[8], wf2[8];
  float sw_;
  auto unpack_xw = [&]() {
    f32x2 a = {0.f, 0.f};
#pragma unroll
    for (int i = 0; i < 8; ++i) { xf[i] = unpk(xv[i >> 2][i & 3]); wf2[i] = unpk(wv[i >> 2][i & 3]); a += wf2[i]; }
    sw_ = oct_sum(a[0] + a[1]);
#pragma unroll
    for (int i = 0; i < 8; ++i) asm volatile("" : "+v"(xf[i]), "+v"(wf2[i]));
    asm volatile("" : "+v"(sw_));
  };
  u32x4 wf[24];

  auto wtr = [&](auto I) {
    constexpr int m = I.v >> 3, c = I.v & 7, R = VW + 32 * m + (c >> 1);
    const unsigned sel = (c & 1) ? 0x07060302u : 0x05040100u;
    wf[I.v] = (u32x4){wperm<R, R + 4>(sel), wperm<R + 8, R + 12>(sel), wperm<R + 16, R + 20>(sel), wperm<R + 24, R + 28>(sel)};
  };

  if (wave < 4) {
    LANES();
    const int c16 = lane & 15, g4 = lane >> 4;
    const int trl = (c16 >> 2) * 64 + (c16 & 3) * 8;
    const int wr = wave >> 1, wc = wave & 1;
    const int head = 2 * hp + wc;
    const unsigned kvp_off = WS_KVP + (head * 16 + rb) * 4096 + wr * 2048 + lane * 16;
    const unsigned zp_off = WS_ZP + (head * 16 + rb) * 128;
    const unsigned o_off = WS_O + ((rb * 4 + 2 * wr) * 32 + head) * 2048 + lane * 16;
    const unsigned fb_off = lane < 8 ? WS_FB + ((rb * 2 + wr) * 32 + head) * 128 + lane * 16 : OOB;
    unsigned pfa, pfb = OOB;
    unsigned pfc = lane < 8 ? fb_off : lane < 24 ? rs_slot + wave * 256 + (lane - 8) * 16
        : lane < 40 ? (wave < 3 ? cs_slot + wave * 256 : WS_KVF + hp * 4096 + rb * 256) + (lane - 24) * 16
        : lane < 48 ? (wave < 2 ? zp_off + (lane & 7) * 16 : (wave == 2 && rb < 2) ? WS_ZF + (2 * hp + rb) * 128 + (lane & 7) * 16 : OOB) : OOB;
    if (wave == 0) pfa = WS_ST2 + (rp * 8 + cb) * 1024 + lane * 16;
    else if (wave == 1) pfa = WS_GU + (g8 * 4 + kq) * 1024 + lane * 16;
    else pfa = WS_YP + (((wave - 2) * 8 + 2 * kq + (lane >> 5)) * 64 + g8) * 512 + (lane & 31) * 16;
    if (wave == 0) pfb = lane < 32 ? WS_XS + (rp * 2 + (lane >> 4)) * 2048 + cb * 256 + (lane & 15) * 16 : lane < 40 ? WS_FD + (cb * 32 + rp) * 128 + (lane & 7) * 16 : OOB;
    u32x4 S4 = {SENT, SENT, SENT, SENT};
    asm volatile("" : "+v"(pfa), "+v"(pfb), "+v"(pfc), "+v"(S4));
    __builtin_amdgcn_s_setprio(1);
    f32x4 acc[4][6];
    int ao[2][2], bo[2][2];
    {
#pragma unroll
      for (int i = 0; i < 2; ++i) {
        ao[0][i] = (64 * wr + c16) * 128 + (((4 * i + g4) ^ (c16 >> 1)) * 16); ao[1][i] = ao[0][i] + 2 * K1_STAGE;
        bo[0][i] = 16384 + g4 * 3072 + wc * 256 + trl + ((i ^ (g4 & 1)) * 32); bo[1][i] = bo[0][i] + 2 * K1_STAGE;
        asm volatile("" : "+v"(ao[0][i]), "+v"(ao[1][i]), "+v"(bo[0][i]), "+v"(bo[1][i]));
      }
#pragma unroll
      for (int i = 0; i < 4; ++i)
#pragma unroll
        for (int j = 0; j < 6; ++j) { acc[i][j] = (f32x4){0.f, 0.f, 0.f, 0.f}; asm volatile("" : "+v"(acc[i][j])); }
      struct Frag { u32x4 a[4]; u32x2 bl[6], bh[6]; };
      Frag FA, FB;
#define K1_B(j) ((u32x4){C.bl[j][0], C.bl[j][1], C.bh[j][0], C.bh[j][1]})
#define K1_LA(i) N.a[i] = *(const u32x4*)(smem + ao[sn >> 1][hf] + (sn & 1) * K1_STAGE + (i) * 2048)
#define K1_LL(j) N.bl[j] = trread(smem + bo[sn >> 1][(j) & 1] + (sn & 1) * K1_STAGE + hf * 12288 + ((j) >> 1) * 1024)
#define K1_LH(j) N.bh[j] = trread(smem + bo[sn >> 1][(j) & 1] + (sn & 1) * K1_STAGE + hf * 12288 + ((j) >> 1) * 1024 + 512)
#define K1_M(i, j) if ((j) < 2) mfma16i(K1_B(j), C.a[i], acc[i][j]); else mfma16i(C.a[i], K1_B(j), acc[i][j]); SB0()
      auto sub = [&](Frag& C, Frag& N, int sn, int hf) {
        sfor<0, 24>([&](auto Q) {
          constexpr int q = Q.v;
          constexpr int j = q / 4, i = (j & 1) ? 3 - q % 4 : q % 4;
          if (q == 23) asm volatile("s_waitcnt lgkmcnt(0)" : "+v"(N.bh[5]));
          K1_M(i, j);
          if constexpr (q < 4) { K1_LA(q); } else if constexpr (q < 16) { if (q & 1) { K1_LH((q - 4) >> 1); } else { K1_LL((q - 4) >> 1); } }
          SB0();
        });
      };
      auto subkv = [&](Frag& C, Frag& N, int sn, int hf, bool ld) {
#define K1_QL(i, j, l) K1_M(i, j); if (ld) { l; } SB0()
        K1_QL(0, 2, K1_LL(2)); K1_QL(1, 2, K1_LH(2)); K1_QL(0, 3, K1_LA(0)); K1_QL(1, 3, K1_LA(1)); K1_QL(2, 2, K1_LL(3)); K1_QL(3, 2, K1_LH(3));
        K1_QL(2, 3, K1_LA(2)); K1_QL(3, 3, K1_LA(3)); K1_QL(0, 4, K1_LL(4)); K1_QL(1, 4, K1_LH(4)); K1_QL(2, 4, K1_LL(5)); K1_QL(3, 4, K1_LH(5));
        K1_M(0, 5); K1_M(1, 5); K1_M(2, 5); K1_M(3, 5);
      };
      WAIT_V(0);
      RAW_BAR();
      {
        Frag& N = FA; const int sn = 0, hf = 0;
        K1_LL(0); K1_LH(0); K1_LA(0); K1_LA(1); K1_LL(1); K1_LH(1); K1_LL(2); K1_LH(2); K1_LL(3); K1_LH(3); K1_LA(2); K1_LA(3); K1_LL(4); K1_LH(4); K1_LL(5); K1_LH(5);
      }
#pragma unroll 1
      for (int it = 0; it < 4; ++it) {
#pragma unroll
        for (int s = 0; s < 4; ++s) {
          if (s == 2 && it == 3) break;
          sub(FA, FB, s, 1);
          if (s == 0) WAIT_V(0);
          if (it == 0) { if (s == 1) STW(S4, pfc); if (s == 2) STW(S4, kvp_off); if (s == 3) STW(S4, kvp_off + 1024); }
          if (it == 1) { if (s == 0) STW(S4, pfa); if (s == 1) STW(S4, pfb); }
          if (s != 1 || it < 3) RAW_BAR();
          sub(FB, FA, (s + 1) & 3, 0);
        }
      }
      subkv(FA, FB, 2, 1, true);
      RAW_BAR();
      subkv(FB, FA, 3, 0, true);
      subkv(FA, FB, 3, 1, true);
      subkv(FB, FA, 0, 0, false);
    }
    asm volatile("s_nop 15" ::: "memory");
    {
      f32x4 m4[4], i4[4]; float cs[4];
      SPIN {
        const int ok = misc[12 + (lane & 3)];
        asm volatile("" ::: "memory");
#pragma unroll
        for (int i = 0; i < 4; ++i) {
          cs[i] = stm[256 + wc * 96 + 32 + 16 * i + c16];
          m4[i] = *(const f32x4*)(stm + 64 * wr + 16 * i + 4 * g4); i4[i] = *(const f32x4*)(stm + 128 + 64 * wr + 16 * i + 4 * g4);
        }
        SB0();
        if (__all(ok != 0)) break;
      }
      const u32x4 ONES4 = {0x3F803F80u, 0x3F803F80u, 0x3F803F80u, 0x3F803F80u};
      f32x2 tk[16], tv[16];
#define PR(v, i) ((f32x2){(v)[2 * (i)], (v)[2 * (i) + 1]})
#pragma unroll
      for (int i = 0; i < 16; ++i) {
        tk[i] = PR(acc[(i >> 1) & 3][2 + (i >> 3)], i & 1) - PR(m4[(i >> 1) & 3], i & 1) * cs[i >> 3];
        tv[i] = PR(acc[(i >> 1) & 3][4 + (i >> 3)], i & 1) - PR(m4[(i >> 1) & 3], i & 1) * cs[2 + (i >> 3)];
      }
      SB0();
#pragma unroll
      for (int i = 0; i < 16; ++i) { tk[i] *= PR(i4[(i >> 1) & 3], i & 1); tv[i] *= PR(i4[(i >> 1) & 3], i & 1); }
      SB0();
      unsigned kp[16], vp[16];
#pragma unroll
      for (int i = 0; i < 16; ++i) vp[i] = pack2o(tv[i][0], tv[i][1]);
      phi_n<16>(tk, kp);
#define F4_(p, i) ((u32x4){(p)[4 * (i)], (p)[4 * (i) + 1], (p)[4 * (i) + 2], (p)[4 * (i) + 3]})
      const f32x4 Z4 = {0.f, 0.f, 0.f, 0.f};
      u32x4 vk[2], vg[2];
#pragma unroll
      for (int j = 0; j < 2; ++j) { vk[j] = wr ? F4_(vp, 2 + j) : F4_(vp, j); vg[j] = wr ? F4_(vp, j) : F4_(vp, 2 + j); }
      f32x4 keep[2], give[2], zq[2];
#pragma unroll
      for (int d = 0; d < 2; ++d) { give[d] = mfma16(F4_(kp, 2 * d), vg[0], Z4); zq[d] = mfma16(ONES4, F4_(kp, 2 * d), Z4); }
#pragma unroll
      for (int d = 0; d < 2; ++d) { give[d] = mfma16(F4_(kp, 2 * d + 1), vg[1], give[d]); zq[d] = mfma16(ONES4, F4_(kp, 2 * d + 1), zq[d]); }
#pragma unroll
      for (int d = 0; d < 2; ++d) keep[d] = mfma16(F4_(kp, 2 * d), vk[0], Z4);
#pragma unroll
      for (int d = 0; d < 2; ++d) keep[d] = mfma16(F4_(kp, 2 * d + 1), vk[1], keep[d]);
      SB0();
      const float zv = (lane & 16) ? zq[1][0] : zq[0][0];
      char* xk = smem + L_XK + wave * 2304 + lane * 16;
      const char* xp = smem + L_XK + (wave ^ 2) * 2304 + lane * 16;
      *(f32x4*)xk = give[0]; *(f32x4*)(xk + 1024) = give[1]; *(float*)(xk + 2048 - lane * 12) = zv;
      asm volatile("" ::: "memory");
      misc[16 + wave] = 1;
      f32x4 p0, p1; float pz;
      SPIN {
        const int ok = misc[16 + (wave ^ 2)];
        asm volatile("" ::: "memory");
        p0 = *(const f32x4*)xp; p1 = *(const f32x4*)(xp + 1024); pz = *(const float*)(xp + 2048 - lane * 12);
        SB0();
        if (ok == 1) break;
      }
      STW(sane(__builtin_bit_cast(u32x4, keep[0] + p0)), kvp_off);
      STW(sane(__builtin_bit_cast(u32x4, keep[1] + p1)), kvp_off + 1024);
      STW1(sane1(__float_as_uint(zv + pz)), (lane < 32 && wr == 0) ? zp_off + lane * 4 : OOB);
    }
    {
      u32x4 qa[4][4]; u32x2 ql[4][2], qh[4][2];
#pragma unroll
      for (int u = 0; u < 4; ++u) {
#pragma unroll
        for (int j = 0; j < 2; ++j) {
          ql[u][j] = trread(smem + bo[1][j] + (u >> 1) * K1_STAGE + (u & 1) * 12288);
          qh[u][j] = trread(smem + bo[1][j] + (u >> 1) * K1_STAGE + (u & 1) * 12288 + 512);
        }
#pragma unroll
        for (int i = 0; i < 4; ++i) qa[u][i] = *(const u32x4*)(smem + ao[1][u & 1] + (u >> 1) * K1_STAGE + i * 2048);
      }
      asm volatile("" ::: "memory");
      misc[8 + wave] = 3;
      SB0();
#pragma unroll
      for (int u = 0; u < 4; ++u)
#pragma unroll
        for (int j = 0; j < 2; ++j)
#pragma unroll
          for (int i = 0; i < 4; ++i) acc[i][j] = mfma16((u32x4){ql[u][j][0], ql[u][j][1], qh[u][j][0], qh[u][j][1]}, qa[u][i], acc[i][j]);
      SB0();
    }
    unsigned qp[16];
    Phi<16> qs;
    const unsigned po = WS_KVP + ((2 * hp + (rb >> 3)) * 16 + h) * 4096 + (2 * ((rb >> 2) & 1) + ((lane >> 4) & 1)) * 1024 + (16 * (rb & 3) + (lane & 15)) * 16;
    u32x4 pv[8] = {};
    {
      f32x4 c4[2]; float m_b[4], rinv[4];
#pragma unroll
      for (int i = 0; i < 2; ++i) c4[i] = *(const f32x4*)(stm + 256 + wc * 96 + 16 * i + 4 * g4);
#pragma unroll
      for (int i = 0; i < 4; ++i) { m_b[i] = stm[64 * wr + 16 * i + c16]; rinv[i] = stm[128 + 64 * wr + 16 * i + c16]; }
      SB0();
      f32x2 tq[16];
#pragma unroll
      for (int i = 0; i < 16; ++i) tq[i] = PR(acc[i >> 2][(i >> 1) & 1], i & 1) - PR(c4[(i >> 1) & 1], i & 1) * m_b[i >> 2];
      SB0();
#pragma unroll
      for (int i = 0; i < 16; ++i) tq[i] *= rinv[i >> 2];
      SB0();
      phi_a<16>(tq, qs);
#pragma unroll
      for (int i = 0; i < 16; ++i) asm volatile("" : "+v"(qs.tb[i]), "+v"(qs.tr[i]), "+v"(qs.e[i]));
    }
    if (wave == 0) {
      SLEEP(P_KVP);
#pragma unroll
      for (int p = 0; p < 8; ++p) pv[p] = LDW(po + p * 8192);
    }
    asm volatile("" ::: "memory");
    if (wave == 1 && rb < 2) {
      SLEEP(P_KVP);
      SPIN {
        unsigned m = 0;
#pragma unroll
        for (int p = 0; p < 2; ++p) { pv[p] = LDW(WS_ZP + (2 * hp + rb) * 2048 + p * 1024 + lane * 16); m = chk(max(max(m, pv[p][0]), pv[p][2]), pv[p]); }
        if (wave_ok(m)) break;
        asm volatile("" ::: "memory");
      }
      f32x4 s = __builtin_bit_cast(f32x4, pv[0]) + __builtin_bit_cast(f32x4, pv[1]);
#pragma unroll
      for (int i = 0; i < 4; ++i) { s[i] += dppf<DPP_ROR8>(s[i]); s[i] = xh_sum(x16_sum(s[i])); }
      if (lane < 16) st64(ws, WS_ZF + (2 * hp + rb) * 128 + (lane >> 3) * 64 + (lane & 3) * 16 + ((lane >> 2) & 1) * 8,
                          ((u64)sane1(pack2(s[2], s[3])) << 32) | pack2(s[0], s[1]));
    }
    if (wave == 0) {
      SPIN {
        unsigned m = 0;
#pragma unroll
        for (int p = 0; p < 8; ++p) m = chk(m, pv[p]);
        if (wave_ok(m)) break;
        asm volatile("" ::: "memory");
#pragma unroll
        for (int p = 0; p < 8; ++p) pv[p] = LDW(po + p * 8192);
      }
      f32x4 s = __builtin_bit_cast(f32x4, pv[0]);
#pragma unroll
      for (int p = 1; p < 8; ++p) s += __builtin_bit_cast(f32x4, pv[p]);
      const unsigned p0 = pack2(xh_sum(s[0]), xh_sum(s[1])), p1 = pack2(xh_sum(s[2]), xh_sum(s[3]));
      auto a = __builtin_amdgcn_permlane16_swap(p0, p0, false, false);
      auto b = __builtin_amdgcn_permlane16_swap(p1, p1, false, false);
      STW(sane((u32x4){a[0], b[0], a[1], b[1]}), lane < 16 ? WS_KVF + hp * 4096 + rb * 256 + lane * 16 : OOB);
    }
    phi_b<16>(qs, qp);
#pragma unroll
    for (int i = 0; i < 16; ++i) asm volatile("" : "+v"(qp[i]));
    if (wave == 0) misc[7] = 1;
    else SPIN { if (misc[7] == 1) break; __builtin_amdgcn_s_sleep(1); }
    SLEEP(P_KVF);
    {
      u32x4 k0, k1, z0;
      SPIN {
        k0 = LDW(WS_KVF + head * 2048 + lane * 16); k1 = LDW(WS_KVF + head * 2048 + 1024 + lane * 16);
        z0 = LDW(WS_ZF + head * 128 + g4 * 16);
        if (wave_ok(chk(chk(chk(0u, k0), k1), z0))) break;
        asm volatile("" ::: "memory");
      }
      const f32x4 Z4 = {0.f, 0.f, 0.f, 0.f};
      f32x4 X[8], Dn[4];
#pragma unroll
      for (int i = 0; i < 4; ++i) Dn[i] = mfma16(z0, F4_(qp, i), Z4);
#pragma unroll
      for (int i = 0; i < 8; ++i) X[i] = mfma16((i & 1) ? k1 : k0, F4_(qp, i >> 1), Z4);
      SB0();
      float ri[4]; unsigned a[16]; f32x2 f[16];
#pragma unroll
      for (int i = 0; i < 4; ++i) ri[i] = __builtin_amdgcn_rcpf(rbf(rbf(Dn[i][0]) + 1e-6f));
      SB0();
#pragma unroll
      for (int i = 0; i < 16; ++i) a[i] = pack2o(X[i >> 1][2 * (i & 1)], X[i >> 1][2 * (i & 1) + 1]);
      SB0();
#pragma unroll
      for (int i = 0; i < 16; ++i) f[i] = unpk(a[i]);
      SB0();
#pragma unroll
      for (int i = 0; i < 16; ++i) f[i] *= ri[i >> 2];
      SB0();
#pragma unroll
      for (int i = 0; i < 4; ++i) {
        STW(((u32x4){pack2(f[4 * i][0], f[4 * i][1]), pack2(f[4 * i + 1][0], f[4 * i + 1][1]), pack2(f[4 * i + 2][0], f[4 * i + 2][1]), pack2(f[4 * i + 3][0], f[4 * i + 3][1])}),
            o_off + (i >> 1) * 65536 + (i & 1) * 1024);
        SB0();
      }
    }
    {
      EPI();
      const int rt = wave & 1, kh = wave >> 1;
      const unsigned ob = WS_O + ((rp * 2 + rt) * 32 + 16 * kh) * 2048 + lane * 16;
#define LDO(t, st) __builtin_amdgcn_raw_buffer_load_b128(wsr, ob + (st) * 1024, whead(t) * 2048, 16)
      int woff[2];
#pragma unroll
      for (int i = 0; i < 2; ++i) woff[i] = kh * 8192 + (g4 >> 1) * 2048 + (g4 & 1) * 512 + trl + ((i ^ (g4 & 1)) * 32);
      struct Frag2 { u32x2 lo[8], hi[8]; };
      Frag2 G[2];
      auto ld2 = [&](Frag2& N, int sn, int nt, int hi) {
        const char* p = smem + sn + woff[nt & 1] + (nt >> 2) * 1024 + ((nt >> 1) & 1) * 256;
        if (hi) N.hi[nt] = trread(p + 4096); else N.lo[nt] = trread(p);
      };
      WAIT_V(0);
      STW(((u32x4){1u, 1u, 1u, 1u}), fb_off);
      RAW_BAR();
#pragma unroll
      for (int i = 0; i < 16; ++i) ld2(G[0], wo_slot(0), i >> 1, i & 1);
      f32x4 a2[2][8];
#pragma unroll
      for (int i = 0; i < 16; ++i) a2[i & 1][i >> 1] = (f32x4){0.f, 0.f, 0.f, 0.f};
      SLEEP(P_FB);
      SPIN { if (wave_ok(ld32(ws, WS_FB + (rp * 32 + 16 * kh + (lane & 15)) * 128))) break; }
      asm volatile("" ::: "memory");
      char* pr[2];
#pragma unroll
      for (int i = 0; i < 2; ++i) pr[i] = smem + L_P + kh * L_PK + (32 * rt + c16) * 512 + (((4 * i + g4) ^ (c16 & 7)) * 16);
      u32x4 of[16][2];
#pragma unroll
      for (int t = 0; t < OW; ++t) { of[t][0] = LDO(t, 0); of[t][1] = LDO(t, 1); }
      SB0();
      sfor<0, 16>([&](auto T) {
        constexpr int t = T.v;
        Frag2& C = G[t & 1]; Frag2& N = G[(t + 1) & 1];
#pragma unroll
        for (int i = 0; i < 16; ++i) {
          const int nt = i >> 1, st = i & 1;
          a2[st][nt] = mfma16((u32x4){C.lo[nt][0], C.lo[nt][1], C.hi[nt][0], C.hi[nt][1]}, of[t][st], a2[st][nt]);
          SB0();
          if ((t & 1) && t <= 11 && i == 8) {
            asm volatile("s_waitcnt lgkmcnt(8)" ::: "memory"); __builtin_amdgcn_s_barrier(); asm volatile("" ::: "memory");
          }
          if (t < 15) ld2(N, wo_slot(t + 1), nt, st);
          if (t > 0 && t - 1 + OW < 16 && (i == 3 || i == 11)) of[(t - 1 + OW) & 15][i >> 3] = LDO(t - 1 + OW, i >> 3);
          if (t - 1 + OW == 16 && (i == 3 || i == 11)) { xv[i >> 3] = *(const u32x4*)(x + ego + (i >> 3) * 64); wv[i >> 3] = *(const u32x4*)(w_r + cb * 128 + eseg * 8 + (i >> 3) * 64); }
          if (t == 15 && i >= 4) *(f32x4*)(pr[(nt - 2) & 1] + ((nt - 2) >> 1) * 128 + st * 8192) = a2[st][(nt - 2) & 7];
          SB0();
        }
      });
#pragma unroll
      for (int i = 12; i < 16; ++i) *(f32x4*)(pr[(i >> 1) & 1] + (i >> 2) * 128 + (i & 1) * 8192) = a2[i & 1][i >> 1];
    }
    unpack_xw();
    __builtin_amdgcn_s_setprio(0);
  } else {
    LANES();
    const int sw = wave - 4, t2 = tid - 256;
    __builtin_amdgcn_s_setprio(3);
    const bool real = hs_ != 7;
    unsigned hs_g0 = 0, hs_cur = 0;
    u32x4 ms_exp = {0, 0, 0, 0};
    bool hs_act = true, ms_act = !real && hp < 8 && rb == 0 && sw == 1;
    const unsigned hs_off = (lane == 1 ? SY_GO : SY_CH) + bid * 4;
    const unsigned re_off = t2 == 0 ? SY_RE + bid * 4 : OOB;
    const unsigned ms_off = lane * 16;
    const char* wsu = uni(ws);
#define HS_PROBE() asm volatile("s_nop 4\n\tglobal_load_dword v156, %0, %1 sc1" :: "v"(hs_off), "s"(wsu) : "memory", RSV)
#define MS_PROBE() asm volatile("s_nop 4\n\tglobal_load_dwordx4 v[152:155], %0, %1 offset:2048 sc1" :: "v"(ms_off), "s"(wsu) : "memory", RSV)
    auto hs_step = [&](bool first) {
      if (hs_act) {
        const unsigned hv_ = rget<156>();
        hs_act = __builtin_amdgcn_readlane(hv_, 1) == hs_g0;
        const unsigned c = __builtin_amdgcn_readlane(hv_, 0);
        if (sw == 0 && (first || c != hs_cur)) STW1(hv_, re_off);
        hs_cur = c;
        HS_PROBE();
      }
      if (ms_act) {
        const unsigned d = ((rget<152>() ^ ms_exp[0]) | (rget<153>() ^ ms_exp[1])) | ((rget<154>() ^ ms_exp[2]) | (rget<155>() ^ ms_exp[3]));
        ms_act = __any(d != 0u);
        STW(ms_exp, ms_act ? OOB : SY_GO + ms_off);
        MS_PROBE();
      }
    };
    HS_PROBE();
    if (ms_act) {
      MS_PROBE();
      asm volatile("s_nop 4\n\tglobal_load_dwordx4 v[148:151], %0, %1 offset:1024 sc1" :: "v"(ms_off), "s"(wsu) : "memory", RSV);
    }
    const int gr = (g8 >> 3) * 7 + min(hs_, 6);
    const unsigned voG = (unsigned)((lane >> 3) * 8 * F_ + (lane & 7) * 8) * 2u, voD = (unsigned)((lane >> 3) * 8 * D_ + (lane & 7) * 8) * 2u;
    const size_t gofs = ((size_t)(kq * 256 + sw * 64) * F_ + gr * 64) * 2;
    const char* pG = (const char*)wg + gofs;
    const char* pU = (const char*)wu + gofs;
    const char* pD = (const char*)(wd + (size_t)(gr * 64) * D_ + kq * 256 + sw * 64);
    auto wstep = [&](auto I) {
      constexpr int i = I.v;
      if (i < 8) wload<VW + 4 * i>(voG, pG + (i & 7) * (F_ * 2));
      else if (i < 16) wload<VW + 4 * i>(voG, pU + (i & 7) * (F_ * 2));
      else wload2<VW + 4 * i>(voD, pD + (i & 7) * (D_ * 2));
    };
    unsigned voW;
    {
      const int bi = lane >> 4, q = (lane >> 2) & 3, ch = lane & 3;
      voW = (unsigned)((4 * (bi >> 1) + q) * D_ + (bi & 1) * 32 + (ch ^ (bi & 2)) * 8) * 2u;
    }
    const char* baseW = (const char*)(wo + (size_t)((16 * (sw >> 1)) * 32 + 16 * (sw & 1)) * D_ + cb * 128) - 1024;
    auto stageW = [&](auto J, bool rsv) {
      const char* s_ = baseW + (size_t)whead(J.v) * (32 * D_ * 2);
      if (rsv) glds4r<wo_slot(J.v)>(voW + 1024, voW + 128, voW + 8 * D_ * 2 - 1024, voW + 8 * D_ * 2 + 128 - 2048, s_, l4);
      else glds4<wo_slot(J.v)>(voW + 1024, voW + 128, voW + 8 * D_ * 2 - 1024, voW + 8 * D_ * 2 + 128 - 2048, s_, l4);
    };
    int sta[2], stb[3];
    {
#pragma unroll
      for (int c = 0; c < 2; ++c) { sta[c] = (posA & 3) * K1_STAGE + (32 * sw + (lane & 15)) * 128 + (((4 * c + (lane >> 4)) ^ ((lane & 15) >> 1)) * 16); asm volatile("" : "+v"(sta[c]) :: RSV); }
#pragma unroll
      for (int i = 0; i < 3; ++i) {
        const int G = sw * 3 + i, hd = G >= 6 ? 1 : 0, mat = (G - 6 * hd) >> 1;
        stb[i] = (posB & 3) * K1_STAGE + 16384 + (lane >> 4) * 6144 + mat * 1024 + hd * 256 + ((lane & 15) >> 2) * 64 + (G & 1) * 32 + (lane & 3) * 8;
        asm volatile("" : "+v"(stb[i]) :: RSV);
      }
    }
    unsigned rs_dst = (lane >> 4) == ((lane & 15) >> 2) ? rs_slot + (32 * sw + (lane & 15)) * 8 : OOB, cs_dst = lane < 48 ? cs_slot + (sw * 48 + lane) * 4 : OOB;
    unsigned rs_off = WS_RS + rb * 16384 + (lane >> 4) * 1024 + sw * 256 + (lane & 15) * 16;
    unsigned cs_off = WS_CS + hp * 12288 + (lane >> 4) * 768 + sw * 192 + min(lane & 15, 11) * 16;
    asm volatile("" : "+v"(rs_dst), "+v"(cs_dst), "+v"(rs_off), "+v"(cs_off) :: RSV);
    int stm_dst = ((lane >> 5) * 128 + sw * 32 + (lane & 15) * 2 + ((lane >> 4) & 1)) * 4, cs_put = (lane & 15) < 12 ? (256 + sw * 48 + (lane & 15) * 4 + (lane >> 4)) * 4 : 1792 + lane * 4;
    asm volatile("" : "+v"(stm_dst), "+v"(cs_put) :: RSV);
    auto st_load = [&]() { sfor<0, 4>([&](auto I) { sload<224 + 4 * I.v>(rs_off, wsu + I.v * 4096); sload<240 + 4 * I.v>(cs_off, wsu + I.v * 3072); }); };
    sfor<3, GS[0]>(group);
    WAIT_VR(gops(GS[0]) - 10 + 1); RAW_BARR();
    sfor<GS[0], GS[1]>(group);
    WAIT_VR(gops(GS[1]) - 20); RAW_BARR();
    hs_g0 = __builtin_amdgcn_readlane(rget<156>(), 1);
    if (ms_act) {
      const u32x4 re = {rget<152>(), rget<153>(), rget<154>(), rget<155>()}, g0 = {rget<148>(), rget<149>(), rget<150>(), rget<151>()};
#pragma unroll
      for (int j = 0; j < 4; ++j) { const unsigned c = re[j] + 1u; ms_exp[j] = (c == g0[j]) ? c + 1u : c; }
      STW(ms_exp, SY_CH + ms_off);
    }
    u32x4 ra[4] = {}; u32x2 rc[3][4] = {};
    auto chunk = [&](auto C) {
      const u32x4 ONES4 = {0x3F803F80u, 0x3F803F80u, 0x3F803F80u, 0x3F803F80u};
      const f32x4 Z4 = {0.f, 0.f, 0.f, 0.f};
      if constexpr (C.v == 0) {
        asm volatile("" : "+v"(ra[0]), "+v"(ra[1]), "+v"(ra[2]), "+v"(ra[3]) :: RSV);
        f32x4 s[2], q[2];
#pragma unroll
        for (int i = 0; i < 2; ++i) { q[i] = mfma16(ra[2 * i], ra[2 * i], Z4); s[i] = mfma16(ONES4, ra[2 * i], Z4); }
#pragma unroll
        for (int i = 0; i < 2; ++i) { q[i] = mfma16(ra[2 * i + 1], ra[2 * i + 1], q[i]); s[i] = mfma16(ONES4, ra[2 * i + 1], s[i]); }
#pragma unroll
        for (int i = 0; i < 2; ++i) {
          float a = q[i][0], c = q[i][1], d = q[i][2], e = q[i][3];
          asm volatile("" : "+v"(a), "+v"(c), "+v"(d), "+v"(e) :: RSV);
          const float lo = (lane & 1) ? c : a, hi = (lane & 1) ? e : d;
          __builtin_amdgcn_raw_buffer_store_b64((u32x2){sane1(__float_as_uint(s[i][0])), sane1(__float_as_uint((lane & 2) ? hi : lo))}, wsr, rs_dst + i * 128, 0, 16);
        }
      } else {
        f32x4 c[3];
#pragma unroll
        for (int i = 0; i < 3; ++i) { asm volatile("" : "+v"(rc[i][0]), "+v"(rc[i][1]), "+v"(rc[i][2]), "+v"(rc[i][3]) :: RSV); c[i] = mfma16(ONES4, (u32x4){rc[i][0][0], rc[i][0][1], rc[i][1][0], rc[i][1][1]}, Z4); }
#pragma unroll
        for (int i = 0; i < 3; ++i) c[i] = mfma16(ONES4, (u32x4){rc[i][2][0], rc[i][2][1], rc[i][3][0], rc[i][3][1]}, c[i]);
        float c0 = c[0][0], c1 = c[1][0], c2 = c[2][0];
        asm volatile("" : "+v"(c0), "+v"(c1), "+v"(c2) :: RSV);
        STW1(sane1(__float_as_uint(lane < 16 ? c0 : lane < 32 ? c1 : c2)), cs_dst);
      }
    };
    sfor<0, 12>([&](auto T) {
      constexpr int t = T.v;
      if (t < 4 && posA == t + 1) {
#pragma unroll
        for (int c = 0; c < 4; ++c) ra[c] = *(const u32x4*)(smem + sta[c & 1] + (c >> 1) * 2048);
      }
      if (t < 4 && posB == t + 1) {
#pragma unroll
        for (int i = 0; i < 3; ++i)
#pragma unroll
          for (int j = 0; j < 4; ++j) rc[i][j] = trread(smem + (stb[i] ^ ((j >> 1) * 32)) + (j >> 1) * 3072 + (j & 1) * 512);
      }
      if (t == 2) HS_PROBE();
      if (t >= 4) hs_step(t == 4);
      sfor<GS[t + 1], GS[t + 2]>([&](auto G) {
        group(G);
        if constexpr ((t == 4 || t == 5) && G.v == GS[t + 1]) chunk(IC<t - 4>{});
      });
      if (real) sfor<WS_[t], WS_[t + 1]>(wstep);
      WAIT_VR(t < 11 ? gops(GS[t + 2]) - 10 * (t + 3) : gops(GS[13]) - gops(GS[12]));
      RAW_BARR();
    });
    hs_step(false);
    auto st_put = [&]() -> bool {
      float a[8]; unsigned m = 0, n = 0;
      rsum4<224>(a, m, n); rsum4<240>(a + 4, m, n);
      const float s1 = sw16(a[0], a[2]), q1 = sw16(a[1], a[3]);
      const float cs = sw32(sw16(a[4], a[5]), sw16(a[6], a[7]));
      const float mean = sw32(s1, s1) * (1.0f / 1024.0f);
      const float var = fmaxf(__builtin_fmaf(sw32(q1, q1), 1.0f / 1024.0f, -(mean * mean)), 0.f);
      const float rinv = __builtin_amdgcn_rcpf(rbf(__builtin_amdgcn_sqrtf(rbf(rbf(var) + 1e-5f))));
      *(float*)(smem + L_STM + stm_dst) = (lane & 32) ? rinv : rbf(mean);
      *(float*)(smem + L_STM + cs_put) = cs;
      return wave_ok(max(m, n));
    };
    st_load();
    if (real) sfor<WS_[12], W1A>(wstep);
    if (real) WAIT_VR(8 + W1A - WS_[11]); else WAIT_VR(8);
    if (lane == 0) { misc[8 + sw] = 0; misc[12 + sw] = 0; misc[16 + sw] = 0; misc[7] = 0; }
    RAW_BARR();
    if (real) WAIT_VR(W1A - WS_[12]); else WAIT_VR(0);
    if (!st_put() || hs_act) {
      if (hs_act) hs_act = __builtin_amdgcn_readlane(rget<156>(), 1) == hs_g0;
      if (ms_act) {
        SPIN {
          bool ok = true;
#pragma unroll
          for (int j = 0; j < 4; ++j) ok &= (ld32(ws, SY_RE + lane * 16 + j * 4) == ms_exp[j]);
          if (__all(ok)) break;
        }
        STW(ms_exp, SY_GO + lane * 16);
      }
      if (hs_act) {
        SPIN {
          if (ld32(ws, SY_GO + bid * 4) != hs_g0) break;
          const unsigned c = ld32(ws, SY_CH + bid * 4);
          if (c != hs_cur) { hs_cur = c; STW1(c, re_off); }
        }
      }
      SPIN { st_load(); WAIT_VR(0); if (st_put()) break; }
    }
    asm volatile("" ::: "memory", RSV);
    if (lane == 0) misc[12 + sw] = 1;
    __builtin_amdgcn_s_setprio(0);
    if (real) sfor<W1A, 16>(wstep);
    auto mwait = [&](int v) { SPIN { const int a = misc[8 + (lane & 3)]; if (__all(a >= v)) break; __builtin_amdgcn_s_sleep(1); } asm volatile("" ::: "memory"); };
    sfor<0, 9>([&](auto J) {
      if (J.v == 4) mwait(3);
      stageW(J, J.v < 2);
      if (J.v >= 1) WAIT_V(4 * PACE);
      if (J.v == 1) { WAIT_VR(8); sfor<0, 16>(wtr); }
    });
    EPI();
#pragma unroll
    for (int i = 0; i < 2; ++i) { xv[i] = *(const u32x4*)(x + ego + i * 64); wv[i] = *(const u32x4*)(w_r + cb * 128 + eseg * 8 + i * 64); }
    asm volatile("s_waitcnt vmcnt(0)" : "+v"(xv[0]), "+v"(xv[1]), "+v"(wv[0]), "+v"(wv[1]) :: "memory");
    unpack_xw();
    RAW_BAR();
    RAW_BAR(); stageW(IC<9>{}, false); stageW(IC<10>{}, false);
    RAW_BAR(); stageW(IC<11>{}, false); stageW(IC<12>{}, false);
    RAW_BAR(); stageW(IC<13>{}, false); stageW(IC<14>{}, false);
    WAIT_V(16); RAW_BAR(); stageW(IC<15>{}, false);
    WAIT_V(12); RAW_BAR();
    WAIT_V(0); RAW_BAR();
    if (real) sfor<16, 24>(wstep);
  }
  LANES(); EPI();
  const int grow = rp * 64 + erow;
  int pa0 = L_P + erow * 512 + ((2 * eseg) ^ (erow & 7)) * 16, pa1 = pa0 ^ 16;
  unsigned st_dst = eseg == 0 ? WS_ST2 + ((rp * 8 + cb) * 64 + erow) * 16 : OOB, st_src = WS_ST2 + rp * 8192 + lane * 16;
  unsigned fd_dst = lane < 8 ? WS_FD + (cb * 32 + rp) * 128 + lane * 16 : OOB, kidx = 0xFFFFu - grow;
  asm volatile("" : "+v"(pa0), "+v"(pa1), "+v"(st_dst), "+v"(st_src), "+v"(fd_dst), "+v"(kidx));
  RAW_BAR2();
  unsigned hb[8];
  f32x2 hv[8];
  {
    f32x4 pp[2][4];
#pragma unroll
    for (int k = 0; k < 2; ++k)
#pragma unroll
      for (int i = 0; i < 4; ++i) pp[k][i] = *(const f32x4*)(smem + ((i & 1) ? pa1 : pa0) + (i >> 1) * 256 + k * L_PK);
    SB0();
    f32x2 s = {0.f, 0.f}, q = {0.f, 0.f}, p2 = {0.f, 0.f};
#pragma unroll
    for (int i = 0; i < 8; ++i) {
      const f32x4 a = pp[0][i >> 1] + pp[1][i >> 1];
      const f32x2 u = xf[i] + unpk(pack2o(a[2 * (i & 1)], a[2 * (i & 1) + 1]));
      hb[i] = pack2o(u[0], u[1]);
      hv[i] = unpk(hb[i]);
      s += hv[i]; q += hv[i] * hv[i]; p2 += hv[i] * wf2[i];
    }
    STW(sane((u32x4){__float_as_uint(oct_sum(s[0] + s[1])), __float_as_uint(oct_sum(q[0] + q[1])), __float_as_uint(oct_sum(p2[0] + p2[1])), __float_as_uint(sw_)}), st_dst);
  }
  KEEP2();
  const int sw = wave - 4;
  const unsigned fd_src = WS_FD + xcd * 4096 + r * 128;
  u32x2 kpk = {SENT, SENT};
  if (wave == 0) {
    u32x4 pa[8];
    SLEEP(P_C);
    SPIN {
      unsigned m = 0;
#pragma unroll
      for (int j = 0; j < 8; ++j) { pa[j] = LDW(st_src + j * 1024); m = chk(m, pa[j]); }
      if (wave_ok(m)) break;
      KEEP2();
    }
#define F4(v) __builtin_bit_cast(f32x4, v)
    const f32x4 t = ((F4(pa[0]) + F4(pa[1])) + (F4(pa[2]) + F4(pa[3]))) + ((F4(pa[4]) + F4(pa[5])) + (F4(pa[6]) + F4(pa[7])));
    const float mean = t[0] * (1.0f / 1024.0f);
    const float var = fmaxf(__builtin_fmaf(t[1], 1.0f / 1024.0f, -(mean * mean)), 0.f);
    const float mb = rbf(mean), rs = __builtin_amdgcn_rcpf(rbf(__builtin_amdgcn_sqrtf(rbf(rbf(var) + 1e-5f))));
    *(f32x2*)(smem + L_KX + lane * 8) = (f32x2){mb, rs};
    const float sc = fmaxf((t[2] - mb * t[3]) * rs, -INFINITY);
    const int u = (int)(pack2(0.f, sc) & 0xffff0000u);
    unsigned b0 = ((unsigned)(u ^ ((u >> 31) | (int)0x80000000)) & 0xffff0000u) | (0xFFFFu - (rp * 64 + lane)), b1 = 0;
    top2_lo(b0, b1); top2_hi(b0, b1);
    STW(((u32x4){b0, b1, b0, b1}), fd_dst);
    if (lane == 0) *(u32x2*)(smem + L_MISC + 112) = (u32x2){b0, b1};
  } else if (wave >= 4) {
    asm volatile("s_waitcnt vmcnt(0)" ::: "memory", RSV2);
    sfor<16, 24>(wtr);
  }
  RAW_BAR();
  bool cand;
  u32x4 ob[2];
  {
    const f32x2 mr = *(const f32x2*)(smem + L_KX + erow * 8);
    const u32x2 kk = *(const u32x2*)(smem + L_MISC + 112);
    const float mb = mr[0], rs = mr[1];
    const unsigned b0 = kk[0], b1 = kk[1];
    u32x4 nq[2];
#pragma unroll
    for (int i = 0; i < 8; ++i) {
      const f32x2 d = hv[i] - mb;
      const f32x2 n = unpk(pack2o(d[0], d[1])) * rs;
      nq[i >> 2][i & 3] = pack2o(n[0], n[1]);
    }
    const bool c2 = (b1 & 0xffffu) == kidx;
    cand = c2 || (b0 & 0xffffu) == kidx;
    if (cand) {
      const unsigned xo = WS_XS + (rp * 2 + (c2 ? 1 : 0)) * 2048 + cb * 256 + eseg * 16;
      STW(sane(nq[0]), xo); STW(sane(nq[1]), xo + 128);
      char* cp = smem + L_CAND + (c2 ? 512 : 0) + eseg * 16;
      *(u32x4*)cp = (u32x4){hb[0], hb[1], hb[2], hb[3]}; *(u32x4*)(cp + 128) = (u32x4){hb[4], hb[5], hb[6], hb[7]};
      *(u32x4*)(cp + 256) = nq[0]; *(u32x4*)(cp + 384) = nq[1];
    }
    asm volatile("" ::: "memory");
    if (wave >= 4) { SLEEP(P_D); kpk = __builtin_amdgcn_raw_buffer_load_b64(wsr, fd_src, 0, 16); }
    asm volatile("" ::: "memory");
#pragma unroll
    for (int i = 0; i < 8; ++i) { const f32x2 o = hv[i] + unpk(nq[i >> 2][i & 3]); ob[i >> 2][i & 3] = pack2(o[0], o[1]); }
  }
#define OUT_ST() do { *(u32x4*)(out + ego) = ob[0]; *(u32x4*)(out + ego + 64) = ob[1]; } while (0)
  if (wave < 4) {
    if (!cand) OUT_ST();
    RAW_BAR();
    const unsigned w0 = misc[24], w1 = misc[25];
    if (cand && grow != (int)key_idx(w0) && grow != (int)key_idx(w1)) OUT_ST();
#pragma unroll 1
    for (int tk = 0; tk < 2; ++tk) {
      const unsigned wk = tk ? w1 : w0, slot = misc[26 + tk];
      if ((int)(slot >> 1) != rp) continue;
      u32x4 pv[7];
      const unsigned yo = WS_YP + ((tk * 8 + cb) * 64 + (lane >> 3) * 8) * 512 + wave * 128 + (lane & 7) * 16;
      const int col = (wave >> 1) * 64 + (lane & 7) * 8 + (wave & 1) * 4 + (lane >> 4);
      float sg = rbf(__builtin_amdgcn_rcpf(1.0f + __expf(-key_val(wk))));
      float hh = bflo(*(const u16*)(smem + L_CAND + (slot & 1) * 512 + col * 2)), nn = bflo(*(const u16*)(smem + L_CAND + (slot & 1) * 512 + 256 + col * 2));
      u16* op = out + (size_t)key_idx(wk) * D_ + cb * 128 + col;
      asm volatile("" : "+v"(sg), "+v"(hh), "+v"(nn), "+v"(op));
      if (tk == 0 || (int)(misc[26] >> 1) != rp) {
        SPIN { if (misc[30] == (int)w0) break; __builtin_amdgcn_s_sleep(1); }
        SLEEP(P_E);
      }
      SPIN {
        unsigned m = 0;
#pragma unroll
        for (int p = 0; p < 7; ++p) { pv[p] = LDW(yo + p * 512); m = chk(m, pv[p]); }
        if (wave_ok(m)) break;
        asm volatile("" ::: "memory");
      }
      f32x4 s = __builtin_bit_cast(f32x4, pv[0]);
#pragma unroll
      for (int p = 1; p < 7; ++p) s += __builtin_bit_cast(f32x4, pv[p]);
      float y = sw32(sw16(s[0], s[1]), sw16(s[2], s[3]));
      y += dppf<DPP_ROR8>(y);
      if (!(lane & 8)) *(GAS u16*)op = (u16)pack2(hh + rbf(nn + rbf(rbf(y) * sg)), 0.f);
    }
  } else {
    unsigned xl = WS_XS + (kq * 256 + sw * 64 + (lane >> 4) * 16 + ((lane >> 2) & 1) * 8) * 2;
    int red_dst = (lane < 8 || (lane >= 24 && lane < 32)) ? L_RED + sw * 2048 + ((lane >> 3) & 1) * 1024 + (lane & 7) * 16 : L_RED + 8192 + lane * 16;
    unsigned gu_src = WS_GU + g8 * 4096 + lane * 16;
    int act_dst = L_ACT + sw * 256 + ((lane & 7) * 8 + (lane >> 3)) * 2, act_src = L_ACT + sw * 256 + (lane & 1) * 128 + (lane >> 4) * 32 + ((lane >> 1) & 1) * 16;
    unsigned yp_dst = lane < 16 ? WS_YP + (((lane >> 3) * 8 + 2 * kq + (sw >> 1)) * 64 + g8) * 512 + (sw & 1) * 256 + (lane & 7) * 16 : OOB;
    asm volatile("" : "+v"(xl), "+v"(red_dst), "+v"(gu_src), "+v"(act_dst), "+v"(act_src), "+v"(yp_dst));
    unsigned w0, w1; int s0 = -1, s1 = -1;
    u32x4 xa = {SENT, SENT, SENT, SENT};
    SPIN {
      const bool all = wave_ok(kpk[1]);
      const unsigned k0 = kpk[1] == SENT ? 0u : kpk[0], k1 = kpk[1] == SENT ? 0u : kpk[1];
      w0 = k0; w1 = k1;
      top2_lo(w0, w1); top2_mid(w0, w1);
      const int n0 = 2 * __builtin_ctzll(__ballot(k0 == w0));
      const u64 m1 = __ballot(k0 == w1);
      const int n1 = m1 ? 2 * __builtin_ctzll(m1) : n0 + 1;
      const bool same = n0 == s0 && n1 == s1;
      s0 = n0; s1 = n1;
      asm volatile("" ::: "memory");
      if (all && same) break;
      if (!all) { kpk = __builtin_amdgcn_raw_buffer_load_b64(wsr, fd_src, 0, 16); }
      xa = LDW(xl + ((lane & 1) ? s1 : s0) * 2048);
      if (all) break;
    }
    SPIN {
      if (wave_ok(chk(0u, xa))) break;
      asm volatile("" ::: "memory");
      xa = LDW(xl + ((lane & 1) ? s1 : s0) * 2048);
    }
    {
      const u32x4 z4 = {0u, 0u, 0u, 0u};
      const u32x4 xg = (lane & 2) ? z4 : xa, xu = (lane & 2) ? xa : z4;
      f32x4 d[8];
#pragma unroll
      for (int c = 0; c < 8; ++c) d[c] = mfma16(xg, wf[c], (f32x4){0.f, 0.f, 0.f, 0.f});
      if (tid == 256) { *(u32x4*)(smem + L_MISC + 96) = (u32x4){w0, w1, (unsigned)s0, (unsigned)s1}; misc[30] = 0; }
      SB0();
#pragma unroll
      for (int c = 0; c < 10; ++c) {
        if (c < 8) d[c & 7] = mfma16(xu, wf[8 + (c & 7)], d[c & 7]);
        if (c >= 2) *(f32x4*)(smem + red_dst + (c - 2) * 128) = d[(c - 2) & 7];
        SB0();
      }
    }
    RAW_BAR();
    if (sw == 0) {
      f32x4 t[8];
#pragma unroll
      for (int p = 0; p < 8; ++p) t[p] = *(const f32x4*)(smem + L_RED + p * 1024 + lane * 16);
      SB0();
      STW(sane(__builtin_bit_cast(u32x4, ((t[0] + t[1]) + (t[2] + t[3])) + ((t[4] + t[5]) + (t[6] + t[7])))), gu_src + kq * 1024);
    } else __builtin_amdgcn_s_sleep(2);
    {
      u32x4 q[4];
      if (grow != (int)key_idx(w0) && grow != (int)key_idx(w1)) OUT_ST();
      asm volatile("" ::: "memory");
      SLEEP(P_GU);
      SPIN {
        unsigned m = 0;
#pragma unroll
        for (int j = 0; j < 4; ++j) { q[j] = LDW(gu_src + j * 1024); m = chk(m, q[j]); }
        if (wave_ok(m)) break;
        asm volatile("" ::: "memory");
      }
      const f32x4 tot = (__builtin_bit_cast(f32x4, q[0]) + __builtin_bit_cast(f32x4, q[1])) + (__builtin_bit_cast(f32x4, q[2]) + __builtin_bit_cast(f32x4, q[3]));
      const f32x2 gs = unpk(pack2o(tot[0], tot[1])), us = unpk(pack2o(tot[2], tot[3]));
      const f32x2 sg = unpk(pack2o(gs[0] * __builtin_amdgcn_rcpf(1.0f + __expf(-gs[0])), gs[1] * __builtin_amdgcn_rcpf(1.0f + __expf(-gs[1]))));
      const unsigned a2 = pack2o(sg[0] * us[0], sg[1] * us[1]);
      *(u16*)(smem + act_dst) = (u16)a2; *(u16*)(smem + act_dst + 128) = (u16)(a2 >> 16);
      __builtin_amdgcn_fence(__ATOMIC_RELEASE, "wavefront"); __builtin_amdgcn_wave_barrier(); __builtin_amdgcn_fence(__ATOMIC_ACQUIRE, "wavefront");
      const u32x4 aa = *(const u32x4*)(smem + act_src);
      f32x4 d[8];
#pragma unroll
      for (int c = 0; c < 8; ++c) d[c] = mfma16(aa, wf[16 + c], (f32x4){0.f, 0.f, 0.f, 0.f});
      SB0();
      u32x4 y[2];
#pragma unroll
      for (int c = 0; c < 8; ++c) {
        const float t0 = d[c][0] + dppf<DPP_ROR8>(d[c][2]), t1 = d[c][3] + dppf<DPP_ROR8>(d[c][1]);
        y[c >> 2][c & 3] = __float_as_uint((lane & 8) ? t1 : t0);
      }
      STW(sane(y[0]), yp_dst); STW(sane(y[1]), yp_dst + 128);
      if (tid == 256) misc[30] = w0;
    }
  }
}

extern "C" void kernel_launch(void* const* d_in, const int* in_sizes, int n_in, void* d_out, int out_size,
                              void* d_ws, size_t ws_size, hipStream_t stream) {
  (void)hipFuncSetAttribute((const void*)fused, hipFuncAttributeMaxDynamicSharedMemorySize, LDS_TOTAL);
  fused<<<256, 512, LDS_TOTAL, stream>>>((const u16*)d_in[0], (const u16*)d_in[1], (const u16*)d_in[2], (const u16*)d_in[4],
                                         (const u16*)d_in[5], (const u16*)d_in[6], (const u16*)d_in[7], (u16*)d_out, (char*)d_ws);
}
```

```cpp
#include <hip/hip_runtime.h>
#include <stdint.h>

#define LAS __attribute__((address_space(3)))
#define GAS __attribute__((address_space(1)))
#define DI __device__ __forceinline__

typedef unsigned short u16;
typedef unsigned long long u64;
typedef __attribute__((ext_vector_type(2))) __bf16 bf16x2;
typedef __attribute__((ext_vector_type(8))) __bf16 bf16x8;
typedef __attribute__((ext_vector_type(4))) short s16x4;
typedef __attribute__((ext_vector_type(2))) unsigned short u16x2;
typedef __attribute__((ext_vector_type(4))) float f32x4;
typedef __attribute__((ext_vector_type(2))) float f32x2;
typedef __attribute__((ext_vector_type(4))) unsigned u32x4;
typedef __attribute__((ext_vector_type(2))) unsigned u32x2;
typedef GAS unsigned gu32;
typedef GAS unsigned long long gu64;

constexpr int S_ = 2048, D_ = 1024, F_ = 3584;

constexpr unsigned SY_CH = 0, SY_GO = 1024, SY_RE = 2048;
constexpr unsigned WS_FD  = 98304;
constexpr unsigned WS_ZF  = 8192;
constexpr unsigned WS_KVF = 16384;
constexpr unsigned WS_RS  = 131072;
constexpr unsigned WS_CS  = WS_RS + 16 * 16 * 1024;
constexpr unsigned WS_ZP  = WS_CS + 16 * 16 * 768;
constexpr unsigned WS_FB  = WS_ZP + 32 * 16 * 128;
constexpr unsigned WS_ST2 = WS_FB + 32 * 32 * 128;
constexpr unsigned WS_XS  = WS_ST2 + 32 * 8 * 1024;
constexpr unsigned WS_GU  = WS_XS + 64 * 2048;
constexpr unsigned WS_YP  = WS_GU + 256 * 1024;
constexpr unsigned WS_KVP = WS_YP + 2 * 8 * 64 * 512;
constexpr unsigned WS_O   = WS_KVP + 32 * 16 * 4096;
constexpr unsigned WS_END = WS_O + S_ * D_ * 2;
constexpr unsigned OOB = 0x80000000u;
constexpr size_t WS_DIAG  = 64u << 20;
#define CNT(i) do {} while (0)
#define STAMPT(kid, i, T) do {} while (0)

#define TSL(idx) do {} while (0)
#define TSDUMP(k) do {} while (0)
template <int V> struct IC { static constexpr int v = V; };
template <int B, int E, class F> DI void sfor(F&& f) { if constexpr (B < E) { f(IC<B>{}); sfor<B + 1, E>(f); } }

DI float bflo(unsigned u) { return __uint_as_float(u << 16); }
DI float bfhi(unsigned u) { return __uint_as_float(u & 0xffff0000u); }
DI float rbf(float x) { return (float)(__bf16)x; }
DI unsigned pack2(float a, float b) { bf16x2 c; c[0] = (__bf16)a; c[1] = (__bf16)b; return __builtin_bit_cast(unsigned, c); }
DI unsigned pack2o(float a, float b) { unsigned p = pack2(a, b); asm("" : "+v"(p)); return p; }
DI f32x4 mfma16(u32x4 a, u32x4 b, f32x4 c) {
  return __builtin_amdgcn_mfma_f32_16x16x32_bf16(__builtin_bit_cast(bf16x8, a), __builtin_bit_cast(bf16x8, b), c, 0, 0, 0);
}
DI void mfma16i(u32x4 a, u32x4 b, f32x4& c) { asm volatile("v_mfma_f32_16x16x32_bf16 %0, %1, %2, %0" : "+v"(c) : "v"(a), "v"(b)); }
DI f32x2 unpk(unsigned u) { return (f32x2){bflo(u), bfhi(u)}; }
DI u32x2 trread(const char* p) {
  s16x4 v = __builtin_amdgcn_ds_read_tr16_b64_v4i16((LAS s16x4*)p);
  return __builtin_bit_cast(u32x2, v);
}
DI const char* uni(const void* p) {
  const u64 v = (u64)p;
  return (const char*)(((u64)(unsigned)__builtin_amdgcn_readfirstlane((int)(v >> 32)) << 32) | (unsigned)__builtin_amdgcn_readfirstlane((int)v));
}
#define RV4(a, b, c, d) "v" #a, "v" #b, "v" #c, "v" #d
#define RV10(p) "v" #p "0", "v" #p "1", "v" #p "2", "v" #p "3", "v" #p "4", "v" #p "5", "v" #p "6", "v" #p "7", "v" #p "8", "v" #p "9"
#define RSV2 RV4(224, 225, 226, 227), "v228", "v229", RV10(23), RV10(24), RV4(250, 251, 252, 253), "v254", "v255"
#define RSV1 RV4(192, 193, 194, 195), RV4(196, 197, 198, 199), RV10(20), RV10(21), RV4(220, 221, 222, 223), RSV2
#define RSV "v148", "v149", RV10(15), RV10(16), RV10(17), RV10(18), "v190", "v191", RSV1
#define NTW " nt"
#define GL_(v, s, o) "global_load_lds_dwordx4 " v ", " s " offset:" #o
#define GLDS4(SC, CL...) asm volatile("s_add_u32 m0, %5, %c6\n\ts_nop 3\n\t" GL_("%0", "%4", 0) SC "\n\t" GL_("%1", "%4", 1024) SC "\n\t" \
  GL_("%2", "%4", 2048) SC "\n\t" GL_("%3", "%4", 3072) SC :: "v"(v0), "v"(v1), "v"(v2), "v"(v3), "s"(uni(sbase)), "s"(lds), "i"(IMM) : "memory", "scc", ##CL)
#define GLDS3(SC, CL...) asm volatile("s_add_u32 m0, %4, %c5\n\ts_nop 3\n\t" GL_("%0", "%3", 0) SC "\n\t" GL_("%1", "%3", 1024) SC "\n\t" \
  GL_("%2", "%3", 2048) SC :: "v"(v0), "v"(v1), "v"(v2), "s"(uni(sbase)), "s"(lds), "i"(IMM) : "memory", "scc", ##CL)
template <int IMM> DI void glds4(unsigned v0, unsigned v1, unsigned v2, unsigned v3, const void* sbase, unsigned lds) { GLDS4(""); }
template <int IMM> DI void glds4r(unsigned v0, unsigned v1, unsigned v2, unsigned v3, const void* sbase, unsigned lds) { GLDS4("", RSV); }
template <int IMM> DI void glds3r(unsigned v0, unsigned v1, unsigned v2, const void* sbase, unsigned lds) { GLDS3("", RSV); }
template <int R> DI void sload(unsigned voff, const void* sbase) {
  asm volatile("s_nop 4\n\tglobal_load_dwordx4 v[%c0:%c1], %2, %3 sc1" ::"i"(R), "i"(R + 3), "v"(voff), "s"(uni(sbase)) : "memory", RSV);
}
DI float fadd(float a, float b) { float o; asm("v_add_f32 %0, %1, %2" : "=v"(o) : "v"(a), "v"(b)); return o; }
DI float fsq(float a, float c) { asm("v_fmac_f32 %0, %1, %1" : "+v"(c) : "v"(a)); return c; }
#define RA_(o, a, b) "v_add_f32 %" #o ", v[%c6+" #a "], v[%c6+" #b "]\n\t"
#define RB2_(o, a) "v_add_f32 %" #o ", v[%c6+" #a "], %" #o "\n\t"
#define RM_(o, a, b) "v_max3_u32 %" #o ", v[%c6+" #a "], v[%c6+" #b "], %" #o "\n\t"
template <int R> DI void rsum4(float* a, unsigned& m, unsigned& n) {
  asm volatile(RA_(0, 0, 4) RA_(1, 1, 5) RM_(4, 0, 1) RA_(2, 2, 6) RA_(3, 3, 7) RM_(5, 2, 3) RB2_(0, 8) RM_(4, 4, 5) RB2_(1, 9) RM_(5, 6, 7)
               RB2_(2, 10) RM_(4, 8, 9) RB2_(3, 11) RM_(5, 10, 11) RB2_(0, 12) RB2_(1, 13) RM_(4, 12, 13) RB2_(2, 14) RB2_(3, 15) RM_(5, 14, 15)
               : "=&v"(a[0]), "=&v"(a[1]), "=&v"(a[2]), "=&v"(a[3]), "+v"(m), "+v"(n) : "i"(R) : RSV);
}
template <int R> DI void wload2(unsigned voff, const void* sbase) {
  asm volatile("s_nop 4\n\tglobal_load_dwordx4 v[%c0:%c1], %2, %3" NTW ::"i"(R), "i"(R + 3), "v"(voff), "s"(uni(sbase)) : "memory", RSV2);
}
template <int R> DI void wload(unsigned voff, const void* sbase) {
  asm volatile("s_nop 4\n\tglobal_load_dwordx4 v[%c0:%c1], %2, %3" NTW ::"i"(R), "i"(R + 3), "v"(voff), "s"(uni(sbase)) : "memory", RSV);
}
template <int RA, int RB> DI unsigned wperm(unsigned sel) {
  unsigned o;
  if (RA < 192) asm volatile("v_perm_b32 %0, v%c2, v%c1, %3" : "=v"(o) : "i"(RA), "i"(RB), "s"(sel) : RSV);
  else if (RA < 224) asm volatile("v_perm_b32 %0, v%c2, v%c1, %3" : "=v"(o) : "i"(RA), "i"(RB), "s"(sel) : RSV1);
  else asm volatile("v_perm_b32 %0, v%c2, v%c1, %3" : "=v"(o) : "i"(RA), "i"(RB), "s"(sel) : RSV2);
  return o;
}
template <int R> DI unsigned rget() { unsigned o; asm volatile("v_mov_b32 %0, v%c1" : "=v"(o) : "i"(R) : RSV); return o; }
DI float xh_sum(float v) {
  auto r = __builtin_amdgcn_permlane32_swap(__float_as_uint(v), __float_as_uint(v), false, false);
  return __uint_as_float(r[0]) + __uint_as_float(r[1]);
}
template <int CTRL> DI unsigned dppu(unsigned v) { return (unsigned)__builtin_amdgcn_update_dpp(0, (int)v, CTRL, 0xf, 0xf, true); }
template <int CTRL> DI float dppf(float v) { return __uint_as_float(dppu<CTRL>(__float_as_uint(v))); }
constexpr int DPP_X1 = 0xB1, DPP_X2 = 0x4E, DPP_HMIR = 0x141, DPP_MIR = 0x140, DPP_ROR8 = 0x128;
DI float quad_sum(float v) { v += dppf<DPP_X1>(v); v += dppf<DPP_X2>(v); return v; }
DI float oct_sum(float v) { v = quad_sum(v); v += dppf<DPP_HMIR>(v); return v; }
DI float x16_sum(float v) {
  auto r = __builtin_amdgcn_permlane16_swap(__float_as_uint(v), __float_as_uint(v), false, false);
  return __uint_as_float(r[0]) + __uint_as_float(r[1]);
}
DI float sw16(float a, float b) {
  auto r = __builtin_amdgcn_permlane16_swap(__float_as_uint(a), __float_as_uint(b), false, false);
  return fadd(__uint_as_float(r[0]), __uint_as_float(r[1]));
}
DI float sw32(float a, float b) {
  auto r = __builtin_amdgcn_permlane32_swap(__float_as_uint(a), __float_as_uint(b), false, false);
  return fadd(__uint_as_float(r[0]), __uint_as_float(r[1]));
}
template <int N> struct Phi { unsigned tb[N]; f32x2 tr[N], e[N]; };
template <int N> DI void phi_a(const f32x2* t, Phi<N>& s) {
#pragma unroll
  for (int i = 0; i < N; ++i) s.tb[i] = pack2o(t[i][0], t[i][1]);
  __builtin_amdgcn_sched_barrier(0);
#pragma unroll
  for (int i = 0; i < N; ++i) s.tr[i] = unpk(s.tb[i]);
  __builtin_amdgcn_sched_barrier(0);
#pragma unroll
  for (int i = 0; i < N; ++i) s.e[i] = s.tr[i] * 1.44269504088896340736f;
  __builtin_amdgcn_sched_barrier(0);
}
template <int N> DI void phi_b(Phi<N>& s, unsigned* p) {
  u16x2 tm[2] = {{0, 0}, {0, 0}};
#pragma unroll
  for (int i = 0; i < N; ++i) {
    s.e[i][0] = __builtin_amdgcn_fmed3f(__builtin_amdgcn_exp2f(s.e[i][0]), 0.f, 1.f);
    tm[i & 1] = __builtin_elementwise_max(tm[i & 1], __builtin_bit_cast(u16x2, s.tb[i]));
    s.e[i][1] = __builtin_amdgcn_fmed3f(__builtin_amdgcn_exp2f(s.e[i][1]), 0.f, 1.f);
    __builtin_amdgcn_sched_barrier(0);
  }
#pragma unroll
  for (int i = 0; i < N; ++i) s.tr[i] += s.e[i];
  __builtin_amdgcn_sched_barrier(0);
#pragma unroll
  for (int i = 0; i < N; ++i) { s.tr[i][0] = fmaxf(s.tr[i][0], s.e[i][0]); s.tr[i][1] = fmaxf(s.tr[i][1], s.e[i][1]); }
  const u16x2 tx = __builtin_elementwise_max(tm[0], tm[1]);
  __builtin_amdgcn_sched_barrier(0);
#pragma unroll
  for (int i = 0; i < N; ++i) p[i] = pack2(s.tr[i][0], s.tr[i][1]);
  if (__any(tx[0] > 0xC0F0 || tx[1] > 0xC0F0)) {
#pragma unroll
    for (int i = 0; i < N; ++i) p[i] = pack2(bflo(p[i]) + 1e-6f, bfhi(p[i]) + 1e-6f);
  }
}
template <int N> DI void phi_n(const f32x2* t, unsigned* p) { Phi<N> s; phi_a<N>(t, s); phi_b<N>(s, p); }
#define RLX_AGENT __ATOMIC_RELAXED, __HIP_MEMORY_SCOPE_AGENT
DI void st64(char* ws, unsigned off, u64 v) { __hip_atomic_store((gu64*)(ws + off), v, RLX_AGENT); }
DI unsigned ld32(char* ws, unsigned off) { return __hip_atomic_load((gu32*)(ws + off), RLX_AGENT); }
#define WAIT_V(n) asm volatile("s_waitcnt vmcnt(%c0)" ::"i"(n) : "memory")
#define RAW_BAR() do { asm volatile("s_waitcnt lgkmcnt(0)" ::: "memory"); __builtin_amdgcn_s_barrier(); asm volatile("" ::: "memory"); } while (0)
#define SB0() __builtin_amdgcn_sched_barrier(0)
#define WAIT_VR(n) asm volatile("s_waitcnt vmcnt(%c0)" ::"i"(n) : "memory", RSV)
#define RAW_BARR() do { asm volatile("s_waitcnt lgkmcnt(0)" ::: "memory", RSV); __builtin_amdgcn_s_barrier(); asm volatile("" ::: "memory", RSV); } while (0)
#define KEEP2() asm volatile("" ::: "memory", RSV2)
#define RAW_BAR2() do { asm volatile("s_waitcnt lgkmcnt(0)" ::: "memory", RSV2); __builtin_amdgcn_s_barrier(); KEEP2(); } while (0)
constexpr unsigned SPIN_CAP = 1u << 18;
#define SPIN _Pragma("nounroll") for (unsigned s_ = 0; s_ < SPIN_CAP; ++s_)
constexpr unsigned SENT = 0xFFFFFFFFu;
DI unsigned sane1(unsigned v) { return min(v, 0xFFFFFFFEu); }
DI u32x4 sane(u32x4 v) { v[1] = sane1(v[1]); v[3] = sane1(v[3]); return v; }
DI unsigned chk(unsigned m, u32x4 v) { return max(max(m, v[1]), v[3]); }
DI bool wave_ok(unsigned m) { return !__any(m == SENT); }
DI unsigned key_idx(unsigned k) { return (0xFFFFu - (k & 0xFFFFu)) & (S_ - 1); }
DI float key_val(unsigned k) { const unsigned o = k >> 16; return __uint_as_float(((o & 0x8000u) ? (o & 0x7fffu) : (~o & 0xffffu)) << 16); }
DI void top2_mix(unsigned& b0, unsigned& b1, unsigned c0, unsigned c1) {
  const unsigned lo = min(b0, c0), mx = max(b1, c1);
  b0 = max(b0, c0); b1 = max(lo, mx);
}
DI void top2_lo(unsigned& b0, unsigned& b1) {
  top2_mix(b0, b1, dppu<DPP_X1>(b0), dppu<DPP_X1>(b1));
  top2_mix(b0, b1, dppu<DPP_X2>(b0), dppu<DPP_X2>(b1));
  top2_mix(b0, b1, dppu<DPP_HMIR>(b0), dppu<DPP_HMIR>(b1));
}
DI void top2_mid(unsigned& b0, unsigned& b1) {
  top2_mix(b0, b1, dppu<DPP_MIR>(b0), dppu<DPP_MIR>(b1));
  auto p = __builtin_amdgcn_permlane16_swap(b0, b0, false, false); auto q = __builtin_amdgcn_permlane16_swap(b1, b1, false, false);
  b0 = p[0]; b1 = q[0]; top2_mix(b0, b1, p[1], q[1]);
}
DI void top2_hi(unsigned& b0, unsigned& b1) {
  top2_mid(b0, b1);
  auto p = __builtin_amdgcn_permlane32_swap(b0, b0, false, false); auto q = __builtin_amdgcn_permlane32_swap(b1, b1, false, false);
  b0 = p[0]; b1 = q[0]; top2_mix(b0, b1, p[1], q[1]);
}

constexpr int K1_STAGE = 40960;
constexpr int LDS_TOTAL = 4 * K1_STAGE;
constexpr int L_STM = 0;
constexpr int L_MISC = 2048;
constexpr int L_XK = 5120;
constexpr int L_KX = 3328;
constexpr int L_ACT = 2304;
constexpr int L_CAND = 4096;
constexpr int L_WO = 16384;
constexpr int L_P = 16384, L_PK = 7 * 16384;
constexpr int L_RED = 65536;
constexpr int wo_slot(int j) { return L_WO + (j % 9) * 16384; }
constexpr int CODE_LINES = 12;
#define OW_ 8
constexpr int OW = OW_;
#define W1A_ 16
constexpr int W1A = W1A_;
#define GSCHED 5, 9, 13, 17, 21, 24, 27, 30, 33, 36, 39, 42, 45, 48, 48
constexpr int GS[15] = {GSCHED};
constexpr int gops(int g) { return (g / 3) * 10 + (g % 3 == 0 ? 0 : g % 3 == 1 ? 4 : 7); }
constexpr bool gs_ok() {
  bool ok = GS[0] >= 3 && GS[0] <= 9 && GS[1] >= 6 && GS[1] <= 12 && GS[0] <= GS[1] && GS[12] == 45 && GS[13] == 48 && GS[14] == 48;
  for (int t = 0; t < 12; ++t) ok = ok && GS[t + 2] <= 3 * (t + 5) && GS[t + 2] >= 3 * (t + 3) && GS[t + 2] >= GS[t + 1] + 3;
  return ok;
}
static_assert(gs_ok(), "staging schedule");
#define WSCHED 0, 0, 0, 0, 0, 0, 0, 0, 0, 2, 4, 6, 8
constexpr int WS_[13] = {WSCHED};
#define PACE 1
#define P_KVP 1
#define P_KVF 1
#define P_FB 8
#define P_C 14
#define P_D 2
#define P_GU 12
#define P_E 10
#define SLEEP(n) do { if (n) __builtin_amdgcn_s_sleep(n); } while (0)
constexpr int VW = 160;

__global__ __launch_bounds__(512, 2) void fused(const u16* __restrict__ x, const u16* __restrict__ wqkv,
                                                const u16* __restrict__ wo, const u16* __restrict__ w_r,
                                                const u16* __restrict__ wg, const u16* __restrict__ wu,
                                                const u16* __restrict__ wd, u16* __restrict__ out, char* __restrict__ ws) {
  extern __shared__ __attribute__((aligned(16))) char smem[];
  STAMPT(0, 0, 0);
  u64 pc0; asm volatile("s_getpc_b64 %0" : "=s"(pc0));
  const int wave = __builtin_amdgcn_readfirstlane(threadIdx.x >> 6);
  const unsigned lds0 = (unsigned)(size_t)smem;
  if (wave == 0) {
    const int l = threadIdx.x;
    asm volatile("s_mov_b32 m0, %1\n\ts_nop 0\n\tglobal_load_lds_dword %0, off"
                 :: "v"((const char*)(pc0 - (pc0 & 127)) + ((blockIdx.x >> 3) + (l < CODE_LINES ? l : 0) * 32) * 128), "s"(__builtin_amdgcn_readfirstlane(lds0 + 3 * K1_STAGE)) : "memory");
  }
#define RB_(b) (((b) & 1) * 8 + (((b) >> 3) & 7))
#define HP_(b) ((((b) & 7) >> 1) * 4 + ((b) >> 6))
#define KROT_(b) ((4 * ((HP_(b) >> 2) + (RB_(b) >> 3) + 2 * ((RB_(b) >> 2) & 1))) & 15)
  unsigned voA0 = 0, voA1 = 0, voA2 = 0, voA3 = 0, voB0 = 0, voB1 = 0, voB2 = 0, voB3 = 0, voB4 = 0, voB5 = 0;
  const unsigned l4 = __builtin_amdgcn_readfirstlane(lds0 + (wave - 4) * 4096), l6 = __builtin_amdgcn_readfirstlane(lds0 + (wave - 4) * 6144);
  const int krot0 = KROT_(blockIdx.x);
  auto group = [&](auto G) {
    constexpr int t = G.v / 3, p = G.v % 3, sl = (t & 3) * K1_STAGE;
    const int kt = (t + krot0) & 15;
    if (p == 0) glds4r<sl>(voA0, voA1, voA2, voA3, (const char*)x + kt * 128, l4);
    else if (p == 1) glds3r<sl + 16384>(voB0, voB1, voB2, (const char*)wqkv + kt * 64 * (3 * D_ * 2), l6);
    else glds3r<sl + 16384 + 3072>(voB3, voB4, voB5, (const char*)wqkv + kt * 64 * (3 * D_ * 2), l6);
  };
  if (wave >= 4) {
    const int lane = threadIdx.x & 63, sw = wave - 4;
    const int r0 = RB_(blockIdx.x) * 128 + 32 * sw + (lane >> 3);
    voA0 = (unsigned)(r0 * D_ + ((lane & 7) ^ (lane >> 4)) * 8) * 2u;
    voA1 = ((voA0 + 8 * D_ * 2) ^ 64) - 1024; voA2 = voA0 + 16 * D_ * 2 - 2048; voA3 = voA1 + 16 * D_ * 2 - 2048;
    voB0 = (unsigned)((16 * sw + 4 * (lane >> 5) + ((lane >> 2) & 3)) * (3 * D_) + HP_(blockIdx.x) * 64 + ((lane >> 4) & 1) * 32 + (lane & 3) * 8) * 2u;
    voB1 = voB0 + 1024; voB2 = voB0 + 2048;
    voB3 = (voB0 ^ 32) + 8 * 3 * D_ * 2; voB4 = voB3 + 1024; voB5 = voB3 + 2048;
    asm volatile("" : "+v"(voA0), "+v"(voA1), "+v"(voA2), "+v"(voA3), "+v"(voB0), "+v"(voB1), "+v"(voB2), "+v"(voB3), "+v"(voB4), "+v"(voB5));
    STAMPT(1, 7, 256);
    sfor<0, 3>(group);
    STAMPT(1, 8, 256);
  }
  int bid = blockIdx.x; asm volatile("" : "+s"(bid));
  const int xcd = bid & 7, idx = bid >> 3;
  const int rb = RB_(bid), hp = HP_(bid);
  const int rp = xcd * 4 + (idx >> 3), cb = idx & 7;
  const int hs_ = (hp + 4 + (int)(((size_t)wqkv >> 7) & 7)) & 7;
  const int g8 = ((hp >> 3) * 4 + (rb >> 2)) * 8 + hs_, kq = rb & 3;
  const int krot = KROT_(bid), posA = 1 + (hp & 3), posB = 1 + (rb & 3);
  const int wrot = 4 * rp + (xcd >> 1), wdir = (xcd & 1) ? -1 : 1;
  auto whead = [&](int t) { return (wrot + wdir * t) & 15; };
  const unsigned rs_slot = WS_RS + (rb * 16 + ((krot + posA) & 15)) * 1024, cs_slot = WS_CS + (hp * 16 + ((krot + posB) & 15)) * 768;
  const __amdgpu_buffer_rsrc_t wsr = __builtin_amdgcn_make_buffer_rsrc(ws, 0, WS_END, 0x00020000);
#define LDW(off) __builtin_amdgcn_raw_buffer_load_b128(wsr, (off), 0, 16)
#define STW(v, off) __builtin_amdgcn_raw_buffer_store_b128((v), wsr, (off), 0, 16)
#define STW1(v, off) __builtin_amdgcn_raw_buffer_store_b32((v), wsr, (off), 0, 16)
  float* stm = (float*)(smem + L_STM);
  volatile LAS int* misc = (volatile LAS int*)(smem + L_MISC);
#define LANES() int tid = threadIdx.x; asm volatile("" : "+v"(tid)); const int lane = tid & 63, r = lane & 31, h = lane >> 5; (void)r; (void)h
#define EPI() const int erow = tid >> 3, eseg = tid & 7; const size_t ego = (size_t)(rp * 64 + erow) * D_ + cb * 128 + eseg * 8
  u32x4 xv[2], wv[2];
  f32x2 xf[8], wf2[8];
  float sw_;
  auto unpack_xw = [&]() {
    f32x2 a = {0.f, 0.f};
#pragma unroll
    for (int i = 0; i < 8; ++i) { xf[i] = unpk(xv[i >> 2][i & 3]); wf2[i] = unpk(wv[i >> 2][i & 3]); a += wf2[i]; }
    sw_ = oct_sum(a[0] + a[1]);
#pragma unroll
    for (int i = 0; i < 8; ++i) asm volatile("" : "+v"(xf[i]), "+v"(wf2[i]));
    asm volatile("" : "+v"(sw_));
  };
  unsigned tsv = 0; (void)tsv;
  u32x4 wf[24];

  auto wtr = [&](auto I) {
    constexpr int m = I.v >> 3, c = I.v & 7, R = VW + 32 * m + (c >> 1);
    const unsigned sel = (c & 1) ? 0x07060302u : 0x05040100u;
    wf[I.v] = (u32x4){wperm<R, R + 4>(sel), wperm<R + 8, R + 12>(sel), wperm<R + 16, R + 20>(sel), wperm<R + 24, R + 28>(sel)};
  };

  if (wave < 4) {
    LANES();
    const int c16 = lane & 15, g4 = lane >> 4;
    const int trl = (c16 >> 2) * 64 + (c16 & 3) * 8;
    const int wr = wave >> 1, wc = wave & 1;
    const int head = 2 * hp + wc;
    const unsigned kvp_off = WS_KVP + (head * 16 + rb) * 4096 + wr * 2048 + lane * 16;
    const unsigned zp_off = WS_ZP + (head * 16 + rb) * 128;
    const unsigned o_off = WS_O + ((rb * 4 + 2 * wr) * 32 + head) * 2048 + lane * 16;
    const unsigned fb_off = lane < 8 ? WS_FB + ((rb * 2 + wr) * 32 + head) * 128 + lane * 16 : OOB;
    unsigned pfa, pfb = OOB;
    unsigned pfc = lane < 8 ? fb_off : lane < 24 ? rs_slot + wave * 256 + (lane - 8) * 16
        : lane < 40 ? (wave < 3 ? cs_slot + wave * 256 : WS_KVF + hp * 4096 + rb * 256) + (lane - 24) * 16
        : lane < 48 ? (wave < 2 ? zp_off + (lane & 7) * 16 : (wave == 2 && rb < 2) ? WS_ZF + (2 * hp + rb) * 128 + (lane & 7) * 16 : OOB) : OOB;
    if (wave == 0) pfa = WS_ST2 + (rp * 8 + cb) * 1024 + lane * 16;
    else if (wave == 1) pfa = WS_GU + (g8 * 4 + kq) * 1024 + lane * 16;
    else pfa = WS_YP + (((wave - 2) * 8 + 2 * kq + (lane >> 5)) * 64 + g8) * 512 + (lane & 31) * 16;
    if (wave == 0) pfb = lane < 32 ? WS_XS + (rp * 2 + (lane >> 4)) * 2048 + cb * 256 + (lane & 15) * 16 : lane < 40 ? WS_FD + (cb * 32 + rp) * 128 + (lane & 7) * 16 : OOB;
    u32x4 S4 = {SENT, SENT, SENT, SENT};
    asm volatile("" : "+v"(pfa), "+v"(pfb), "+v"(pfc), "+v"(S4));
    __builtin_amdgcn_s_setprio(1);
    f32x4 acc[4][6];
    int ao[2][2], bo[2][2];
    {
#pragma unroll
      for (int i = 0; i < 2; ++i) {
        ao[0][i] = (64 * wr + c16) * 128 + (((4 * i + g4) ^ (c16 >> 1)) * 16); ao[1][i] = ao[0][i] + 2 * K1_STAGE;
        bo[0][i] = 16384 + g4 * 3072 + wc * 256 + trl + ((i ^ (g4 & 1)) * 32); bo[1][i] = bo[0][i] + 2 * K1_STAGE;
        asm volatile("" : "+v"(ao[0][i]), "+v"(ao[1][i]), "+v"(bo[0][i]), "+v"(bo[1][i]));
      }
#pragma unroll
      for (int i = 0; i < 4; ++i)
#pragma unroll
        for (int j = 0; j < 6; ++j) { acc[i][j] = (f32x4){0.f, 0.f, 0.f, 0.f}; asm volatile("" : "+v"(acc[i][j])); }
      struct Frag { u32x4 a[4]; u32x2 bl[6], bh[6]; };
      Frag FA, FB;
#define K1_B(j) ((u32x4){C.bl[j][0], C.bl[j][1], C.bh[j][0], C.bh[j][1]})
#define K1_LA(i) N.a[i] = *(const u32x4*)(smem + ao[sn >> 1][hf] + (sn & 1) * K1_STAGE + (i) * 2048)
#define K1_LL(j) N.bl[j] = trread(smem + bo[sn >> 1][(j) & 1] + (sn & 1) * K1_STAGE + hf * 12288 + ((j) >> 1) * 1024)
#define K1_LH(j) N.bh[j] = trread(smem + bo[sn >> 1][(j) & 1] + (sn & 1) * K1_STAGE + hf * 12288 + ((j) >> 1) * 1024 + 512)
#define K1_M(i, j) if ((j) < 2) mfma16i(K1_B(j), C.a[i], acc[i][j]); else mfma16i(C.a[i], K1_B(j), acc[i][j]); SB0()
#define K1_Q(i, j, ld) K1_M(i, j); ld; SB0()
      auto sub = [&](Frag& C, Frag& N, int sn, int hf) {
        K1_Q(0, 0, K1_LL(0)); K1_Q(1, 0, K1_LH(0)); K1_Q(0, 1, K1_LA(0)); K1_Q(1, 1, K1_LA(1)); K1_Q(0, 2, K1_LL(1)); K1_Q(1, 2, K1_LH(1));
        K1_Q(0, 3, K1_LL(2)); K1_Q(1, 3, K1_LH(2)); K1_Q(2, 0, K1_LL(3)); K1_Q(3, 0, K1_LH(3)); K1_Q(2, 1, K1_LA(2)); K1_Q(3, 1, K1_LA(3));
        K1_Q(2, 2, K1_LL(4)); K1_Q(3, 2, K1_LH(4)); K1_Q(2, 3, K1_LL(5)); K1_Q(3, 3, K1_LH(5));
        K1_M(0, 4); K1_M(1, 4); K1_M(2, 4); K1_M(3, 4); K1_M(0, 5); K1_M(1, 5); K1_M(2, 5);
        asm volatile("s_waitcnt lgkmcnt(0)" : "+v"(N.bh[5]));
        K1_M(3, 5);
      };
      auto subkv = [&](Frag& C, Frag& N, int sn, int hf, bool ld) {
#define K1_QL(i, j, l) K1_M(i, j); if (ld) { l; } SB0()
        K1_QL(0, 2, K1_LL(2)); K1_QL(1, 2, K1_LH(2)); K1_QL(0, 3, K1_LA(0)); K1_QL(1, 3, K1_LA(1)); K1_QL(2, 2, K1_LL(3)); K1_QL(3, 2, K1_LH(3));
        K1_QL(2, 3, K1_LA(2)); K1_QL(3, 3, K1_LA(3)); K1_QL(0, 4, K1_LL(4)); K1_QL(1, 4, K1_LH(4)); K1_QL(2, 4, K1_LL(5)); K1_QL(3, 4, K1_LH(5));
        K1_M(0, 5); K1_M(1, 5); K1_M(2, 5); K1_M(3, 5);
      };
      WAIT_V(0);
      RAW_BAR();
      STAMPT(0, 1, 0);
      {
        Frag& N = FA; const int sn = 0, hf = 0;
        K1_LL(0); K1_LH(0); K1_LA(0); K1_LA(1); K1_LL(1); K1_LH(1); K1_LL(2); K1_LH(2); K1_LL(3); K1_LH(3); K1_LA(2); K1_LA(3); K1_LL(4); K1_LH(4); K1_LL(5); K1_LH(5);
      }
#pragma unroll 1
      for (int it = 0; it < 4; ++it) {
#pragma unroll
        for (int s = 0; s < 4; ++s) {
          if (s == 2 && it == 3) break;
          sub(FA, FB, s, 1);
          if (s == 0) WAIT_V(0);
          if (it == 0) { if (s == 1) STW(S4, pfc); if (s == 2) STW(S4, kvp_off); if (s == 3) STW(S4, kvp_off + 1024); }
          if (it == 1) { if (s == 0) STW(S4, pfa); if (s == 1) STW(S4, pfb); }
          TSL(it * 4 + s);
          if (s != 1 || it < 3) RAW_BAR();
          sub(FB, FA, (s + 1) & 3, 0);
        }
      }
      subkv(FA, FB, 2, 1, true);
      TSL(14);
      RAW_BAR();
      subkv(FB, FA, 3, 0, true);
      subkv(FA, FB, 3, 1, true);
      TSL(15);
      subkv(FB, FA, 0, 0, false);
    }
    asm volatile("s_nop 15" ::: "memory");
    TSL(16); if (wave == 0) TSDUMP(0);
    STAMPT(0, 2, 0);
    {
      f32x4 m4[4], i4[4]; float cs[4];
      SPIN {
        const int ok = misc[12 + (lane & 3)];
        asm volatile("" ::: "memory");
#pragma unroll
        for (int i = 0; i < 4; ++i) {
          cs[i] = stm[256 + wc * 96 + 32 + 16 * i + c16];
          m4[i] = *(const f32x4*)(stm + 64 * wr + 16 * i + 4 * g4); i4[i] = *(const f32x4*)(stm + 128 + 64 * wr + 16 * i + 4 * g4);
        }
        SB0();
        if (__all(ok != 0)) break;
      }
      const u32x4 ONES4 = {0x3F803F80u, 0x3F803F80u, 0x3F803F80u, 0x3F803F80u};
      f32x2 tk[16], tv[16];
#define PR(v, i) ((f32x2){(v)[2 * (i)], (v)[2 * (i) + 1]})
#pragma unroll
      for (int i = 0; i < 16; ++i) {
        tk[i] = PR(acc[(i >> 1) & 3][2 + (i >> 3)], i & 1) - PR(m4[(i >> 1) & 3], i & 1) * cs[i >> 3];
        tv[i] = PR(acc[(i >> 1) & 3][4 + (i >> 3)], i & 1) - PR(m4[(i >> 1) & 3], i & 1) * cs[2 + (i >> 3)];
      }
      SB0();
#pragma unroll
      for (int i = 0; i < 16; ++i) { tk[i] *= PR(i4[(i >> 1) & 3], i & 1); tv[i] *= PR(i4[(i >> 1) & 3], i & 1); }
      SB0();
      unsigned kp[16], vp[16];
#pragma unroll
      for (int i = 0; i < 16; ++i) vp[i] = pack2o(tv[i][0], tv[i][1]);
      phi_n<16>(tk, kp);
#define F4_(p, i) ((u32x4){(p)[4 * (i)], (p)[4 * (i) + 1], (p)[4 * (i) + 2], (p)[4 * (i) + 3]})
      const f32x4 Z4 = {0.f, 0.f, 0.f, 0.f};
      u32x4 vk[2], vg[2];
#pragma unroll
      for (int j = 0; j < 2; ++j) { vk[j] = wr ? F4_(vp, 2 + j) : F4_(vp, j); vg[j] = wr ? F4_(vp, j) : F4_(vp, 2 + j); }
      f32x4 keep[2], give[2], zq[2];
#pragma unroll
      for (int d = 0; d < 2; ++d) { give[d] = mfma16(F4_(kp, 2 * d), vg[0], Z4); zq[d] = mfma16(ONES4, F4_(kp, 2 * d), Z4); }
#pragma unroll
      for (int d = 0; d < 2; ++d) { give[d] = mfma16(F4_(kp, 2 * d + 1), vg[1], give[d]); zq[d] = mfma16(ONES4, F4_(kp, 2 * d + 1), zq[d]); }
#pragma unroll
      for (int d = 0; d < 2; ++d) keep[d] = mfma16(F4_(kp, 2 * d), vk[0], Z4);
#pragma unroll
      for (int d = 0; d < 2; ++d) keep[d] = mfma16(F4_(kp, 2 * d + 1), vk[1], keep[d]);
      SB0();
      const float zv = (lane & 16) ? zq[1][0] : zq[0][0];
      char* xk = smem + L_XK + wave * 2304 + lane * 16;
      const char* xp = smem + L_XK + (wave ^ 2) * 2304 + lane * 16;
      *(f32x4*)xk = give[0]; *(f32x4*)(xk + 1024) = give[1]; *(float*)(xk + 2048 - lane * 12) = zv;
      asm volatile("" ::: "memory");
      misc[16 + wave] = 1;
      f32x4 p0, p1; float pz;
      SPIN {
        const int ok = misc[16 + (wave ^ 2)];
        asm volatile("" ::: "memory");
        p0 = *(const f32x4*)xp; p1 = *(const f32x4*)(xp + 1024); pz = *(const float*)(xp + 2048 - lane * 12);
        SB0();
        if (ok == 1) break;
      }
      STW(sane(__builtin_bit_cast(u32x4, keep[0] + p0)), kvp_off);
      STW(sane(__builtin_bit_cast(u32x4, keep[1] + p1)), kvp_off + 1024);
      STW1(sane1(__float_as_uint(zv + pz)), (lane < 32 && wr == 0) ? zp_off + lane * 4 : OOB);
    }
    STAMPT(0, 3, 0);
    {
      u32x4 qa[4][4]; u32x2 ql[4][2], qh[4][2];
#pragma unroll
      for (int u = 0; u < 4; ++u) {
#pragma unroll
        for (int j = 0; j < 2; ++j) {
          ql[u][j] = trread(smem + bo[1][j] + (u >> 1) * K1_STAGE + (u & 1) * 12288);
          qh[u][j] = trread(smem + bo[1][j] + (u >> 1) * K1_STAGE + (u & 1) * 12288 + 512);
        }
#pragma unroll
        for (int i = 0; i < 4; ++i) qa[u][i] = *(const u32x4*)(smem + ao[1][u & 1] + (u >> 1) * K1_STAGE + i * 2048);
      }
      asm volatile("" ::: "memory");
      misc[8 + wave] = 3;
      SB0();
#pragma unroll
      for (int u = 0; u < 4; ++u)
#pragma unroll
        for (int j = 0; j < 2; ++j)
#pragma unroll
          for (int i = 0; i < 4; ++i) acc[i][j] = mfma16((u32x4){ql[u][j][0], ql[u][j][1], qh[u][j][0], qh[u][j][1]}, qa[u][i], acc[i][j]);
      SB0();
    }
    unsigned qp[16];
    Phi<16> qs;
    const unsigned po = WS_KVP + ((2 * hp + (rb >> 3)) * 16 + h) * 4096 + (2 * ((rb >> 2) & 1) + ((lane >> 4) & 1)) * 1024 + (16 * (rb & 3) + (lane & 15)) * 16;
    u32x4 pv[8] = {};
    {
      f32x4 c4[2]; float m_b[4], rinv[4];
#pragma unroll
      for (int i = 0; i < 2; ++i) c4[i] = *(const f32x4*)(stm + 256 + wc * 96 + 16 * i + 4 * g4);
#pragma unroll
      for (int i = 0; i < 4; ++i) { m_b[i] = stm[64 * wr + 16 * i + c16]; rinv[i] = stm[128 + 64 * wr + 16 * i + c16]; }
      SB0();
      f32x2 tq[16];
#pragma unroll
      for (int i = 0; i < 16; ++i) tq[i] = PR(acc[i >> 2][(i >> 1) & 1], i & 1) - PR(c4[(i >> 1) & 1], i & 1) * m_b[i >> 2];
      SB0();
#pragma unroll
      for (int i = 0; i < 16; ++i) tq[i] *= rinv[i >> 2];
      SB0();
      phi_a<16>(tq, qs);
#pragma unroll
      for (int i = 0; i < 16; ++i) asm volatile("" : "+v"(qs.tb[i]), "+v"(qs.tr[i]), "+v"(qs.e[i]));
    }
    if (wave == 0) {
      SLEEP(P_KVP);
#pragma unroll
      for (int p = 0; p < 8; ++p) pv[p] = LDW(po + p * 8192);
    }
    asm volatile("" ::: "memory");
    STAMPT(0, 4, 0);
    if (wave == 1 && rb < 2) {
      SLEEP(P_KVP);
      SPIN {
        unsigned m = 0;
#pragma unroll
        for (int p = 0; p < 2; ++p) { pv[p] = LDW(WS_ZP + (2 * hp + rb) * 2048 + p * 1024 + lane * 16); m = chk(max(max(m, pv[p][0]), pv[p][2]), pv[p]); }
        if (wave_ok(m)) break;
        asm volatile("" ::: "memory");
      }
      f32x4 s = __builtin_bit_cast(f32x4, pv[0]) + __builtin_bit_cast(f32x4, pv[1]);
#pragma unroll
      for (int i = 0; i < 4; ++i) { s[i] += dppf<DPP_ROR8>(s[i]); s[i] = xh_sum(x16_sum(s[i])); }
      if (lane < 16) st64(ws, WS_ZF + (2 * hp + rb) * 128 + (lane >> 3) * 64 + (lane & 3) * 16 + ((lane >> 2) & 1) * 8,
                          ((u64)sane1(pack2(s[2], s[3])) << 32) | pack2(s[0], s[1]));
    }
    if (wave == 0) {
      SPIN {
        unsigned m = 0;
#pragma unroll
        for (int p = 0; p < 8; ++p) m = chk(m, pv[p]);
        CNT(0);
        if (wave_ok(m)) break;
        asm volatile("" ::: "memory");
#pragma unroll
        for (int p = 0; p < 8; ++p) pv[p] = LDW(po + p * 8192);
      }
      f32x4 s = __builtin_bit_cast(f32x4, pv[0]);
#pragma unroll
      for (int p = 1; p < 8; ++p) s += __builtin_bit_cast(f32x4, pv[p]);
      const unsigned p0 = pack2(xh_sum(s[0]), xh_sum(s[1])), p1 = pack2(xh_sum(s[2]), xh_sum(s[3]));
      auto a = __builtin_amdgcn_permlane16_swap(p0, p0, false, false);
      auto b = __builtin_amdgcn_permlane16_swap(p1, p1, false, false);
      STW(sane((u32x4){a[0], b[0], a[1], b[1]}), lane < 16 ? WS_KVF + hp * 4096 + rb * 256 + lane * 16 : OOB);
      STAMPT(1, 2, 0);
    }
    phi_b<16>(qs, qp);
#pragma unroll
    for (int i = 0; i < 16; ++i) asm volatile("" : "+v"(qp[i]));
    if (wave == 0) misc[7] = 1;
    else SPIN { if (misc[7] == 1) break; __builtin_amdgcn_s_sleep(1); }
    SLEEP(P_KVF);
    {
      u32x4 k0, k1, z0;
      SPIN {
        k0 = LDW(WS_KVF + head * 2048 + lane * 16); k1 = LDW(WS_KVF + head * 2048 + 1024 + lane * 16);
        z0 = LDW(WS_ZF + head * 128 + g4 * 16);
        CNT(1);
        if (wave_ok(chk(chk(chk(0u, k0), k1), z0))) break;
        asm volatile("" ::: "memory");
      }
      STAMPT(0, 5, 0);
      const f32x4 Z4 = {0.f, 0.f, 0.f, 0.f};
      f32x4 X[8], Dn[4];
#pragma unroll
      for (int i = 0; i < 4; ++i) Dn[i] = mfma16(z0, F4_(qp, i), Z4);
#pragma unroll
      for (int i = 0; i < 8; ++i) X[i] = mfma16((i & 1) ? k1 : k0, F4_(qp, i >> 1), Z4);
      SB0();
      float ri[4]; unsigned a[16]; f32x2 f[16];
#pragma unroll
      for (int i = 0; i < 4; ++i) ri[i] = __builtin_amdgcn_rcpf(rbf(rbf(Dn[i][0]) + 1e-6f));
      SB0();
#pragma unroll
      for (int i = 0; i < 16; ++i) a[i] = pack2o(X[i >> 1][2 * (i & 1)], X[i >> 1][2 * (i & 1) + 1]);
      SB0();
#pragma unroll
      for (int i = 0; i < 16; ++i) f[i] = unpk(a[i]);
      SB0();
#pragma unroll
      for (int i = 0; i < 16; ++i) f[i] *= ri[i >> 2];
      SB0();
#pragma unroll
      for (int i = 0; i < 4; ++i) {
        STW(((u32x4){pack2(f[4 * i][0], f[4 * i][1]), pack2(f[4 * i + 1][0], f[4 * i + 1][1]), pack2(f[4 * i + 2][0], f[4 * i + 2][1]), pack2(f[4 * i + 3][0], f[4 * i + 3][1])}),
            o_off + (i >> 1) * 65536 + (i & 1) * 1024);
        SB0();
      }
    }
    STAMPT(0, 6, 0);
    {
      EPI();
      const int rt = wave & 1, kh = wave >> 1;
      const unsigned ob = WS_O + ((rp * 2 + rt) * 32 + 16 * kh) * 2048 + lane * 16;
#define LDO(t, st) __builtin_amdgcn_raw_buffer_load_b128(wsr, ob + (st) * 1024, whead(t) * 2048, 16)
      int woff[2];
#pragma unroll
      for (int i = 0; i < 2; ++i) woff[i] = kh * 8192 + (g4 >> 1) * 2048 + (g4 & 1) * 512 + trl + ((i ^ (g4 & 1)) * 32);
      struct Frag2 { u32x2 lo[8], hi[8]; };
      Frag2 G[2];
      auto ld2 = [&](Frag2& N, int sn, int nt, int hi) {
        const char* p = smem + sn + woff[nt & 1] + (nt >> 2) * 1024 + ((nt >> 1) & 1) * 256;
        if (hi) N.hi[nt] = trread(p + 4096); else N.lo[nt] = trread(p);
      };
      WAIT_V(0);
      STW(((u32x4){1u, 1u, 1u, 1u}), fb_off);
      RAW_BAR();
#pragma unroll
      for (int i = 0; i < 16; ++i) ld2(G[0], wo_slot(0), i >> 1, i & 1);
      f32x4 a2[2][8];
#pragma unroll
      for (int i = 0; i < 16; ++i) a2[i & 1][i >> 1] = (f32x4){0.f, 0.f, 0.f, 0.f};
      SLEEP(P_FB);
      SPIN { CNT(2); if (wave_ok(ld32(ws, WS_FB + (rp * 32 + 16 * kh + (lane & 15)) * 128))) break; }
      asm volatile("" ::: "memory");
      STAMPT(0, 7, 0);
      char* pr[2];
#pragma unroll
      for (int i = 0; i < 2; ++i) pr[i] = smem + L_P + kh * L_PK + (32 * rt + c16) * 512 + (((4 * i + g4) ^ (c16 & 7)) * 16);
      u32x4 of[16][2];
#pragma unroll
      for (int t = 0; t < OW; ++t) { of[t][0] = LDO(t, 0); of[t][1] = LDO(t, 1); }
      SB0();
      sfor<0, 16>([&](auto T) {
        constexpr int t = T.v;
        Frag2& C = G[t & 1]; Frag2& N = G[(t + 1) & 1];
#pragma unroll
        for (int i = 0; i < 16; ++i) {
          const int nt = i >> 1, st = i & 1;
          a2[st][nt] = mfma16((u32x4){C.lo[nt][0], C.lo[nt][1], C.hi[nt][0], C.hi[nt][1]}, of[t][st], a2[st][nt]);
          SB0();
          if ((t & 1) && t <= 11 && i == 8) {
            asm volatile("s_waitcnt lgkmcnt(8)" ::: "memory"); __builtin_amdgcn_s_barrier(); asm volatile("" ::: "memory");
          }
          if (t < 15) ld2(N, wo_slot(t + 1), nt, st);
          if (t > 0 && t - 1 + OW < 16 && (i == 3 || i == 11)) of[(t - 1 + OW) & 15][i >> 3] = LDO(t - 1 + OW, i >> 3);
          if (t - 1 + OW == 16 && (i == 3 || i == 11)) { xv[i >> 3] = *(const u32x4*)(x + ego + (i >> 3) * 64); wv[i >> 3] = *(const u32x4*)(w_r + cb * 128 + eseg * 8 + (i >> 3) * 64); }
          if (t == 15 && i >= 4) *(f32x4*)(pr[(nt - 2) & 1] + ((nt - 2) >> 1) * 128 + st * 8192) = a2[st][(nt - 2) & 7];
          SB0();
        }
      });
      STAMPT(0, 8, 0);
#pragma unroll
      for (int i = 12; i < 16; ++i) *(f32x4*)(pr[(i >> 1) & 1] + (i >> 2) * 128 + (i & 1) * 8192) = a2[i & 1][i >> 1];
    }
    unpack_xw();
    __builtin_amdgcn_s_setprio(0);
  } else {
    LANES();
    const int sw = wave - 4, t2 = tid - 256;
    __builtin_amdgcn_s_setprio(3);
    const bool real = hs_ != 7;
    unsigned hs_g0 = 0, hs_cur = 0;
    u32x4 ms_exp = {0, 0, 0, 0};
    bool hs_act = true, ms_act = !real && hp < 8 && rb == 0 && sw == 1;
    const unsigned hs_off = (lane == 1 ? SY_GO : SY_CH) + bid * 4;
    const unsigned re_off = t2 == 0 ? SY_RE + bid * 4 : OOB;
    const unsigned ms_off = lane * 16;
    const char* wsu = uni(ws);
#define HS_PROBE() asm volatile("s_nop 4\n\tglobal_load_dword v156, %0, %1 sc1" :: "v"(hs_off), "s"(wsu) : "memory", RSV)
#define MS_PROBE() asm volatile("s_nop 4\n\tglobal_load_dwordx4 v[152:155], %0, %1 offset:2048 sc1" :: "v"(ms_off), "s"(wsu) : "memory", RSV)
    auto hs_step = [&](bool first) {
      if (hs_act) {
        const unsigned hv_ = rget<156>();
        hs_act = __builtin_amdgcn_readlane(hv_, 1) == hs_g0;
        const unsigned c = __builtin_amdgcn_readlane(hv_, 0);
        if (sw == 0 && (first || c != hs_cur)) STW1(hv_, re_off);
        hs_cur = c;
        HS_PROBE();
      }
      if (ms_act) {
        const unsigned d = ((rget<152>() ^ ms_exp[0]) | (rget<153>() ^ ms_exp[1])) | ((rget<154>() ^ ms_exp[2]) | (rget<155>() ^ ms_exp[3]));
        ms_act = __any(d != 0u);
        STW(ms_exp, ms_act ? OOB : SY_GO + ms_off);
        MS_PROBE();
      }
    };
    HS_PROBE();
    if (ms_act) {
      MS_PROBE();
      asm volatile("s_nop 4\n\tglobal_load_dwordx4 v[148:151], %0, %1 offset:1024 sc1" :: "v"(ms_off), "s"(wsu) : "memory", RSV);
    }
    const int gr = (g8 >> 3) * 7 + min(hs_, 6);
    const unsigned voG = (unsigned)((lane >> 3) * 8 * F_ + (lane & 7) * 8) * 2u, voD = (unsigned)((lane >> 3) * 8 * D_ + (lane & 7) * 8) * 2u;
    const size_t gofs = ((size_t)(kq * 256 + sw * 64) * F_ + gr * 64) * 2;
    const char* pG = (const char*)wg + gofs;
    const char* pU = (const char*)wu + gofs;
    const char* pD = (const char*)(wd + (size_t)(gr * 64) * D_ + kq * 256 + sw * 64);
    auto wstep = [&](auto I) {
      constexpr int i = I.v;
      if (i < 8) wload<VW + 4 * i>(voG, pG + (i & 7) * (F_ * 2));
      else if (i < 16) wload<VW + 4 * i>(voG, pU + (i & 7) * (F_ * 2));
      else wload2<VW + 4 * i>(voD, pD + (i & 7) * (D_ * 2));
    };
    unsigned voW;
    {
      const int bi = lane >> 4, q = (lane >> 2) & 3, ch = lane & 3;
      voW = (unsigned)((4 * (bi >> 1) + q) * D_ + (bi & 1) * 32 + (ch ^ (bi & 2)) * 8) * 2u;
    }
    const char* baseW = (const char*)(wo + (size_t)((16 * (sw >> 1)) * 32 + 16 * (sw & 1)) * D_ + cb * 128) - 1024;
    auto stageW = [&](auto J, bool rsv) {
      const char* s_ = baseW + (size_t)whead(J.v) * (32 * D_ * 2);
      if (rsv) glds4r<wo_slot(J.v)>(voW + 1024, voW + 128, voW + 8 * D_ * 2 - 1024, voW + 8 * D_ * 2 + 128 - 2048, s_, l4);
      else glds4<wo_slot(J.v)>(voW + 1024, voW + 128, voW + 8 * D_ * 2 - 1024, voW + 8 * D_ * 2 + 128 - 2048, s_, l4);
    };
    int sta[4], stb[3];
    {
      const int row = t2 >> 1, key = (row >> 1) & 7;
#pragma unroll
      for (int c = 0; c < 4; ++c) { sta[c] = (posA & 3) * K1_STAGE + row * 128 + (((4 * (t2 & 1) + c) ^ key) * 16); asm volatile("" : "+v"(sta[c]) :: RSV); }
#pragma unroll
      for (int i = 0; i < 3; ++i) {
        const int G = sw * 3 + i, hd = G >= 6 ? 1 : 0, mat = (G - 6 * hd) >> 1;
        stb[i] = (posB & 3) * K1_STAGE + 16384 + (lane >> 4) * 6144 + mat * 1024 + hd * 256 + ((lane & 15) >> 2) * 64 + (G & 1) * 32 + (lane & 3) * 8;
        asm volatile("" : "+v"(stb[i]) :: RSV);
      }
    }
    unsigned rs_dst = rs_slot + t2 * 4, cs_dst = lane < 48 ? cs_slot + (sw * 48 + lane) * 4 : OOB;
    unsigned rs_off = WS_RS + rb * 16384 + (lane >> 4) * 1024 + sw * 256 + (lane & 15) * 16;
    unsigned cs_off = WS_CS + hp * 12288 + (lane >> 4) * 768 + sw * 192 + min(lane & 15, 11) * 16;
    asm volatile("" : "+v"(rs_dst), "+v"(cs_dst), "+v"(rs_off), "+v"(cs_off) :: RSV);
    int stm_dst = ((lane >> 5) * 128 + sw * 32 + (lane & 15) * 2 + ((lane >> 4) & 1)) * 4, cs_put = (lane & 15) < 12 ? (256 + sw * 48 + (lane & 15) * 4 + (lane >> 4)) * 4 : 1792 + lane * 4;
    asm volatile("" : "+v"(stm_dst), "+v"(cs_put) :: RSV);
    auto st_load = [&]() { sfor<0, 4>([&](auto I) { sload<224 + 4 * I.v>(rs_off, wsu + I.v * 4096); sload<240 + 4 * I.v>(cs_off, wsu + I.v * 3072); }); };
    sfor<3, GS[0]>(group);
    WAIT_VR(gops(GS[0]) - 10 + 1); RAW_BARR();
    TSL(14);
    sfor<GS[0], GS[1]>(group);
    WAIT_VR(gops(GS[1]) - 20); RAW_BARR();
    hs_g0 = __builtin_amdgcn_readlane(rget<156>(), 1);
    if (ms_act) {
      const u32x4 re = {rget<152>(), rget<153>(), rget<154>(), rget<155>()}, g0 = {rget<148>(), rget<149>(), rget<150>(), rget<151>()};
#pragma unroll
      for (int j = 0; j < 4; ++j) { const unsigned c = re[j] + 1u; ms_exp[j] = (c == g0[j]) ? c + 1u : c; }
      STW(ms_exp, SY_CH + ms_off);
    }
    u32x4 ra[4] = {}; u32x2 rc[3][4] = {};
    float sa = 0.f, sb = 0.f, qa = 0.f, qb = 0.f, ca[3] = {0.f, 0.f, 0.f}, cb_[3] = {0.f, 0.f, 0.f};
    auto chunk = [&](auto C) {
      constexpr int c = C.v;
      if constexpr (c < 4) {
        asm volatile("" : "+v"(ra[c]) :: RSV);
#pragma unroll
        for (int i = 0; i < 4; ++i) { const float lo = bflo(ra[c][i]), hi = bfhi(ra[c][i]); sa = fadd(sa, lo); sb = fadd(sb, hi); qa = fsq(lo, qa); qb = fsq(hi, qb); }
        asm volatile("" : "+v"(sa), "+v"(sb), "+v"(qa), "+v"(qb) :: RSV);
      } else if constexpr (c == 4) {
        float s = fadd(sa, sb), sq = fadd(qa, qb);
        s = fadd(s, dppf<DPP_X1>(s)); sq = fadd(sq, dppf<DPP_X1>(sq));
        STW1(sane1(__float_as_uint((t2 & 1) ? sq : s)), rs_dst);
      } else if constexpr (c < 11) {
        constexpr int i = (c - 5) >> 1, j = ((c - 5) & 1) * 2;
        asm volatile("" : "+v"(rc[i][j]), "+v"(rc[i][j + 1]) :: RSV);
#pragma unroll
        for (int e = 0; e < 4; ++e) { const unsigned u = rc[i][j + (e >> 1)][e & 1]; ca[i] = fadd(ca[i], bflo(u)); cb_[i] = fadd(cb_[i], bfhi(u)); }
        asm volatile("" : "+v"(ca[i]), "+v"(cb_[i]) :: RSV);
      } else {
        const float c2 = fadd(ca[2], cb_[2]);
        STW1(sane1(__float_as_uint(sw32(sw16(fadd(ca[0], cb_[0]), fadd(ca[1], cb_[1])), sw16(c2, c2)))), cs_dst);
      }
    };
    sfor<0, 12>([&](auto T) {
      constexpr int t = T.v;
      TSL(t);
      if (t == 4) STAMPT(1, 9, 256);
      if (t < 4 && posA == t + 1) {
#pragma unroll
        for (int c = 0; c < 4; ++c) ra[c] = *(const u32x4*)(smem + sta[c]);
      }
      if (t < 4 && posB == t + 1) {
#pragma unroll
        for (int i = 0; i < 3; ++i)
#pragma unroll
          for (int j = 0; j < 4; ++j) rc[i][j] = trread(smem + (stb[i] ^ ((j >> 1) * 32)) + (j >> 1) * 3072 + (j & 1) * 512);
      }
      if (t == 2) HS_PROBE();
      if (t >= 4) hs_step(t == 4);
      sfor<GS[t + 1], GS[t + 2]>([&](auto G) {
        group(G);
        constexpr int u = t - 4, p = G.v - GS[t + 1];
        constexpr int ord[12] = {0, 5, 6, 1, 7, 8, 2, 9, 10, 3, 11, 4};
        if constexpr (u >= 0 && u < 4 && p < 3) chunk(IC<ord[(3 * u + p) % 12]>{});
      });
      if (real) sfor<WS_[t], WS_[t + 1]>(wstep);
      TSL(16 + t);
      WAIT_VR(t < 11 ? gops(GS[t + 2]) - 10 * (t + 3) : gops(GS[13]) - gops(GS[12]));
      TSL(32 + t);
      RAW_BARR();
    });
    TSL(12);
    STAMPT(1, 10, 256);
    hs_step(false);
    auto st_put = [&]() -> bool {
      float a[8]; unsigned m = 0, n = 0;
      rsum4<224>(a, m, n); rsum4<240>(a + 4, m, n);
      const float s1 = sw16(a[0], a[2]), q1 = sw16(a[1], a[3]);
      const float cs = sw32(sw16(a[4], a[5]), sw16(a[6], a[7]));
      const float mean = sw32(s1, s1) * (1.0f / 1024.0f);
      const float var = fmaxf(__builtin_fmaf(sw32(q1, q1), 1.0f / 1024.0f, -(mean * mean)), 0.f);
      const float rinv = __builtin_amdgcn_rcpf(rbf(__builtin_amdgcn_sqrtf(rbf(rbf(var) + 1e-5f))));
      *(float*)(smem + L_STM + stm_dst) = (lane & 32) ? rinv : rbf(mean);
      *(float*)(smem + L_STM + cs_put) = cs;
      return wave_ok(max(m, n));
    };
    st_load();
    if (real) sfor<WS_[12], W1A>(wstep);
    TSL(28);
    if (real) WAIT_VR(8 + W1A - WS_[11]); else WAIT_VR(8);
    if (lane == 0) { misc[8 + sw] = 0; misc[12 + sw] = 0; misc[16 + sw] = 0; misc[7] = 0; }
    TSL(44);
    RAW_BARR();
    TSL(13); TSDUMP(1 + sw);
    STAMPT(1, 0, 256);
    if (real) WAIT_VR(W1A - WS_[12]); else WAIT_VR(0);
    if (!st_put() || hs_act) {
      if (hs_act) hs_act = __builtin_amdgcn_readlane(rget<156>(), 1) == hs_g0;
      if (ms_act) {
        SPIN {
          bool ok = true;
#pragma unroll
          for (int j = 0; j < 4; ++j) ok &= (ld32(ws, SY_RE + lane * 16 + j * 4) == ms_exp[j]);
          if (__all(ok)) break;
        }
        STW(ms_exp, SY_GO + lane * 16);
      }
      if (hs_act) {
        SPIN {
          if (ld32(ws, SY_GO + bid * 4) != hs_g0) break;
          const unsigned c = ld32(ws, SY_CH + bid * 4);
          if (c != hs_cur) { hs_cur = c; STW1(c, re_off); }
        }
      }
      SPIN { st_load(); WAIT_VR(0); if (st_put()) break; }
    }
    asm volatile("" ::: "memory", RSV);
    if (lane == 0) misc[12 + sw] = 1;
    __builtin_amdgcn_s_setprio(0);
    if (real) sfor<W1A, 16>(wstep);
    auto mwait = [&](int v) { SPIN { const int a = misc[8 + (lane & 3)]; if (__all(a >= v)) break; __builtin_amdgcn_s_sleep(1); } asm volatile("" ::: "memory"); };
    sfor<0, 9>([&](auto J) {
      if (J.v == 4) mwait(3);
      stageW(J, J.v < 2);
      if (J.v >= 1) WAIT_V(4 * PACE);
      if (J.v == 1) { WAIT_VR(8); sfor<0, 16>(wtr); }
    });
    STAMPT(1, 1, 256);
    EPI();
#pragma unroll
    for (int i = 0; i < 2; ++i) { xv[i] = *(const u32x4*)(x + ego + i * 64); wv[i] = *(const u32x4*)(w_r + cb * 128 + eseg * 8 + i * 64); }
    asm volatile("s_waitcnt vmcnt(0)" : "+v"(xv[0]), "+v"(xv[1]), "+v"(wv[0]), "+v"(wv[1]) :: "memory");
    unpack_xw();
    RAW_BAR();
    RAW_BAR(); stageW(IC<9>{}, false); stageW(IC<10>{}, false);
    RAW_BAR(); stageW(IC<11>{}, false); stageW(IC<12>{}, false);
    RAW_BAR(); stageW(IC<13>{}, false); stageW(IC<14>{}, false);
    WAIT_V(16); RAW_BAR(); stageW(IC<15>{}, false);
    WAIT_V(12); RAW_BAR();
    WAIT_V(0); RAW_BAR();
    if (real) sfor<16, 24>(wstep);
  }
  LANES(); EPI();
  const int grow = rp * 64 + erow;
  int pa0 = L_P + erow * 512 + ((2 * eseg) ^ (erow & 7)) * 16, pa1 = pa0 ^ 16;
  unsigned st_dst = eseg == 0 ? WS_ST2 + ((rp * 8 + cb) * 64 + erow) * 16 : OOB, st_src = WS_ST2 + rp * 8192 + lane * 16;
  unsigned fd_dst = lane < 8 ? WS_FD + (cb * 32 + rp) * 128 + lane * 16 : OOB, kidx = 0xFFFFu - grow;
  asm volatile("" : "+v"(pa0), "+v"(pa1), "+v"(st_dst), "+v"(st_src), "+v"(fd_dst), "+v"(kidx));
  RAW_BAR2();
  STAMPT(0, 9, 0);
  unsigned hb[8];
  f32x2 hv[8];
  {
    f32x4 pp[2][4];
#pragma unroll
    for (int k = 0; k < 2; ++k)
#pragma unroll
      for (int i = 0; i < 4; ++i) pp[k][i] = *(const f32x4*)(smem + ((i & 1) ? pa1 : pa0) + (i >> 1) * 256 + k * L_PK);
    SB0();
    f32x2 s = {0.f, 0.f}, q = {0.f, 0.f}, p2 = {0.f, 0.f};
#pragma unroll
    for (int i = 0; i < 8; ++i) {
      const f32x4 a = pp[0][i >> 1] + pp[1][i >> 1];
      const f32x2 u = xf[i] + unpk(pack2o(a[2 * (i & 1)], a[2 * (i & 1) + 1]));
      hb[i] = pack2o(u[0], u[1]);
      hv[i] = unpk(hb[i]);
      s += hv[i]; q += hv[i] * hv[i]; p2 += hv[i] * wf2[i];
    }
    STW(sane((u32x4){__float_as_uint(oct_sum(s[0] + s[1])), __float_as_uint(oct_sum(q[0] + q[1])), __float_as_uint(oct_sum(p2[0] + p2[1])), __float_as_uint(sw_)}), st_dst);
  }
  KEEP2();
  STAMPT(0, 10, 0);
  const int sw = wave - 4;
  const unsigned fd_src = WS_FD + xcd * 4096 + r * 128;
  u32x2 kpk = {SENT, SENT};
  if (wave == 0) {
    u32x4 pa[8];
    SLEEP(P_C);
    SPIN {
      unsigned m = 0;
#pragma unroll
      for (int j = 0; j < 8; ++j) { pa[j] = LDW(st_src + j * 1024); m = chk(m, pa[j]); }
      CNT(3);
      if (wave_ok(m)) break;
      KEEP2();
    }
    STAMPT(0, 11, 0);
#define F4(v) __builtin_bit_cast(f32x4, v)
    const f32x4 t = ((F4(pa[0]) + F4(pa[1])) + (F4(pa[2]) + F4(pa[3]))) + ((F4(pa[4]) + F4(pa[5])) + (F4(pa[6]) + F4(pa[7])));
    const float mean = t[0] * (1.0f / 1024.0f);
    const float var = fmaxf(__builtin_fmaf(t[1], 1.0f / 1024.0f, -(mean * mean)), 0.f);
    const float mb = rbf(mean), rs = __builtin_amdgcn_rcpf(rbf(__builtin_amdgcn_sqrtf(rbf(rbf(var) + 1e-5f))));
    *(f32x2*)(smem + L_KX + lane * 8) = (f32x2){mb, rs};
    const float sc = fmaxf((t[2] - mb * t[3]) * rs, -INFINITY);
    const int u = (int)(pack2(0.f, sc) & 0xffff0000u);
    unsigned b0 = ((unsigned)(u ^ ((u >> 31) | (int)0x80000000)) & 0xffff0000u) | (0xFFFFu - (rp * 64 + lane)), b1 = 0;
    top2_lo(b0, b1); top2_hi(b0, b1);
    STW(((u32x4){b0, b1, b0, b1}), fd_dst);
    if (lane == 0) *(u32x2*)(smem + L_MISC + 112) = (u32x2){b0, b1};
  } else if (wave >= 4) {
    asm volatile("s_waitcnt vmcnt(0)" ::: "memory", RSV2);
    sfor<16, 24>(wtr);
  }
  RAW_BAR();
  bool cand;
  u32x4 ob[2];
  {
    const f32x2 mr = *(const f32x2*)(smem + L_KX + erow * 8);
    const u32x2 kk = *(const u32x2*)(smem + L_MISC + 112);
    const float mb = mr[0], rs = mr[1];
    const unsigned b0 = kk[0], b1 = kk[1];
    u32x4 nq[2];
#pragma unroll
    for (int i = 0; i < 8; ++i) {
      const f32x2 d = hv[i] - mb;
      const f32x2 n = unpk(pack2o(d[0], d[1])) * rs;
      nq[i >> 2][i & 3] = pack2o(n[0], n[1]);
    }
    const bool c2 = (b1 & 0xffffu) == kidx;
    cand = c2 || (b0 & 0xffffu) == kidx;
    if (cand) {
      const unsigned xo = WS_XS + (rp * 2 + (c2 ? 1 : 0)) * 2048 + cb * 256 + eseg * 16;
      STW(sane(nq[0]), xo); STW(sane(nq[1]), xo + 128);
      char* cp = smem + L_CAND + (c2 ? 512 : 0) + eseg * 16;
      *(u32x4*)cp = (u32x4){hb[0], hb[1], hb[2], hb[3]}; *(u32x4*)(cp + 128) = (u32x4){hb[4], hb[5], hb[6], hb[7]};
      *(u32x4*)(cp + 256) = nq[0]; *(u32x4*)(cp + 384) = nq[1];
    }
    asm volatile("" ::: "memory");
    if (wave >= 4) { SLEEP(P_D); kpk = __builtin_amdgcn_raw_buffer_load_b64(wsr, fd_src, 0, 16); }
    asm volatile("" ::: "memory");
#pragma unroll
    for (int i = 0; i < 8; ++i) { const f32x2 o = hv[i] + unpk(nq[i >> 2][i & 3]); ob[i >> 2][i & 3] = pack2(o[0], o[1]); }
  }
#define OUT_ST() do { *(u32x4*)(out + ego) = ob[0]; *(u32x4*)(out + ego + 64) = ob[1]; } while (0)
  STAMPT(0, 12, 0);
  if (wave < 4) {
    if (!cand) OUT_ST();
    RAW_BAR();
    const unsigned w0 = misc[24], w1 = misc[25];
    if (cand && grow != (int)key_idx(w0) && grow != (int)key_idx(w1)) OUT_ST();
#pragma unroll 1
    for (int tk = 0; tk < 2; ++tk) {
      const unsigned wk = tk ? w1 : w0, slot = misc[26 + tk];
      if ((int)(slot >> 1) != rp) continue;
      u32x4 pv[7];
      const unsigned yo = WS_YP + ((tk * 8 + cb) * 64 + (lane >> 3) * 8) * 512 + wave * 128 + (lane & 7) * 16;
      const int col = (wave >> 1) * 64 + (lane & 7) * 8 + (wave & 1) * 4 + (lane >> 4);
      float sg = rbf(__builtin_amdgcn_rcpf(1.0f + __expf(-key_val(wk))));
      float hh = bflo(*(const u16*)(smem + L_CAND + (slot & 1) * 512 + col * 2)), nn = bflo(*(const u16*)(smem + L_CAND + (slot & 1) * 512 + 256 + col * 2));
      u16* op = out + (size_t)key_idx(wk) * D_ + cb * 128 + col;
      asm volatile("" : "+v"(sg), "+v"(hh), "+v"(nn), "+v"(op));
      if (tk == 0 || (int)(misc[26] >> 1) != rp) {
        SPIN { if (misc[30] == (int)w0) break; __builtin_amdgcn_s_sleep(1); }
        SLEEP(P_E);
      }
      SPIN {
        unsigned m = 0;
#pragma unroll
        for (int p = 0; p < 7; ++p) { pv[p] = LDW(yo + p * 512); m = chk(m, pv[p]); }
        CNT(6);
        if (wave_ok(m)) break;
        asm volatile("" ::: "memory");
      }
      STAMPT(0, 13, 0);
      f32x4 s = __builtin_bit_cast(f32x4, pv[0]);
#pragma unroll
      for (int p = 1; p < 7; ++p) s += __builtin_bit_cast(f32x4, pv[p]);
      float y = sw32(sw16(s[0], s[1]), sw16(s[2], s[3]));
      y += dppf<DPP_ROR8>(y);
      if (!(lane & 8)) *(GAS u16*)op = (u16)pack2(hh + rbf(nn + rbf(rbf(y) * sg)), 0.f);
      STAMPT(0, 14, 0);
    }
  } else {
    unsigned xl = WS_XS + (kq * 256 + sw * 64 + (lane >> 4) * 16 + ((lane >> 2) & 1) * 8) * 2;
    int red_dst = (lane < 8 || (lane >= 24 && lane < 32)) ? L_RED + sw * 2048 + ((lane >> 3) & 1) * 1024 + (lane & 7) * 16 : L_RED + 8192 + lane * 16;
    unsigned gu_src = WS_GU + g8 * 4096 + lane * 16;
    int act_dst = L_ACT + sw * 256 + ((lane & 7) * 8 + (lane >> 3)) * 2, act_src = L_ACT + sw * 256 + (lane & 1) * 128 + (lane >> 4) * 32 + ((lane >> 1) & 1) * 16;
    unsigned yp_dst = lane < 16 ? WS_YP + (((lane >> 3) * 8 + 2 * kq + (sw >> 1)) * 64 + g8) * 512 + (sw & 1) * 256 + (lane & 7) * 16 : OOB;
    asm volatile("" : "+v"(xl), "+v"(red_dst), "+v"(gu_src), "+v"(act_dst), "+v"(act_src), "+v"(yp_dst));
    unsigned w0, w1; int s0 = -1, s1 = -1;
    u32x4 xa = {SENT, SENT, SENT, SENT};
    SPIN {
      CNT(4);
      const bool all = wave_ok(kpk[1]);
      const unsigned k0 = kpk[1] == SENT ? 0u : kpk[0], k1 = kpk[1] == SENT ? 0u : kpk[1];
      w0 = k0; w1 = k1;
      top2_lo(w0, w1); top2_mid(w0, w1);
      const int n0 = 2 * __builtin_ctzll(__ballot(k0 == w0));
      const u64 m1 = __ballot(k0 == w1);
      const int n1 = m1 ? 2 * __builtin_ctzll(m1) : n0 + 1;
      const bool same = n0 == s0 && n1 == s1;
      s0 = n0; s1 = n1;
      asm volatile("" ::: "memory");
      if (all && same) break;
      if (!all) { kpk = __builtin_amdgcn_raw_buffer_load_b64(wsr, fd_src, 0, 16); }
      xa = LDW(xl + ((lane & 1) ? s1 : s0) * 2048);
      if (all) break;
    }
    SPIN {
      if (wave_ok(chk(0u, xa))) break;
      asm volatile("" ::: "memory");
      xa = LDW(xl + ((lane & 1) ? s1 : s0) * 2048);
    }
    STAMPT(1, 3, 256);
    {
      const u32x4 z4 = {0u, 0u, 0u, 0u};
      const u32x4 xg = (lane & 2) ? z4 : xa, xu = (lane & 2) ? xa : z4;
      f32x4 d[8];
#pragma unroll
      for (int c = 0; c < 8; ++c) d[c] = mfma16(xg, wf[c], (f32x4){0.f, 0.f, 0.f, 0.f});
      if (tid == 256) { *(u32x4*)(smem + L_MISC + 96) = (u32x4){w0, w1, (unsigned)s0, (unsigned)s1}; misc[30] = 0; }
      SB0();
#pragma unroll
      for (int c = 0; c < 10; ++c) {
        if (c < 8) d[c & 7] = mfma16(xu, wf[8 + (c & 7)], d[c & 7]);
        if (c >= 2) *(f32x4*)(smem + red_dst + (c - 2) * 128) = d[(c - 2) & 7];
        SB0();
      }
    }
    RAW_BAR();
    if (sw == 0) {
      f32x4 t[8];
#pragma unroll
      for (int p = 0; p < 8; ++p) t[p] = *(const f32x4*)(smem + L_RED + p * 1024 + lane * 16);
      SB0();
      STW(sane(__builtin_bit_cast(u32x4, ((t[0] + t[1]) + (t[2] + t[3])) + ((t[4] + t[5]) + (t[6] + t[7])))), gu_src + kq * 1024);
    } else __builtin_amdgcn_s_sleep(2);
    STAMPT(1, 4, 256);
    {
      u32x4 q[4];
      if (grow != (int)key_idx(w0) && grow != (int)key_idx(w1)) OUT_ST();
      asm volatile("" ::: "memory");
      SLEEP(P_GU);
      SPIN {
        unsigned m = 0;
#pragma unroll
        for (int j = 0; j < 4; ++j) { q[j] = LDW(gu_src + j * 1024); m = chk(m, q[j]); }
        CNT(5);
        if (wave_ok(m)) break;
        asm volatile("" ::: "memory");
      }
      STAMPT(1, 5, 256);
      const f32x4 tot = (__builtin_bit_cast(f32x4, q[0]) + __builtin_bit_cast(f32x4, q[1])) + (__builtin_bit_cast(f32x4, q[2]) + __builtin_bit_cast(f32x4, q[3]));
      const f32x2 gs = unpk(pack2o(tot[0], tot[1])), us = unpk(pack2o(tot[2], tot[3]));
      const f32x2 sg = unpk(pack2o(gs[0] * __builtin_amdgcn_rcpf(1.0f + __expf(-gs[0])), gs[1] * __builtin_amdgcn_rcpf(1.0f + __expf(-gs[1]))));
      const unsigned a2 = pack2o(sg[0] * us[0], sg[1] * us[1]);
      *(u16*)(smem + act_dst) = (u16)a2; *(u16*)(smem + act_dst + 128) = (u16)(a2 >> 16);
      __builtin_amdgcn_fence(__ATOMIC_RELEASE, "wavefront"); __builtin_amdgcn_wave_barrier(); __builtin_amdgcn_fence(__ATOMIC_ACQUIRE, "wavefront");
      const u32x4 aa = *(const u32x4*)(smem + act_src);
      f32x4 d[8];
#pragma unroll
      for (int c = 0; c < 8; ++c) d[c] = mfma16(aa, wf[16 + c], (f32x4){0.f, 0.f, 0.f, 0.f});
      SB0();
      u32x4 y[2];
#pragma unroll
      for (int c = 0; c < 8; ++c) {
        const float t0 = d[c][0] + dppf<DPP_ROR8>(d[c][2]), t1 = d[c][3] + dppf<DPP_ROR8>(d[c][1]);
        y[c >> 2][c & 3] = __float_as_uint((lane & 8) ? t1 : t0);
      }
      STW(sane(y[0]), yp_dst); STW(sane(y[1]), yp_dst + 128);
      if (tid == 256) misc[30] = w0;
    }
    STAMPT(1, 6, 256);
  }
}

extern "C" void kernel_launch(void* const* d_in, const int* in_sizes, int n_in, void* d_out, int out_size,
                              void* d_ws, size_t ws_size, hipStream_t stream) {
  (void)hipFuncSetAttribute((const void*)fused, hipFuncAttributeMaxDynamicSharedMemorySize, LDS_TOTAL);
  fused<<<256, 512, LDS_TOTAL, stream>>>((const u16*)d_in[0], (const u16*)d_in[1], (const u16*)d_in[2], (const u16*)d_in[4],
                                         (const u16*)d_in[5], (const u16*)d_in[6], (const u16*)d_in[7], (u16*)d_out, (char*)d_ws);
}
```

```cpp
#include <hip/hip_runtime.h>
#include <stdint.h>

#define LAS __attribute__((address_space(3)))
#define GAS __attribute__((address_space(1)))
#define DI __device__ __forceinline__

typedef unsigned short u16;
typedef unsigned long long u64;
typedef __attribute__((ext_vector_type(2))) __bf16 bf16x2;
typedef __attribute__((ext_vector_type(8))) __bf16 bf16x8;
typedef __attribute__((ext_vector_type(4))) short s16x4;
typedef __attribute__((ext_vector_type(2))) unsigned short u16x2;
typedef __attribute__((ext_vector_type(4))) float f32x4;
typedef __attribute__((ext_vector_type(2))) float f32x2;
typedef __attribute__((ext_vector_type(4))) unsigned u32x4;
typedef __attribute__((ext_vector_type(2))) unsigned u32x2;
typedef GAS unsigned gu32;
typedef GAS unsigned long long gu64;

constexpr int S_ = 2048, D_ = 1024, F_ = 3584;

constexpr unsigned SY_CH = 0, SY_GO = 1024, SY_RE = 2048;
constexpr unsigned WS_FD  = 98304;
constexpr unsigned WS_ZF  = 8192;
constexpr unsigned WS_KVF = 16384;
constexpr unsigned WS_RS  = 131072;
constexpr unsigned WS_CS  = WS_RS + 16 * 16 * 1024;
constexpr unsigned WS_ZP  = WS_CS + 16 * 16 * 768;
constexpr unsigned WS_FB  = WS_ZP + 32 * 16 * 128;
constexpr unsigned WS_ST2 = WS_FB + 32 * 32 * 128;
constexpr unsigned WS_XS  = WS_ST2 + 32 * 8 * 1024;
constexpr unsigned WS_GU  = WS_XS + 64 * 2048;
constexpr unsigned WS_YP  = WS_GU + 256 * 1024;
constexpr unsigned WS_KVP = WS_YP + 2 * 8 * 64 * 512;
constexpr unsigned WS_O   = WS_KVP + 32 * 16 * 4096;
constexpr unsigned WS_END = WS_O + S_ * D_ * 2;
constexpr unsigned OOB = 0x80000000u;
constexpr size_t WS_DIAG  = 64u << 20;
#define CNT(i) do {} while (0)
#define STAMPT(kid, i, T) do {} while (0)

#define TSL(idx) do {} while (0)
#define TSDUMP(k) do {} while (0)
template <int V> struct IC { static constexpr int v = V; };
template <int B, int E, class F> DI void sfor(F&& f) { if constexpr (B < E) { f(IC<B>{}); sfor<B + 1, E>(f); } }

DI float bflo(unsigned u) { return __uint_as_float(u << 16); }
DI float bfhi(unsigned u) { return __uint_as_float(u & 0xffff0000u); }
DI float rbf(float x) { return (float)(__bf16)x; }
DI unsigned pack2(float a, float b) { bf16x2 c; c[0] = (__bf16)a; c[1] = (__bf16)b; return __builtin_bit_cast(unsigned, c); }
DI unsigned pack2o(float a, float b) { unsigned p = pack2(a, b); asm("" : "+v"(p)); return p; }
DI f32x4 mfma16(u32x4 a, u32x4 b, f32x4 c) {
  return __builtin_amdgcn_mfma_f32_16x16x32_bf16(__builtin_bit_cast(bf16x8, a), __builtin_bit_cast(bf16x8, b), c, 0, 0, 0);
}
DI void mfma16i(u32x4 a, u32x4 b, f32x4& c) { asm volatile("v_mfma_f32_16x16x32_bf16 %0, %1, %2, %0" : "+v"(c) : "v"(a), "v"(b)); }
DI f32x2 unpk(unsigned u) { return (f32x2){bflo(u), bfhi(u)}; }
DI u32x2 trread(const char* p) {
  s16x4 v = __builtin_amdgcn_ds_read_tr16_b64_v4i16((LAS s16x4*)p);
  return __builtin_bit_cast(u32x2, v);
}
DI const char* uni(const void* p) {
  const u64 v = (u64)p;
  return (const char*)(((u64)(unsigned)__builtin_amdgcn_readfirstlane((int)(v >> 32)) << 32) | (unsigned)__builtin_amdgcn_readfirstlane((int)v));
}
#define RV4(a, b, c, d) "v" #a, "v" #b, "v" #c, "v" #d
#define RV10(p) "v" #p "0", "v" #p "1", "v" #p "2", "v" #p "3", "v" #p "4", "v" #p "5", "v" #p "6", "v" #p "7", "v" #p "8", "v" #p "9"
#define RSV2 RV4(224, 225, 226, 227), "v228", "v229", RV10(23), RV10(24), RV4(250, 251, 252, 253), "v254", "v255"
#define RSV1 RV4(192, 193, 194, 195), RV4(196, 197, 198, 199), RV10(20), RV10(21), RV4(220, 221, 222, 223), RSV2
#define RSV "v148", "v149", RV10(15), RV10(16), RV10(17), RV10(18), "v190", "v191", RSV1
#define NTW " nt"
#define GL_(v, s, o) "global_load_lds_dwordx4 " v ", " s " offset:" #o
#define GLDS4(SC, CL...) asm volatile("s_add_u32 m0, %5, %c6\n\ts_nop 3\n\t" GL_("%0", "%4", 0) SC "\n\t" GL_("%1", "%4", 1024) SC "\n\t" \
  GL_("%2", "%4", 2048) SC "\n\t" GL_("%3", "%4", 3072) SC :: "v"(v0), "v"(v1), "v"(v2), "v"(v3), "s"(uni(sbase)), "s"(lds), "i"(IMM) : "memory", "scc", ##CL)
#define GLDS3(SC, CL...) asm volatile("s_add_u32 m0, %4, %c5\n\ts_nop 3\n\t" GL_("%0", "%3", 0) SC "\n\t" GL_("%1", "%3", 1024) SC "\n\t" \
  GL_("%2", "%3", 2048) SC :: "v"(v0), "v"(v1), "v"(v2), "s"(uni(sbase)), "s"(lds), "i"(IMM) : "memory", "scc", ##CL)
template <int IMM> DI void glds4(unsigned v0, unsigned v1, unsigned v2, unsigned v3, const void* sbase, unsigned lds) { GLDS4(""); }
template <int IMM> DI void glds4r(unsigned v0, unsigned v1, unsigned v2, unsigned v3, const void* sbase, unsigned lds) { GLDS4("", RSV); }
template <int IMM> DI void glds3r(unsigned v0, unsigned v1, unsigned v2, const void* sbase, unsigned lds) { GLDS3("", RSV); }
template <int R> DI void sload(unsigned voff, const void* sbase) {
  asm volatile("s_nop 4\n\tglobal_load_dwordx4 v[%c0:%c1], %2, %3 sc1" ::"i"(R), "i"(R + 3), "v"(voff), "s"(uni(sbase)) : "memory", RSV);
}
DI float fadd(float a, float b) { float o; asm("v_add_f32 %0, %1, %2" : "=v"(o) : "v"(a), "v"(b)); return o; }
#define RA_(o, a, b) "v_add_f32 %" #o ", v[%c6+" #a "], v[%c6+" #b "]\n\t"
#define RB2_(o, a) "v_add_f32 %" #o ", v[%c6+" #a "], %" #o "\n\t"
#define RM_(o, a, b) "v_max3_u32 %" #o ", v[%c6+" #a "], v[%c6+" #b "], %" #o "\n\t"
template <int R> DI void rsum4(float* a, unsigned& m, unsigned& n) {
  asm volatile(RA_(0, 0, 4) RA_(1, 1, 5) RM_(4, 0, 1) RA_(2, 2, 6) RA_(3, 3, 7) RM_(5, 2, 3) RB2_(0, 8) RM_(4, 4, 5) RB2_(1, 9) RM_(5, 6, 7)
               RB2_(2, 10) RM_(4, 8, 9) RB2_(3, 11) RM_(5, 10, 11) RB2_(0, 12) RB2_(1, 13) RM_(4, 12, 13) RB2_(2, 14) RB2_(3, 15) RM_(5, 14, 15)
               : "=&v"(a[0]), "=&v"(a[1]), "=&v"(a[2]), "=&v"(a[3]), "+v"(m), "+v"(n) : "i"(R) : RSV);
}
template <int R> DI void wload2(unsigned voff, const void* sbase) {
  asm volatile("s_nop 4\n\tglobal_load_dwordx4 v[%c0:%c1], %2, %3" NTW ::"i"(R), "i"(R + 3), "v"(voff), "s"(uni(sbase)) : "memory", RSV2);
}
template <int R> DI void wload(unsigned voff, const void* sbase) {
  asm volatile("s_nop 4\n\tglobal_load_dwordx4 v[%c0:%c1], %2, %3" NTW ::"i"(R), "i"(R + 3), "v"(voff), "s"(uni(sbase)) : "memory", RSV);
}
template <int RA, int RB> DI unsigned wperm(unsigned sel) {
  unsigned o;
  if (RA < 192) asm volatile("v_perm_b32 %0, v%c2, v%c1, %3" : "=v"(o) : "i"(RA), "i"(RB), "s"(sel) : RSV);
  else if (RA < 224) asm volatile("v_perm_b32 %0, v%c2, v%c1, %3" : "=v"(o) : "i"(RA), "i"(RB), "s"(sel) : RSV1);
  else asm volatile("v_perm_b32 %0, v%c2, v%c1, %3" : "=v"(o) : "i"(RA), "i"(RB), "s"(sel) : RSV2);
  return o;
}
template <int R> DI unsigned rget() { unsigned o; asm volatile("v_mov_b32 %0, v%c1" : "=v"(o) : "i"(R) : RSV); return o; }
DI float xh_sum(float v) {
  auto r = __builtin_amdgcn_permlane32_swap(__float_as_uint(v), __float_as_uint(v), false, false);
  return __uint_as_float(r[0]) + __uint_as_float(r[1]);
}
template <int CTRL> DI unsigned dppu(unsigned v) { return (unsigned)__builtin_amdgcn_update_dpp(0, (int)v, CTRL, 0xf, 0xf, true); }
template <int CTRL> DI float dppf(float v) { return __uint_as_float(dppu<CTRL>(__float_as_uint(v))); }
constexpr int DPP_X1 = 0xB1, DPP_X2 = 0x4E, DPP_HMIR = 0x141, DPP_MIR = 0x140, DPP_ROR8 = 0x128;
DI float quad_sum(float v) { v += dppf<DPP_X1>(v); v += dppf<DPP_X2>(v); return v; }
DI float oct_sum(float v) { v = quad_sum(v); v += dppf<DPP_HMIR>(v); return v; }
DI float x16_sum(float v) {
  auto r = __builtin_amdgcn_permlane16_swap(__float_as_uint(v), __float_as_uint(v), false, false);
  return __uint_as_float(r[0]) + __uint_as_float(r[1]);
}
DI float sw16(float a, float b) {
  auto r = __builtin_amdgcn_permlane16_swap(__float_as_uint(a), __float_as_uint(b), false, false);
  return fadd(__uint_as_float(r[0]), __uint_as_float(r[1]));
}
DI float sw32(float a, float b) {
  auto r = __builtin_amdgcn_permlane32_swap(__float_as_uint(a), __float_as_uint(b), false, false);
  return fadd(__uint_as_float(r[0]), __uint_as_float(r[1]));
}
template <int N> struct Phi { unsigned tb[N]; f32x2 tr[N], e[N]; };
template <int N> DI void phi_a(const f32x2* t, Phi<N>& s) {
#pragma unroll
  for (int i = 0; i < N; ++i) s.tb[i] = pack2o(t[i][0], t[i][1]);
  __builtin_amdgcn_sched_barrier(0);
#pragma unroll
  for (int i = 0; i < N; ++i) s.tr[i] = unpk(s.tb[i]);
  __builtin_amdgcn_sched_barrier(0);
#pragma unroll
  for (int i = 0; i < N; ++i) s.e[i] = s.tr[i] * 1.44269504088896340736f;
  __builtin_amdgcn_sched_barrier(0);
}
template <int N> DI void phi_b(Phi<N>& s, unsigned* p) {
  u16x2 tm[2] = {{0, 0}, {0, 0}};
#pragma unroll
  for (int i = 0; i < N; ++i) {
    s.e[i][0] = __builtin_amdgcn_fmed3f(__builtin_amdgcn_exp2f(s.e[i][0]), 0.f, 1.f);
    tm[i & 1] = __builtin_elementwise_max(tm[i & 1], __builtin_bit_cast(u16x2, s.tb[i]));
    s.e[i][1] = __builtin_amdgcn_fmed3f(__builtin_amdgcn_exp2f(s.e[i][1]), 0.f, 1.f);
    __builtin_amdgcn_sched_barrier(0);
  }
#pragma unroll
  for (int i = 0; i < N; ++i) s.tr[i] += s.e[i];
  __builtin_amdgcn_sched_barrier(0);
#pragma unroll
  for (int i = 0; i < N; ++i) { s.tr[i][0] = fmaxf(s.tr[i][0], s.e[i][0]); s.tr[i][1] = fmaxf(s.tr[i][1], s.e[i][1]); }
  const u16x2 tx = __builtin_elementwise_max(tm[0], tm[1]);
  __builtin_amdgcn_sched_barrier(0);
#pragma unroll
  for (int i = 0; i < N; ++i) p[i] = pack2(s.tr[i][0], s.tr[i][1]);
  if (__any(tx[0] > 0xC0F0 || tx[1] > 0xC0F0)) {
#pragma unroll
    for (int i = 0; i < N; ++i) p[i] = pack2(bflo(p[i]) + 1e-6f, bfhi(p[i]) + 1e-6f);
  }
}
template <int N> DI void phi_n(const f32x2* t, unsigned* p) { Phi<N> s; phi_a<N>(t, s); phi_b<N>(s, p); }
#define RLX_AGENT __ATOMIC_RELAXED, __HIP_MEMORY_SCOPE_AGENT
DI void st64(char* ws, unsigned off, u64 v) { __hip_atomic_store((gu64*)(ws + off), v, RLX_AGENT); }
DI unsigned ld32(char* ws, unsigned off) { return __hip_atomic_load((gu32*)(ws + off), RLX_AGENT); }
#define WAIT_V(n) asm volatile("s_waitcnt vmcnt(%c0)" ::"i"(n) : "memory")
#define RAW_BAR() do { asm volatile("s_waitcnt lgkmcnt(0)" ::: "memory"); __builtin_amdgcn_s_barrier(); asm volatile("" ::: "memory"); } while (0)
#define SB0() __builtin_amdgcn_sched_barrier(0)
#define WAIT_VR(n) asm volatile("s_waitcnt vmcnt(%c0)" ::"i"(n) : "memory", RSV)
#define RAW_BARR() do { asm volatile("s_waitcnt lgkmcnt(0)" ::: "memory", RSV); __builtin_amdgcn_s_barrier(); asm volatile("" ::: "memory", RSV); } while (0)
#define KEEP2() asm volatile("" ::: "memory", RSV2)
#define RAW_BAR2() do { asm volatile("s_waitcnt lgkmcnt(0)" ::: "memory", RSV2); __builtin_amdgcn_s_barrier(); KEEP2(); } while (0)
constexpr unsigned SPIN_CAP = 1u << 18;
#define SPIN _Pragma("nounroll") for (unsigned s_ = 0; s_ < SPIN_CAP; ++s_)
constexpr unsigned SENT = 0xFFFFFFFFu;
DI unsigned sane1(unsigned v) { return min(v, 0xFFFFFFFEu); }
DI u32x4 sane(u32x4 v) { v[1] = sane1(v[1]); v[3] = sane1(v[3]); return v; }
DI unsigned chk(unsigned m, u32x4 v) { return max(max(m, v[1]), v[3]); }
DI bool wave_ok(unsigned m) { return !__any(m == SENT); }
DI unsigned key_idx(unsigned k) { return (0xFFFFu - (k & 0xFFFFu)) & (S_ - 1); }
DI float key_val(unsigned k) { const unsigned o = k >> 16; return __uint_as_float(((o & 0x8000u) ? (o & 0x7fffu) : (~o & 0xffffu)) << 16); }
DI void top2_mix(unsigned& b0, unsigned& b1, unsigned c0, unsigned c1) {
  const unsigned lo = min(b0, c0), mx = max(b1, c1);
  b0 = max(b0, c0); b1 = max(lo, mx);
}
DI void top2_lo(unsigned& b0, unsigned& b1) {
  top2_mix(b0, b1, dppu<DPP_X1>(b0), dppu<DPP_X1>(b1));
  top2_mix(b0, b1, dppu<DPP_X2>(b0), dppu<DPP_X2>(b1));
  top2_mix(b0, b1, dppu<DPP_HMIR>(b0), dppu<DPP_HMIR>(b1));
}
DI void top2_mid(unsigned& b0, unsigned& b1) {
  top2_mix(b0, b1, dppu<DPP_MIR>(b0), dppu<DPP_MIR>(b1));
  auto p = __builtin_amdgcn_permlane16_swap(b0, b0, false, false); auto q = __builtin_amdgcn_permlane16_swap(b1, b1, false, false);
  b0 = p[0]; b1 = q[0]; top2_mix(b0, b1, p[1], q[1]);
}
DI void top2_hi(unsigned& b0, unsigned& b1) {
  top2_mid(b0, b1);
  auto p = __builtin_amdgcn_permlane32_swap(b0, b0, false, false); auto q = __builtin_amdgcn_permlane32_swap(b1, b1, false, false);
  b0 = p[0]; b1 = q[0]; top2_mix(b0, b1, p[1], q[1]);
}

constexpr int K1_STAGE = 40960;
constexpr int LDS_TOTAL = 4 * K1_STAGE;
constexpr int L_STM = 0;
constexpr int L_MISC = 2048;
constexpr int L_XK = 5120;
constexpr int L_KX = 3328;
constexpr int L_ACT = 2304;
constexpr int L_CAND = 4096;
constexpr int L_WO = 16384;
constexpr int L_P = 16384, L_PK = 7 * 16384;
constexpr int L_RED = 65536;
constexpr int wo_slot(int j) { return L_WO + (j % 9) * 16384; }
constexpr int CODE_LINES = 12;
#define OW_ 8
constexpr int OW = OW_;
#define W1A_ 16
constexpr int W1A = W1A_;
#define GSCHED 5, 9, 13, 17, 21, 24, 27, 30, 33, 36, 39, 42, 45, 48, 48
constexpr int GS[15] = {GSCHED};
constexpr int gops(int g) { return (g / 3) * 10 + (g % 3 == 0 ? 0 : g % 3 == 1 ? 4 : 7); }
constexpr bool gs_ok() {
  bool ok = GS[0] >= 3 && GS[0] <= 9 && GS[1] >= 6 && GS[1] <= 12 && GS[0] <= GS[1] && GS[12] == 45 && GS[13] == 48 && GS[14] == 48;
  for (int t = 0; t < 12; ++t) ok = ok && GS[t + 2] <= 3 * (t + 5) && GS[t + 2] >= 3 * (t + 3) && GS[t + 2] >= GS[t + 1] + 3;
  return ok;
}
static_assert(gs_ok(), "staging schedule");
#define WSCHED 0, 0, 0, 0, 0, 0, 0, 0, 0, 2, 4, 6, 8
constexpr int WS_[13] = {WSCHED};
#define PACE 1
#define P_KVP 1
#define P_KVF 1
#define P_FB 8
#define P_C 14
#define P_D 2
#define P_GU 12
#define P_E 10
#define SLEEP(n) do { if (n) __builtin_amdgcn_s_sleep(n); } while (0)
constexpr int VW = 160;

__global__ __launch_bounds__(512, 2) void fused(const u16* __restrict__ x, const u16* __restrict__ wqkv,
                                                const u16* __restrict__ wo, const u16* __restrict__ w_r,
                                                const u16* __restrict__ wg, const u16* __restrict__ wu,
                                                const u16* __restrict__ wd, u16* __restrict__ out, char* __restrict__ ws) {
  extern __shared__ __attribute__((aligned(16))) char smem[];
  STAMPT(0, 0, 0);
  u64 pc0; asm volatile("s_getpc_b64 %0" : "=s"(pc0));
  const int wave = __builtin_amdgcn_readfirstlane(threadIdx.x >> 6);
  const unsigned lds0 = (unsigned)(size_t)smem;
  if (wave == 0) {
    const int l = threadIdx.x;
    asm volatile("s_mov_b32 m0, %1\n\ts_nop 0\n\tglobal_load_lds_dword %0, off"
                 :: "v"((const char*)(pc0 - (pc0 & 127)) + ((blockIdx.x >> 3) + (l < CODE_LINES ? l : 0) * 32) * 128), "s"(__builtin_amdgcn_readfirstlane(lds0 + 3 * K1_STAGE)) : "memory");
  }
#define RB_(b) (((b) & 1) * 8 + (((b) >> 3) & 7))
#define HP_(b) ((((b) & 7) >> 1) * 4 + ((b) >> 6))
#define KROT_(b) ((4 * ((HP_(b) >> 2) + (RB_(b) >> 3) + 2 * ((RB_(b) >> 2) & 1))) & 15)
  unsigned voA0, voA1, voA2, voA3, voB0, voB1, voB2, voB3, voB4, voB5;
  const unsigned l4 = __builtin_amdgcn_readfirstlane(lds0 + (wave - 4) * 4096), l6 = __builtin_amdgcn_readfirstlane(lds0 + (wave - 4) * 6144);
  const int krot0 = KROT_(blockIdx.x);
  auto group = [&](auto G) {
    constexpr int t = G.v / 3, p = G.v % 3, sl = (t & 3) * K1_STAGE;
    const int kt = (t + krot0) & 15;
    if (p == 0) glds4r<sl>(voA0, voA1, voA2, voA3, (const char*)x + kt * 128, l4);
    else if (p == 1) glds3r<sl + 16384>(voB0, voB1, voB2, (const char*)wqkv + kt * 64 * (3 * D_ * 2), l6);
    else glds3r<sl + 16384 + 3072>(voB3, voB4, voB5, (const char*)wqkv + kt * 64 * (3 * D_ * 2), l6);
  };
  if (wave >= 4) {
    const int lane = threadIdx.x & 63, sw = wave - 4;
    const int r0 = RB_(blockIdx.x) * 128 + 32 * sw + (lane >> 3);
    voA0 = (unsigned)(r0 * D_ + ((lane & 7) ^ (lane >> 4)) * 8) * 2u;
    voA1 = ((voA0 + 8 * D_ * 2) ^ 64) - 1024; voA2 = voA0 + 16 * D_ * 2 - 2048; voA3 = voA1 + 16 * D_ * 2 - 2048;
    asm volatile("" : "+v"(voA0), "+v"(voA1), "+v"(voA2), "+v"(voA3));
    STAMPT(1, 7, 256);
    group(IC<0>{});
    int l2 = lane; asm volatile("" : "+v"(l2));
    voB0 = (unsigned)((16 * sw + 4 * (l2 >> 5) + ((l2 >> 2) & 3)) * (3 * D_) + HP_(blockIdx.x) * 64 + ((l2 >> 4) & 1) * 32 + (l2 & 3) * 8) * 2u;
    voB1 = voB0 + 1024; voB2 = voB0 + 2048;
    voB3 = (voB0 ^ 32) + 8 * 3 * D_ * 2; voB4 = voB3 + 1024; voB5 = voB3 + 2048;
    asm volatile("" : "+v"(voB0), "+v"(voB1), "+v"(voB2), "+v"(voB3), "+v"(voB4), "+v"(voB5));
    sfor<1, 3>(group);
    STAMPT(1, 8, 256);
  }
  int bid = blockIdx.x; asm volatile("" : "+s"(bid));
  const int xcd = bid & 7, idx = bid >> 3;
  const int rb = RB_(bid), hp = HP_(bid);
  const int rp = xcd * 4 + (idx >> 3), cb = idx & 7;
  const int hs_ = (hp + 4 + (int)(((size_t)wqkv >> 7) & 7)) & 7;
  const int g8 = ((hp >> 3) * 4 + (rb >> 2)) * 8 + hs_, kq = rb & 3;
  const int krot = KROT_(bid), posA = 1 + (hp & 3), posB = 1 + (rb & 3);
  const int wrot = 4 * rp + (xcd >> 1), wdir = (xcd & 1) ? -1 : 1;
  auto whead = [&](int t) { return (wrot + wdir * t) & 15; };
  const unsigned rs_slot = WS_RS + (rb * 16 + ((krot + posA) & 15)) * 1024, cs_slot = WS_CS + (hp * 16 + ((krot + posB) & 15)) * 768;
  const __amdgpu_buffer_rsrc_t wsr = __builtin_amdgcn_make_buffer_rsrc(ws, 0, WS_END, 0x00020000);
#define LDW(off) __builtin_amdgcn_raw_buffer_load_b128(wsr, (off), 0, 16)
#define STW(v, off) __builtin_amdgcn_raw_buffer_store_b128((v), wsr, (off), 0, 16)
#define STW1(v, off) __builtin_amdgcn_raw_buffer_store_b32((v), wsr, (off), 0, 16)
  float* stm = (float*)(smem + L_STM);
  volatile LAS int* misc = (volatile LAS int*)(smem + L_MISC);
#define LANES() int tid = threadIdx.x; asm volatile("" : "+v"(tid)); const int lane = tid & 63, r = lane & 31, h = lane >> 5; (void)r; (void)h
#define EPI() const int erow = tid >> 3, eseg = tid & 7; const size_t ego = (size_t)(rp * 64 + erow) * D_ + cb * 128 + eseg * 8
  u32x4 xv[2], wv[2];
  f32x2 xf[8], wf2[8];
  float sw_;
  auto unpack_xw = [&]() {
    f32x2 a = {0.f, 0.f};
#pragma unroll
    for (int i = 0; i < 8; ++i) { xf[i] = unpk(xv[i >> 2][i & 3]); wf2[i] = unpk(wv[i >> 2][i & 3]); a += wf2[i]; }
    sw_ = oct_sum(a[0] + a[1]);
#pragma unroll
    for (int i = 0; i < 8; ++i) asm volatile("" : "+v"(xf[i]), "+v"(wf2[i]));
    asm volatile("" : "+v"(sw_));
  };
  unsigned tsv = 0; (void)tsv;
  u32x4 wf[24];

  auto wtr = [&](auto I) {
    constexpr int m = I.v >> 3, c = I.v & 7, R = VW + 32 * m + (c >> 1);
    const unsigned sel = (c & 1) ? 0x07060302u : 0x05040100u;
    wf[I.v] = (u32x4){wperm<R, R + 4>(sel), wperm<R + 8, R + 12>(sel), wperm<R + 16, R + 20>(sel), wperm<R + 24, R + 28>(sel)};
  };

  if (wave < 4) {
    LANES();
    const int c16 = lane & 15, g4 = lane >> 4;
    const int trl = (c16 >> 2) * 64 + (c16 & 3) * 8;
    const int wr = wave >> 1, wc = wave & 1;
    const int head = 2 * hp + wc;
    const unsigned kvp_off = WS_KVP + (head * 16 + rb) * 4096 + wr * 2048 + lane * 16;
    const unsigned zp_off = WS_ZP + (head * 16 + rb) * 128;
    const unsigned o_off = WS_O + ((rb * 4 + 2 * wr) * 32 + head) * 2048 + lane * 16;
    const unsigned fb_off = lane < 8 ? WS_FB + ((rb * 2 + wr) * 32 + head) * 128 + lane * 16 : OOB;
    unsigned pfa, pfb = OOB;
    unsigned pfc = lane < 8 ? fb_off : lane < 24 ? rs_slot + wave * 256 + (lane - 8) * 16
        : lane < 40 ? (wave < 3 ? cs_slot + wave * 256 : WS_KVF + hp * 4096 + rb * 256) + (lane - 24) * 16
        : lane < 48 ? (wave < 2 ? zp_off + (lane & 7) * 16 : (wave == 2 && rb < 2) ? WS_ZF + (2 * hp + rb) * 128 + (lane & 7) * 16 : OOB) : OOB;
    if (wave == 0) pfa = WS_ST2 + (rp * 8 + cb) * 1024 + lane * 16;
    else if (wave == 1) pfa = WS_GU + (g8 * 4 + kq) * 1024 + lane * 16;
    else pfa = WS_YP + (((wave - 2) * 8 + 2 * kq + (lane >> 5)) * 64 + g8) * 512 + (lane & 31) * 16;
    if (wave == 0) pfb = lane < 32 ? WS_XS + (rp * 2 + (lane >> 4)) * 2048 + cb * 256 + (lane & 15) * 16 : lane < 40 ? WS_FD + (cb * 32 + rp) * 128 + (lane & 7) * 16 : OOB;
    u32x4 S4 = {SENT, SENT, SENT, SENT};
    asm volatile("" : "+v"(pfa), "+v"(pfb), "+v"(pfc), "+v"(S4));
    __builtin_amdgcn_s_setprio(1);
    f32x4 acc[4][6];
    int ao[2][2], bo[2][2];
    {
#pragma unroll
      for (int i = 0; i < 2; ++i) {
        ao[0][i] = (64 * wr + c16) * 128 + (((4 * i + g4) ^ (c16 >> 1)) * 16); ao[1][i] = ao[0][i] + 2 * K1_STAGE;
        bo[0][i] = 16384 + g4 * 3072 + wc * 256 + trl + ((i ^ (g4 & 1)) * 32); bo[1][i] = bo[0][i] + 2 * K1_STAGE;
        asm volatile("" : "+v"(ao[0][i]), "+v"(ao[1][i]), "+v"(bo[0][i]), "+v"(bo[1][i]));
      }
#pragma unroll
      for (int i = 0; i < 4; ++i)
#pragma unroll
        for (int j = 0; j < 6; ++j) { acc[i][j] = (f32x4){0.f, 0.f, 0.f, 0.f}; asm volatile("" : "+v"(acc[i][j])); }
      struct Frag { u32x4 a[4]; u32x2 bl[6], bh[6]; };
      Frag FA, FB;
#define K1_B(j) ((u32x4){C.bl[j][0], C.bl[j][1], C.bh[j][0], C.bh[j][1]})
#define K1_LA(i) N.a[i] = *(const u32x4*)(smem + ao[sn >> 1][hf] + (sn & 1) * K1_STAGE + (i) * 2048)
#define K1_LL(j) N.bl[j] = trread(smem + bo[sn >> 1][(j) & 1] + (sn & 1) * K1_STAGE + hf * 12288 + ((j) >> 1) * 1024)
#define K1_LH(j) N.bh[j] = trread(smem + bo[sn >> 1][(j) & 1] + (sn & 1) * K1_STAGE + hf * 12288 + ((j) >> 1) * 1024 + 512)
#define K1_M(i, j) if ((j) < 2) mfma16i(K1_B(j), C.a[i], acc[i][j]); else mfma16i(C.a[i], K1_B(j), acc[i][j]); SB0()
      auto sub = [&](Frag& C, Frag& N, int sn, int hf) {
        sfor<0, 24>([&](auto Q) {
          constexpr int q = Q.v;
          constexpr int j = q / 4, i = (j & 1) ? 3 - q % 4 : q % 4;
          if (q == 23) asm volatile("s_waitcnt lgkmcnt(0)" : "+v"(N.bh[5]));
          K1_M(i, j);
          if constexpr (q < 4) { K1_LA(q); } else if constexpr (q < 16) { if (q & 1) { K1_LH((q - 4) >> 1); } else { K1_LL((q - 4) >> 1); } }
          SB0();
        });
      };
      auto subkv = [&](Frag& C, Frag& N, int sn, int hf, bool ld) {
#define K1_QL(i, j, l) K1_M(i, j); if (ld) { l; } SB0()
        K1_QL(0, 2, K1_LL(2)); K1_QL(1, 2, K1_LH(2)); K1_QL(0, 3, K1_LA(0)); K1_QL(1, 3, K1_LA(1)); K1_QL(2, 2, K1_LL(3)); K1_QL(3, 2, K1_LH(3));
        K1_QL(2, 3, K1_LA(2)); K1_QL(3, 3, K1_LA(3)); K1_QL(0, 4, K1_LL(4)); K1_QL(1, 4, K1_LH(4)); K1_QL(2, 4, K1_LL(5)); K1_QL(3, 4, K1_LH(5));
        K1_M(0, 5); K1_M(1, 5); K1_M(2, 5); K1_M(3, 5);
      };
      WAIT_V(0);
      RAW_BAR();
      STAMPT(0, 1, 0);
      {
        Frag& N = FA; const int sn = 0, hf = 0;
        K1_LL(0); K1_LH(0); K1_LA(0); K1_LA(1); K1_LL(1); K1_LH(1); K1_LL(2); K1_LH(2); K1_LL(3); K1_LH(3); K1_LA(2); K1_LA(3); K1_LL(4); K1_LH(4); K1_LL(5); K1_LH(5);
      }
#pragma unroll 1
      for (int it = 0; it < 4; ++it) {
#pragma unroll
        for (int s = 0; s < 4; ++s) {
          if (s == 2 && it == 3) break;
          sub(FA, FB, s, 1);
          if (s == 0) WAIT_V(0);
          if (it == 0) { if (s == 1) STW(S4, pfc); if (s == 2) STW(S4, kvp_off); if (s == 3) STW(S4, kvp_off + 1024); }
          if (it == 1) { if (s == 0) STW(S4, pfa); if (s == 1) STW(S4, pfb); }
          TSL(it * 4 + s);
          if (s != 1 || it < 3) RAW_BAR();
          sub(FB, FA, (s + 1) & 3, 0);
        }
      }
      subkv(FA, FB, 2, 1, true);
      TSL(14);
      RAW_BAR();
      subkv(FB, FA, 3, 0, true);
      subkv(FA, FB, 3, 1, true);
      TSL(15);
      subkv(FB, FA, 0, 0, false);
    }
    asm volatile("s_nop 15" ::: "memory");
    TSL(16); if (wave == 0) TSDUMP(0);
    STAMPT(0, 2, 0);
    {
      f32x4 m4[4], i4[4]; float cs[4];
      SPIN {
        const int ok = misc[12 + (lane & 3)];
        asm volatile("" ::: "memory");
#pragma unroll
        for (int i = 0; i < 4; ++i) {
          cs[i] = stm[256 + wc * 96 + 32 + 16 * i + c16];
          m4[i] = *(const f32x4*)(stm + 64 * wr + 16 * i + 4 * g4); i4[i] = *(const f32x4*)(stm + 128 + 64 * wr + 16 * i + 4 * g4);
        }
        SB0();
        if (__all(ok != 0)) break;
      }
      const u32x4 ONES4 = {0x3F803F80u, 0x3F803F80u, 0x3F803F80u, 0x3F803F80u};
      f32x2 tk[16], tv[16];
#define PR(v, i) ((f32x2){(v)[2 * (i)], (v)[2 * (i) + 1]})
#pragma unroll
      for (int i = 0; i < 16; ++i) {
        tk[i] = PR(acc[(i >> 1) & 3][2 + (i >> 3)], i & 1) - PR(m4[(i >> 1) & 3], i & 1) * cs[i >> 3];
        tv[i] = PR(acc[(i >> 1) & 3][4 + (i >> 3)], i & 1) - PR(m4[(i >> 1) & 3], i & 1) * cs[2 + (i >> 3)];
      }
      SB0();
#pragma unroll
      for (int i = 0; i < 16; ++i) { tk[i] *= PR(i4[(i >> 1) & 3], i & 1); tv[i] *= PR(i4[(i >> 1) & 3], i & 1); }
      SB0();
      unsigned kp[16], vp[16];
#pragma unroll
      for (int i = 0; i < 16; ++i) vp[i] = pack2o(tv[i][0], tv[i][1]);
      phi_n<16>(tk, kp);
#define F4_(p, i) ((u32x4){(p)[4 * (i)], (p)[4 * (i) + 1], (p)[4 * (i) + 2], (p)[4 * (i) + 3]})
      const f32x4 Z4 = {0.f, 0.f, 0.f, 0.f};
      u32x4 vk[2], vg[2];
#pragma unroll
      for (int j = 0; j < 2; ++j) { vk[j] = wr ? F4_(vp, 2 + j) : F4_(vp, j); vg[j] = wr ? F4_(vp, j) : F4_(vp, 2 + j); }
      f32x4 keep[2], give[2], zq[2];
#pragma unroll
      for (int d = 0; d < 2; ++d) { give[d] = mfma16(F4_(kp, 2 * d), vg[0], Z4); zq[d] = mfma16(ONES4, F4_(kp, 2 * d), Z4); }
#pragma unroll
      for (int d = 0; d < 2; ++d) { give[d] = mfma16(F4_(kp, 2 * d + 1), vg[1], give[d]); zq[d] = mfma16(ONES4, F4_(kp, 2 * d + 1), zq[d]); }
#pragma unroll
      for (int d = 0; d < 2; ++d) keep[d] = mfma16(F4_(kp, 2 * d), vk[0], Z4);
#pragma unroll
      for (int d = 0; d < 2; ++d) keep[d] = mfma16(F4_(kp, 2 * d + 1), vk[1], keep[d]);
      SB0();
      const float zv = (lane & 16) ? zq[1][0] : zq[0][0];
      char* xk = smem + L_XK + wave * 2304 + lane * 16;
      const char* xp = smem + L_XK + (wave ^ 2) * 2304 + lane * 16;
      *(f32x4*)xk = give[0]; *(f32x4*)(xk + 1024) = give[1]; *(float*)(xk + 2048 - lane * 12) = zv;
      asm volatile("" ::: "memory");
      misc[16 + wave] = 1;
      f32x4 p0, p1; float pz;
      SPIN {
        const int ok = misc[16 + (wave ^ 2)];
        asm volatile("" ::: "memory");
        p0 = *(const f32x4*)xp; p1 = *(const f32x4*)(xp + 1024); pz = *(const float*)(xp + 2048 - lane * 12);
        SB0();
        if (ok == 1) break;
      }
      STW(sane(__builtin_bit_cast(u32x4, keep[0] + p0)), kvp_off);
      STW(sane(__builtin_bit_cast(u32x4, keep[1] + p1)), kvp_off + 1024);
      STW1(sane1(__float_as_uint(zv + pz)), (lane < 32 && wr == 0) ? zp_off + lane * 4 : OOB);
    }
    STAMPT(0, 3, 0);
    {
      u32x4 qa[4][4]; u32x2 ql[4][2], qh[4][2];
#pragma unroll
      for (int u = 0; u < 4; ++u) {
#pragma unroll
        for (int j = 0; j < 2; ++j) {
          ql[u][j] = trread(smem + bo[1][j] + (u >> 1) * K1_STAGE + (u & 1) * 12288);
          qh[u][j] = trread(smem + bo[1][j] + (u >> 1) * K1_STAGE + (u & 1) * 12288 + 512);
        }
#pragma unroll
        for (int i = 0; i < 4; ++i) qa[u][i] = *(const u32x4*)(smem + ao[1][u & 1] + (u >> 1) * K1_STAGE + i * 2048);
      }
      asm volatile("" ::: "memory");
      misc[8 + wave] = 3;
      SB0();
#pragma unroll
      for (int u = 0; u < 4; ++u)
#pragma unroll
        for (int j = 0; j < 2; ++j)
#pragma unroll
          for (int i = 0; i < 4; ++i) acc[i][j] = mfma16((u32x4){ql[u][j][0], ql[u][j][1], qh[u][j][0], qh[u][j][1]}, qa[u][i], acc[i][j]);
      SB0();
    }
    unsigned qp[16];
    Phi<16> qs;
    const unsigned po = WS_KVP + ((2 * hp + (rb >> 3)) * 16 + h) * 4096 + (2 * ((rb >> 2) & 1) + ((lane >> 4) & 1)) * 1024 + (16 * (rb & 3) + (lane & 15)) * 16;
    u32x4 pv[8] = {};
    {
      f32x4 c4[2]; float m_b[4], rinv[4];
#pragma unroll
      for (int i = 0; i < 2; ++i) c4[i] = *(const f32x4*)(stm + 256 + wc * 96 + 16 * i + 4 * g4);
#pragma unroll
      for (int i = 0; i < 4; ++i) { m_b[i] = stm[64 * wr + 16 * i + c16]; rinv[i] = stm[128 + 64 * wr + 16 * i + c16]; }
      SB0();
      f32x2 tq[16];
#pragma unroll
      for (int i = 0; i < 16; ++i) tq[i] = PR(acc[i >> 2][(i >> 1) & 1], i & 1) - PR(c4[(i >> 1) & 1], i & 1) * m_b[i >> 2];
      SB0();
#pragma unroll
      for (int i = 0; i < 16; ++i) tq[i] *= rinv[i >> 2];
      SB0();
      phi_a<16>(tq, qs);
#pragma unroll
      for (int i = 0; i < 16; ++i) asm volatile("" : "+v"(qs.tb[i]), "+v"(qs.tr[i]), "+v"(qs.e[i]));
    }
    if (wave == 0) {
      SLEEP(P_KVP);
#pragma unroll
      for (int p = 0; p < 8; ++p) pv[p] = LDW(po + p * 8192);
    }
    asm volatile("" ::: "memory");
    STAMPT(0, 4, 0);
    if (wave == 1 && rb < 2) {
      SLEEP(P_KVP);
      SPIN {
        unsigned m = 0;
#pragma unroll
        for (int p = 0; p < 2; ++p) { pv[p] = LDW(WS_ZP + (2 * hp + rb) * 2048 + p * 1024 + lane * 16); m = chk(max(max(m, pv[p][0]), pv[p][2]), pv[p]); }
        if (wave_ok(m)) break;
        asm volatile("" ::: "memory");
      }
      f32x4 s = __builtin_bit_cast(f32x4, pv[0]) + __builtin_bit_cast(f32x4, pv[1]);
#pragma unroll
      for (int i = 0; i < 4; ++i) { s[i] += dppf<DPP_ROR8>(s[i]); s[i] = xh_sum(x16_sum(s[i])); }
      if (lane < 16) st64(ws, WS_ZF + (2 * hp + rb) * 128 + (lane >> 3) * 64 + (lane & 3) * 16 + ((lane >> 2) & 1) * 8,
                          ((u64)sane1(pack2(s[2], s[3])) << 32) | pack2(s[0], s[1]));
    }
    if (wave == 0) {
      SPIN {
        unsigned m = 0;
#pragma unroll
        for (int p = 0; p < 8; ++p) m = chk(m, pv[p]);
        CNT(0);
        if (wave_ok(m)) break;
        asm volatile("" ::: "memory");
#pragma unroll
        for (int p = 0; p < 8; ++p) pv[p] = LDW(po + p * 8192);
      }
      f32x4 s = __builtin_bit_cast(f32x4, pv[0]);
#pragma unroll
      for (int p = 1; p < 8; ++p) s += __builtin_bit_cast(f32x4, pv[p]);
      const unsigned p0 = pack2(xh_sum(s[0]), xh_sum(s[1])), p1 = pack2(xh_sum(s[2]), xh_sum(s[3]));
      auto a = __builtin_amdgcn_permlane16_swap(p0, p0, false, false);
      auto b = __builtin_amdgcn_permlane16_swap(p1, p1, false, false);
      STW(sane((u32x4){a[0], b[0], a[1], b[1]}), lane < 16 ? WS_KVF + hp * 4096 + rb * 256 + lane * 16 : OOB);
      STAMPT(1, 2, 0);
    }
    phi_b<16>(qs, qp);
#pragma unroll
    for (int i = 0; i < 16; ++i) asm volatile("" : "+v"(qp[i]));
    if (wave == 0) misc[7] = 1;
    else SPIN { if (misc[7] == 1) break; __builtin_amdgcn_s_sleep(1); }
    SLEEP(P_KVF);
    {
      u32x4 k0, k1, z0;
      SPIN {
        k0 = LDW(WS_KVF + head * 2048 + lane * 16); k1 = LDW(WS_KVF + head * 2048 + 1024 + lane * 16);
        z0 = LDW(WS_ZF + head * 128 + g4 * 16);
        CNT(1);
        if (wave_ok(chk(chk(chk(0u, k0), k1), z0))) break;
        asm volatile("" ::: "memory");
      }
      STAMPT(0, 5, 0);
      const f32x4 Z4 = {0.f, 0.f, 0.f, 0.f};
      f32x4 X[8], Dn[4];
#pragma unroll
      for (int i = 0; i < 4; ++i) Dn[i] = mfma16(z0, F4_(qp, i), Z4);
#pragma unroll
      for (int i = 0; i < 8; ++i) X[i] = mfma16((i & 1) ? k1 : k0, F4_(qp, i >> 1), Z4);
      SB0();
      float ri[4]; unsigned a[16]; f32x2 f[16];
#pragma unroll
      for (int i = 0; i < 4; ++i) ri[i] = __builtin_amdgcn_rcpf(rbf(rbf(Dn[i][0]) + 1e-6f));
      SB0();
#pragma unroll
      for (int i = 0; i < 16; ++i) a[i] = pack2o(X[i >> 1][2 * (i & 1)], X[i >> 1][2 * (i & 1) + 1]);
      SB0();
#pragma unroll
      for (int i = 0; i < 16; ++i) f[i] = unpk(a[i]);
      SB0();
#pragma unroll
      for (int i = 0; i < 16; ++i) f[i] *= ri[i >> 2];
      SB0();
#pragma unroll
      for (int i = 0; i < 4; ++i) {
        STW(((u32x4){pack2(f[4 * i][0], f[4 * i][1]), pack2(f[4 * i + 1][0], f[4 * i + 1][1]), pack2(f[4 * i + 2][0], f[4 * i + 2][1]), pack2(f[4 * i + 3][0], f[4 * i + 3][1])}),
            o_off + (i >> 1) * 65536 + (i & 1) * 1024);
        SB0();
      }
    }
    STAMPT(0, 6, 0);
    {
      EPI();
      const int rt = wave & 1, kh = wave >> 1;
      const unsigned ob = WS_O + ((rp * 2 + rt) * 32 + 16 * kh) * 2048 + lane * 16;
#define LDO(t, st) __builtin_amdgcn_raw_buffer_load_b128(wsr, ob + (st) * 1024, whead(t) * 2048, 16)
      int woff[2];
#pragma unroll
      for (int i = 0; i < 2; ++i) woff[i] = kh * 8192 + (g4 >> 1) * 2048 + (g4 & 1) * 512 + trl + ((i ^ (g4 & 1)) * 32);
      struct Frag2 { u32x2 lo[8], hi[8]; };
      Frag2 G[2];
      auto ld2 = [&](Frag2& N, int sn, int nt, int hi) {
        const char* p = smem + sn + woff[nt & 1] + (nt >> 2) * 1024 + ((nt >> 1) & 1) * 256;
        if (hi) N.hi[nt] = trread(p + 4096); else N.lo[nt] = trread(p);
      };
      WAIT_V(0);
      STW(((u32x4){1u, 1u, 1u, 1u}), fb_off);
      RAW_BAR();
#pragma unroll
      for (int i = 0; i < 16; ++i) ld2(G[0], wo_slot(0), i >> 1, i & 1);
      f32x4 a2[2][8];
#pragma unroll
      for (int i = 0; i < 16; ++i) a2[i & 1][i >> 1] = (f32x4){0.f, 0.f, 0.f, 0.f};
      SLEEP(P_FB);
      SPIN { CNT(2); if (wave_ok(ld32(ws, WS_FB + (rp * 32 + 16 * kh + (lane & 15)) * 128))) break; }
      asm volatile("" ::: "memory");
      STAMPT(0, 7, 0);
      char* pr[2];
#pragma unroll
      for (int i = 0; i < 2; ++i) pr[i] = smem + L_P + kh * L_PK + (32 * rt + c16) * 512 + (((4 * i + g4) ^ (c16 & 7)) * 16);
      u32x4 of[16][2];
#pragma unroll
      for (int t = 0; t < OW; ++t) { of[t][0] = LDO(t, 0); of[t][1] = LDO(t, 1); }
      SB0();
      sfor<0, 16>([&](auto T) {
        constexpr int t = T.v;
        Frag2& C = G[t & 1]; Frag2& N = G[(t + 1) & 1];
#pragma unroll
        for (int i = 0; i < 16; ++i) {
          const int nt = i >> 1, st = i & 1;
          a2[st][nt] = mfma16((u32x4){C.lo[nt][0], C.lo[nt][1], C.hi[nt][0], C.hi[nt][1]}, of[t][st], a2[st][nt]);
          SB0();
          if ((t & 1) && t <= 11 && i == 8) {
            asm volatile("s_waitcnt lgkmcnt(8)" ::: "memory"); __builtin_amdgcn_s_barrier(); asm volatile("" ::: "memory");
          }
          if (t < 15) ld2(N, wo_slot(t + 1), nt, st);
          if (t > 0 && t - 1 + OW < 16 && (i == 3 || i == 11)) of[(t - 1 + OW) & 15][i >> 3] = LDO(t - 1 + OW, i >> 3);
          if (t - 1 + OW == 16 && (i == 3 || i == 11)) { xv[i >> 3] = *(const u32x4*)(x + ego + (i >> 3) * 64); wv[i >> 3] = *(const u32x4*)(w_r + cb * 128 + eseg * 8 + (i >> 3) * 64); }
          if (t == 15 && i >= 4) *(f32x4*)(pr[(nt - 2) & 1] + ((nt - 2) >> 1) * 128 + st * 8192) = a2[st][(nt - 2) & 7];
          SB0();
        }
      });
      STAMPT(0, 8, 0);
#pragma unroll
      for (int i = 12; i < 16; ++i) *(f32x4*)(pr[(i >> 1) & 1] + (i >> 2) * 128 + (i & 1) * 8192) = a2[i & 1][i >> 1];
    }
    unpack_xw();
    __builtin_amdgcn_s_setprio(0);
  } else {
    LANES();
    const int sw = wave - 4, t2 = tid - 256;
    __builtin_amdgcn_s_setprio(3);
    const bool real = hs_ != 7;
    unsigned hs_g0 = 0, hs_cur = 0;
    u32x4 ms_exp = {0, 0, 0, 0};
    bool hs_act = true, ms_act = !real && hp < 8 && rb == 0 && sw == 1;
    const unsigned hs_off = (lane == 1 ? SY_GO : SY_CH) + bid * 4;
    const unsigned re_off = t2 == 0 ? SY_RE + bid * 4 : OOB;
    const unsigned ms_off = lane * 16;
    const char* wsu = uni(ws);
#define HS_PROBE() asm volatile("s_nop 4\n\tglobal_load_dword v156, %0, %1 sc1" :: "v"(hs_off), "s"(wsu) : "memory", RSV)
#define MS_PROBE() asm volatile("s_nop 4\n\tglobal_load_dwordx4 v[152:155], %0, %1 offset:2048 sc1" :: "v"(ms_off), "s"(wsu) : "memory", RSV)
    auto hs_step = [&](bool first) {
      if (hs_act) {
        const unsigned hv_ = rget<156>();
        hs_act = __builtin_amdgcn_readlane(hv_, 1) == hs_g0;
        const unsigned c = __builtin_amdgcn_readlane(hv_, 0);
        if (sw == 0 && (first || c != hs_cur)) STW1(hv_, re_off);
        hs_cur = c;
        HS_PROBE();
      }
      if (ms_act) {
        const unsigned d = ((rget<152>() ^ ms_exp[0]) | (rget<153>() ^ ms_exp[1])) | ((rget<154>() ^ ms_exp[2]) | (rget<155>() ^ ms_exp[3]));
        ms_act = __any(d != 0u);
        STW(ms_exp, ms_act ? OOB : SY_GO + ms_off);
        MS_PROBE();
      }
    };
    HS_PROBE();
    if (ms_act) {
      MS_PROBE();
      asm volatile("s_nop 4\n\tglobal_load_dwordx4 v[148:151], %0, %1 offset:1024 sc1" :: "v"(ms_off), "s"(wsu) : "memory", RSV);
    }
    const int gr = (g8 >> 3) * 7 + min(hs_, 6);
    const unsigned voG = (unsigned)((lane >> 3) * 8 * F_ + (lane & 7) * 8) * 2u, voD = (unsigned)((lane >> 3) * 8 * D_ + (lane & 7) * 8) * 2u;
    const size_t gofs = ((size_t)(kq * 256 + sw * 64) * F_ + gr * 64) * 2;
    const char* pG = (const char*)wg + gofs;
    const char* pU = (const char*)wu + gofs;
    const char* pD = (const char*)(wd + (size_t)(gr * 64) * D_ + kq * 256 + sw * 64);
    auto wstep = [&](auto I) {
      constexpr int i = I.v;
      if (i < 8) wload<VW + 4 * i>(voG, pG + (i & 7) * (F_ * 2));
      else if (i < 16) wload<VW + 4 * i>(voG, pU + (i & 7) * (F_ * 2));
      else wload2<VW + 4 * i>(voD, pD + (i & 7) * (D_ * 2));
    };
    unsigned voW;
    {
      const int bi = lane >> 4, q = (lane >> 2) & 3, ch = lane & 3;
      voW = (unsigned)((4 * (bi >> 1) + q) * D_ + (bi & 1) * 32 + (ch ^ (bi & 2)) * 8) * 2u;
    }
    const char* baseW = (const char*)(wo + (size_t)((16 * (sw >> 1)) * 32 + 16 * (sw & 1)) * D_ + cb * 128) - 1024;
    auto stageW = [&](auto J, bool rsv) {
      const char* s_ = baseW + (size_t)whead(J.v) * (32 * D_ * 2);
      if (rsv) glds4r<wo_slot(J.v)>(voW + 1024, voW + 128, voW + 8 * D_ * 2 - 1024, voW + 8 * D_ * 2 + 128 - 2048, s_, l4);
      else glds4<wo_slot(J.v)>(voW + 1024, voW + 128, voW + 8 * D_ * 2 - 1024, voW + 8 * D_ * 2 + 128 - 2048, s_, l4);
    };
    int sta[2], stb[3];
    {
#pragma unroll
      for (int c = 0; c < 2; ++c) { sta[c] = (posA & 3) * K1_STAGE + (32 * sw + (lane & 15)) * 128 + (((4 * c + (lane >> 4)) ^ ((lane & 15) >> 1)) * 16); asm volatile("" : "+v"(sta[c]) :: RSV); }
#pragma unroll
      for (int i = 0; i < 3; ++i) {
        const int G = sw * 3 + i, hd = G >= 6 ? 1 : 0, mat = (G - 6 * hd) >> 1;
        stb[i] = (posB & 3) * K1_STAGE + 16384 + (lane >> 4) * 6144 + mat * 1024 + hd * 256 + ((lane & 15) >> 2) * 64 + (G & 1) * 32 + (lane & 3) * 8;
        asm volatile("" : "+v"(stb[i]) :: RSV);
      }
    }
    unsigned rs_dst = (lane >> 4) == ((lane & 15) >> 2) ? rs_slot + (32 * sw + (lane & 15)) * 8 : OOB, cs_dst = lane < 48 ? cs_slot + (sw * 48 + lane) * 4 : OOB;
    unsigned rs_off = WS_RS + rb * 16384 + (lane >> 4) * 1024 + sw * 256 + (lane & 15) * 16;
    unsigned cs_off = WS_CS + hp * 12288 + (lane >> 4) * 768 + sw * 192 + min(lane & 15, 11) * 16;
    asm volatile("" : "+v"(rs_dst), "+v"(cs_dst), "+v"(rs_off), "+v"(cs_off) :: RSV);
    int stm_dst = ((lane >> 5) * 128 + sw * 32 + (lane & 15) * 2 + ((lane >> 4) & 1)) * 4, cs_put = (lane & 15) < 12 ? (256 + sw * 48 + (lane & 15) * 4 + (lane >> 4)) * 4 : 1792 + lane * 4;
    asm volatile("" : "+v"(stm_dst), "+v"(cs_put) :: RSV);
    auto st_load = [&]() { sfor<0, 4>([&](auto I) { sload<224 + 4 * I.v>(rs_off, wsu + I.v * 4096); sload<240 + 4 * I.v>(cs_off, wsu + I.v * 3072); }); };
    sfor<3, GS[0]>(group);
    WAIT_VR(gops(GS[0]) - 10 + 1); RAW_BARR();
    TSL(14);
    sfor<GS[0], GS[1]>(group);
    WAIT_VR(gops(GS[1]) - 20); RAW_BARR();
    hs_g0 = __builtin_amdgcn_readlane(rget<156>(), 1);
    if (ms_act) {
      const u32x4 re = {rget<152>(), rget<153>(), rget<154>(), rget<155>()}, g0 = {rget<148>(), rget<149>(), rget<150>(), rget<151>()};
#pragma unroll
      for (int j = 0; j < 4; ++j) { const unsigned c = re[j] + 1u; ms_exp[j] = (c == g0[j]) ? c + 1u : c; }
      STW(ms_exp, SY_CH + ms_off);
    }
    u32x4 ra[4] = {}; u32x2 rc[3][4] = {};
    auto chunk = [&](auto C) {
      const u32x4 ONES4 = {0x3F803F80u, 0x3F803F80u, 0x3F803F80u, 0x3F803F80u};
      const f32x4 Z4 = {0.f, 0.f, 0.f, 0.f};
      if constexpr (C.v == 0) {
        asm volatile("" : "+v"(ra[0]), "+v"(ra[1]), "+v"(ra[2]), "+v"(ra[3]) :: RSV);
        f32x4 s[2], q[2];
#pragma unroll
        for (int i = 0; i < 2; ++i) { q[i] = mfma16(ra[2 * i], ra[2 * i], Z4); s[i] = mfma16(ONES4, ra[2 * i], Z4); }
#pragma unroll
        for (int i = 0; i < 2; ++i) { q[i] = mfma16(ra[2 * i + 1], ra[2 * i + 1], q[i]); s[i] = mfma16(ONES4, ra[2 * i + 1], s[i]); }
#pragma unroll
        for (int i = 0; i < 2; ++i) {
          float a = q[i][0], c = q[i][1], d = q[i][2], e = q[i][3];
          asm volatile("" : "+v"(a), "+v"(c), "+v"(d), "+v"(e) :: RSV);
          const float lo = (lane & 1) ? c : a, hi = (lane & 1) ? e : d;
          __builtin_amdgcn_raw_buffer_store_b64((u32x2){sane1(__float_as_uint(s[i][0])), sane1(__float_as_uint((lane & 2) ? hi : lo))}, wsr, rs_dst + i * 128, 0, 16);
        }
      } else {
        f32x4 c[3];
#pragma unroll
        for (int i = 0; i < 3; ++i) { asm volatile("" : "+v"(rc[i][0]), "+v"(rc[i][1]), "+v"(rc[i][2]), "+v"(rc[i][3]) :: RSV); c[i] = mfma16(ONES4, (u32x4){rc[i][0][0], rc[i][0][1], rc[i][1][0], rc[i][1][1]}, Z4); }
#pragma unroll
        for (int i = 0; i < 3; ++i) c[i] = mfma16(ONES4, (u32x4){rc[i][2][0], rc[i][2][1], rc[i][3][0], rc[i][3][1]}, c[i]);
        float c0 = c[0][0], c1 = c[1][0], c2 = c[2][0];
        asm volatile("" : "+v"(c0), "+v"(c1), "+v"(c2) :: RSV);
        STW1(sane1(__float_as_uint(lane < 16 ? c0 : lane < 32 ? c1 : c2)), cs_dst);
      }
    };
    sfor<0, 12>([&](auto T) {
      constexpr int t = T.v;
      TSL(t);
      if (t == 4) STAMPT(1, 9, 256);
      if (t < 4 && posA == t + 1) {
#pragma unroll
        for (int c = 0; c < 4; ++c) ra[c] = *(const u32x4*)(smem + sta[c & 1] + (c >> 1) * 2048);
      }
      if (t < 4 && posB == t + 1) {
#pragma unroll
        for (int i = 0; i < 3; ++i)
#pragma unroll
          for (int j = 0; j < 4; ++j) rc[i][j] = trread(smem + (stb[i] ^ ((j >> 1) * 32)) + (j >> 1) * 3072 + (j & 1) * 512);
      }
      if (t == 2) HS_PROBE();
      if (t >= 4) hs_step(t == 4);
      sfor<GS[t + 1], GS[t + 2]>([&](auto G) {
        group(G);
        if constexpr ((t == 4 || t == 5) && G.v == GS[t + 1]) chunk(IC<t - 4>{});
      });
      if (real) sfor<WS_[t], WS_[t + 1]>(wstep);
      TSL(16 + t);
      WAIT_VR(t < 11 ? gops(GS[t + 2]) - 10 * (t + 3) : gops(GS[13]) - gops(GS[12]));
      TSL(32 + t);
      RAW_BARR();
    });
    TSL(12);
    STAMPT(1, 10, 256);
    hs_step(false);
    auto st_put = [&]() -> bool {
      float a[8]; unsigned m = 0, n = 0;
      rsum4<224>(a, m, n); rsum4<240>(a + 4, m, n);
      const float s1 = sw16(a[0], a[2]), q1 = sw16(a[1], a[3]);
      const float cs = sw32(sw16(a[4], a[5]), sw16(a[6], a[7]));
      const float mean = sw32(s1, s1) * (1.0f / 1024.0f);
      const float var = fmaxf(__builtin_fmaf(sw32(q1, q1), 1.0f / 1024.0f, -(mean * mean)), 0.f);
      const float rinv = __builtin_amdgcn_rcpf(rbf(__builtin_amdgcn_sqrtf(rbf(rbf(var) + 1e-5f))));
      *(float*)(smem + L_STM + stm_dst) = (lane & 32) ? rinv : rbf(mean);
      *(float*)(smem + L_STM + cs_put) = cs;
      return wave_ok(max(m, n));
    };
    st_load();
    if (real) sfor<WS_[12], W1A>(wstep);
    TSL(28);
    if (real) WAIT_VR(8 + W1A - WS_[11]); else WAIT_VR(8);
    if (lane == 0) { misc[8 + sw] = 0; misc[12 + sw] = 0; misc[16 + sw] = 0; misc[7] = 0; }
    TSL(44);
    RAW_BARR();
    TSL(13); TSDUMP(1 + sw);
    STAMPT(1, 0, 256);
    if (real) WAIT_VR(W1A - WS_[12]); else WAIT_VR(0);
    if (!st_put() || hs_act) {
      if (hs_act) hs_act = __builtin_amdgcn_readlane(rget<156>(), 1) == hs_g0;
      if (ms_act) {
        SPIN {
          bool ok = true;
#pragma unroll
          for (int j = 0; j < 4; ++j) ok &= (ld32(ws, SY_RE + lane * 16 + j * 4) == ms_exp[j]);
          if (__all(ok)) break;
        }
        STW(ms_exp, SY_GO + lane * 16);
      }
      if (hs_act) {
        SPIN {
          if (ld32(ws, SY_GO + bid * 4) != hs_g0) break;
          const unsigned c = ld32(ws, SY_CH + bid * 4);
          if (c != hs_cur) { hs_cur = c; STW1(c, re_off); }
        }
      }
      SPIN { st_load(); WAIT_VR(0); if (st_put()) break; }
    }
    asm volatile("" ::: "memory", RSV);
    if (lane == 0) misc[12 + sw] = 1;
    __builtin_amdgcn_s_setprio(0);
    if (real) sfor<W1A, 16>(wstep);
    auto mwait = [&](int v) { SPIN { const int a = misc[8 + (lane & 3)]; if (__all(a >= v)) break; __builtin_amdgcn_s_sleep(1); } asm volatile("" ::: "memory"); };
    sfor<0, 9>([&](auto J) {
      if (J.v == 4) mwait(3);
      stageW(J, J.v < 2);
      if (J.v >= 1) WAIT_V(4 * PACE);
      if (J.v == 1) { WAIT_VR(8); sfor<0, 16>(wtr); }
    });
    STAMPT(1, 1, 256);
    EPI();
#pragma unroll
    for (int i = 0; i < 2; ++i) { xv[i] = *(const u32x4*)(x + ego + i * 64); wv[i] = *(const u32x4*)(w_r + cb * 128 + eseg * 8 + i * 64); }
    asm volatile("s_waitcnt vmcnt(0)" : "+v"(xv[0]), "+v"(xv[1]), "+v"(wv[0]), "+v"(wv[1]) :: "memory");
    unpack_xw();
    RAW_BAR();
    RAW_BAR(); stageW(IC<9>{}, false); stageW(IC<10>{}, false);
    RAW_BAR(); stageW(IC<11>{}, false); stageW(IC<12>{}, false);
    RAW_BAR(); stageW(IC<13>{}, false); stageW(IC<14>{}, false);
    WAIT_V(16); RAW_BAR(); stageW(IC<15>{}, false);
    WAIT_V(12); RAW_BAR();
    WAIT_V(0); RAW_BAR();
    if (real) sfor<16, 24>(wstep);
  }
  LANES(); EPI();
  const int grow = rp * 64 + erow;
  int pa0 = L_P + erow * 512 + ((2 * eseg) ^ (erow & 7)) * 16, pa1 = pa0 ^ 16;
  unsigned st_dst = eseg == 0 ? WS_ST2 + ((rp * 8 + cb) * 64 + erow) * 16 : OOB, st_src = WS_ST2 + rp * 8192 + lane * 16;
  unsigned fd_dst = lane < 8 ? WS_FD + (cb * 32 + rp) * 128 + lane * 16 : OOB, kidx = 0xFFFFu - grow;
  asm volatile("" : "+v"(pa0), "+v"(pa1), "+v"(st_dst), "+v"(st_src), "+v"(fd_dst), "+v"(kidx));
  RAW_BAR2();
  STAMPT(0, 9, 0);
  unsigned hb[8];
  f32x2 hv[8];
  {
    f32x4 pp[2][4];
#pragma unroll
    for (int k = 0; k < 2; ++k)
#pragma unroll
      for (int i = 0; i < 4; ++i) pp[k][i] = *(const f32x4*)(smem + ((i & 1) ? pa1 : pa0) + (i >> 1) * 256 + k * L_PK);
    SB0();
    f32x2 s = {0.f, 0.f}, q = {0.f, 0.f}, p2 = {0.f, 0.f};
#pragma unroll
    for (int i = 0; i < 8; ++i) {
      const f32x4 a = pp[0][i >> 1] + pp[1][i >> 1];
      const f32x2 u = xf[i] + unpk(pack2o(a[2 * (i & 1)], a[2 * (i & 1) + 1]));
      hb[i] = pack2o(u[0], u[1]);
      hv[i] = unpk(hb[i]);
      s += hv[i]; q += hv[i] * hv[i]; p2 += hv[i] * wf2[i];
    }
    STW(sane((u32x4){__float_as_uint(oct_sum(s[0] + s[1])), __float_as_uint(oct_sum(q[0] + q[1])), __float_as_uint(oct_sum(p2[0] + p2[1])), __float_as_uint(sw_)}), st_dst);
  }
  KEEP2();
  STAMPT(0, 10, 0);
  const int sw = wave - 4;
  const unsigned fd_src = WS_FD + xcd * 4096 + r * 128;
  u32x2 kpk = {SENT, SENT};
  if (wave == 0) {
    u32x4 pa[8];
    SLEEP(P_C);
    SPIN {
      unsigned m = 0;
#pragma unroll
      for (int j = 0; j < 8; ++j) { pa[j] = LDW(st_src + j * 1024); m = chk(m, pa[j]); }
      CNT(3);
      if (wave_ok(m)) break;
      KEEP2();
    }
    STAMPT(0, 11, 0);
#define F4(v) __builtin_bit_cast(f32x4, v)
    const f32x4 t = ((F4(pa[0]) + F4(pa[1])) + (F4(pa[2]) + F4(pa[3]))) + ((F4(pa[4]) + F4(pa[5])) + (F4(pa[6]) + F4(pa[7])));
    const float mean = t[0] * (1.0f / 1024.0f);
    const float var = fmaxf(__builtin_fmaf(t[1], 1.0f / 1024.0f, -(mean * mean)), 0.f);
    const float mb = rbf(mean), rs = __builtin_amdgcn_rcpf(rbf(__builtin_amdgcn_sqrtf(rbf(rbf(var) + 1e-5f))));
    *(f32x2*)(smem + L_KX + lane * 8) = (f32x2){mb, rs};
    const float sc = fmaxf((t[2] - mb * t[3]) * rs, -INFINITY);
    const int u = (int)(pack2(0.f, sc) & 0xffff0000u);
    unsigned b0 = ((unsigned)(u ^ ((u >> 31) | (int)0x80000000)) & 0xffff0000u) | (0xFFFFu - (rp * 64 + lane)), b1 = 0;
    top2_lo(b0, b1); top2_hi(b0, b1);
    STW(((u32x4){b0, b1, b0, b1}), fd_dst);
    if (lane == 0) *(u32x2*)(smem + L_MISC + 112) = (u32x2){b0, b1};
  } else if (wave >= 4) {
    asm volatile("s_waitcnt vmcnt(0)" ::: "memory", RSV2);
    sfor<16, 24>(wtr);
  }
  RAW_BAR();
  bool cand;
  u32x4 ob[2];
  {
    const f32x2 mr = *(const f32x2*)(smem + L_KX + erow * 8);
    const u32x2 kk = *(const u32x2*)(smem + L_MISC + 112);
    const float mb = mr[0], rs = mr[1];
    const unsigned b0 = kk[0], b1 = kk[1];
    u32x4 nq[2];
#pragma unroll
    for (int i = 0; i < 8; ++i) {
      const f32x2 d = hv[i] - mb;
      const f32x2 n = unpk(pack2o(d[0], d[1])) * rs;
      nq[i >> 2][i & 3] = pack2o(n[0], n[1]);
    }
    const bool c2 = (b1 & 0xffffu) == kidx;
    cand = c2 || (b0 & 0xffffu) == kidx;
    if (cand) {
      const unsigned xo = WS_XS + (rp * 2 + (c2 ? 1 : 0)) * 2048 + cb * 256 + eseg * 16;
      STW(sane(nq[0]), xo); STW(sane(nq[1]), xo + 128);
      char* cp = smem + L_CAND + (c2 ? 512 : 0) + eseg * 16;
      *(u32x4*)cp = (u32x4){hb[0], hb[1], hb[2], hb[3]}; *(u32x4*)(cp + 128) = (u32x4){hb[4], hb[5], hb[6], hb[7]};
      *(u32x4*)(cp + 256) = nq[0]; *(u32x4*)(cp + 384) = nq[1];
    }
    asm volatile("" ::: "memory");
    if (wave >= 4) { SLEEP(P_D); kpk = __builtin_amdgcn_raw_buffer_load_b64(wsr, fd_src, 0, 16); }
    asm volatile("" ::: "memory");
#pragma unroll
    for (int i = 0; i < 8; ++i) { const f32x2 o = hv[i] + unpk(nq[i >> 2][i & 3]); ob[i >> 2][i & 3] = pack2(o[0], o[1]); }
  }
#define OUT_ST() do { *(u32x4*)(out + ego) = ob[0]; *(u32x4*)(out + ego + 64) = ob[1]; } while (0)
  STAMPT(0, 12, 0);
  if (wave < 4) {
    if (!cand) OUT_ST();
    RAW_BAR();
    const unsigned w0 = misc[24], w1 = misc[25];
    if (cand && grow != (int)key_idx(w0) && grow != (int)key_idx(w1)) OUT_ST();
#pragma unroll 1
    for (int tk = 0; tk < 2; ++tk) {
      const unsigned wk = tk ? w1 : w0, slot = misc[26 + tk];
      if ((int)(slot >> 1) != rp) continue;
      u32x4 pv[7];
      const unsigned yo = WS_YP + ((tk * 8 + cb) * 64 + (lane >> 3) * 8) * 512 + wave * 128 + (lane & 7) * 16;
      const int col = (wave >> 1) * 64 + (lane & 7) * 8 + (wave & 1) * 4 + (lane >> 4);
      float sg = rbf(__builtin_amdgcn_rcpf(1.0f + __expf(-key_val(wk))));
      float hh = bflo(*(const u16*)(smem + L_CAND + (slot & 1) * 512 + col * 2)), nn = bflo(*(const u16*)(smem + L_CAND + (slot & 1) * 512 + 256 + col * 2));
      u16* op = out + (size_t)key_idx(wk) * D_ + cb * 128 + col;
      asm volatile("" : "+v"(sg), "+v"(hh), "+v"(nn), "+v"(op));
      if (tk == 0 || (int)(misc[26] >> 1) != rp) {
        SPIN { if (misc[30] == (int)w0) break; __builtin_amdgcn_s_sleep(1); }
        SLEEP(P_E);
      }
      SPIN {
        unsigned m = 0;
#pragma unroll
        for (int p = 0; p < 7; ++p) { pv[p] = LDW(yo + p * 512); m = chk(m, pv[p]); }
        CNT(6);
        if (wave_ok(m)) break;
        asm volatile("" ::: "memory");
      }
      STAMPT(0, 13, 0);
      f32x4 s = __builtin_bit_cast(f32x4, pv[0]);
#pragma unroll
      for (int p = 1; p < 7; ++p) s += __builtin_bit_cast(f32x4, pv[p]);
      float y = sw32(sw16(s[0], s[1]), sw16(s[2], s[3]));
      y += dppf<DPP_ROR8>(y);
      if (!(lane & 8)) *(GAS u16*)op = (u16)pack2(hh + rbf(nn + rbf(rbf(y) * sg)), 0.f);
      STAMPT(0, 14, 0);
    }
  } else {
    unsigned xl = WS_XS + (kq * 256 + sw * 64 + (lane >> 4) * 16 + ((lane >> 2) & 1) * 8) * 2;
    int red_dst = (lane < 8 || (lane >= 24 && lane < 32)) ? L_RED + sw * 2048 + ((lane >> 3) & 1) * 1024 + (lane & 7) * 16 : L_RED + 8192 + lane * 16;
    unsigned gu_src = WS_GU + g8 * 4096 + lane * 16;
    int act_dst = L_ACT + sw * 256 + ((lane & 7) * 8 + (lane >> 3)) * 2, act_src = L_ACT + sw * 256 + (lane & 1) * 128 + (lane >> 4) * 32 + ((lane >> 1) & 1) * 16;
    unsigned yp_dst = lane < 16 ? WS_YP + (((lane >> 3) * 8 + 2 * kq + (sw >> 1)) * 64 + g8) * 512 + (sw & 1) * 256 + (lane & 7) * 16 : OOB;
    asm volatile("" : "+v"(xl), "+v"(red_dst), "+v"(gu_src), "+v"(act_dst), "+v"(act_src), "+v"(yp_dst));
    unsigned w0, w1; int s0 = -1, s1 = -1;
    u32x4 xa = {SENT, SENT, SENT, SENT};
    SPIN {
      CNT(4);
      const bool all = wave_ok(kpk[1]);
      const unsigned k0 = kpk[1] == SENT ? 0u : kpk[0], k1 = kpk[1] == SENT ? 0u : kpk[1];
      w0 = k0; w1 = k1;
      top2_lo(w0, w1); top2_mid(w0, w1);
      const int n0 = 2 * __builtin_ctzll(__ballot(k0 == w0));
      const u64 m1 = __ballot(k0 == w1);
      const int n1 = m1 ? 2 * __builtin_ctzll(m1) : n0 + 1;
      const bool same = n0 == s0 && n1 == s1;
      s0 = n0; s1 = n1;
      asm volatile("" ::: "memory");
      if (all && same) break;
      if (!all) { kpk = __builtin_amdgcn_raw_buffer_load_b64(wsr, fd_src, 0, 16); }
      xa = LDW(xl + ((lane & 1) ? s1 : s0) * 2048);
      if (all) break;
    }
    SPIN {
      if (wave_ok(chk(0u, xa))) break;
      asm volatile("" ::: "memory");
      xa = LDW(xl + ((lane & 1) ? s1 : s0) * 2048);
    }
    STAMPT(1, 3, 256);
    {
      const u32x4 z4 = {0u, 0u, 0u, 0u};
      const u32x4 xg = (lane & 2) ? z4 : xa, xu = (lane & 2) ? xa : z4;
      f32x4 d[8];
#pragma unroll
      for (int c = 0; c < 8; ++c) d[c] = mfma16(xg, wf[c], (f32x4){0.f, 0.f, 0.f, 0.f});
      if (tid == 256) { *(u32x4*)(smem + L_MISC + 96) = (u32x4){w0, w1, (unsigned)s0, (unsigned)s1}; misc[30] = 0; }
      SB0();
#pragma unroll
      for (int c = 0; c < 10; ++c) {
        if (c < 8) d[c & 7] = mfma16(xu, wf[8 + (c & 7)], d[c & 7]);
        if (c >= 2) *(f32x4*)(smem + red_dst + (c - 2) * 128) = d[(c - 2) & 7];
        SB0();
      }
    }
    RAW_BAR();
    if (sw == 0) {
      f32x4 t[8];
#pragma unroll
      for (int p = 0; p < 8; ++p) t[p] = *(const f32x4*)(smem + L_RED + p * 1024 + lane * 16);
      SB0();
      STW(sane(__builtin_bit_cast(u32x4, ((t[0] + t[1]) + (t[2] + t[3])) + ((t[4] + t[5]) + (t[6] + t[7])))), gu_src + kq * 1024);
    } else __builtin_amdgcn_s_sleep(2);
    STAMPT(1, 4, 256);
    {
      u32x4 q[4];
      if (grow != (int)key_idx(w0) && grow != (int)key_idx(w1)) OUT_ST();
      asm volatile("" ::: "memory");
      SLEEP(P_GU);
      SPIN {
        unsigned m = 0;
#pragma unroll
        for (int j = 0; j < 4; ++j) { q[j] = LDW(gu_src + j * 1024); m = chk(m, q[j]); }
        CNT(5);
        if (wave_ok(m)) break;
        asm volatile("" ::: "memory");
      }
      STAMPT(1, 5, 256);
      const f32x4 tot = (__builtin_bit_cast(f32x4, q[0]) + __builtin_bit_cast(f32x4, q[1])) + (__builtin_bit_cast(f32x4, q[2]) + __builtin_bit_cast(f32x4, q[3]));
      const f32x2 gs = unpk(pack2o(tot[0], tot[1])), us = unpk(pack2o(tot[2], tot[3]));
      const f32x2 sg = unpk(pack2o(gs[0] * __builtin_amdgcn_rcpf(1.0f + __expf(-gs[0])), gs[1] * __builtin_amdgcn_rcpf(1.0f + __expf(-gs[1]))));
      const unsigned a2 = pack2o(sg[0] * us[0], sg[1] * us[1]);
      *(u16*)(smem + act_dst) = (u16)a2; *(u16*)(smem + act_dst + 128) = (u16)(a2 >> 16);
      __builtin_amdgcn_fence(__ATOMIC_RELEASE, "wavefront"); __builtin_amdgcn_wave_barrier(); __builtin_amdgcn_fence(__ATOMIC_ACQUIRE, "wavefront");
      const u32x4 aa = *(const u32x4*)(smem + act_src);
      f32x4 d[8];
#pragma unroll
      for (int c = 0; c < 8; ++c) d[c] = mfma16(aa, wf[16 + c], (f32x4){0.f, 0.f, 0.f, 0.f});
      SB0();
      u32x4 y[2];
#pragma unroll
      for (int c = 0; c < 8; ++c) {
        const float t0 = d[c][0] + dppf<DPP_ROR8>(d[c][2]), t1 = d[c][3] + dppf<DPP_ROR8>(d[c][1]);
        y[c >> 2][c & 3] = __float_as_uint((lane & 8) ? t1 : t0);
      }
      STW(sane(y[0]), yp_dst); STW(sane(y[1]), yp_dst + 128);
      if (tid == 256) misc[30] = w0;
    }
    STAMPT(1, 6, 256);
  }
}

extern "C" void kernel_launch(void* const* d_in, const int* in_sizes, int n_in, void* d_out, int out_size,
                              void* d_ws, size_t ws_size, hipStream_t stream) {
  (void)hipFuncSetAttribute((const void*)fused, hipFuncAttributeMaxDynamicSharedMemorySize, LDS_TOTAL);
  fused<<<256, 512, LDS_TOTAL, stream>>>((const u16*)d_in[0], (const u16*)d_in[1], (const u16*)d_in[2], (const u16*)d_in[4],
                                         (const u16*)d_in[5], (const u16*)d_in[6], (const u16*)d_in[7], (u16*)d_out, (char*)d_ws);
}
```

```cpp
#include <hip/hip_runtime.h>
#include <stdint.h>

#define LAS __attribute__((address_space(3)))
#define GAS __attribute__((address_space(1)))
#define DI __device__ __forceinline__

typedef unsigned short u16;
typedef unsigned long long u64;
typedef __attribute__((ext_vector_type(2))) __bf16 bf16x2;
typedef __attribute__((ext_vector_type(8))) __bf16 bf16x8;
typedef __attribute__((ext_vector_type(4))) short s16x4;
typedef __attribute__((ext_vector_type(2))) unsigned short u16x2;
typedef __attribute__((ext_vector_type(4))) float f32x4;
typedef __attribute__((ext_vector_type(2))) float f32x2;
typedef __attribute__((ext_vector_type(4))) unsigned u32x4;
typedef __attribute__((ext_vector_type(2))) unsigned u32x2;
typedef GAS unsigned gu32;
typedef GAS unsigned long long gu64;

constexpr int S_ = 2048, D_ = 1024, F_ = 3584;

constexpr unsigned SY_CH = 0, SY_GO = 1024, SY_RE = 2048;
constexpr unsigned WS_FD  = 98304;
constexpr unsigned WS_ZF  = 8192;
constexpr unsigned WS_KVF = 16384;
constexpr unsigned WS_RS  = 131072;
constexpr unsigned WS_CS  = WS_RS + 16 * 16 * 1024;
constexpr unsigned WS_ZP  = WS_CS + 16 * 16 * 768;
constexpr unsigned WS_FB  = WS_ZP + 32 * 16 * 128;
constexpr unsigned WS_ST2 = WS_FB + 32 * 32 * 128;
constexpr unsigned WS_XS  = WS_ST2 + 32 * 8 * 1024;
constexpr unsigned WS_GU  = WS_XS + 64 * 2048;
constexpr unsigned WS_YP  = WS_GU + 256 * 1024;
constexpr unsigned WS_KVP = WS_YP + 2 * 8 * 64 * 512;
constexpr unsigned WS_O   = WS_KVP + 32 * 16 * 4096;
constexpr unsigned WS_END = WS_O + S_ * D_ * 2;
constexpr unsigned OOB = 0x80000000u;

template <int V> struct IC { static constexpr int v = V; };
template <int B, int E, class F> DI void sfor(F&& f) { if constexpr (B < E) { f(IC<B>{}); sfor<B + 1, E>(f); } }

DI float bflo(unsigned u) { return __uint_as_float(u << 16); }
DI float bfhi(unsigned u) { return __uint_as_float(u & 0xffff0000u); }
DI float rbf(float x) { return (float)(__bf16)x; }
DI unsigned pack2(float a, float b) { bf16x2 c; c[0] = (__bf16)a; c[1] = (__bf16)b; return __builtin_bit_cast(unsigned, c); }
DI unsigned pack2o(float a, float b) { unsigned p = pack2(a, b); asm("" : "+v"(p)); return p; }
DI f32x4 mfma16(u32x4 a, u32x4 b, f32x4 c) {
  return __builtin_amdgcn_mfma_f32_16x16x32_bf16(__builtin_bit_cast(bf16x8, a), __builtin_bit_cast(bf16x8, b), c, 0, 0, 0);
}
DI void mfma16i(u32x4 a, u32x4 b, f32x4& c) { asm volatile("v_mfma_f32_16x16x32_bf16 %0, %1, %2, %0" : "+v"(c) : "v"(a), "v"(b)); }
DI f32x2 unpk(unsigned u) { return (f32x2){bflo(u), bfhi(u)}; }
DI u32x2 trread(const char* p) {
  s16x4 v = __builtin_amdgcn_ds_read_tr16_b64_v4i16((LAS s16x4*)p);
  return __builtin_bit_cast(u32x2, v);
}
DI const char* uni(const void* p) {
  const u64 v = (u64)p;
  return (const char*)(((u64)(unsigned)__builtin_amdgcn_readfirstlane((int)(v >> 32)) << 32) | (unsigned)__builtin_amdgcn_readfirstlane((int)v));
}
#define RV4(a, b, c, d) "v" #a, "v" #b, "v" #c, "v" #d
#define RV10(p) "v" #p "0", "v" #p "1", "v" #p "2", "v" #p "3", "v" #p "4", "v" #p "5", "v" #p "6", "v" #p "7", "v" #p "8", "v" #p "9"
#define RSV2 RV4(224, 225, 226, 227), "v228", "v229", RV10(23), RV10(24), RV4(250, 251, 252, 253), "v254", "v255"
#define RSV1 RV4(192, 193, 194, 195), RV4(196, 197, 198, 199), RV10(20), RV10(21), RV4(220, 221, 222, 223), RSV2
#define RSV "v148", "v149", RV10(15), RV10(16), RV10(17), RV10(18), "v190", "v191", RSV1
#define NTW " nt"
#define GL_(v, s, o) "global_load_lds_dwordx4 " v ", " s " offset:" #o
#define GLDS4(SC, CL...) asm volatile("s_add_u32 m0, %5, %c6\n\ts_nop 3\n\t" GL_("%0", "%4", 0) SC "\n\t" GL_("%1", "%4", 1024) SC "\n\t" \
  GL_("%2", "%4", 2048) SC "\n\t" GL_("%3", "%4", 3072) SC :: "v"(v0), "v"(v1), "v"(v2), "v"(v3), "s"(uni(sbase)), "s"(lds), "i"(IMM) : "memory", "scc", ##CL)
#define GLDS3(SC, CL...) asm volatile("s_add_u32 m0, %4, %c5\n\ts_nop 3\n\t" GL_("%0", "%3", 0) SC "\n\t" GL_("%1", "%3", 1024) SC "\n\t" \
  GL_("%2", "%3", 2048) SC :: "v"(v0), "v"(v1), "v"(v2), "s"(uni(sbase)), "s"(lds), "i"(IMM) : "memory", "scc", ##CL)
template <int IMM> DI void glds4(unsigned v0, unsigned v1, unsigned v2, unsigned v3, const void* sbase, unsigned lds) { GLDS4(""); }
template <int IMM> DI void glds4r(unsigned v0, unsigned v1, unsigned v2, unsigned v3, const void* sbase, unsigned lds) { GLDS4("", RSV); }
template <int IMM> DI void glds3r(unsigned v0, unsigned v1, unsigned v2, const void* sbase, unsigned lds) { GLDS3("", RSV); }
template <int R> DI void sload(unsigned voff, const void* sbase) {
  asm volatile("s_nop 4\n\tglobal_load_dwordx4 v[%c0:%c1], %2, %3 sc1" ::"i"(R), "i"(R + 3), "v"(voff), "s"(uni(sbase)) : "memory", RSV);
}
DI float fadd(float a, float b) { float o; asm("v_add_f32 %0, %1, %2" : "=v"(o) : "v"(a), "v"(b)); return o; }
#define RA_(o, a, b) "v_add_f32 %" #o ", v[%c6+" #a "], v[%c6+" #b "]\n\t"
#define RB2_(o, a) "v_add_f32 %" #o ", v[%c6+" #a "], %" #o "\n\t"
#define RM_(o, a, b) "v_max3_u32 %" #o ", v[%c6+" #a "], v[%c6+" #b "], %" #o "\n\t"
template <int R> DI void rsum4(float* a, unsigned& m, unsigned& n) {
  asm volatile(RA_(0, 0, 4) RA_(1, 1, 5) RM_(4, 0, 1) RA_(2, 2, 6) RA_(3, 3, 7) RM_(5, 2, 3) RB2_(0, 8) RM_(4, 4, 5) RB2_(1, 9) RM_(5, 6, 7)
               RB2_(2, 10) RM_(4, 8, 9) RB2_(3, 11) RM_(5, 10, 11) RB2_(0, 12) RB2_(1, 13) RM_(4, 12, 13) RB2_(2, 14) RB2_(3, 15) RM_(5, 14, 15)
               : "=&v"(a[0]), "=&v"(a[1]), "=&v"(a[2]), "=&v"(a[3]), "+v"(m), "+v"(n) : "i"(R) : RSV);
}
template <int R> DI void wload2(unsigned voff, const void* sbase) {
  asm volatile("s_nop 4\n\tglobal_load_dwordx4 v[%c0:%c1], %2, %3" NTW ::"i"(R), "i"(R + 3), "v"(voff), "s"(uni(sbase)) : "memory", RSV2);
}
template <int R> DI void wload(unsigned voff, const void* sbase) {
  asm volatile("s_nop 4\n\tglobal_load_dwordx4 v[%c0:%c1], %2, %3" NTW ::"i"(R), "i"(R + 3), "v"(voff), "s"(uni(sbase)) : "memory", RSV);
}
template <int RA, int RB> DI unsigned wperm(unsigned sel) {
  unsigned o;
  if (RA < 192) asm volatile("v_perm_b32 %0, v%c2, v%c1, %3" : "=v"(o) : "i"(RA), "i"(RB), "s"(sel) : RSV);
  else if (RA < 224) asm volatile("v_perm_b32 %0, v%c2, v%c1, %3" : "=v"(o) : "i"(RA), "i"(RB), "s"(sel) : RSV1);
  else asm volatile("v_perm_b32 %0, v%c2, v%c1, %3" : "=v"(o) : "i"(RA), "i"(RB), "s"(sel) : RSV2);
  return o;
}
template <int R> DI unsigned rget() { unsigned o; asm volatile("v_mov_b32 %0, v%c1" : "=v"(o) : "i"(R) : RSV); return o; }
DI float xh_sum(float v) {
  auto r = __builtin_amdgcn_permlane32_swap(__float_as_uint(v), __float_as_uint(v), false, false);
  return __uint_as_float(r[0]) + __uint_as_float(r[1]);
}
template <int CTRL> DI unsigned dppu(unsigned v) { return (unsigned)__builtin_amdgcn_update_dpp(0, (int)v, CTRL, 0xf, 0xf, true); }
template <int CTRL> DI float dppf(float v) { return __uint_as_float(dppu<CTRL>(__float_as_uint(v))); }
constexpr int DPP_X1 = 0xB1, DPP_X2 = 0x4E, DPP_HMIR = 0x141, DPP_MIR = 0x140, DPP_ROR8 = 0x128;
DI float quad_sum(float v) { v += dppf<DPP_X1>(v); v += dppf<DPP_X2>(v); return v; }
DI float oct_sum(float v) { v = quad_sum(v); v += dppf<DPP_HMIR>(v); return v; }
DI float x16_sum(float v) {
  auto r = __builtin_amdgcn_permlane16_swap(__float_as_uint(v), __float_as_uint(v), false, false);
  return __uint_as_float(r[0]) + __uint_as_float(r[1]);
}
DI float sw16(float a, float b) {
  auto r = __builtin_amdgcn_permlane16_swap(__float_as_uint(a), __float_as_uint(b), false, false);
  return fadd(__uint_as_float(r[0]), __uint_as_float(r[1]));
}
DI float sw32(float a, float b) {
  auto r = __builtin_amdgcn_permlane32_swap(__float_as_uint(a), __float_as_uint(b), false, false);
  return fadd(__uint_as_float(r[0]), __uint_as_float(r[1]));
}
template <int N> struct Phi { unsigned tb[N]; f32x2 tr[N], e[N]; };
template <int N> DI void phi_a(const f32x2* t, Phi<N>& s) {
#pragma unroll
  for (int i = 0; i < N; ++i) s.tb[i] = pack2o(t[i][0], t[i][1]);
  __builtin_amdgcn_sched_barrier(0);
#pragma unroll
  for (int i = 0; i < N; ++i) s.tr[i] = unpk(s.tb[i]);
  __builtin_amdgcn_sched_barrier(0);
#pragma unroll
  for (int i = 0; i < N; ++i) s.e[i] = s.tr[i] * 1.44269504088896340736f;
  __builtin_amdgcn_sched_barrier(0);
}
template <int N> DI void phi_b(Phi<N>& s, unsigned* p) {
  u16x2 tm[2] = {{0, 0}, {0, 0}};
#pragma unroll
  for (int i = 0; i < N; ++i) {
    s.e[i][0] = __builtin_amdgcn_fmed3f(__builtin_amdgcn_exp2f(s.e[i][0]), 0.f, 1.f);
    tm[i & 1] = __builtin_elementwise_max(tm[i & 1], __builtin_bit_cast(u16x2, s.tb[i]));
    s.e[i][1] = __builtin_amdgcn_fmed3f(__builtin_amdgcn_exp2f(s.e[i][1]), 0.f, 1.f);
    __builtin_amdgcn_sched_barrier(0);
  }
#pragma unroll
  for (int i = 0; i < N; ++i) s.tr[i] += s.e[i];
  __builtin_amdgcn_sched_barrier(0);
#pragma unroll
  for (int i = 0; i < N; ++i) { s.tr[i][0] = fmaxf(s.tr[i][0], s.e[i][0]); s.tr[i][1] = fmaxf(s.tr[i][1], s.e[i][1]); }
  const u16x2 tx = __builtin_elementwise_max(tm[0], tm[1]);
  __builtin_amdgcn_sched_barrier(0);
#pragma unroll
  for (int i = 0; i < N; ++i) p[i] = pack2(s.tr[i][0], s.tr[i][1]);
  if (__any(tx[0] > 0xC0F0 || tx[1] > 0xC0F0)) {
#pragma unroll
    for (int i = 0; i < N; ++i) p[i] = pack2(bflo(p[i]) + 1e-6f, bfhi(p[i]) + 1e-6f);
  }
}
template <int N> DI void phi_n(const f32x2* t, unsigned* p) { Phi<N> s; phi_a<N>(t, s); phi_b<N>(s, p); }
#define RLX_AGENT __ATOMIC_RELAXED, __HIP_MEMORY_SCOPE_AGENT
DI void st64(char* ws, unsigned off, u64 v) { __hip_atomic_store((gu64*)(ws + off), v, RLX_AGENT); }
DI unsigned ld32(char* ws, unsigned off) { return __hip_atomic_load((gu32*)(ws + off), RLX_AGENT); }
#define WAIT_V(n) asm volatile("s_waitcnt vmcnt(%c0)" ::"i"(n) : "memory")
#define RAW_BAR() do { asm volatile("s_waitcnt lgkmcnt(0)" ::: "memory"); __builtin_amdgcn_s_barrier(); asm volatile("" ::: "memory"); } while (0)
#define SB0() __builtin_amdgcn_sched_barrier(0)
#define WAIT_VR(n) asm volatile("s_waitcnt vmcnt(%c0)" ::"i"(n) : "memory", RSV)
#define RAW_BARR() do { asm volatile("s_waitcnt lgkmcnt(0)" ::: "memory", RSV); __builtin_amdgcn_s_barrier(); asm volatile("" ::: "memory", RSV); } while (0)
#define KEEP2() asm volatile("" ::: "memory", RSV2)
#define RAW_BAR2() do { asm volatile("s_waitcnt lgkmcnt(0)" ::: "memory", RSV2); __builtin_amdgcn_s_barrier(); KEEP2(); } while (0)
constexpr unsigned SPIN_CAP = 1u << 18;
#define SPIN _Pragma("nounroll") for (unsigned s_ = 0; s_ < SPIN_CAP; ++s_)
constexpr unsigned SENT = 0xFFFFFFFFu;
DI unsigned sane1(unsigned v) { return min(v, 0xFFFFFFFEu); }
DI u32x4 sane(u32x4 v) { v[1] = sane1(v[1]); v[3] = sane1(v[3]); return v; }
DI unsigned chk(unsigned m, u32x4 v) { return max(max(m, v[1]), v[3]); }
DI bool wave_ok(unsigned m) { return !__any(m == SENT); }
DI unsigned key_idx(unsigned k) { return (0xFFFFu - (k & 0xFFFFu)) & (S_ - 1); }
DI float key_val(unsigned k) { const unsigned o = k >> 16; return __uint_as_float(((o & 0x8000u) ? (o & 0x7fffu) : (~o & 0xffffu)) << 16); }
DI void top2_mix(unsigned& b0, unsigned& b1, unsigned c0, unsigned c1) {
  const unsigned lo = min(b0, c0), mx = max(b1, c1);
  b0 = max(b0, c0); b1 = max(lo, mx);
}
DI void top2_lo(unsigned& b0, unsigned& b1) {
  top2_mix(b0, b1, dppu<DPP_X1>(b0), dppu<DPP_X1>(b1));
  top2_mix(b0, b1, dppu<DPP_X2>(b0), dppu<DPP_X2>(b1));
  top2_mix(b0, b1, dppu<DPP_HMIR>(b0), dppu<DPP_HMIR>(b1));
}
DI void top2_mid(unsigned& b0, unsigned& b1) {
  top2_mix(b0, b1, dppu<DPP_MIR>(b0), dppu<DPP_MIR>(b1));
  auto p = __builtin_amdgcn_permlane16_swap(b0, b0, false, false); auto q = __builtin_amdgcn_permlane16_swap(b1, b1, false, false);
  b0 = p[0]; b1 = q[0]; top2_mix(b0, b1, p[1], q[1]);
}
DI void top2_hi(unsigned& b0, unsigned& b1) {
  top2_mid(b0, b1);
  auto p = __builtin_amdgcn_permlane32_swap(b0, b0, false, false); auto q = __builtin_amdgcn_permlane32_swap(b1, b1, false, false);
  b0 = p[0]; b1 = q[0]; top2_mix(b0, b1, p[1], q[1]);
}

constexpr int K1_STAGE = 40960;
constexpr int LDS_TOTAL = 4 * K1_STAGE;
constexpr int L_STM = 0;
constexpr int L_MISC = 2048;
constexpr int L_XK = 5120;
constexpr int L_KX = 3328;
constexpr int L_ACT = 2304;
constexpr int L_CAND = 4096;
constexpr int L_WO = 16384;
constexpr int L_P = 16384, L_PK = 7 * 16384;
constexpr int L_RED = 65536;
constexpr int wo_slot(int j) { return L_WO + (j % 9) * 16384; }
constexpr int CODE_LINES = 12;
#define OW_ 8
constexpr int OW = OW_;
#define W1A_ 16
constexpr int W1A = W1A_;
#define GSCHED 5, 9, 13, 17, 21, 24, 27, 30, 33, 36, 39, 42, 45, 48, 48
constexpr int GS[15] = {GSCHED};
constexpr int gops(int g) { return (g / 3) * 10 + (g % 3 == 0 ? 0 : g % 3 == 1 ? 4 : 7); }
constexpr bool gs_ok() {
  bool ok = GS[0] >= 3 && GS[0] <= 9 && GS[1] >= 6 && GS[1] <= 12 && GS[0] <= GS[1] && GS[12] == 45 && GS[13] == 48 && GS[14] == 48;
  for (int t = 0; t < 12; ++t) ok = ok && GS[t + 2] <= 3 * (t + 5) && GS[t + 2] >= 3 * (t + 3) && GS[t + 2] >= GS[t + 1] + 3;
  return ok;
}
static_assert(gs_ok(), "staging schedule");
#define WSCHED 0, 0, 0, 0, 0, 0, 0, 0, 0, 2, 4, 6, 8
constexpr int WS_[13] = {WSCHED};
#define PACE 1
#define P_KVP 1
#define P_KVF 1
#define P_FB 8
#define P_C 14
#define P_D 2
#define P_GU 12
#define P_E 10
#define SLEEP(n) do { if (n) __builtin_amdgcn_s_sleep(n); } while (0)
constexpr int VW = 160;

__global__ __launch_bounds__(512, 2) void fused(const u16* __restrict__ x, const u16* __restrict__ wqkv,
                                                const u16* __restrict__ wo, const u16* __restrict__ w_r,
                                                const u16* __restrict__ wg, const u16* __restrict__ wu,
                                                const u16* __restrict__ wd, u16* __restrict__ out, char* __restrict__ ws) {
  extern __shared__ __attribute__((aligned(16))) char smem[];
  u64 pc0; asm volatile("s_getpc_b64 %0" : "=s"(pc0));
  const int wave = __builtin_amdgcn_readfirstlane(threadIdx.x >> 6);
  const unsigned lds0 = (unsigned)(size_t)smem;
  if (wave == 0) {
    const int l = threadIdx.x;
    asm volatile("s_mov_b32 m0, %1\n\ts_nop 0\n\tglobal_load_lds_dword %0, off"
                 :: "v"((const char*)(pc0 - (pc0 & 127)) + ((blockIdx.x >> 3) + (l < CODE_LINES ? l : 0) * 32) * 128), "s"(__builtin_amdgcn_readfirstlane(lds0 + 3 * K1_STAGE)) : "memory");
  }
#define RB_(b) (((b) & 1) * 8 + (((b) >> 3) & 7))
#define HP_(b) ((((b) & 7) >> 1) * 4 + ((b) >> 6))
#define KROT_(b) ((4 * ((HP_(b) >> 2) + (RB_(b) >> 3) + 2 * ((RB_(b) >> 2) & 1))) & 15)
  unsigned voA0, voA1, voA2, voA3, voB0, voB1, voB2, voB3, voB4, voB5;
  const unsigned l4 = __builtin_amdgcn_readfirstlane(lds0 + (wave - 4) * 4096), l6 = __builtin_amdgcn_readfirstlane(lds0 + (wave - 4) * 6144);
  const int krot0 = KROT_(blockIdx.x);
  auto group = [&](auto G) {
    constexpr int t = G.v / 3, p = G.v % 3, sl = (t & 3) * K1_STAGE;
    const int kt = (t + krot0) & 15;
    if (p == 0) glds4r<sl>(voA0, voA1, voA2, voA3, (const char*)x + kt * 128, l4);
    else if (p == 1) glds3r<sl + 16384>(voB0, voB1, voB2, (const char*)wqkv + kt * 64 * (3 * D_ * 2), l6);
    else glds3r<sl + 16384 + 3072>(voB3, voB4, voB5, (const char*)wqkv + kt * 64 * (3 * D_ * 2), l6);
  };
  if (wave >= 4) {
    const int lane = threadIdx.x & 63, sw = wave - 4;
    const int r0 = RB_(blockIdx.x) * 128 + 32 * sw + (lane >> 3);
    voA0 = (unsigned)(r0 * D_ + ((lane & 7) ^ (lane >> 4)) * 8) * 2u;
    voA1 = ((voA0 + 8 * D_ * 2) ^ 64) - 1024; voA2 = voA0 + 16 * D_ * 2 - 2048; voA3 = voA1 + 16 * D_ * 2 - 2048;
    asm volatile("" : "+v"(voA0), "+v"(voA1), "+v"(voA2), "+v"(voA3));
    group(IC<0>{});
    int l2 = lane; asm volatile("" : "+v"(l2));
    voB0 = (unsigned)((16 * sw + 4 * (l2 >> 5) + ((l2 >> 2) & 3)) * (3 * D_) + HP_(blockIdx.x) * 64 + ((l2 >> 4) & 1) * 32 + (l2 & 3) * 8) * 2u;
    voB1 = voB0 + 1024; voB2 = voB0 + 2048;
    voB3 = (voB0 ^ 32) + 8 * 3 * D_ * 2; voB4 = voB3 + 1024; voB5 = voB3 + 2048;
    asm volatile("" : "+v"(voB0), "+v"(voB1), "+v"(voB2), "+v"(voB3), "+v"(voB4), "+v"(voB5));
    sfor<1, 3>(group);
  }
  int bid = blockIdx.x; asm volatile("" : "+s"(bid));
  const int xcd = bid & 7, idx = bid >> 3;
  const int rb = RB_(bid), hp = HP_(bid);
  const int rp = xcd * 4 + (idx >> 3), cb = idx & 7;
  const int hs_ = (hp + 4 + (int)(((size_t)wqkv >> 7) & 7)) & 7;
  const int g8 = ((hp >> 3) * 4 + (rb >> 2)) * 8 + hs_, kq = rb & 3;
  const int krot = KROT_(bid), posA = 1 + (hp & 3), posB = 1 + (rb & 3);
  const int wrot = 4 * rp + (xcd >> 1), wdir = (xcd & 1) ? -1 : 1;
  auto whead = [&](int t) { return (wrot + wdir * t) & 15; };
  const unsigned rs_slot = WS_RS + (rb * 16 + ((krot + posA) & 15)) * 1024, cs_slot = WS_CS + (hp * 16 + ((krot + posB) & 15)) * 768;
  const __amdgpu_buffer_rsrc_t wsr = __builtin_amdgcn_make_buffer_rsrc(ws, 0, WS_END, 0x00020000);
#define LDW(off) __builtin_amdgcn_raw_buffer_load_b128(wsr, (off), 0, 16)
#define STW(v, off) __builtin_amdgcn_raw_buffer_store_b128((v), wsr, (off), 0, 16)
#define STW1(v, off) __builtin_amdgcn_raw_buffer_store_b32((v), wsr, (off), 0, 16)
  float* stm = (float*)(smem + L_STM);
  volatile LAS int* misc = (volatile LAS int*)(smem + L_MISC);
#define LANES() int tid = threadIdx.x; asm volatile("" : "+v"(tid)); const int lane = tid & 63, r = lane & 31, h = lane >> 5; (void)r; (void)h
#define EPI() const int erow = tid >> 3, eseg = tid & 7; const size_t ego = (size_t)(rp * 64 + erow) * D_ + cb * 128 + eseg * 8
  u32x4 xv[2], wv[2];
  f32x2 xf[8], wf2[8];
  float sw_;
  auto unpack_xw = [&]() {
    f32x2 a = {0.f, 0.f};
#pragma unroll
    for (int i = 0; i < 8; ++i) { xf[i] = unpk(xv[i >> 2][i & 3]); wf2[i] = unpk(wv[i >> 2][i & 3]); a += wf2[i]; }
    sw_ = oct_sum(a[0] + a[1]);
#pragma unroll
    for (int i = 0; i < 8; ++i) asm volatile("" : "+v"(xf[i]), "+v"(wf2[i]));
    asm volatile("" : "+v"(sw_));
  };
  u32x4 wf[24];

  auto wtr = [&](auto I) {
    constexpr int m = I.v >> 3, c = I.v & 7, R = VW + 32 * m + (c >> 1);
    const unsigned sel = (c & 1) ? 0x07060302u : 0x05040100u;
    wf[I.v] = (u32x4){wperm<R, R + 4>(sel), wperm<R + 8, R + 12>(sel), wperm<R + 16, R + 20>(sel), wperm<R + 24, R + 28>(sel)};
  };

  if (wave < 4) {
    LANES();
    const int c16 = lane & 15, g4 = lane >> 4;
    const int trl = (c16 >> 2) * 64 + (c16 & 3) * 8;
    const int wr = wave >> 1, wc = wave & 1;
    const int head = 2 * hp + wc;
    const unsigned kvp_off = WS_KVP + (head * 16 + rb) * 4096 + wr * 2048 + lane * 16;
    const unsigned zp_off = WS_ZP + (head * 16 + rb) * 128;
    const unsigned o_off = WS_O + ((rb * 4 + 2 * wr) * 32 + head) * 2048 + lane * 16;
    const unsigned fb_off = lane < 8 ? WS_FB + ((rb * 2 + wr) * 32 + head) * 128 + lane * 16 : OOB;
    unsigned pfa, pfb = OOB;
    unsigned pfc = lane < 8 ? fb_off : lane < 24 ? rs_slot + wave * 256 + (lane - 8) * 16
        : lane < 40 ? (wave < 3 ? cs_slot + wave * 256 : WS_KVF + hp * 4096 + rb * 256) + (lane - 24) * 16
        : lane < 48 ? (wave < 2 ? zp_off + (lane & 7) * 16 : (wave == 2 && rb < 2) ? WS_ZF + (2 * hp + rb) * 128 + (lane & 7) * 16 : OOB) : OOB;
    if (wave == 0) pfa = WS_ST2 + (rp * 8 + cb) * 1024 + lane * 16;
    else if (wave == 1) pfa = WS_GU + (g8 * 4 + kq) * 1024 + lane * 16;
    else pfa = WS_YP + (((wave - 2) * 8 + 2 * kq + (lane >> 5)) * 64 + g8) * 512 + (lane & 31) * 16;
    if (wave == 0) pfb = lane < 32 ? WS_XS + (rp * 2 + (lane >> 4)) * 2048 + cb * 256 + (lane & 15) * 16 : lane < 40 ? WS_FD + (cb * 32 + rp) * 128 + (lane & 7) * 16 : OOB;
    u32x4 S4 = {SENT, SENT, SENT, SENT};
    asm volatile("" : "+v"(pfa), "+v"(pfb), "+v"(pfc), "+v"(S4));
    __builtin_amdgcn_s_setprio(1);
    f32x4 acc[4][6];
    int ao[2][2], bo[2][2];
    {
#pragma unroll
      for (int i = 0; i < 2; ++i) {
        ao[0][i] = (64 * wr + c16) * 128 + (((4 * i + g4) ^ (c16 >> 1)) * 16); ao[1][i] = ao[0][i] + 2 * K1_STAGE;
        bo[0][i] = 16384 + g4 * 3072 + wc * 256 + trl + ((i ^ (g4 & 1)) * 32); bo[1][i] = bo[0][i] + 2 * K1_STAGE;
        asm volatile("" : "+v"(ao[0][i]), "+v"(ao[1][i]), "+v"(bo[0][i]), "+v"(bo[1][i]));
      }
#pragma unroll
      for (int i = 0; i < 4; ++i)
#pragma unroll
        for (int j = 0; j < 6; ++j) { acc[i][j] = (f32x4){0.f, 0.f, 0.f, 0.f}; asm volatile("" : "+v"(acc[i][j])); }
      struct Frag { u32x4 a[4]; u32x2 bl[6], bh[6]; };
      Frag FA, FB;
#define K1_B(j) ((u32x4){C.bl[j][0], C.bl[j][1], C.bh[j][0], C.bh[j][1]})
#define K1_LA(i) N.a[i] = *(const u32x4*)(smem + ao[sn >> 1][hf] + (sn & 1) * K1_STAGE + (i) * 2048)
#define K1_LL(j) N.bl[j] = trread(smem + bo[sn >> 1][(j) & 1] + (sn & 1) * K1_STAGE + hf * 12288 + ((j) >> 1) * 1024)
#define K1_LH(j) N.bh[j] = trread(smem + bo[sn >> 1][(j) & 1] + (sn & 1) * K1_STAGE + hf * 12288 + ((j) >> 1) * 1024 + 512)
#define K1_M(i, j) if ((j) < 2) mfma16i(K1_B(j), C.a[i], acc[i][j]); else mfma16i(C.a[i], K1_B(j), acc[i][j]); SB0()
      auto sub = [&](Frag& C, Frag& N, int sn, int hf) {
        sfor<0, 24>([&](auto Q) {
          constexpr int q = Q.v;
          constexpr int j = q / 4, i = (j & 1) ? 3 - q % 4 : q % 4;
          if (q == 23) asm volatile("s_waitcnt lgkmcnt(0)" : "+v"(N.bh[5]));
          K1_M(i, j);
          if constexpr (q < 4) { K1_LA(q); } else if constexpr (q < 16) { if (q & 1) { K1_LH((q - 4) >> 1); } else { K1_LL((q - 4) >> 1); } }
          SB0();
        });
      };
      auto subkv = [&](Frag& C, Frag& N, int sn, int hf, bool ld) {
#define K1_QL(i, j, l) K1_M(i, j); if (ld) { l; } SB0()
        K1_QL(0, 2, K1_LL(2)); K1_QL(1, 2, K1_LH(2)); K1_QL(0, 3, K1_LA(0)); K1_QL(1, 3, K1_LA(1)); K1_QL(2, 2, K1_LL(3)); K1_QL(3, 2, K1_LH(3));
        K1_QL(2, 3, K1_LA(2)); K1_QL(3, 3, K1_LA(3)); K1_QL(0, 4, K1_LL(4)); K1_QL(1, 4, K1_LH(4)); K1_QL(2, 4, K1_LL(5)); K1_QL(3, 4, K1_LH(5));
        K1_M(0, 5); K1_M(1, 5); K1_M(2, 5); K1_M(3, 5);
      };
      WAIT_V(0);
      RAW_BAR();
      {
        Frag& N = FA; const int sn = 0, hf = 0;
        K1_LL(0); K1_LH(0); K1_LA(0); K1_LA(1); K1_LL(1); K1_LH(1); K1_LL(2); K1_LH(2); K1_LL(3); K1_LH(3); K1_LA(2); K1_LA(3); K1_LL(4); K1_LH(4); K1_LL(5); K1_LH(5);
      }
#pragma unroll 1
      for (int it = 0; it < 4; ++it) {
#pragma unroll
        for (int s = 0; s < 4; ++s) {
          if (s == 2 && it == 3) break;
          sub(FA, FB, s, 1);
          if (s == 0) WAIT_V(0);
          if (it == 0) { if (s == 1) STW(S4, pfc); if (s == 2) STW(S4, kvp_off); if (s == 3) STW(S4, kvp_off + 1024); }
          if (it == 1) { if (s == 0) STW(S4, pfa); if (s == 1) STW(S4, pfb); }
          if (s != 1 || it < 3) RAW_BAR();
          sub(FB, FA, (s + 1) & 3, 0);
        }
      }
      subkv(FA, FB, 2, 1, true);
      RAW_BAR();
      subkv(FB, FA, 3, 0, true);
      subkv(FA, FB, 3, 1, true);
      subkv(FB, FA, 0, 0, false);
    }
    asm volatile("s_nop 15" ::: "memory");
    {
      f32x4 m4[4], i4[4]; float cs[4];
      SPIN {
        const int ok = misc[12 + (lane & 3)];
        asm volatile("" ::: "memory");
#pragma unroll
        for (int i = 0; i < 4; ++i) {
          cs[i] = stm[256 + wc * 96 + 32 + 16 * i + c16];
          m4[i] = *(const f32x4*)(stm + 64 * wr + 16 * i + 4 * g4); i4[i] = *(const f32x4*)(stm + 128 + 64 * wr + 16 * i + 4 * g4);
        }
        SB0();
        if (__all(ok != 0)) break;
      }
      const u32x4 ONES4 = {0x3F803F80u, 0x3F803F80u, 0x3F803F80u, 0x3F803F80u};
      f32x2 tk[16], tv[16];
#define PR(v, i) ((f32x2){(v)[2 * (i)], (v)[2 * (i) + 1]})
#pragma unroll
      for (int i = 0; i < 16; ++i) {
        tk[i] = PR(acc[(i >> 1) & 3][2 + (i >> 3)], i & 1) - PR(m4[(i >> 1) & 3], i & 1) * cs[i >> 3];
        tv[i] = PR(acc[(i >> 1) & 3][4 + (i >> 3)], i & 1) - PR(m4[(i >> 1) & 3], i & 1) * cs[2 + (i >> 3)];
      }
      SB0();
#pragma unroll
      for (int i = 0; i < 16; ++i) { tk[i] *= PR(i4[(i >> 1) & 3], i & 1); tv[i] *= PR(i4[(i >> 1) & 3], i & 1); }
      SB0();
      unsigned kp[16], vp[16];
#pragma unroll
      for (int i = 0; i < 16; ++i) vp[i] = pack2o(tv[i][0], tv[i][1]);
      phi_n<16>(tk, kp);
#define F4_(p, i) ((u32x4){(p)[4 * (i)], (p)[4 * (i) + 1], (p)[4 * (i) + 2], (p)[4 * (i) + 3]})
      const f32x4 Z4 = {0.f, 0.f, 0.f, 0.f};
      u32x4 vk[2], vg[2];
#pragma unroll
      for (int j = 0; j < 2; ++j) { vk[j] = wr ? F4_(vp, 2 + j) : F4_(vp, j); vg[j] = wr ? F4_(vp, j) : F4_(vp, 2 + j); }
      f32x4 keep[2], give[2], zq[2];
#pragma unroll
      for (int d = 0; d < 2; ++d) { give[d] = mfma16(F4_(kp, 2 * d), vg[0], Z4); zq[d] = mfma16(ONES4, F4_(kp, 2 * d), Z4); }
#pragma unroll
      for (int d = 0; d < 2; ++d) { give[d] = mfma16(F4_(kp, 2 * d + 1), vg[1], give[d]); zq[d] = mfma16(ONES4, F4_(kp, 2 * d + 1), zq[d]); }
#pragma unroll
      for (int d = 0; d < 2; ++d) keep[d] = mfma16(F4_(kp, 2 * d), vk[0], Z4);
#pragma unroll
      for (int d = 0; d < 2; ++d) keep[d] = mfma16(F4_(kp, 2 * d + 1), vk[1], keep[d]);
      SB0();
      const float zv = (lane & 16) ? zq[1][0] : zq[0][0];
      char* xk = smem + L_XK + wave * 2304 + lane * 16;
      const char* xp = smem + L_XK + (wave ^ 2) * 2304 + lane * 16;
      *(f32x4*)xk = give[0]; *(f32x4*)(xk + 1024) = give[1]; *(float*)(xk + 2048 - lane * 12) = zv;
      asm volatile("" ::: "memory");
      misc[16 + wave] = 1;
      f32x4 p0, p1; float pz;
      SPIN {
        const int ok = misc[16 + (wave ^ 2)];
        asm volatile("" ::: "memory");
        p0 = *(const f32x4*)xp; p1 = *(const f32x4*)(xp + 1024); pz = *(const float*)(xp + 2048 - lane * 12);
        SB0();
        if (ok == 1) break;
      }
      STW(sane(__builtin_bit_cast(u32x4, keep[0] + p0)), kvp_off);
      STW(sane(__builtin_bit_cast(u32x4, keep[1] + p1)), kvp_off + 1024);
      STW1(sane1(__float_as_uint(zv + pz)), (lane < 32 && wr == 0) ? zp_off + lane * 4 : OOB);
    }
    {
      u32x4 qa[4][4]; u32x2 ql[4][2], qh[4][2];
#pragma unroll
      for (int u = 0; u < 4; ++u) {
#pragma unroll
        for (int j = 0; j < 2; ++j) {
          ql[u][j] = trread(smem + bo[1][j] + (u >> 1) * K1_STAGE + (u & 1) * 12288);
          qh[u][j] = trread(smem + bo[1][j] + (u >> 1) * K1_STAGE + (u & 1) * 12288 + 512);
        }
#pragma unroll
        for (int i = 0; i < 4; ++i) qa[u][i] = *(const u32x4*)(smem + ao[1][u & 1] + (u >> 1) * K1_STAGE + i * 2048);
      }
      asm volatile("" ::: "memory");
      misc[8 + wave] = 3;
      SB0();
#pragma unroll
      for (int u = 0; u < 4; ++u)
#pragma unroll
        for (int j = 0; j < 2; ++j)
#pragma unroll
          for (int i = 0; i < 4; ++i) acc[i][j] = mfma16((u32x4){ql[u][j][0], ql[u][j][1], qh[u][j][0], qh[u][j][1]}, qa[u][i], acc[i][j]);
      SB0();
    }
    unsigned qp[16];
    Phi<16> qs;
    const unsigned po = WS_KVP + ((2 * hp + (rb >> 3)) * 16 + h) * 4096 + (2 * ((rb >> 2) & 1) + ((lane >> 4) & 1)) * 1024 + (16 * (rb & 3) + (lane & 15)) * 16;
    u32x4 pv[8] = {};
    {
      f32x4 c4[2]; float m_b[4], rinv[4];
#pragma unroll
      for (int i = 0; i < 2; ++i) c4[i] = *(const f32x4*)(stm + 256 + wc * 96 + 16 * i + 4 * g4);
#pragma unroll
      for (int i = 0; i < 4; ++i) { m_b[i] = stm[64 * wr + 16 * i + c16]; rinv[i] = stm[128 + 64 * wr + 16 * i + c16]; }
      SB0();
      f32x2 tq[16];
#pragma unroll
      for (int i = 0; i < 16; ++i) tq[i] = PR(acc[i >> 2][(i >> 1) & 1], i & 1) - PR(c4[(i >> 1) & 1], i & 1) * m_b[i >> 2];
      SB0();
#pragma unroll
      for (int i = 0; i < 16; ++i) tq[i] *= rinv[i >> 2];
      SB0();
      phi_a<16>(tq, qs);
#pragma unroll
      for (int i = 0; i < 16; ++i) asm volatile("" : "+v"(qs.tb[i]), "+v"(qs.tr[i]), "+v"(qs.e[i]));
    }
    if (wave == 0) {
      SLEEP(P_KVP);
#pragma unroll
      for (int p = 0; p < 8; ++p) pv[p] = LDW(po + p * 8192);
    }
    asm volatile("" ::: "memory");
    if (wave == 1 && rb < 2) {
      SLEEP(P_KVP);
      SPIN {
        unsigned m = 0;
#pragma unroll
        for (int p = 0; p < 2; ++p) { pv[p] = LDW(WS_ZP + (2 * hp + rb) * 2048 + p * 1024 + lane * 16); m = chk(max(max(m, pv[p][0]), pv[p][2]), pv[p]); }
        if (wave_ok(m)) break;
        asm volatile("" ::: "memory");
      }
      f32x4 s = __builtin_bit_cast(f32x4, pv[0]) + __builtin_bit_cast(f32x4, pv[1]);
#pragma unroll
      for (int i = 0; i < 4; ++i) { s[i] += dppf<DPP_ROR8>(s[i]); s[i] = xh_sum(x16_sum(s[i])); }
      if (lane < 16) st64(ws, WS_ZF + (2 * hp + rb) * 128 + (lane >> 3) * 64 + (lane & 3) * 16 + ((lane >> 2) & 1) * 8,
                          ((u64)sane1(pack2(s[2], s[3])) << 32) | pack2(s[0], s[1]));
    }
    if (wave == 0) {
      SPIN {
        unsigned m = 0;
#pragma unroll
        for (int p = 0; p < 8; ++p) m = chk(m, pv[p]);
        if (wave_ok(m)) break;
        asm volatile("" ::: "memory");
#pragma unroll
        for (int p = 0; p < 8; ++p) pv[p] = LDW(po + p * 8192);
      }
      f32x4 s = __builtin_bit_cast(f32x4, pv[0]);
#pragma unroll
      for (int p = 1; p < 8; ++p) s += __builtin_bit_cast(f32x4, pv[p]);
      const unsigned p0 = pack2(xh_sum(s[0]), xh_sum(s[1])), p1 = pack2(xh_sum(s[2]), xh_sum(s[3]));
      auto a = __builtin_amdgcn_permlane16_swap(p0, p0, false, false);
      auto b = __builtin_amdgcn_permlane16_swap(p1, p1, false, false);
      STW(sane((u32x4){a[0], b[0], a[1], b[1]}), lane < 16 ? WS_KVF + hp * 4096 + rb * 256 + lane * 16 : OOB);
    }
    phi_b<16>(qs, qp);
#pragma unroll
    for (int i = 0; i < 16; ++i) asm volatile("" : "+v"(qp[i]));
    if (wave == 0) misc[7] = 1;
    else SPIN { if (misc[7] == 1) break; __builtin_amdgcn_s_sleep(1); }
    SLEEP(P_KVF);
    {
      u32x4 k0, k1, z0;
      SPIN {
        k0 = LDW(WS_KVF + head * 2048 + lane * 16); k1 = LDW(WS_KVF + head * 2048 + 1024 + lane * 16);
        z0 = LDW(WS_ZF + head * 128 + g4 * 16);
        if (wave_ok(chk(chk(chk(0u, k0), k1), z0))) break;
        asm volatile("" ::: "memory");
      }
      const f32x4 Z4 = {0.f, 0.f, 0.f, 0.f};
      f32x4 X[8], Dn[4];
#pragma unroll
      for (int i = 0; i < 4; ++i) Dn[i] = mfma16(z0, F4_(qp, i), Z4);
#pragma unroll
      for (int i = 0; i < 8; ++i) X[i] = mfma16((i & 1) ? k1 : k0, F4_(qp, i >> 1), Z4);
      SB0();
      float ri[4]; unsigned a[16]; f32x2 f[16];
#pragma unroll
      for (int i = 0; i < 4; ++i) ri[i] = __builtin_amdgcn_rcpf(rbf(rbf(Dn[i][0]) + 1e-6f));
      SB0();
#pragma unroll
      for (int i = 0; i < 16; ++i) a[i] = pack2o(X[i >> 1][2 * (i & 1)], X[i >> 1][2 * (i & 1) + 1]);
      SB0();
#pragma unroll
      for (int i = 0; i < 16; ++i) f[i] = unpk(a[i]);
      SB0();
#pragma unroll
      for (int i = 0; i < 16; ++i) f[i] *= ri[i >> 2];
      SB0();
#pragma unroll
      for (int i = 0; i < 4; ++i) {
        STW(((u32x4){pack2(f[4 * i][0], f[4 * i][1]), pack2(f[4 * i + 1][0], f[4 * i + 1][1]), pack2(f[4 * i + 2][0], f[4 * i + 2][1]), pack2(f[4 * i + 3][0], f[4 * i + 3][1])}),
            o_off + (i >> 1) * 65536 + (i & 1) * 1024);
        SB0();
      }
    }
    {
      EPI();
      const int rt = wave & 1, kh = wave >> 1;
      const unsigned ob = WS_O + ((rp * 2 + rt) * 32 + 16 * kh) * 2048 + lane * 16;
#define LDO(t, st) __builtin_amdgcn_raw_buffer_load_b128(wsr, ob + (st) * 1024, whead(t) * 2048, 16)
      int woff[2];
#pragma unroll
      for (int i = 0; i < 2; ++i) woff[i] = kh * 8192 + (g4 >> 1) * 2048 + (g4 & 1) * 512 + trl + ((i ^ (g4 & 1)) * 32);
      struct Frag2 { u32x2 lo[8], hi[8]; };
      Frag2 G[2];
      auto ld2 = [&](Frag2& N, int sn, int nt, int hi) {
        const char* p = smem + sn + woff[nt & 1] + (nt >> 2) * 1024 + ((nt >> 1) & 1) * 256;
        if (hi) N.hi[nt] = trread(p + 4096); else N.lo[nt] = trread(p);
      };
      WAIT_V(0);
      STW(((u32x4){1u, 1u, 1u, 1u}), fb_off);
      RAW_BAR();
#pragma unroll
      for (int i = 0; i < 16; ++i) ld2(G[0], wo_slot(0), i >> 1, i & 1);
      f32x4 a2[2][8];
#pragma unroll
      for (int i = 0; i < 16; ++i) a2[i & 1][i >> 1] = (f32x4){0.f, 0.f, 0.f, 0.f};
      SLEEP(P_FB);
      SPIN { if (wave_ok(ld32(ws, WS_FB + (rp * 32 + 16 * kh + (lane & 15)) * 128))) break; }
      asm volatile("" ::: "memory");
      char* pr[2];
#pragma unroll
      for (int i = 0; i < 2; ++i) pr[i] = smem + L_P + kh * L_PK + (32 * rt + c16) * 512 + (((4 * i + g4) ^ (c16 & 7)) * 16);
      u32x4 of[16][2];
#pragma unroll
      for (int t = 0; t < OW; ++t) { of[t][0] = LDO(t, 0); of[t][1] = LDO(t, 1); }
      SB0();
      sfor<0, 16>([&](auto T) {
        constexpr int t = T.v;
        Frag2& C = G[t & 1]; Frag2& N = G[(t + 1) & 1];
#pragma unroll
        for (int i = 0; i < 16; ++i) {
          const int nt = i >> 1, st = i & 1;
          a2[st][nt] = mfma16((u32x4){C.lo[nt][0], C.lo[nt][1], C.hi[nt][0], C.hi[nt][1]}, of[t][st], a2[st][nt]);
          SB0();
          if ((t & 1) && t <= 11 && i == 8) {
            asm volatile("s_waitcnt lgkmcnt(8)" ::: "memory"); __builtin_amdgcn_s_barrier(); asm volatile("" ::: "memory");
          }
          if (t < 15) ld2(N, wo_slot(t + 1), nt, st);
          if (t > 0 && t - 1 + OW < 16 && (i == 3 || i == 11)) of[(t - 1 + OW) & 15][i >> 3] = LDO(t - 1 + OW, i >> 3);
          if (t - 1 + OW == 16 && (i == 3 || i == 11)) { xv[i >> 3] = *(const u32x4*)(x + ego + (i >> 3) * 64); wv[i >> 3] = *(const u32x4*)(w_r + cb * 128 + eseg * 8 + (i >> 3) * 64); }
          if (t == 15 && i >= 4) *(f32x4*)(pr[(nt - 2) & 1] + ((nt - 2) >> 1) * 128 + st * 8192) = a2[st][(nt - 2) & 7];
          SB0();
        }
      });
#pragma unroll
      for (int i = 12; i < 16; ++i) *(f32x4*)(pr[(i >> 1) & 1] + (i >> 2) * 128 + (i & 1) * 8192) = a2[i & 1][i >> 1];
    }
    unpack_xw();
    __builtin_amdgcn_s_setprio(0);
  } else {
    LANES();
    const int sw = wave - 4, t2 = tid - 256;
    __builtin_amdgcn_s_setprio(3);
    const bool real = hs_ != 7;
    unsigned hs_g0 = 0, hs_cur = 0;
    u32x4 ms_exp = {0, 0, 0, 0};
    bool hs_act = true, ms_act = !real && hp < 8 && rb == 0 && sw == 1;
    const unsigned hs_off = (lane == 1 ? SY_GO : SY_CH) + bid * 4;
    const unsigned re_off = t2 == 0 ? SY_RE + bid * 4 : OOB;
    const unsigned ms_off = lane * 16;
    const char* wsu = uni(ws);
#define HS_PROBE() asm volatile("s_nop 4\n\tglobal_load_dword v156, %0, %1 sc1" :: "v"(hs_off), "s"(wsu) : "memory", RSV)
#define MS_PROBE() asm volatile("s_nop 4\n\tglobal_load_dwordx4 v[152:155], %0, %1 offset:2048 sc1" :: "v"(ms_off), "s"(wsu) : "memory", RSV)
    auto hs_step = [&](bool first) {
      if (hs_act) {
        const unsigned hv_ = rget<156>();
        hs_act = __builtin_amdgcn_readlane(hv_, 1) == hs_g0;
        const unsigned c = __builtin_amdgcn_readlane(hv_, 0);
        if (sw == 0 && (first || c != hs_cur)) STW1(hv_, re_off);
        hs_cur = c;
        HS_PROBE();
      }
      if (ms_act) {
        const unsigned d = ((rget<152>() ^ ms_exp[0]) | (rget<153>() ^ ms_exp[1])) | ((rget<154>() ^ ms_exp[2]) | (rget<155>() ^ ms_exp[3]));
        ms_act = __any(d != 0u);
        STW(ms_exp, ms_act ? OOB : SY_GO + ms_off);
        MS_PROBE();
      }
    };
    HS_PROBE();
    if (ms_act) {
      MS_PROBE();
      asm volatile("s_nop 4\n\tglobal_load_dwordx4 v[148:151], %0, %1 offset:1024 sc1" :: "v"(ms_off), "s"(wsu) : "memory", RSV);
    }
    const int gr = (g8 >> 3) * 7 + min(hs_, 6);
    const unsigned voG = (unsigned)((lane >> 3) * 8 * F_ + (lane & 7) * 8) * 2u, voD = (unsigned)((lane >> 3) * 8 * D_ + (lane & 7) * 8) * 2u;
    const size_t gofs = ((size_t)(kq * 256 + sw * 64) * F_ + gr * 64) * 2;
    const char* pG = (const char*)wg + gofs;
    const char* pU = (const char*)wu + gofs;
    const char* pD = (const char*)(wd + (size_t)(gr * 64) * D_ + kq * 256 + sw * 64);
    auto wstep = [&](auto I) {
      constexpr int i = I.v;
      if (i < 8) wload<VW + 4 * i>(voG, pG + (i & 7) * (F_ * 2));
      else if (i < 16) wload<VW + 4 * i>(voG, pU + (i & 7) * (F_ * 2));
      else wload2<VW + 4 * i>(voD, pD + (i & 7) * (D_ * 2));
    };
    unsigned voW;
    {
      const int bi = lane >> 4, q = (lane >> 2) & 3, ch = lane & 3;
      voW = (unsigned)((4 * (bi >> 1) + q) * D_ + (bi & 1) * 32 + (ch ^ (bi & 2)) * 8) * 2u;
    }
    const char* baseW = (const char*)(wo + (size_t)((16 * (sw >> 1)) * 32 + 16 * (sw & 1)) * D_ + cb * 128) - 1024;
    auto stageW = [&](auto J, bool rsv) {
      const char* s_ = baseW + (size_t)whead(J.v) * (32 * D_ * 2);
      if (rsv) glds4r<wo_slot(J.v)>(voW + 1024, voW + 128, voW + 8 * D_ * 2 - 1024, voW + 8 * D_ * 2 + 128 - 2048, s_, l4);
      else glds4<wo_slot(J.v)>(voW + 1024, voW + 128, voW + 8 * D_ * 2 - 1024, voW + 8 * D_ * 2 + 128 - 2048, s_, l4);
    };
    int sta[2], stb[3];
    {
#pragma unroll
      for (int c = 0; c < 2; ++c) { sta[c] = (posA & 3) * K1_STAGE + (32 * sw + (lane & 15)) * 128 + (((4 * c + (lane >> 4)) ^ ((lane & 15) >> 1)) * 16); asm volatile("" : "+v"(sta[c]) :: RSV); }
#pragma unroll
      for (int i = 0; i < 3; ++i) {
        const int G = sw * 3 + i, hd = G >= 6 ? 1 : 0, mat = (G - 6 * hd) >> 1;
        stb[i] = (posB & 3) * K1_STAGE + 16384 + (lane >> 4) * 6144 + mat * 1024 + hd * 256 + ((lane & 15) >> 2) * 64 + (G & 1) * 32 + (lane & 3) * 8;
        asm volatile("" : "+v"(stb[i]) :: RSV);
      }
    }
    unsigned rs_dst = (lane >> 4) == ((lane & 15) >> 2) ? rs_slot + (32 * sw + (lane & 15)) * 8 : OOB, cs_dst = lane < 48 ? cs_slot + (sw * 48 + lane) * 4 : OOB;
    unsigned rs_off = WS_RS + rb * 16384 + (lane >> 4) * 1024 + sw * 256 + (lane & 15) * 16;
    unsigned cs_off = WS_CS + hp * 12288 + (lane >> 4) * 768 + sw * 192 + min(lane & 15, 11) * 16;
    asm volatile("" : "+v"(rs_dst), "+v"(cs_dst), "+v"(rs_off), "+v"(cs_off) :: RSV);
    int stm_dst = ((lane >> 5) * 128 + sw * 32 + (lane & 15) * 2 + ((lane >> 4) & 1)) * 4, cs_put = (lane & 15) < 12 ? (256 + sw * 48 + (lane & 15) * 4 + (lane >> 4)) * 4 : 1792 + lane * 4;
    asm volatile("" : "+v"(stm_dst), "+v"(cs_put) :: RSV);
    auto st_load = [&]() { sfor<0, 4>([&](auto I) { sload<224 + 4 * I.v>(rs_off, wsu + I.v * 4096); sload<240 + 4 * I.v>(cs_off, wsu + I.v * 3072); }); };
    sfor<3, GS[0]>(group);
    WAIT_VR(gops(GS[0]) - 10 + 1); RAW_BARR();
    sfor<GS[0], GS[1]>(group);
    WAIT_VR(gops(GS[1]) - 20); RAW_BARR();
    hs_g0 = __builtin_amdgcn_readlane(rget<156>(), 1);
    if (ms_act) {
      const u32x4 re = {rget<152>(), rget<153>(), rget<154>(), rget<155>()}, g0 = {rget<148>(), rget<149>(), rget<150>(), rget<151>()};
#pragma unroll
      for (int j = 0; j < 4; ++j) { const unsigned c = re[j] + 1u; ms_exp[j] = (c == g0[j]) ? c + 1u : c; }
      STW(ms_exp, SY_CH + ms_off);
    }
    u32x4 ra[4] = {}; u32x2 rc[3][4] = {};
    auto chunk = [&](auto C) {
      const u32x4 ONES4 = {0x3F803F80u, 0x3F803F80u, 0x3F803F80u, 0x3F803F80u};
      const f32x4 Z4 = {0.f, 0.f, 0.f, 0.f};
      if constexpr (C.v == 0) {
        asm volatile("" : "+v"(ra[0]), "+v"(ra[1]), "+v"(ra[2]), "+v"(ra[3]) :: RSV);
        f32x4 s[2], q[2];
#pragma unroll
        for (int i = 0; i < 2; ++i) { q[i] = mfma16(ra[2 * i], ra[2 * i], Z4); s[i] = mfma16(ONES4, ra[2 * i], Z4); }
#pragma unroll
        for (int i = 0; i < 2; ++i) { q[i] = mfma16(ra[2 * i + 1], ra[2 * i + 1], q[i]); s[i] = mfma16(ONES4, ra[2 * i + 1], s[i]); }
#pragma unroll
        for (int i = 0; i < 2; ++i) {
          float a = q[i][0], c = q[i][1], d = q[i][2], e = q[i][3];
          asm volatile("" : "+v"(a), "+v"(c), "+v"(d), "+v"(e) :: RSV);
          const float lo = (lane & 1) ? c : a, hi = (lane & 1) ? e : d;
          __builtin_amdgcn_raw_buffer_store_b64((u32x2){sane1(__float_as_uint(s[i][0])), sane1(__float_as_uint((lane & 2) ? hi : lo))}, wsr, rs_dst + i * 128, 0, 16);
        }
      } else {
        f32x4 c[3];
#pragma unroll
        for (int i = 0; i < 3; ++i) { asm volatile("" : "+v"(rc[i][0]), "+v"(rc[i][1]), "+v"(rc[i][2]), "+v"(rc[i][3]) :: RSV); c[i] = mfma16(ONES4, (u32x4){rc[i][0][0], rc[i][0][1], rc[i][1][0], rc[i][1][1]}, Z4); }
#pragma unroll
        for (int i = 0; i < 3; ++i) c[i] = mfma16(ONES4, (u32x4){rc[i][2][0], rc[i][2][1], rc[i][3][0], rc[i][3][1]}, c[i]);
        float c0 = c[0][0], c1 = c[1][0], c2 = c[2][0];
        asm volatile("" : "+v"(c0), "+v"(c1), "+v"(c2) :: RSV);
        STW1(sane1(__float_as_uint(lane < 16 ? c0 : lane < 32 ? c1 : c2)), cs_dst);
      }
    };
    sfor<0, 12>([&](auto T) {
      constexpr int t = T.v;
      if (t < 4 && posA == t + 1) {
#pragma unroll
        for (int c = 0; c < 4; ++c) ra[c] = *(const u32x4*)(smem + sta[c & 1] + (c >> 1) * 2048);
      }
      if (t < 4 && posB == t + 1) {
#pragma unroll
        for (int i = 0; i < 3; ++i)
#pragma unroll
          for (int j = 0; j < 4; ++j) rc[i][j] = trread(smem + (stb[i] ^ ((j >> 1) * 32)) + (j >> 1) * 3072 + (j & 1) * 512);
      }
      if (t == 2) HS_PROBE();
      if (t >= 4) hs_step(t == 4);
      sfor<GS[t + 1], GS[t + 2]>([&](auto G) {
        group(G);
        if constexpr ((t == 4 || t == 5) && G.v == GS[t + 1]) chunk(IC<t - 4>{});
      });
      if (real) sfor<WS_[t], WS_[t + 1]>(wstep);
      WAIT_VR(t < 11 ? gops(GS[t + 2]) - 10 * (t + 3) : gops(GS[13]) - gops(GS[12]));
      RAW_BARR();
    });
    hs_step(false);
    auto st_put = [&]() -> bool {
      float a[8]; unsigned m = 0, n = 0;
      rsum4<224>(a, m, n); rsum4<240>(a + 4, m, n);
      const float s1 = sw16(a[0], a[2]), q1 = sw16(a[1], a[3]);
      const float cs = sw32(sw16(a[4], a[5]), sw16(a[6], a[7]));
      const float mean = sw32(s1, s1) * (1.0f / 1024.0f);
      const float var = fmaxf(__builtin_fmaf(sw32(q1, q1), 1.0f / 1024.0f, -(mean * mean)), 0.f);
      const float rinv = __builtin_amdgcn_rcpf(rbf(__builtin_amdgcn_sqrtf(rbf(rbf(var) + 1e-5f))));
      *(float*)(smem + L_STM + stm_dst) = (lane & 32) ? rinv : rbf(mean);
      *(float*)(smem + L_STM + cs_put) = cs;
      return wave_ok(max(m, n));
    };
    st_load();
    if (real) sfor<WS_[12], W1A>(wstep);
    if (real) WAIT_VR(8 + W1A - WS_[11]); else WAIT_VR(8);
    if (lane == 0) { misc[8 + sw] = 0; misc[12 + sw] = 0; misc[16 + sw] = 0; misc[7] = 0; }
    RAW_BARR();
    if (real) WAIT_VR(W1A - WS_[12]); else WAIT_VR(0);
    if (!st_put() || hs_act) {
      if (hs_act) hs_act = __builtin_amdgcn_readlane(rget<156>(), 1) == hs_g0;
      if (ms_act) {
        SPIN {
          bool ok = true;
#pragma unroll
          for (int j = 0; j < 4; ++j) ok &= (ld32(ws, SY_RE + lane * 16 + j * 4) == ms_exp[j]);
          if (__all(ok)) break;
        }
        STW(ms_exp, SY_GO + lane * 16);
      }
      if (hs_act) {
        SPIN {
          if (ld32(ws, SY_GO + bid * 4) != hs_g0) break;
          const unsigned c = ld32(ws, SY_CH + bid * 4);
          if (c != hs_cur) { hs_cur = c; STW1(c, re_off); }
        }
      }
      SPIN { st_load(); WAIT_VR(0); if (st_put()) break; }
    }
    asm volatile("" ::: "memory", RSV);
    if (lane == 0) misc[12 + sw] = 1;
    __builtin_amdgcn_s_setprio(0);
    if (real) sfor<W1A, 16>(wstep);
    auto mwait = [&](int v) { SPIN { const int a = misc[8 + (lane & 3)]; if (__all(a >= v)) break; __builtin_amdgcn_s_sleep(1); } asm volatile("" ::: "memory"); };
    sfor<0, 9>([&](auto J) {
      if (J.v == 4) mwait(3);
      stageW(J, J.v < 2);
      if (J.v >= 1) WAIT_V(4 * PACE);
      if (J.v == 1) { WAIT_VR(8); sfor<0, 16>(wtr); }
    });
    EPI();
#pragma unroll
    for (int i = 0; i < 2; ++i) { xv[i] = *(const u32x4*)(x + ego + i * 64); wv[i] = *(const u32x4*)(w_r + cb * 128 + eseg * 8 + i * 64); }
    asm volatile("s_waitcnt vmcnt(0)" : "+v"(xv[0]), "+v"(xv[1]), "+v"(wv[0]), "+v"(wv[1]) :: "memory");
    unpack_xw();
    RAW_BAR();
    RAW_BAR(); stageW(IC<9>{}, false); stageW(IC<10>{}, false);
    RAW_BAR(); stageW(IC<11>{}, false); stageW(IC<12>{}, false);
    RAW_BAR(); stageW(IC<13>{}, false); stageW(IC<14>{}, false);
    WAIT_V(16); RAW_BAR(); stageW(IC<15>{}, false);
    WAIT_V(12); RAW_BAR();
    WAIT_V(0); RAW_BAR();
    if (real) sfor<16, 24>(wstep);
  }
  LANES(); EPI();
  const int grow = rp * 64 + erow;
  int pa0 = L_P + erow * 512 + ((2 * eseg) ^ (erow & 7)) * 16, pa1 = pa0 ^ 16;
  unsigned st_dst = eseg == 0 ? WS_ST2 + ((rp * 8 + cb) * 64 + erow) * 16 : OOB, st_src = WS_ST2 + rp * 8192 + lane * 16;
  unsigned fd_dst = lane < 8 ? WS_FD + (cb * 32 + rp) * 128 + lane * 16 : OOB, kidx = 0xFFFFu - grow;
  asm volatile("" : "+v"(pa0), "+v"(pa1), "+v"(st_dst), "+v"(st_src), "+v"(fd_dst), "+v"(kidx));
  RAW_BAR2();
  unsigned hb[8];
  f32x2 hv[8];
  {
    f32x4 pp[2][4];
#pragma unroll
    for (int k = 0; k < 2; ++k)
#pragma unroll
      for (int i = 0; i < 4; ++i) pp[k][i] = *(const f32x4*)(smem + ((i & 1) ? pa1 : pa0) + (i >> 1) * 256 + k * L_PK);
    SB0();
    f32x2 s = {0.f, 0.f}, q = {0.f, 0.f}, p2 = {0.f, 0.f};
#pragma unroll
    for (int i = 0; i < 8; ++i) {
      const f32x4 a = pp[0][i >> 1] + pp[1][i >> 1];
      const f32x2 u = xf[i] + unpk(pack2o(a[2 * (i & 1)], a[2 * (i & 1) + 1]));
      hb[i] = pack2o(u[0], u[1]);
      hv[i] = unpk(hb[i]);
      s += hv[i]; q += hv[i] * hv[i]; p2 += hv[i] * wf2[i];
    }
    STW(sane((u32x4){__float_as_uint(oct_sum(s[0] + s[1])), __float_as_uint(oct_sum(q[0] + q[1])), __float_as_uint(oct_sum(p2[0] + p2[1])), __float_as_uint(sw_)}), st_dst);
  }
  KEEP2();
  const int sw = wave - 4;
  const unsigned fd_src = WS_FD + xcd * 4096 + r * 128;
  u32x2 kpk = {SENT, SENT};
  if (wave == 0) {
    u32x4 pa[8];
    SLEEP(P_C);
    SPIN {
      unsigned m = 0;
#pragma unroll
      for (int j = 0; j < 8; ++j) { pa[j] = LDW(st_src + j * 1024); m = chk(m, pa[j]); }
      if (wave_ok(m)) break;
      KEEP2();
    }
#define F4(v) __builtin_bit_cast(f32x4, v)
    const f32x4 t = ((F4(pa[0]) + F4(pa[1])) + (F4(pa[2]) + F4(pa[3]))) + ((F4(pa[4]) + F4(pa[5])) + (F4(pa[6]) + F4(pa[7])));
    const float mean = t[0] * (1.0f / 1024.0f);
    const float var = fmaxf(__builtin_fmaf(t[1], 1.0f / 1024.0f, -(mean * mean)), 0.f);
    const float mb = rbf(mean), rs = __builtin_amdgcn_rcpf(rbf(__builtin_amdgcn_sqrtf(rbf(rbf(var) + 1e-5f))));
    *(f32x2*)(smem + L_KX + lane * 8) = (f32x2){mb, rs};
    const float sc = fmaxf((t[2] - mb * t[3]) * rs, -INFINITY);
    const int u = (int)(pack2(0.f, sc) & 0xffff0000u);
    unsigned b0 = ((unsigned)(u ^ ((u >> 31) | (int)0x80000000)) & 0xffff0000u) | (0xFFFFu - (rp * 64 + lane)), b1 = 0;
    top2_lo(b0, b1); top2_hi(b0, b1);
    STW(((u32x4){b0, b1, b0, b1}), fd_dst);
    if (lane == 0) *(u32x2*)(smem + L_MISC + 112) = (u32x2){b0, b1};
  } else if (wave >= 4) {
    asm volatile("s_waitcnt vmcnt(0)" ::: "memory", RSV2);
    sfor<16, 24>(wtr);
  }
  RAW_BAR();
  bool cand;
  u32x4 ob[2];
  {
    const f32x2 mr = *(const f32x2*)(smem + L_KX + erow * 8);
    const u32x2 kk = *(const u32x2*)(smem + L_MISC + 112);
    const float mb = mr[0], rs = mr[1];
    const unsigned b0 = kk[0], b1 = kk[1];
    u32x4 nq[2];
#pragma unroll
    for (int i = 0; i < 8; ++i) {
      const f32x2 d = hv[i] - mb;
      const f32x2 n = unpk(pack2o(d[0], d[1])) * rs;
      nq[i >> 2][i & 3] = pack2o(n[0], n[1]);
    }
    const bool c2 = (b1 & 0xffffu) == kidx;
    cand = c2 || (b0 & 0xffffu) == kidx;
    if (cand) {
      const unsigned xo = WS_XS + (rp * 2 + (c2 ? 1 : 0)) * 2048 + cb * 256 + eseg * 16;
      STW(sane(nq[0]), xo); STW(sane(nq[1]), xo + 128);
      char* cp = smem + L_CAND + (c2 ? 512 : 0) + eseg * 16;
      *(u32x4*)cp = (u32x4){hb[0], hb[1], hb[2], hb[3]}; *(u32x4*)(cp + 128) = (u32x4){hb[4], hb[5], hb[6], hb[7]};
      *(u32x4*)(cp + 256) = nq[0]; *(u32x4*)(cp + 384) = nq[1];
    }
    asm volatile("" ::: "memory");
    if (wave >= 4) { SLEEP(P_D); kpk = __builtin_amdgcn_raw_buffer_load_b64(wsr, fd_src, 0, 16); }
    asm volatile("" ::: "memory");
#pragma unroll
    for (int i = 0; i < 8; ++i) { const f32x2 o = hv[i] + unpk(nq[i >> 2][i & 3]); ob[i >> 2][i & 3] = pack2(o[0], o[1]); }
  }
#define OUT_ST() do { *(u32x4*)(out + ego) = ob[0]; *(u32x4*)(out + ego + 64) = ob[1]; } while (0)
  if (wave < 4) {
    if (!cand) OUT_ST();
    RAW_BAR();
    const unsigned w0 = misc[24], w1 = misc[25];
    if (cand && grow != (int)key_idx(w0) && grow != (int)key_idx(w1)) OUT_ST();
#pragma unroll 1
    for (int tk = 0; tk < 2; ++tk) {
      const unsigned wk = tk ? w1 : w0, slot = misc[26 + tk];
      if ((int)(slot >> 1) != rp) continue;
      u32x4 pv[7];
      const unsigned yo = WS_YP + ((tk * 8 + cb) * 64 + (lane >> 3) * 8) * 512 + wave * 128 + (lane & 7) * 16;
      const int col = (wave >> 1) * 64 + (lane & 7) * 8 + (wave & 1) * 4 + (lane >> 4);
      float sg = rbf(__builtin_amdgcn_rcpf(1.0f + __expf(-key_val(wk))));
      float hh = bflo(*(const u16*)(smem + L_CAND + (slot & 1) * 512 + col * 2)), nn = bflo(*(const u16*)(smem + L_CAND + (slot & 1) * 512 + 256 + col * 2));
      u16* op = out + (size_t)key_idx(wk) * D_ + cb * 128 + col;
      asm volatile("" : "+v"(sg), "+v"(hh), "+v"(nn), "+v"(op));
      if (tk == 0 || (int)(misc[26] >> 1) != rp) {
        SPIN { if (misc[30] == (int)w0) break; __builtin_amdgcn_s_sleep(1); }
        SLEEP(P_E);
      }
      SPIN {
        unsigned m = 0;
#pragma unroll
        for (int p = 0; p < 7; ++p) { pv[p] = LDW(yo + p * 512); m = chk(m, pv[p]); }
        if (wave_ok(m)) break;
        asm volatile("" ::: "memory");
      }
      f32x4 s = __builtin_bit_cast(f32x4, pv[0]);
#pragma unroll
      for (int p = 1; p < 7; ++p) s += __builtin_bit_cast(f32x4, pv[p]);
      float y = sw32(sw16(s[0], s[1]), sw16(s[2], s[3]));
      y += dppf<DPP_ROR8>(y);
      if (!(lane & 8)) *(GAS u16*)op = (u16)pack2(hh + rbf(nn + rbf(rbf(y) * sg)), 0.f);
    }
  } else {
    unsigned xl = WS_XS + (kq * 256 + sw * 64 + (lane >> 4) * 16 + ((lane >> 2) & 1) * 8) * 2;
    int red_dst = (lane < 8 || (lane >= 24 && lane < 32)) ? L_RED + sw * 2048 + ((lane >> 3) & 1) * 1024 + (lane & 7) * 16 : L_RED + 8192 + lane * 16;
    unsigned gu_src = WS_GU + g8 * 4096 + lane * 16;
    int act_dst = L_ACT + sw * 256 + ((lane & 7) * 8 + (lane >> 3)) * 2, act_src = L_ACT + sw * 256 + (lane & 1) * 128 + (lane >> 4) * 32 + ((lane >> 1) & 1) * 16;
    unsigned yp_dst = lane < 16 ? WS_YP + (((lane >> 3) * 8 + 2 * kq + (sw >> 1)) * 64 + g8) * 512 + (sw & 1) * 256 + (lane & 7) * 16 : OOB;
    asm volatile("" : "+v"(xl), "+v"(red_dst), "+v"(gu_src), "+v"(act_dst), "+v"(act_src), "+v"(yp_dst));
    unsigned w0, w1; int s0 = -1, s1 = -1;
    u32x4 xa = {SENT, SENT, SENT, SENT};
    SPIN {
      const bool all = wave_ok(kpk[1]);
      const unsigned k0 = kpk[1] == SENT ? 0u : kpk[0], k1 = kpk[1] == SENT ? 0u : kpk[1];
      w0 = k0; w1 = k1;
      top2_lo(w0, w1); top2_mid(w0, w1);
      const int n0 = 2 * __builtin_ctzll(__ballot(k0 == w0));
      const u64 m1 = __ballot(k0 == w1);
      const int n1 = m1 ? 2 * __builtin_ctzll(m1) : n0 + 1;
      const bool same = n0 == s0 && n1 == s1;
      s0 = n0; s1 = n1;
      asm volatile("" ::: "memory");
      if (all && same) break;
      if (!all) { kpk = __builtin_amdgcn_raw_buffer_load_b64(wsr, fd_src, 0, 16); }
      xa = LDW(xl + ((lane & 1) ? s1 : s0) * 2048);
      if (all) break;
    }
    SPIN {
      if (wave_ok(chk(0u, xa))) break;
      asm volatile("" ::: "memory");
      xa = LDW(xl + ((lane & 1) ? s1 : s0) * 2048);
    }
    {
      const u32x4 z4 = {0u, 0u, 0u, 0u};
      const u32x4 xg = (lane & 2) ? z4 : xa, xu = (lane & 2) ? xa : z4;
      f32x4 d[8];
#pragma unroll
      for (int c = 0; c < 8; ++c) d[c] = mfma16(xg, wf[c], (f32x4){0.f, 0.f, 0.f, 0.f});
      if (tid == 256) { *(u32x4*)(smem + L_MISC + 96) = (u32x4){w0, w1, (unsigned)s0, (unsigned)s1}; misc[30] = 0; }
      SB0();
#pragma unroll
      for (int c = 0; c < 10; ++c) {
        if (c < 8) d[c & 7] = mfma16(xu, wf[8 + (c & 7)], d[c & 7]);
        if (c >= 2) *(f32x4*)(smem + red_dst + (c - 2) * 128) = d[(c - 2) & 7];
        SB0();
      }
    }
    RAW_BAR();
    if (sw == 0) {
      f32x4 t[8];
#pragma unroll
      for (int p = 0; p < 8; ++p) t[p] = *(const f32x4*)(smem + L_RED + p * 1024 + lane * 16);
      SB0();
      STW(sane(__builtin_bit_cast(u32x4, ((t[0] + t[1]) + (t[2] + t[3])) + ((t[4] + t[5]) + (t[6] + t[7])))), gu_src + kq * 1024);
    } else __builtin_amdgcn_s_sleep(2);
    {
      u32x4 q[4];
      if (grow != (int)key_idx(w0) && grow != (int)key_idx(w1)) OUT_ST();
      asm volatile("" ::: "memory");
      SLEEP(P_GU);
      SPIN {
        unsigned m = 0;
#pragma unroll
        for (int j = 0; j < 4; ++j) { q[j] = LDW(gu_src + j * 1024); m = chk(m, q[j]); }
        if (wave_ok(m)) break;
        asm volatile("" ::: "memory");
      }
      const f32x4 tot = (__builtin_bit_cast(f32x4, q[0]) + __builtin_bit_cast(f32x4, q[1])) + (__builtin_bit_cast(f32x4, q[2]) + __builtin_bit_cast(f32x4, q[3]));
      const f32x2 gs = unpk(pack2o(tot[0], tot[1])), us = unpk(pack2o(tot[2], tot[3]));
      const f32x2 sg = unpk(pack2o(gs[0] * __builtin_amdgcn_rcpf(1.0f + __expf(-gs[0])), gs[1] * __builtin_amdgcn_rcpf(1.0f + __expf(-gs[1]))));
      const unsigned a2 = pack2o(sg[0] * us[0], sg[1] * us[1]);
      *(u16*)(smem + act_dst) = (u16)a2; *(u16*)(smem + act_dst + 128) = (u16)(a2 >> 16);
      __builtin_amdgcn_fence(__ATOMIC_RELEASE, "wavefront"); __builtin_amdgcn_wave_barrier(); __builtin_amdgcn_fence(__ATOMIC_ACQUIRE, "wavefront");
      const u32x4 aa = *(const u32x4*)(smem + act_src);
      f32x4 d[8];
#pragma unroll
      for (int c = 0; c < 8; ++c) d[c] = mfma16(aa, wf[16 + c], (f32x4){0.f, 0.f, 0.f, 0.f});
      SB0();
      u32x4 y[2];
#pragma unroll
      for (int c = 0; c < 8; ++c) {
        const float t0 = d[c][0] + dppf<DPP_ROR8>(d[c][2]), t1 = d[c][3] + dppf<DPP_ROR8>(d[c][1]);
        y[c >> 2][c & 3] = __float_as_uint((lane & 8) ? t1 : t0);
      }
      STW(sane(y[0]), yp_dst); STW(sane(y[1]), yp_dst + 128);
      if (tid == 256) misc[30] = w0;
    }
  }
}

extern "C" void kernel_launch(void* const* d_in, const int* in_sizes, int n_in, void* d_out, int out_size,
                              void* d_ws, size_t ws_size, hipStream_t stream) {
  (void)hipFuncSetAttribute((const void*)fused, hipFuncAttributeMaxDynamicSharedMemorySize, LDS_TOTAL);
  fused<<<256, 512, LDS_TOTAL, stream>>>((const u16*)d_in[0], (const u16*)d_in[1], (const u16*)d_in[2], (const u16*)d_in[4],
                                         (const u16*)d_in[5], (const u16*)d_in[6], (const u16*)d_in[7], (u16*)d_out, (char*)d_ws);
}
```

```cpp
#include <hip/hip_runtime.h>
#include <stdint.h>

#define LAS __attribute__((address_space(3)))
#define GAS __attribute__((address_space(1)))
#define DI __device__ __forceinline__

typedef unsigned short u16;
typedef unsigned long long u64;
typedef __attribute__((ext_vector_type(2))) __bf16 bf16x2;
typedef __attribute__((ext_vector_type(8))) __bf16 bf16x8;
typedef __attribute__((ext_vector_type(4))) short s16x4;
typedef __attribute__((ext_vector_type(2))) unsigned short u16x2;
typedef __attribute__((ext_vector_type(4))) float f32x4;
typedef __attribute__((ext_vector_type(2))) float f32x2;
typedef __attribute__((ext_vector_type(4))) unsigned u32x4;
typedef __attribute__((ext_vector_type(2))) unsigned u32x2;
typedef GAS unsigned gu32;
typedef GAS unsigned long long gu64;

constexpr int S_ = 2048, D_ = 1024, F_ = 3584;

constexpr unsigned SY_CH = 0, SY_GO = 1024, SY_RE = 2048;
constexpr unsigned WS_FD  = 98304;
constexpr unsigned WS_ZF  = 8192;
constexpr unsigned WS_KVF = 16384;
constexpr unsigned WS_RS  = 131072;
constexpr unsigned WS_CS  = WS_RS + 16 * 16 * 1024;
constexpr unsigned WS_ZP  = WS_CS + 16 * 16 * 768;
constexpr unsigned WS_FB  = WS_ZP + 32 * 16 * 128;
constexpr unsigned WS_ST2 = WS_FB + 32 * 32 * 128;
constexpr unsigned WS_XS  = WS_ST2 + 32 * 8 * 1024;
constexpr unsigned WS_GU  = WS_XS + 64 * 2048;
constexpr unsigned WS_YP  = WS_GU + 256 * 1024;
constexpr unsigned WS_KVP = WS_YP + 2 * 8 * 64 * 512;
constexpr unsigned WS_O   = WS_KVP + 32 * 16 * 4096;
constexpr unsigned WS_END = WS_O + S_ * D_ * 2;
constexpr unsigned OOB = 0x80000000u;

template <int V> struct IC { static constexpr int v = V; };
template <int B, int E, class F> DI void sfor(F&& f) { if constexpr (B < E) { f(IC<B>{}); sfor<B + 1, E>(f); } }

DI float bflo(unsigned u) { return __uint_as_float(u << 16); }
DI float bfhi(unsigned u) { return __uint_as_float(u & 0xffff0000u); }
DI float rbf(float x) { return (float)(__bf16)x; }
DI unsigned pack2(float a, float b) { bf16x2 c; c[0] = (__bf16)a; c[1] = (__bf16)b; return __builtin_bit_cast(unsigned, c); }
DI unsigned pack2o(float a, float b) { unsigned p = pack2(a, b); asm("" : "+v"(p)); return p; }
DI f32x4 mfma16(u32x4 a, u32x4 b, f32x4 c) {
  return __builtin_amdgcn_mfma_f32_16x16x32_bf16(__builtin_bit_cast(bf16x8, a), __builtin_bit_cast(bf16x8, b), c, 0, 0, 0);
}
DI void mfma16i(u32x4 a, u32x4 b, f32x4& c) { asm volatile("v_mfma_f32_16x16x32_bf16 %0, %1, %2, %0" : "+v"(c) : "v"(a), "v"(b)); }
DI f32x2 unpk(unsigned u) { return (f32x2){bflo(u), bfhi(u)}; }
DI u32x2 trread(const char* p) {
  s16x4 v = __builtin_amdgcn_ds_read_tr16_b64_v4i16((LAS s16x4*)p);
  return __builtin_bit_cast(u32x2, v);
}
DI const char* uni(const void* p) {
  const u64 v = (u64)p;
  return (const char*)(((u64)(unsigned)__builtin_amdgcn_readfirstlane((int)(v >> 32)) << 32) | (unsigned)__builtin_amdgcn_readfirstlane((int)v));
}
#define RV4(a, b, c, d) "v" #a, "v" #b, "v" #c, "v" #d
#define RV10(p) "v" #p "0", "v" #p "1", "v" #p "2", "v" #p "3", "v" #p "4", "v" #p "5", "v" #p "6", "v" #p "7", "v" #p "8", "v" #p "9"
#define RSV2 RV4(224, 225, 226, 227), "v228", "v229", RV10(23), RV10(24), RV4(250, 251, 252, 253), "v254", "v255"
#define RSV1 RV4(192, 193, 194, 195), RV4(196, 197, 198, 199), RV10(20), RV10(21), RV4(220, 221, 222, 223), RSV2
#define RSV "v148", "v149", RV10(15), RV10(16), RV10(17), RV10(18), "v190", "v191", RSV1
#define NTW " nt"
#define GL_(v, s, o) "global_load_lds_dwordx4 " v ", " s " offset:" #o
#define GLDS4(SC, CL...) asm volatile("s_add_u32 m0, %5, %c6\n\ts_nop 3\n\t" GL_("%0", "%4", 0) SC "\n\t" GL_("%1", "%4", 1024) SC "\n\t" \
  GL_("%2", "%4", 2048) SC "\n\t" GL_("%3", "%4", 3072) SC :: "v"(v0), "v"(v1), "v"(v2), "v"(v3), "s"(uni(sbase)), "s"(lds), "i"(IMM) : "memory", "scc", ##CL)
#define GLDS3(SC, CL...) asm volatile("s_add_u32 m0, %4, %c5\n\ts_nop 3\n\t" GL_("%0", "%3", 0) SC "\n\t" GL_("%1", "%3", 1024) SC "\n\t" \
  GL_("%2", "%3", 2048) SC :: "v"(v0), "v"(v1), "v"(v2), "s"(uni(sbase)), "s"(lds), "i"(IMM) : "memory", "scc", ##CL)
template <int IMM> DI void glds4(unsigned v0, unsigned v1, unsigned v2, unsigned v3, const void* sbase, unsigned lds) { GLDS4(""); }
template <int IMM> DI void glds4r(unsigned v0, unsigned v1, unsigned v2, unsigned v3, const void* sbase, unsigned lds) { GLDS4("", RSV); }
template <int IMM> DI void glds3r(unsigned v0, unsigned v1, unsigned v2, const void* sbase, unsigned lds) { GLDS3("", RSV); }
template <int R> DI void sload(unsigned voff, const void* sbase) {
  asm volatile("s_nop 4\n\tglobal_load_dwordx4 v[%c0:%c1], %2, %3 sc1" ::"i"(R), "i"(R + 3), "v"(voff), "s"(uni(sbase)) : "memory", RSV);
}
DI float fadd(float a, float b) { float o; asm("v_add_f32 %0, %1, %2" : "=v"(o) : "v"(a), "v"(b)); return o; }
#define RA_(o, a, b) "v_add_f32 %" #o ", v[%c6+" #a "], v[%c6+" #b "]\n\t"
#define RB2_(o, a) "v_add_f32 %" #o ", v[%c6+" #a "], %" #o "\n\t"
#define RM_(o, a, b) "v_max3_u32 %" #o ", v[%c6+" #a "], v[%c6+" #b "], %" #o "\n\t"
template <int R> DI void rsum4(float* a, unsigned& m, unsigned& n) {
  asm volatile(RA_(0, 0, 4) RA_(1, 1, 5) RM_(4, 0, 1) RA_(2, 2, 6) RA_(3, 3, 7) RM_(5, 2, 3) RB2_(0, 8) RM_(4, 4, 5) RB2_(1, 9) RM_(5, 6, 7)
               RB2_(2, 10) RM_(4, 8, 9) RB2_(3, 11) RM_(5, 10, 11) RB2_(0, 12) RB2_(1, 13) RM_(4, 12, 13) RB2_(2, 14) RB2_(3, 15) RM_(5, 14, 15)
               : "=&v"(a[0]), "=&v"(a[1]), "=&v"(a[2]), "=&v"(a[3]), "+v"(m), "+v"(n) : "i"(R) : RSV);
}
template <int R> DI void wload2(unsigned voff, const void* sbase) {
  asm volatile("s_nop 4\n\tglobal_load_dwordx4 v[%c0:%c1], %2, %3" NTW ::"i"(R), "i"(R + 3), "v"(voff), "s"(uni(sbase)) : "memory", RSV2);
}
template <int R> DI void wload(unsigned voff, const void* sbase) {
  asm volatile("s_nop 4\n\tglobal_load_dwordx4 v[%c0:%c1], %2, %3" NTW ::"i"(R), "i"(R + 3), "v"(voff), "s"(uni(sbase)) : "memory", RSV);
}
template <int RA, int RB> DI unsigned wperm(unsigned sel) {
  unsigned o;
  if (RA < 192) asm volatile("v_perm_b32 %0, v%c2, v%c1, %3" : "=v"(o) : "i"(RA), "i"(RB), "s"(sel) : RSV);
  else if (RA < 224) asm volatile("v_perm_b32 %0, v%c2, v%c1, %3" : "=v"(o) : "i"(RA), "i"(RB), "s"(sel) : RSV1);
  else asm volatile("v_perm_b32 %0, v%c2, v%c1, %3" : "=v"(o) : "i"(RA), "i"(RB), "s"(sel) : RSV2);
  return o;
}
template <int R> DI unsigned rget() { unsigned o; asm volatile("v_mov_b32 %0, v%c1" : "=v"(o) : "i"(R) : RSV); return o; }
DI float xh_sum(float v) {
  auto r = __builtin_amdgcn_permlane32_swap(__float_as_uint(v), __float_as_uint(v), false, false);
  return __uint_as_float(r[0]) + __uint_as_float(r[1]);
}
template <int CTRL> DI unsigned dppu(unsigned v) { return (unsigned)__builtin_amdgcn_update_dpp(0, (int)v, CTRL, 0xf, 0xf, true); }
template <int CTRL> DI float dppf(float v) { return __uint_as_float(dppu<CTRL>(__float_as_uint(v))); }
constexpr int DPP_X1 = 0xB1, DPP_X2 = 0x4E, DPP_HMIR = 0x141, DPP_MIR = 0x140, DPP_ROR8 = 0x128;
DI float quad_sum(float v) { v += dppf<DPP_X1>(v); v += dppf<DPP_X2>(v); return v; }
DI float oct_sum(float v) { v = quad_sum(v); v += dppf<DPP_HMIR>(v); return v; }
DI float x16_sum(float v) {
  auto r = __builtin_amdgcn_permlane16_swap(__float_as_uint(v), __float_as_uint(v), false, false);
  return __uint_as_float(r[0]) + __uint_as_float(r[1]);
}
DI float sw16(float a, float b) {
  auto r = __builtin_amdgcn_permlane16_swap(__float_as_uint(a), __float_as_uint(b), false, false);
  return fadd(__uint_as_float(r[0]), __uint_as_float(r[1]));
}
DI float sw32(float a, float b) {
  auto r = __builtin_amdgcn_permlane32_swap(__float_as_uint(a), __float_as_uint(b), false, false);
  return fadd(__uint_as_float(r[0]), __uint_as_float(r[1]));
}
template <int N> struct Phi { unsigned tb[N]; f32x2 tr[N], e[N]; };
template <int N> DI void phi_a(const f32x2* t, Phi<N>& s) {
#pragma unroll
  for (int i = 0; i < N; ++i) s.tb[i] = pack2o(t[i][0], t[i][1]);
  __builtin_amdgcn_sched_barrier(0);
#pragma unroll
  for (int i = 0; i < N; ++i) s.tr[i] = unpk(s.tb[i]);
  __builtin_amdgcn_sched_barrier(0);
#pragma unroll
  for (int i = 0; i < N; ++i) s.e[i] = s.tr[i] * 1.44269504088896340736f;
  __builtin_amdgcn_sched_barrier(0);
}
template <int N> DI void phi_b(Phi<N>& s, unsigned* p) {
  u16x2 tm[2] = {{0, 0}, {0, 0}};
#pragma unroll
  for (int i = 0; i < N; ++i) {
    s.e[i][0] = __builtin_amdgcn_fmed3f(__builtin_amdgcn_exp2f(s.e[i][0]), 0.f, 1.f);
    tm[i & 1] = __builtin_elementwise_max(tm[i & 1], __builtin_bit_cast(u16x2, s.tb[i]));
    s.e[i][1] = __builtin_amdgcn_fmed3f(__builtin_amdgcn_exp2f(s.e[i][1]), 0.f, 1.f);
    __builtin_amdgcn_sched_barrier(0);
  }
#pragma unroll
  for (int i = 0; i < N; ++i) s.tr[i] += s.e[i];
  __builtin_amdgcn_sched_barrier(0);
#pragma unroll
  for (int i = 0; i < N; ++i) { s.tr[i][0] = fmaxf(s.tr[i][0], s.e[i][0]); s.tr[i][1] = fmaxf(s.tr[i][1], s.e[i][1]); }
  const u16x2 tx = __builtin_elementwise_max(tm[0], tm[1]);
  __builtin_amdgcn_sched_barrier(0);
#pragma unroll
  for (int i = 0; i < N; ++i) p[i] = pack2(s.tr[i][0], s.tr[i][1]);
  if (__any(tx[0] > 0xC0F0 || tx[1] > 0xC0F0)) {
#pragma unroll
    for (int i = 0; i < N; ++i) p[i] = pack2(bflo(p[i]) + 1e-6f, bfhi(p[i]) + 1e-6f);
  }
}
template <int N> DI void phi_n(const f32x2* t, unsigned* p) { Phi<N> s; phi_a<N>(t, s); phi_b<N>(s, p); }
#define RLX_AGENT __ATOMIC_RELAXED, __HIP_MEMORY_SCOPE_AGENT
DI void st64(char* ws, unsigned off, u64 v) { __hip_atomic_store((gu64*)(ws + off), v, RLX_AGENT); }
DI unsigned ld32(char* ws, unsigned off) { return __hip_atomic_load((gu32*)(ws + off), RLX_AGENT); }
#define WAIT_V(n) asm volatile("s_waitcnt vmcnt(%c0)" ::"i"(n) : "memory")
#define RAW_BAR() do { asm volatile("s_waitcnt lgkmcnt(0)" ::: "memory"); __builtin_amdgcn_s_barrier(); asm volatile("" ::: "memory"); } while (0)
#define SB0() __builtin_amdgcn_sched_barrier(0)
#define WAIT_VR(n) asm volatile("s_waitcnt vmcnt(%c0)" ::"i"(n) : "memory", RSV)
#define RAW_BARR() do { asm volatile("s_waitcnt lgkmcnt(0)" ::: "memory", RSV); __builtin_amdgcn_s_barrier(); asm volatile("" ::: "memory", RSV); } while (0)
#define KEEP2() asm volatile("" ::: "memory", RSV2)
#define RAW_BAR2() do { asm volatile("s_waitcnt lgkmcnt(0)" ::: "memory", RSV2); __builtin_amdgcn_s_barrier(); KEEP2(); } while (0)
constexpr unsigned SPIN_CAP = 1u << 18;
#define SPIN _Pragma("nounroll") for (unsigned s_ = 0; s_ < SPIN_CAP; ++s_)
constexpr unsigned SENT = 0xFFFFFFFFu;
DI unsigned sane1(unsigned v) { return min(v, 0xFFFFFFFEu); }
DI u32x4 sane(u32x4 v) { v[1] = sane1(v[1]); v[3] = sane1(v[3]); return v; }
DI unsigned chk(unsigned m, u32x4 v) { return max(max(m, v[1]), v[3]); }
DI bool wave_ok(unsigned m) { return !__any(m == SENT); }
DI unsigned key_idx(unsigned k) { return (0xFFFFu - (k & 0xFFFFu)) & (S_ - 1); }
DI float key_val(unsigned k) { const unsigned o = k >> 16; return __uint_as_float(((o & 0x8000u) ? (o & 0x7fffu) : (~o & 0xffffu)) << 16); }
DI void top2_mix(unsigned& b0, unsigned& b1, unsigned c0, unsigned c1) {
  const unsigned lo = min(b0, c0), mx = max(b1, c1);
  b0 = max(b0, c0); b1 = max(lo, mx);
}
DI void top2_lo(unsigned& b0, unsigned& b1) {
  top2_mix(b0, b1, dppu<DPP_X1>(b0), dppu<DPP_X1>(b1));
  top2_mix(b0, b1, dppu<DPP_X2>(b0), dppu<DPP_X2>(b1));
  top2_mix(b0, b1, dppu<DPP_HMIR>(b0), dppu<DPP_HMIR>(b1));
}
DI void top2_mid(unsigned& b0, unsigned& b1) {
  top2_mix(b0, b1, dppu<DPP_MIR>(b0), dppu<DPP_MIR>(b1));
  auto p = __builtin_amdgcn_permlane16_swap(b0, b0, false, false); auto q = __builtin_amdgcn_permlane16_swap(b1, b1, false, false);
  b0 = p[0]; b1 = q[0]; top2_mix(b0, b1, p[1], q[1]);
}
DI void top2_hi(unsigned& b0, unsigned& b1) {
  top2_mid(b0, b1);
  auto p = __builtin_amdgcn_permlane32_swap(b0, b0, false, false); auto q = __builtin_amdgcn_permlane32_swap(b1, b1, false, false);
  b0 = p[0]; b1 = q[0]; top2_mix(b0, b1, p[1], q[1]);
}

constexpr int K1_STAGE = 40960;
constexpr int LDS_TOTAL = 4 * K1_STAGE;
constexpr int L_STM = 0;
constexpr int L_MISC = 2048;
constexpr int L_XK = 5120;
constexpr int L_KX = 3328;
constexpr int L_ACT = 2304;
constexpr int L_CAND = 4096;
constexpr int L_WO = 16384;
constexpr int L_P = 16384, L_PK = 7 * 16384;
constexpr int L_RED = 65536;
constexpr int wo_slot(int j) { return L_WO + (j % 9) * 16384; }
constexpr int CODE_LINES = 12;
#define OW_ 8
constexpr int OW = OW_;
#define W1A_ 16
constexpr int W1A = W1A_;
#define GSCHED 5, 9, 13, 17, 21, 24, 27, 30, 33, 36, 39, 42, 45, 48, 48
constexpr int GS[15] = {GSCHED};
constexpr int gops(int g) { return (g / 3) * 10 + (g % 3 == 0 ? 0 : g % 3 == 1 ? 4 : 7); }
constexpr bool gs_ok() {
  bool ok = GS[0] >= 3 && GS[0] <= 9 && GS[1] >= 6 && GS[1] <= 12 && GS[0] <= GS[1] && GS[12] == 45 && GS[13] == 48 && GS[14] == 48;
  for (int t = 0; t < 12; ++t) ok = ok && GS[t + 2] <= 3 * (t + 5) && GS[t + 2] >= 3 * (t + 3) && GS[t + 2] >= GS[t + 1] + 3;
  return ok;
}
static_assert(gs_ok(), "staging schedule");
#define WSCHED 0, 0, 0, 0, 0, 0, 0, 0, 0, 2, 4, 6, 8
constexpr int WS_[13] = {WSCHED};
#define PACE 1
#define P_KVP 5
#define P_KVF 4
#define P_FB 12
#define P_C 16
#define P_D 5
#define P_GU 12
#define P_E 10
#define SLEEP(n) do { if (n) __builtin_amdgcn_s_sleep(n); } while (0)
constexpr int VW = 160;

__global__ __launch_bounds__(512, 2) void fused(const u16* __restrict__ x, const u16* __restrict__ wqkv,
                                                const u16* __restrict__ wo, const u16* __restrict__ w_r,
                                                const u16* __restrict__ wg, const u16* __restrict__ wu,
                                                const u16* __restrict__ wd, u16* __restrict__ out, char* __restrict__ ws) {
  extern __shared__ __attribute__((aligned(16))) char smem[];
  u64 pc0; asm volatile("s_getpc_b64 %0" : "=s"(pc0));
  const int wave = __builtin_amdgcn_readfirstlane(threadIdx.x >> 6);
  const unsigned lds0 = (unsigned)(size_t)smem;
  if (wave == 0) {
    const int l = threadIdx.x;
    asm volatile("s_mov_b32 m0, %1\n\ts_nop 0\n\tglobal_load_lds_dword %0, off"
                 :: "v"((const char*)(pc0 - (pc0 & 127)) + ((blockIdx.x >> 3) + (l < CODE_LINES ? l : 0) * 32) * 128), "s"(__builtin_amdgcn_readfirstlane(lds0 + 3 * K1_STAGE)) : "memory");
  }
#define RB_(b) (((b) & 1) * 8 + (((b) >> 3) & 7))
#define HP_(b) ((((b) & 7) >> 1) * 4 + ((b) >> 6))
#define KROT_(b) ((4 * ((HP_(b) >> 2) + (RB_(b) >> 3) + 2 * ((RB_(b) >> 2) & 1))) & 15)
  unsigned voA0, voA1, voA2, voA3, voB0, voB1, voB2, voB3, voB4, voB5;
  const unsigned l4 = __builtin_amdgcn_readfirstlane(lds0 + (wave - 4) * 4096), l6 = __builtin_amdgcn_readfirstlane(lds0 + (wave - 4) * 6144);
  const int krot0 = KROT_(blockIdx.x);
  auto group = [&](auto G) {
    constexpr int t = G.v / 3, p = G.v % 3, sl = (t & 3) * K1_STAGE;
    const int kt = (t + krot0) & 15;
    if (p == 0) glds4r<sl>(voA0, voA1, voA2, voA3, (const char*)x + kt * 128, l4);
    else if (p == 1) glds3r<sl + 16384>(voB0, voB1, voB2, (const char*)wqkv + kt * 64 * (3 * D_ * 2), l6);
    else glds3r<sl + 16384 + 3072>(voB3, voB4, voB5, (const char*)wqkv + kt * 64 * (3 * D_ * 2), l6);
  };
  if (wave >= 4) {
    const int lane = threadIdx.x & 63, sw = wave - 4;
    const int r0 = RB_(blockIdx.x) * 128 + 32 * sw + (lane >> 3);
    voA0 = (unsigned)(r0 * D_ + ((lane & 7) ^ (lane >> 4)) * 8) * 2u;
    voA1 = ((voA0 + 8 * D_ * 2) ^ 64) - 1024; voA2 = voA0 + 16 * D_ * 2 - 2048; voA3 = voA1 + 16 * D_ * 2 - 2048;
    asm volatile("" : "+v"(voA0), "+v"(voA1), "+v"(voA2), "+v"(voA3));
    group(IC<0>{});
    int l2 = lane; asm volatile("" : "+v"(l2));
    voB0 = (unsigned)((16 * sw + 4 * (l2 >> 5) + ((l2 >> 2) & 3)) * (3 * D_) + HP_(blockIdx.x) * 64 + ((l2 >> 4) & 1) * 32 + (l2 & 3) * 8) * 2u;
    voB1 = voB0 + 1024; voB2 = voB0 + 2048;
    voB3 = (voB0 ^ 32) + 8 * 3 * D_ * 2; voB4 = voB3 + 1024; voB5 = voB3 + 2048;
    asm volatile("" : "+v"(voB0), "+v"(voB1), "+v"(voB2), "+v"(voB3), "+v"(voB4), "+v"(voB5));
    sfor<1, 3>(group);
  }
  int bid = blockIdx.x; asm volatile("" : "+s"(bid));
  const int xcd = bid & 7, idx = bid >> 3;
  const int rb = RB_(bid), hp = HP_(bid);
  const int rp = xcd * 4 + (idx >> 3), cb = idx & 7;
  const int hs_ = (hp + 4 + (int)(((size_t)wqkv >> 7) & 7)) & 7;
  const int g8 = ((hp >> 3) * 4 + (rb >> 2)) * 8 + hs_, kq = rb & 3;
  const int krot = KROT_(bid), posA = 1 + (hp & 3), posB = 1 + (rb & 3);
  const int wrot = 4 * rp + (xcd >> 1), wdir = (xcd & 1) ? -1 : 1;
  auto whead = [&](int t) { return (wrot + wdir * t) & 15; };
  const unsigned rs_slot = WS_RS + (rb * 16 + ((krot + posA) & 15)) * 1024, cs_slot = WS_CS + (hp * 16 + ((krot + posB) & 15)) * 768;
  const __amdgpu_buffer_rsrc_t wsr = __builtin_amdgcn_make_buffer_rsrc(ws, 0, WS_END, 0x00020000);
#define LDW(off) __builtin_amdgcn_raw_buffer_load_b128(wsr, (off), 0, 16)
#define STW(v, off) __builtin_amdgcn_raw_buffer_store_b128((v), wsr, (off), 0, 16)
#define STW1(v, off) __builtin_amdgcn_raw_buffer_store_b32((v), wsr, (off), 0, 16)
  float* stm = (float*)(smem + L_STM);
  volatile LAS int* misc = (volatile LAS int*)(smem + L_MISC);
#define LANES() int tid = threadIdx.x; asm volatile("" : "+v"(tid)); const int lane = tid & 63, r = lane & 31, h = lane >> 5; (void)r; (void)h
#define EPI() const int erow = tid >> 3, eseg = tid & 7; const size_t ego = (size_t)(rp * 64 + erow) * D_ + cb * 128 + eseg * 8
  u32x4 xv[2], wv[2];
  f32x2 xf[8], wf2[8];
  float sw_;
  auto unpack_xw = [&]() {
    f32x2 a = {0.f, 0.f};
#pragma unroll
    for (int i = 0; i < 8; ++i) { xf[i] = unpk(xv[i >> 2][i & 3]); wf2[i] = unpk(wv[i >> 2][i & 3]); a += wf2[i]; }
    sw_ = oct_sum(a[0] + a[1]);
#pragma unroll
    for (int i = 0; i < 8; ++i) asm volatile("" : "+v"(xf[i]), "+v"(wf2[i]));
    asm volatile("" : "+v"(sw_));
  };
  u32x4 wf[24];

  auto wtr = [&](auto I) {
    constexpr int m = I.v >> 3, c = I.v & 7, R = VW + 32 * m + (c >> 1);
    const unsigned sel = (c & 1) ? 0x07060302u : 0x05040100u;
    wf[I.v] = (u32x4){wperm<R, R + 4>(sel), wperm<R + 8, R + 12>(sel), wperm<R + 16, R + 20>(sel), wperm<R + 24, R + 28>(sel)};
  };

  if (wave < 4) {
    LANES();
    const int c16 = lane & 15, g4 = lane >> 4;
    const int trl = (c16 >> 2) * 64 + (c16 & 3) * 8;
    const int wr = wave >> 1, wc = wave & 1;
    const int head = 2 * hp + wc;
    const unsigned kvp_off = WS_KVP + (head * 16 + rb) * 4096 + wr * 2048 + lane * 16;
    const unsigned zp_off = WS_ZP + (head * 16 + rb) * 128;
    const unsigned o_off = WS_O + ((rb * 4 + 2 * wr) * 32 + head) * 2048 + lane * 16;
    const unsigned fb_off = lane < 8 ? WS_FB + ((rb * 2 + wr) * 32 + head) * 128 + lane * 16 : OOB;
    unsigned pfa, pfb = OOB;
    unsigned pfc = lane < 8 ? fb_off : lane < 24 ? rs_slot + wave * 256 + (lane - 8) * 16
        : lane < 40 ? (wave < 3 ? cs_slot + wave * 256 : WS_KVF + hp * 4096 + rb * 256) + (lane - 24) * 16
        : lane < 48 ? (wave < 2 ? zp_off + (lane & 7) * 16 : (wave == 2 && rb < 2) ? WS_ZF + (2 * hp + rb) * 128 + (lane & 7) * 16 : OOB) : OOB;
    if (wave == 0) pfa = WS_ST2 + (rp * 8 + cb) * 1024 + lane * 16;
    else if (wave == 1) pfa = WS_GU + (g8 * 4 + kq) * 1024 + lane * 16;
    else pfa = WS_YP + (((wave - 2) * 8 + 2 * kq + (lane >> 5)) * 64 + g8) * 512 + (lane & 31) * 16;
    if (wave == 0) pfb = lane < 32 ? WS_XS + (rp * 2 + (lane >> 4)) * 2048 + cb * 256 + (lane & 15) * 16 : lane < 40 ? WS_FD + (cb * 32 + rp) * 128 + (lane & 7) * 16 : OOB;
    u32x4 S4 = {SENT, SENT, SENT, SENT};
    asm volatile("" : "+v"(pfa), "+v"(pfb), "+v"(pfc), "+v"(S4));
    __builtin_amdgcn_s_setprio(1);
    f32x4 acc[4][6];
    int ao[2][2], bo[2][2];
    {
#pragma unroll
      for (int i = 0; i < 2; ++i) {
        ao[0][i] = (64 * wr + c16) * 128 + (((4 * i + g4) ^ (c16 >> 1)) * 16); ao[1][i] = ao[0][i] + 2 * K1_STAGE;
        bo[0][i] = 16384 + g4 * 3072 + wc * 256 + trl + ((i ^ (g4 & 1)) * 32); bo[1][i] = bo[0][i] + 2 * K1_STAGE;
        asm volatile("" : "+v"(ao[0][i]), "+v"(ao[1][i]), "+v"(bo[0][i]), "+v"(bo[1][i]));
      }
#pragma unroll
      for (int i = 0; i < 4; ++i)
#pragma unroll
        for (int j = 0; j < 6; ++j) { acc[i][j] = (f32x4){0.f, 0.f, 0.f, 0.f}; asm volatile("" : "+v"(acc[i][j])); }
      struct Frag { u32x4 a[4]; u32x2 bl[6], bh[6]; };
      Frag FA, FB;
#define K1_B(j) ((u32x4){C.bl[j][0], C.bl[j][1], C.bh[j][0], C.bh[j][1]})
#define K1_LA(i) N.a[i] = *(const u32x4*)(smem + ao[sn >> 1][hf] + (sn & 1) * K1_STAGE + (i) * 2048)
#define K1_LL(j) N.bl[j] = trread(smem + bo[sn >> 1][(j) & 1] + (sn & 1) * K1_STAGE + hf * 12288 + ((j) >> 1) * 1024)
#define K1_LH(j) N.bh[j] = trread(smem + bo[sn >> 1][(j) & 1] + (sn & 1) * K1_STAGE + hf * 12288 + ((j) >> 1) * 1024 + 512)
#define K1_M(i, j) if ((j) < 2) mfma16i(K1_B(j), C.a[i], acc[i][j]); else mfma16i(C.a[i], K1_B(j), acc[i][j]); SB0()
      auto sub = [&](Frag& C, Frag& N, int sn, int hf) {
        sfor<0, 24>([&](auto Q) {
          constexpr int q = Q.v;
          constexpr int j = q / 4, i = (j & 1) ? 3 - q % 4 : q % 4;
          if (q == 23) asm volatile("s_waitcnt lgkmcnt(0)" : "+v"(N.bh[5]));
          K1_M(i, j);
          if constexpr (q < 4) { K1_LA(q); } else if constexpr (q < 16) { if (q & 1) { K1_LH((q - 4) >> 1); } else { K1_LL((q - 4) >> 1); } }
          SB0();
        });
      };
      auto subkv = [&](Frag& C, Frag& N, int sn, int hf, bool ld) {
#define K1_QL(i, j, l) K1_M(i, j); if (ld) { l; } SB0()
        K1_QL(0, 2, K1_LL(2)); K1_QL(1, 2, K1_LH(2)); K1_QL(0, 3, K1_LA(0)); K1_QL(1, 3, K1_LA(1)); K1_QL(2, 2, K1_LL(3)); K1_QL(3, 2, K1_LH(3));
        K1_QL(2, 3, K1_LA(2)); K1_QL(3, 3, K1_LA(3)); K1_QL(0, 4, K1_LL(4)); K1_QL(1, 4, K1_LH(4)); K1_QL(2, 4, K1_LL(5)); K1_QL(3, 4, K1_LH(5));
        K1_M(0, 5); K1_M(1, 5); K1_M(2, 5); K1_M(3, 5);
      };
      WAIT_V(0);
      RAW_BAR();
      {
        Frag& N = FA; const int sn = 0, hf = 0;
        K1_LL(0); K1_LH(0); K1_LA(0); K1_LA(1); K1_LL(1); K1_LH(1); K1_LL(2); K1_LH(2); K1_LL(3); K1_LH(3); K1_LA(2); K1_LA(3); K1_LL(4); K1_LH(4); K1_LL(5); K1_LH(5);
      }
#pragma unroll 1
      for (int it = 0; it < 4; ++it) {
#pragma unroll
        for (int s = 0; s < 4; ++s) {
          if (s == 2 && it == 3) break;
          sub(FA, FB, s, 1);
          if (s == 0) WAIT_V(0);
          if (it == 0) { if (s == 1) STW(S4, pfc); if (s == 2) STW(S4, kvp_off); if (s == 3) STW(S4, kvp_off + 1024); }
          if (it == 1) { if (s == 0) STW(S4, pfa); if (s == 1) STW(S4, pfb); }
          if (s != 1 || it < 3) RAW_BAR();
          sub(FB, FA, (s + 1) & 3, 0);
        }
      }
      subkv(FA, FB, 2, 1, true);
      RAW_BAR();
      subkv(FB, FA, 3, 0, true);
      subkv(FA, FB, 3, 1, true);
      subkv(FB, FA, 0, 0, false);
    }
    asm volatile("s_nop 15" ::: "memory");
    {
      f32x4 m4[4], i4[4]; float cs[4];
      SPIN {
        const int ok = misc[12 + (lane & 3)];
        asm volatile("" ::: "memory");
#pragma unroll
        for (int i = 0; i < 4; ++i) {
          cs[i] = stm[256 + wc * 96 + 32 + 16 * i + c16];
          m4[i] = *(const f32x4*)(stm + 64 * wr + 16 * i + 4 * g4); i4[i] = *(const f32x4*)(stm + 128 + 64 * wr + 16 * i + 4 * g4);
        }
        SB0();
        if (__all(ok != 0)) break;
      }
      const u32x4 ONES4 = {0x3F803F80u, 0x3F803F80u, 0x3F803F80u, 0x3F803F80u};
      f32x2 tk[16], tv[16];
#define PR(v, i) ((f32x2){(v)[2 * (i)], (v)[2 * (i) + 1]})
#pragma unroll
      for (int i = 0; i < 16; ++i) {
        tk[i] = PR(acc[(i >> 1) & 3][2 + (i >> 3)], i & 1) - PR(m4[(i >> 1) & 3], i & 1) * cs[i >> 3];
        tv[i] = PR(acc[(i >> 1) & 3][4 + (i >> 3)], i & 1) - PR(m4[(i >> 1) & 3], i & 1) * cs[2 + (i >> 3)];
      }
      SB0();
#pragma unroll
      for (int i = 0; i < 16; ++i) { tk[i] *= PR(i4[(i >> 1) & 3], i & 1); tv[i] *= PR(i4[(i >> 1) & 3], i & 1); }
      SB0();
      unsigned kp[16], vp[16];
#pragma unroll
      for (int i = 0; i < 16; ++i) vp[i] = pack2o(tv[i][0], tv[i][1]);
      phi_n<16>(tk, kp);
#define F4_(p, i) ((u32x4){(p)[4 * (i)], (p)[4 * (i) + 1], (p)[4 * (i) + 2], (p)[4 * (i) + 3]})
      const f32x4 Z4 = {0.f, 0.f, 0.f, 0.f};
      u32x4 vk[2], vg[2];
#pragma unroll
      for (int j = 0; j < 2; ++j) { vk[j] = wr ? F4_(vp, 2 + j) : F4_(vp, j); vg[j] = wr ? F4_(vp, j) : F4_(vp, 2 + j); }
      f32x4 keep[2], give[2], zq[2];
#pragma unroll
      for (int d = 0; d < 2; ++d) { give[d] = mfma16(F4_(kp, 2 * d), vg[0], Z4); zq[d] = mfma16(ONES4, F4_(kp, 2 * d), Z4); }
#pragma unroll
      for (int d = 0; d < 2; ++d) { give[d] = mfma16(F4_(kp, 2 * d + 1), vg[1], give[d]); zq[d] = mfma16(ONES4, F4_(kp, 2 * d + 1), zq[d]); }
#pragma unroll
      for (int d = 0; d < 2; ++d) keep[d] = mfma16(F4_(kp, 2 * d), vk[0], Z4);
#pragma unroll
      for (int d = 0; d < 2; ++d) keep[d] = mfma16(F4_(kp, 2 * d + 1), vk[1], keep[d]);
      SB0();
      const float zv = (lane & 16) ? zq[1][0] : zq[0][0];
      char* xk = smem + L_XK + wave * 2304 + lane * 16;
      const char* xp = smem + L_XK + (wave ^ 2) * 2304 + lane * 16;
      *(f32x4*)xk = give[0]; *(f32x4*)(xk + 1024) = give[1]; *(float*)(xk + 2048 - lane * 12) = zv;
      asm volatile("" ::: "memory");
      misc[16 + wave] = 1;
      f32x4 p0, p1; float pz;
      SPIN {
        const int ok = misc[16 + (wave ^ 2)];
        asm volatile("" ::: "memory");
        p0 = *(const f32x4*)xp; p1 = *(const f32x4*)(xp + 1024); pz = *(const float*)(xp + 2048 - lane * 12);
        SB0();
        if (ok == 1) break;
      }
      STW(sane(__builtin_bit_cast(u32x4, keep[0] + p0)), kvp_off);
      STW(sane(__builtin_bit_cast(u32x4, keep[1] + p1)), kvp_off + 1024);
      STW1(sane1(__float_as_uint(zv + pz)), (lane < 32 && wr == 0) ? zp_off + lane * 4 : OOB);
    }
    {
      u32x4 qa[4][4]; u32x2 ql[4][2], qh[4][2];
#pragma unroll
      for (int u = 0; u < 4; ++u) {
#pragma unroll
        for (int j = 0; j < 2; ++j) {
          ql[u][j] = trread(smem + bo[1][j] + (u >> 1) * K1_STAGE + (u & 1) * 12288);
          qh[u][j] = trread(smem + bo[1][j] + (u >> 1) * K1_STAGE + (u & 1) * 12288 + 512);
        }
#pragma unroll
        for (int i = 0; i < 4; ++i) qa[u][i] = *(const u32x4*)(smem + ao[1][u & 1] + (u >> 1) * K1_STAGE + i * 2048);
      }
      asm volatile("" ::: "memory");
      misc[8 + wave] = 3;
      SB0();
#pragma unroll
      for (int u = 0; u < 4; ++u)
#pragma unroll
        for (int j = 0; j < 2; ++j)
#pragma unroll
          for (int i = 0; i < 4; ++i) acc[i][j] = mfma16((u32x4){ql[u][j][0], ql[u][j][1], qh[u][j][0], qh[u][j][1]}, qa[u][i], acc[i][j]);
      SB0();
    }
    unsigned qp[16];
    Phi<16> qs;
    const unsigned po = WS_KVP + ((2 * hp + (rb >> 3)) * 16 + h) * 4096 + (2 * ((rb >> 2) & 1) + ((lane >> 4) & 1)) * 1024 + (16 * (rb & 3) + (lane & 15)) * 16;
    u32x4 pv[8] = {};
    {
      f32x4 c4[2]; float m_b[4], rinv[4];
#pragma unroll
      for (int i = 0; i < 2; ++i) c4[i] = *(const f32x4*)(stm + 256 + wc * 96 + 16 * i + 4 * g4);
#pragma unroll
      for (int i = 0; i < 4; ++i) { m_b[i] = stm[64 * wr + 16 * i + c16]; rinv[i] = stm[128 + 64 * wr + 16 * i + c16]; }
      SB0();
      f32x2 tq[16];
#pragma unroll
      for (int i = 0; i < 16; ++i) tq[i] = PR(acc[i >> 2][(i >> 1) & 1], i & 1) - PR(c4[(i >> 1) & 1], i & 1) * m_b[i >> 2];
      SB0();
#pragma unroll
      for (int i = 0; i < 16; ++i) tq[i] *= rinv[i >> 2];
      SB0();
      phi_a<16>(tq, qs);
#pragma unroll
      for (int i = 0; i < 16; ++i) asm volatile("" : "+v"(qs.tb[i]), "+v"(qs.tr[i]), "+v"(qs.e[i]));
    }
    if (wave == 0) {
      SLEEP(P_KVP);
#pragma unroll
      for (int p = 0; p < 8; ++p) pv[p] = LDW(po + p * 8192);
    }
    asm volatile("" ::: "memory");
    if (wave == 1 && rb < 2) {
      SLEEP(P_KVP);
      SPIN {
        unsigned m = 0;
#pragma unroll
        for (int p = 0; p < 2; ++p) { pv[p] = LDW(WS_ZP + (2 * hp + rb) * 2048 + p * 1024 + lane * 16); m = chk(max(max(m, pv[p][0]), pv[p][2]), pv[p]); }
        if (wave_ok(m)) break;
        asm volatile("" ::: "memory");
      }
      f32x4 s = __builtin_bit_cast(f32x4, pv[0]) + __builtin_bit_cast(f32x4, pv[1]);
#pragma unroll
      for (int i = 0; i < 4; ++i) { s[i] += dppf<DPP_ROR8>(s[i]); s[i] = xh_sum(x16_sum(s[i])); }
      if (lane < 16) st64(ws, WS_ZF + (2 * hp + rb) * 128 + (lane >> 3) * 64 + (lane & 3) * 16 + ((lane >> 2) & 1) * 8,
                          ((u64)sane1(pack2(s[2], s[3])) << 32) | pack2(s[0], s[1]));
    }
    if (wave == 0) {
      SPIN {
        unsigned m = 0;
#pragma unroll
        for (int p = 0; p < 8; ++p) m = chk(m, pv[p]);
        if (wave_ok(m)) break;
        asm volatile("" ::: "memory");
#pragma unroll
        for (int p = 0; p < 8; ++p) pv[p] = LDW(po + p * 8192);
      }
      f32x4 s = __builtin_bit_cast(f32x4, pv[0]);
#pragma unroll
      for (int p = 1; p < 8; ++p) s += __builtin_bit_cast(f32x4, pv[p]);
      const unsigned p0 = pack2(xh_sum(s[0]), xh_sum(s[1])), p1 = pack2(xh_sum(s[2]), xh_sum(s[3]));
      auto a = __builtin_amdgcn_permlane16_swap(p0, p0, false, false);
      auto b = __builtin_amdgcn_permlane16_swap(p1, p1, false, false);
      STW(sane((u32x4){a[0], b[0], a[1], b[1]}), lane < 16 ? WS_KVF + hp * 4096 + rb * 256 + lane * 16 : OOB);
    }
    phi_b<16>(qs, qp);
#pragma unroll
    for (int i = 0; i < 16; ++i) asm volatile("" : "+v"(qp[i]));
    if (wave == 0) misc[7] = 1;
    else SPIN { if (misc[7] == 1) break; __builtin_amdgcn_s_sleep(1); }
    SLEEP(P_KVF);
    {
      u32x4 k0, k1, z0;
      SPIN {
        k0 = LDW(WS_KVF + head * 2048 + lane * 16); k1 = LDW(WS_KVF + head * 2048 + 1024 + lane * 16);
        z0 = LDW(WS_ZF + head * 128 + g4 * 16);
        if (wave_ok(chk(chk(chk(0u, k0), k1), z0))) break;
        asm volatile("" ::: "memory");
      }
      const f32x4 Z4 = {0.f, 0.f, 0.f, 0.f};
      f32x4 X[8], Dn[4];
#pragma unroll
      for (int i = 0; i < 4; ++i) Dn[i] = mfma16(z0, F4_(qp, i), Z4);
#pragma unroll
      for (int i = 0; i < 8; ++i) X[i] = mfma16((i & 1) ? k1 : k0, F4_(qp, i >> 1), Z4);
      SB0();
      float ri[4]; unsigned a[16]; f32x2 f[16];
#pragma unroll
      for (int i = 0; i < 4; ++i) ri[i] = __builtin_amdgcn_rcpf(rbf(rbf(Dn[i][0]) + 1e-6f));
      SB0();
#pragma unroll
      for (int i = 0; i < 16; ++i) a[i] = pack2o(X[i >> 1][2 * (i & 1)], X[i >> 1][2 * (i & 1) + 1]);
      SB0();
#pragma unroll
      for (int i = 0; i < 16; ++i) f[i] = unpk(a[i]);
      SB0();
#pragma unroll
      for (int i = 0; i < 16; ++i) f[i] *= ri[i >> 2];
      SB0();
#pragma unroll
      for (int i = 0; i < 4; ++i) {
        STW(((u32x4){pack2(f[4 * i][0], f[4 * i][1]), pack2(f[4 * i + 1][0], f[4 * i + 1][1]), pack2(f[4 * i + 2][0], f[4 * i + 2][1]), pack2(f[4 * i + 3][0], f[4 * i + 3][1])}),
            o_off + (i >> 1) * 65536 + (i & 1) * 1024);
        SB0();
      }
    }
    {
      EPI();
      const int rt = wave & 1, kh = wave >> 1;
      const unsigned ob = WS_O + ((rp * 2 + rt) * 32 + 16 * kh) * 2048 + lane * 16;
#define LDO(t, st) __builtin_amdgcn_raw_buffer_load_b128(wsr, ob + (st) * 1024, whead(t) * 2048, 16)
      int woff[2];
#pragma unroll
      for (int i = 0; i < 2; ++i) woff[i] = kh * 8192 + (g4 >> 1) * 2048 + (g4 & 1) * 512 + trl + ((i ^ (g4 & 1)) * 32);
      struct Frag2 { u32x2 lo[8], hi[8]; };
      Frag2 G[2];
      auto ld2 = [&](Frag2& N, int sn, int nt, int hi) {
        const char* p = smem + sn + woff[nt & 1] + (nt >> 2) * 1024 + ((nt >> 1) & 1) * 256;
        if (hi) N.hi[nt] = trread(p + 4096); else N.lo[nt] = trread(p);
      };
      WAIT_V(0);
      STW(((u32x4){1u, 1u, 1u, 1u}), fb_off);
      RAW_BAR();
#pragma unroll
      for (int i = 0; i < 16; ++i) ld2(G[0], wo_slot(0), i >> 1, i & 1);
      f32x4 a2[2][8];
#pragma unroll
      for (int i = 0; i < 16; ++i) a2[i & 1][i >> 1] = (f32x4){0.f, 0.f, 0.f, 0.f};
      SLEEP(P_FB);
      SPIN { if (wave_ok(ld32(ws, WS_FB + (rp * 32 + 16 * kh + (lane & 15)) * 128))) break; }
      asm volatile("" ::: "memory");
      char* pr[2];
#pragma unroll
      for (int i = 0; i < 2; ++i) pr[i] = smem + L_P + kh * L_PK + (32 * rt + c16) * 512 + (((4 * i + g4) ^ (c16 & 7)) * 16);
      u32x4 of[16][2];
#pragma unroll
      for (int t = 0; t < OW; ++t) { of[t][0] = LDO(t, 0); of[t][1] = LDO(t, 1); }
      SB0();
      sfor<0, 16>([&](auto T) {
        constexpr int t = T.v;
        Frag2& C = G[t & 1]; Frag2& N = G[(t + 1) & 1];
#pragma unroll
        for (int i = 0; i < 16; ++i) {
          const int nt = i >> 1, st = i & 1;
          a2[st][nt] = mfma16((u32x4){C.lo[nt][0], C.lo[nt][1], C.hi[nt][0], C.hi[nt][1]}, of[t][st], a2[st][nt]);
          SB0();
          if ((t & 1) && t <= 11 && i == 8) {
            asm volatile("s_waitcnt lgkmcnt(8)" ::: "memory"); __builtin_amdgcn_s_barrier(); asm volatile("" ::: "memory");
          }
          if (t < 15) ld2(N, wo_slot(t + 1), nt, st);
          if (t > 0 && t - 1 + OW < 16 && (i == 3 || i == 11)) of[(t - 1 + OW) & 15][i >> 3] = LDO(t - 1 + OW, i >> 3);
          if (t - 1 + OW == 16 && (i == 3 || i == 11)) { xv[i >> 3] = *(const u32x4*)(x + ego + (i >> 3) * 64); wv[i >> 3] = *(const u32x4*)(w_r + cb * 128 + eseg * 8 + (i >> 3) * 64); }
          if (t == 15 && i >= 4) *(f32x4*)(pr[(nt - 2) & 1] + ((nt - 2) >> 1) * 128 + st * 8192) = a2[st][(nt - 2) & 7];
          SB0();
        }
      });
#pragma unroll
      for (int i = 12; i < 16; ++i) *(f32x4*)(pr[(i >> 1) & 1] + (i >> 2) * 128 + (i & 1) * 8192) = a2[i & 1][i >> 1];
    }
    unpack_xw();
    __builtin_amdgcn_s_setprio(0);
  } else {
    LANES();
    const int sw = wave - 4, t2 = tid - 256;
    __builtin_amdgcn_s_setprio(3);
    const bool real = hs_ != 7;
    unsigned hs_g0 = 0, hs_cur = 0;
    u32x4 ms_exp = {0, 0, 0, 0};
    bool hs_act = true, ms_act = !real && hp < 8 && rb == 0 && sw == 1;
    const unsigned hs_off = (lane == 1 ? SY_GO : SY_CH) + bid * 4;
    const unsigned re_off = t2 == 0 ? SY_RE + bid * 4 : OOB;
    const unsigned ms_off = lane * 16;
    const char* wsu = uni(ws);
#define HS_PROBE() asm volatile("s_nop 4\n\tglobal_load_dword v156, %0, %1 sc1" :: "v"(hs_off), "s"(wsu) : "memory", RSV)
#define MS_PROBE() asm volatile("s_nop 4\n\tglobal_load_dwordx4 v[152:155], %0, %1 offset:2048 sc1" :: "v"(ms_off), "s"(wsu) : "memory", RSV)
    auto hs_step = [&](bool first) {
      if (hs_act) {
        const unsigned hv_ = rget<156>();
        hs_act = __builtin_amdgcn_readlane(hv_, 1) == hs_g0;
        const unsigned c = __builtin_amdgcn_readlane(hv_, 0);
        if (sw == 0 && (first || c != hs_cur)) STW1(hv_, re_off);
        hs_cur = c;
        HS_PROBE();
      }
      if (ms_act) {
        const unsigned d = ((rget<152>() ^ ms_exp[0]) | (rget<153>() ^ ms_exp[1])) | ((rget<154>() ^ ms_exp[2]) | (rget<155>() ^ ms_exp[3]));
        ms_act = __any(d != 0u);
        STW(ms_exp, ms_act ? OOB : SY_GO + ms_off);
        MS_PROBE();
      }
    };
    HS_PROBE();
    if (ms_act) {
      MS_PROBE();
      asm volatile("s_nop 4\n\tglobal_load_dwordx4 v[148:151], %0, %1 offset:1024 sc1" :: "v"(ms_off), "s"(wsu) : "memory", RSV);
    }
    const int gr = (g8 >> 3) * 7 + min(hs_, 6);
    const unsigned voG = (unsigned)((lane >> 3) * 8 * F_ + (lane & 7) * 8) * 2u, voD = (unsigned)((lane >> 3) * 8 * D_ + (lane & 7) * 8) * 2u;
    const size_t gofs = ((size_t)(kq * 256 + sw * 64) * F_ + gr * 64) * 2;
    const char* pG = (const char*)wg + gofs;
    const char* pU = (const char*)wu + gofs;
    const char* pD = (const char*)(wd + (size_t)(gr * 64) * D_ + kq * 256 + sw * 64);
    auto wstep = [&](auto I) {
      constexpr int i = I.v;
      if (i < 8) wload<VW + 4 * i>(voG, pG + (i & 7) * (F_ * 2));
      else if (i < 16) wload<VW + 4 * i>(voG, pU + (i & 7) * (F_ * 2));
      else wload2<VW + 4 * i>(voD, pD + (i & 7) * (D_ * 2));
    };
    unsigned voW;
    {
      const int bi = lane >> 4, q = (lane >> 2) & 3, ch = lane & 3;
      voW = (unsigned)((4 * (bi >> 1) + q) * D_ + (bi & 1) * 32 + (ch ^ (bi & 2)) * 8) * 2u;
    }
    const char* baseW = (const char*)(wo + (size_t)((16 * (sw >> 1)) * 32 + 16 * (sw & 1)) * D_ + cb * 128) - 1024;
    auto stageW = [&](auto J, bool rsv) {
      const char* s_ = baseW + (size_t)whead(J.v) * (32 * D_ * 2);
      if (rsv) glds4r<wo_slot(J.v)>(voW + 1024, voW + 128, voW + 8 * D_ * 2 - 1024, voW + 8 * D_ * 2 + 128 - 2048, s_, l4);
      else glds4<wo_slot(J.v)>(voW + 1024, voW + 128, voW + 8 * D_ * 2 - 1024, voW + 8 * D_ * 2 + 128 - 2048, s_, l4);
    };
    int sta[2], stb[3];
    {
#pragma unroll
      for (int c = 0; c < 2; ++c) { sta[c] = (posA & 3) * K1_STAGE + (32 * sw + (lane & 15)) * 128 + (((4 * c + (lane >> 4)) ^ ((lane & 15) >> 1)) * 16); asm volatile("" : "+v"(sta[c]) :: RSV); }
#pragma unroll
      for (int i = 0; i < 3; ++i) {
        const int G = sw * 3 + i, hd = G >= 6 ? 1 : 0, mat = (G - 6 * hd) >> 1;
        stb[i] = (posB & 3) * K1_STAGE + 16384 + (lane >> 4) * 6144 + mat * 1024 + hd * 256 + ((lane & 15) >> 2) * 64 + (G & 1) * 32 + (lane & 3) * 8;
        asm volatile("" : "+v"(stb[i]) :: RSV);
      }
    }
    unsigned rs_dst = (lane >> 4) == ((lane & 15) >> 2) ? rs_slot + (32 * sw + (lane & 15)) * 8 : OOB, cs_dst = lane < 48 ? cs_slot + (sw * 48 + lane) * 4 : OOB;
    unsigned rs_off = WS_RS + rb * 16384 + (lane >> 4) * 1024 + sw * 256 + (lane & 15) * 16;
    unsigned cs_off = WS_CS + hp * 12288 + (lane >> 4) * 768 + sw * 192 + min(lane & 15, 11) * 16;
    asm volatile("" : "+v"(rs_dst), "+v"(cs_dst), "+v"(rs_off), "+v"(cs_off) :: RSV);
    int stm_dst = ((lane >> 5) * 128 + sw * 32 + (lane & 15) * 2 + ((lane >> 4) & 1)) * 4, cs_put = (lane & 15) < 12 ? (256 + sw * 48 + (lane & 15) * 4 + (lane >> 4)) * 4 : 1792 + lane * 4;
    asm volatile("" : "+v"(stm_dst), "+v"(cs_put) :: RSV);
    auto st_load = [&]() { sfor<0, 4>([&](auto I) { sload<224 + 4 * I.v>(rs_off, wsu + I.v * 4096); sload<240 + 4 * I.v>(cs_off, wsu + I.v * 3072); }); };
    sfor<3, GS[0]>(group);
    WAIT_VR(gops(GS[0]) - 10 + 1); RAW_BARR();
    sfor<GS[0], GS[1]>(group);
    WAIT_VR(gops(GS[1]) - 20); RAW_BARR();
    hs_g0 = __builtin_amdgcn_readlane(rget<156>(), 1);
    if (ms_act) {
      const u32x4 re = {rget<152>(), rget<153>(), rget<154>(), rget<155>()}, g0 = {rget<148>(), rget<149>(), rget<150>(), rget<151>()};
#pragma unroll
      for (int j = 0; j < 4; ++j) { const unsigned c = re[j] + 1u; ms_exp[j] = (c == g0[j]) ? c + 1u : c; }
      STW(ms_exp, SY_CH + ms_off);
    }
    u32x4 ra[4] = {}; u32x2 rc[3][4] = {};
    auto chunk = [&](auto C) {
      const u32x4 ONES4 = {0x3F803F80u, 0x3F803F80u, 0x3F803F80u, 0x3F803F80u};
      const f32x4 Z4 = {0.f, 0.f, 0.f, 0.f};
      if constexpr (C.v == 0) {
        asm volatile("" : "+v"(ra[0]), "+v"(ra[1]), "+v"(ra[2]), "+v"(ra[3]) :: RSV);
        f32x4 s[2], q[2];
#pragma unroll
        for (int i = 0; i < 2; ++i) { q[i] = mfma16(ra[2 * i], ra[2 * i], Z4); s[i] = mfma16(ONES4, ra[2 * i], Z4); }
#pragma unroll
        for (int i = 0; i < 2; ++i) { q[i] = mfma16(ra[2 * i + 1], ra[2 * i + 1], q[i]); s[i] = mfma16(ONES4, ra[2 * i + 1], s[i]); }
#pragma unroll
        for (int i = 0; i < 2; ++i) {
          float a = q[i][0], c = q[i][1], d = q[i][2], e = q[i][3];
          asm volatile("" : "+v"(a), "+v"(c), "+v"(d), "+v"(e) :: RSV);
          const float lo = (lane & 1) ? c : a, hi = (lane & 1) ? e : d;
          __builtin_amdgcn_raw_buffer_store_b64((u32x2){sane1(__float_as_uint(s[i][0])), sane1(__float_as_uint((lane & 2) ? hi : lo))}, wsr, rs_dst + i * 128, 0, 16);
        }
      } else {
        f32x4 c[3];
#pragma unroll
        for (int i = 0; i < 3; ++i) { asm volatile("" : "+v"(rc[i][0]), "+v"(rc[i][1]), "+v"(rc[i][2]), "+v"(rc[i][3]) :: RSV); c[i] = mfma16(ONES4, (u32x4){rc[i][0][0], rc[i][0][1], rc[i][1][0], rc[i][1][1]}, Z4); }
#pragma unroll
        for (int i = 0; i < 3; ++i) c[i] = mfma16(ONES4, (u32x4){rc[i][2][0], rc[i][2][1], rc[i][3][0], rc[i][3][1]}, c[i]);
        float c0 = c[0][0], c1 = c[1][0], c2 = c[2][0];
        asm volatile("" : "+v"(c0), "+v"(c1), "+v"(c2) :: RSV);
        STW1(sane1(__float_as_uint(lane < 16 ? c0 : lane < 32 ? c1 : c2)), cs_dst);
      }
    };
    sfor<0, 12>([&](auto T) {
      constexpr int t = T.v;
      if (t < 4 && posA == t + 1) {
#pragma unroll
        for (int c = 0; c < 4; ++c) ra[c] = *(const u32x4*)(smem + sta[c & 1] + (c >> 1) * 2048);
      }
      if (t < 4 && posB == t + 1) {
#pragma unroll
        for (int i = 0; i < 3; ++i)
#pragma unroll
          for (int j = 0; j < 4; ++j) rc[i][j] = trread(smem + (stb[i] ^ ((j >> 1) * 32)) + (j >> 1) * 3072 + (j & 1) * 512);
      }
      if (t == 2) HS_PROBE();
      if (t >= 4) hs_step(t == 4);
      sfor<GS[t + 1], GS[t + 2]>([&](auto G) {
        group(G);
        if constexpr ((t == 4 || t == 5) && G.v == GS[t + 1]) chunk(IC<t - 4>{});
      });
      if (real) sfor<WS_[t], WS_[t + 1]>(wstep);
      WAIT_VR(t < 11 ? gops(GS[t + 2]) - 10 * (t + 3) : gops(GS[13]) - gops(GS[12]));
      RAW_BARR();
    });
    hs_step(false);
    auto st_put = [&]() -> bool {
      float a[8]; unsigned m = 0, n = 0;
      rsum4<224>(a, m, n); rsum4<240>(a + 4, m, n);
      const float s1 = sw16(a[0], a[2]), q1 = sw16(a[1], a[3]);
      const float cs = sw32(sw16(a[4], a[5]), sw16(a[6], a[7]));
      const float mean = sw32(s1, s1) * (1.0f / 1024.0f);
      const float var = fmaxf(__builtin_fmaf(sw32(q1, q1), 1.0f / 1024.0f, -(mean * mean)), 0.f);
      const float rinv = __builtin_amdgcn_rcpf(rbf(__builtin_amdgcn_sqrtf(rbf(rbf(var) + 1e-5f))));
      *(float*)(smem + L_STM + stm_dst) = (lane & 32) ? rinv : rbf(mean);
      *(float*)(smem + L_STM + cs_put) = cs;
      return wave_ok(max(m, n));
    };
    st_load();
    if (real) sfor<WS_[12], W1A>(wstep);
    if (real) WAIT_VR(8 + W1A - WS_[11]); else WAIT_VR(8);
    if (lane == 0) { misc[8 + sw] = 0; misc[12 + sw] = 0; misc[16 + sw] = 0; misc[7] = 0; }
    RAW_BARR();
    if (real) WAIT_VR(W1A - WS_[12]); else WAIT_VR(0);
    if (!st_put() || hs_act) {
      if (hs_act) hs_act = __builtin_amdgcn_readlane(rget<156>(), 1) == hs_g0;
      if (ms_act) {
        SPIN {
          bool ok = true;
#pragma unroll
          for (int j = 0; j < 4; ++j) ok &= (ld32(ws, SY_RE + lane * 16 + j * 4) == ms_exp[j]);
          if (__all(ok)) break;
        }
        STW(ms_exp, SY_GO + lane * 16);
      }
      if (hs_act) {
        SPIN {
          if (ld32(ws, SY_GO + bid * 4) != hs_g0) break;
          const unsigned c = ld32(ws, SY_CH + bid * 4);
          if (c != hs_cur) { hs_cur = c; STW1(c, re_off); }
        }
      }
      SPIN { st_load(); WAIT_VR(0); if (st_put()) break; }
    }
    asm volatile("" ::: "memory", RSV);
    if (lane == 0) misc[12 + sw] = 1;
    __builtin_amdgcn_s_setprio(0);
    if (real) sfor<W1A, 16>(wstep);
    auto mwait = [&](int v) { SPIN { const int a = misc[8 + (lane & 3)]; if (__all(a >= v)) break; __builtin_amdgcn_s_sleep(1); } asm volatile("" ::: "memory"); };
    sfor<0, 9>([&](auto J) {
      if (J.v == 4) mwait(3);
      stageW(J, J.v < 2);
      if (J.v >= 1) WAIT_V(4 * PACE);
      if (J.v == 1) { WAIT_VR(8); sfor<0, 16>(wtr); }
    });
    EPI();
#pragma unroll
    for (int i = 0; i < 2; ++i) { xv[i] = *(const u32x4*)(x + ego + i * 64); wv[i] = *(const u32x4*)(w_r + cb * 128 + eseg * 8 + i * 64); }
    asm volatile("s_waitcnt vmcnt(0)" : "+v"(xv[0]), "+v"(xv[1]), "+v"(wv[0]), "+v"(wv[1]) :: "memory");
    unpack_xw();
    RAW_BAR();
    RAW_BAR(); stageW(IC<9>{}, false); stageW(IC<10>{}, false);
    RAW_BAR(); stageW(IC<11>{}, false); stageW(IC<12>{}, false);
    RAW_BAR(); stageW(IC<13>{}, false); stageW(IC<14>{}, false);
    WAIT_V(16); RAW_BAR(); stageW(IC<15>{}, false);
    WAIT_V(12); RAW_BAR();
    WAIT_V(0); RAW_BAR();
    if (real) sfor<16, 24>(wstep);
  }
  LANES(); EPI();
  const int grow = rp * 64 + erow;
  int pa0 = L_P + erow * 512 + ((2 * eseg) ^ (erow & 7)) * 16, pa1 = pa0 ^ 16;
  unsigned st_dst = eseg == 0 ? WS_ST2 + ((rp * 8 + cb) * 64 + erow) * 16 : OOB, st_src = WS_ST2 + rp * 8192 + lane * 16;
  unsigned fd_dst = lane < 8 ? WS_FD + (cb * 32 + rp) * 128 + lane * 16 : OOB, kidx = 0xFFFFu - grow;
  asm volatile("" : "+v"(pa0), "+v"(pa1), "+v"(st_dst), "+v"(st_src), "+v"(fd_dst), "+v"(kidx));
  RAW_BAR2();
  unsigned hb[8];
  f32x2 hv[8];
  {
    f32x4 pp[2][4];
#pragma unroll
    for (int k = 0; k < 2; ++k)
#pragma unroll
      for (int i = 0; i < 4; ++i) pp[k][i] = *(const f32x4*)(smem + ((i & 1) ? pa1 : pa0) + (i >> 1) * 256 + k * L_PK);
    SB0();
    f32x2 s = {0.f, 0.f}, q = {0.f, 0.f}, p2 = {0.f, 0.f};
#pragma unroll
    for (int i = 0; i < 8; ++i) {
      const f32x4 a = pp[0][i >> 1] + pp[1][i >> 1];
      const f32x2 u = xf[i] + unpk(pack2o(a[2 * (i & 1)], a[2 * (i & 1) + 1]));
      hb[i] = pack2o(u[0], u[1]);
      hv[i] = unpk(hb[i]);
      s += hv[i]; q += hv[i] * hv[i]; p2 += hv[i] * wf2[i];
    }
    STW(sane((u32x4){__float_as_uint(oct_sum(s[0] + s[1])), __float_as_uint(oct_sum(q[0] + q[1])), __float_as_uint(oct_sum(p2[0] + p2[1])), __float_as_uint(sw_)}), st_dst);
  }
  KEEP2();
  const int sw = wave - 4;
  const unsigned fd_src = WS_FD + xcd * 4096 + r * 128;
  u32x2 kpk = {SENT, SENT};
  if (wave == 0) {
    u32x4 pa[8];
    SLEEP(P_C);
    SPIN {
      unsigned m = 0;
#pragma unroll
      for (int j = 0; j < 8; ++j) { pa[j] = LDW(st_src + j * 1024); m = chk(m, pa[j]); }
      if (wave_ok(m)) break;
      KEEP2();
    }
#define F4(v) __builtin_bit_cast(f32x4, v)
    const f32x4 t = ((F4(pa[0]) + F4(pa[1])) + (F4(pa[2]) + F4(pa[3]))) + ((F4(pa[4]) + F4(pa[5])) + (F4(pa[6]) + F4(pa[7])));
    const float mean = t[0] * (1.0f / 1024.0f);
    const float var = fmaxf(__builtin_fmaf(t[1], 1.0f / 1024.0f, -(mean * mean)), 0.f);
    const float mb = rbf(mean), rs = __builtin_amdgcn_rcpf(rbf(__builtin_amdgcn_sqrtf(rbf(rbf(var) + 1e-5f))));
    *(f32x2*)(smem + L_KX + lane * 8) = (f32x2){mb, rs};
    const float sc = fmaxf((t[2] - mb * t[3]) * rs, -INFINITY);
    const int u = (int)(pack2(0.f, sc) & 0xffff0000u);
    unsigned b0 = ((unsigned)(u ^ ((u >> 31) | (int)0x80000000)) & 0xffff0000u) | (0xFFFFu - (rp * 64 + lane)), b1 = 0;
    top2_lo(b0, b1); top2_hi(b0, b1);
    STW(((u32x4){b0, b1, b0, b1}), fd_dst);
    if (lane == 0) *(u32x2*)(smem + L_MISC + 112) = (u32x2){b0, b1};
  } else if (wave >= 4) {
    asm volatile("s_waitcnt vmcnt(0)" ::: "memory", RSV2);
    sfor<16, 24>(wtr);
  }
  RAW_BAR();
  bool cand;
  u32x4 ob[2];
  {
    const f32x2 mr = *(const f32x2*)(smem + L_KX + erow * 8);
    const u32x2 kk = *(const u32x2*)(smem + L_MISC + 112);
    const float mb = mr[0], rs = mr[1];
    const unsigned b0 = kk[0], b1 = kk[1];
    u32x4 nq[2];
#pragma unroll
    for (int i = 0; i < 8; ++i) {
      const f32x2 d = hv[i] - mb;
      const f32x2 n = unpk(pack2o(d[0], d[1])) * rs;
      nq[i >> 2][i & 3] = pack2o(n[0], n[1]);
    }
    const bool c2 = (b1 & 0xffffu) == kidx;
    cand = c2 || (b0 & 0xffffu) == kidx;
    if (cand) {
      const unsigned xo = WS_XS + (rp * 2 + (c2 ? 1 : 0)) * 2048 + cb * 256 + eseg * 16;
      STW(sane(nq[0]), xo); STW(sane(nq[1]), xo + 128);
      char* cp = smem + L_CAND + (c2 ? 512 : 0) + eseg * 16;
      *(u32x4*)cp = (u32x4){hb[0], hb[1], hb[2], hb[3]}; *(u32x4*)(cp + 128) = (u32x4){hb[4], hb[5], hb[6], hb[7]};
      *(u32x4*)(cp + 256) = nq[0]; *(u32x4*)(cp + 384) = nq[1];
    }
    asm volatile("" ::: "memory");
    if (wave >= 4) { SLEEP(P_D); kpk = __builtin_amdgcn_raw_buffer_load_b64(wsr, fd_src, 0, 16); }
    asm volatile("" ::: "memory");
#pragma unroll
    for (int i = 0; i < 8; ++i) { const f32x2 o = hv[i] + unpk(nq[i >> 2][i & 3]); ob[i >> 2][i & 3] = pack2(o[0], o[1]); }
  }
#define OUT_ST() do { *(u32x4*)(out + ego) = ob[0]; *(u32x4*)(out + ego + 64) = ob[1]; } while (0)
  if (wave < 4) {
    if (!cand) OUT_ST();
    RAW_BAR();
    const unsigned w0 = misc[24], w1 = misc[25];
    if (cand && grow != (int)key_idx(w0) && grow != (int)key_idx(w1)) OUT_ST();
#pragma unroll 1
    for (int tk = 0; tk < 2; ++tk) {
      const unsigned wk = tk ? w1 : w0, slot = misc[26 + tk];
      if ((int)(slot >> 1) != rp) continue;
      u32x4 pv[7];
      const unsigned yo = WS_YP + ((tk * 8 + cb) * 64 + (lane >> 3) * 8) * 512 + wave * 128 + (lane & 7) * 16;
      const int col = (wave >> 1) * 64 + (lane & 7) * 8 + (wave & 1) * 4 + (lane >> 4);
      float sg = rbf(__builtin_amdgcn_rcpf(1.0f + __expf(-key_val(wk))));
      float hh = bflo(*(const u16*)(smem + L_CAND + (slot & 1) * 512 + col * 2)), nn = bflo(*(const u16*)(smem + L_CAND + (slot & 1) * 512 + 256 + col * 2));
      u16* op = out + (size_t)key_idx(wk) * D_ + cb * 128 + col;
      asm volatile("" : "+v"(sg), "+v"(hh), "+v"(nn), "+v"(op));
      if (tk == 0 || (int)(misc[26] >> 1) != rp) {
        SPIN { if (misc[30] == (int)w0) break; __builtin_amdgcn_s_sleep(1); }
        SLEEP(P_E);
      }
      SPIN {
        unsigned m = 0;
#pragma unroll
        for (int p = 0; p < 7; ++p) { pv[p] = LDW(yo + p * 512); m = chk(m, pv[p]); }
        if (wave_ok(m)) break;
        asm volatile("" ::: "memory");
      }
      f32x4 s = __builtin_bit_cast(f32x4, pv[0]);
#pragma unroll
      for (int p = 1; p < 7; ++p) s += __builtin_bit_cast(f32x4, pv[p]);
      float y = sw32(sw16(s[0], s[1]), sw16(s[2], s[3]));
      y += dppf<DPP_ROR8>(y);
      if (!(lane & 8)) *(GAS u16*)op = (u16)pack2(hh + rbf(nn + rbf(rbf(y) * sg)), 0.f);
    }
  } else {
    unsigned xl = WS_XS + (kq * 256 + sw * 64 + (lane >> 4) * 16 + ((lane >> 2) & 1) * 8) * 2;
    int red_dst = (lane < 8 || (lane >= 24 && lane < 32)) ? L_RED + sw * 2048 + ((lane >> 3) & 1) * 1024 + (lane & 7) * 16 : L_RED + 8192 + lane * 16;
    unsigned gu_src = WS_GU + g8 * 4096 + lane * 16;
    int act_dst = L_ACT + sw * 256 + ((lane & 7) * 8 + (lane >> 3)) * 2, act_src = L_ACT + sw * 256 + (lane & 1) * 128 + (lane >> 4) * 32 + ((lane >> 1) & 1) * 16;
    unsigned yp_dst = lane < 16 ? WS_YP + (((lane >> 3) * 8 + 2 * kq + (sw >> 1)) * 64 + g8) * 512 + (sw & 1) * 256 + (lane & 7) * 16 : OOB;
    asm volatile("" : "+v"(xl), "+v"(red_dst), "+v"(gu_src), "+v"(act_dst), "+v"(act_src), "+v"(yp_dst));
    unsigned w0, w1; int s0 = -1, s1 = -1;
    u32x4 xa = {SENT, SENT, SENT, SENT};
    SPIN {
      const bool all = wave_ok(kpk[1]);
      const unsigned k0 = kpk[1] == SENT ? 0u : kpk[0], k1 = kpk[1] == SENT ? 0u : kpk[1];
      w0 = k0; w1 = k1;
      top2_lo(w0, w1); top2_mid(w0, w1);
      const int n0 = 2 * __builtin_ctzll(__ballot(k0 == w0));
      const u64 m1 = __ballot(k0 == w1);
      const int n1 = m1 ? 2 * __builtin_ctzll(m1) : n0 + 1;
      const bool same = n0 == s0 && n1 == s1;
      s0 = n0; s1 = n1;
      asm volatile("" ::: "memory");
      if (all && same) break;
      if (!all) { kpk = __builtin_amdgcn_raw_buffer_load_b64(wsr, fd_src, 0, 16); }
      xa = LDW(xl + ((lane & 1) ? s1 : s0) * 2048);
      if (all) break;
    }
    SPIN {
      if (wave_ok(chk(0u, xa))) break;
      asm volatile("" ::: "memory");
      xa = LDW(xl + ((lane & 1) ? s1 : s0) * 2048);
    }
    {
      const u32x4 z4 = {0u, 0u, 0u, 0u};
      const u32x4 xg = (lane & 2) ? z4 : xa, xu = (lane & 2) ? xa : z4;
      f32x4 d[8];
#pragma unroll
      for (int c = 0; c < 8; ++c) d[c] = mfma16(xg, wf[c], (f32x4){0.f, 0.f, 0.f, 0.f});
      if (tid == 256) { *(u32x4*)(smem + L_MISC + 96) = (u32x4){w0, w1, (unsigned)s0, (unsigned)s1}; misc[30] = 0; }
      SB0();
#pragma unroll
      for (int c = 0; c < 10; ++c) {
        if (c < 8) d[c & 7] = mfma16(xu, wf[8 + (c & 7)], d[c & 7]);
        if (c >= 2) *(f32x4*)(smem + red_dst + (c - 2) * 128) = d[(c - 2) & 7];
        SB0();
      }
    }
    RAW_BAR();
    if (sw == 0) {
      f32x4 t[8];
#pragma unroll
      for (int p = 0; p < 8; ++p) t[p] = *(const f32x4*)(smem + L_RED + p * 1024 + lane * 16);
      SB0();
      STW(sane(__builtin_bit_cast(u32x4, ((t[0] + t[1]) + (t[2] + t[3])) + ((t[4] + t[5]) + (t[6] + t[7])))), gu_src + kq * 1024);
    } else __builtin_amdgcn_s_sleep(2);
    {
      u32x4 q[4];
      if (grow != (int)key_idx(w0) && grow != (int)key_idx(w1)) OUT_ST();
      asm volatile("" ::: "memory");
      SLEEP(P_GU);
      SPIN {
        unsigned m = 0;
#pragma unroll
        for (int j = 0; j < 4; ++j) { q[j] = LDW(gu_src + j * 1024); m = chk(m, q[j]); }
        if (wave_ok(m)) break;
        asm volatile("" ::: "memory");
      }
      const f32x4 tot = (__builtin_bit_cast(f32x4, q[0]) + __builtin_bit_cast(f32x4, q[1])) + (__builtin_bit_cast(f32x4, q[2]) + __builtin_bit_cast(f32x4, q[3]));
      const f32x2 gs = unpk(pack2o(tot[0], tot[1])), us = unpk(pack2o(tot[2], tot[3]));
      const f32x2 sg = unpk(pack2o(gs[0] * __builtin_amdgcn_rcpf(1.0f + __expf(-gs[0])), gs[1] * __builtin_amdgcn_rcpf(1.0f + __expf(-gs[1]))));
      const unsigned a2 = pack2o(sg[0] * us[0], sg[1] * us[1]);
      *(u16*)(smem + act_dst) = (u16)a2; *(u16*)(smem + act_dst + 128) = (u16)(a2 >> 16);
      __builtin_amdgcn_fence(__ATOMIC_RELEASE, "wavefront"); __builtin_amdgcn_wave_barrier(); __builtin_amdgcn_fence(__ATOMIC_ACQUIRE, "wavefront");
      const u32x4 aa = *(const u32x4*)(smem + act_src);
      f32x4 d[8];
#pragma unroll
      for (int c = 0; c < 8; ++c) d[c] = mfma16(aa, wf[16 + c], (f32x4){0.f, 0.f, 0.f, 0.f});
      SB0();
      u32x4 y[2];
#pragma unroll
      for (int c = 0; c < 8; ++c) {
        const float t0 = d[c][0] + dppf<DPP_ROR8>(d[c][2]), t1 = d[c][3] + dppf<DPP_ROR8>(d[c][1]);
        y[c >> 2][c & 3] = __float_as_uint((lane & 8) ? t1 : t0);
      }
      STW(sane(y[0]), yp_dst); STW(sane(y[1]), yp_dst + 128);
      if (tid == 256) misc[30] = w0;
    }
  }
}

extern "C" void kernel_launch(void* const* d_in, const int* in_sizes, int n_in, void* d_out, int out_size,
                              void* d_ws, size_t ws_size, hipStream_t stream) {
  (void)hipFuncSetAttribute((const void*)fused, hipFuncAttributeMaxDynamicSharedMemorySize, LDS_TOTAL);
  fused<<<256, 512, LDS_TOTAL, stream>>>((const u16*)d_in[0], (const u16*)d_in[1], (const u16*)d_in[2], (const u16*)d_in[4],
                                         (const u16*)d_in[5], (const u16*)d_in[6], (const u16*)d_in[7], (u16*)d_out, (char*)d_ws);
}
```

```cpp
#include <hip/hip_runtime.h>
#include <stdint.h>

#define LAS __attribute__((address_space(3)))
#define GAS __attribute__((address_space(1)))
#define DI __device__ __forceinline__

typedef unsigned short u16;
typedef unsigned long long u64;
typedef __attribute__((ext_vector_type(2))) __bf16 bf16x2;
typedef __attribute__((ext_vector_type(8))) __bf16 bf16x8;
typedef __attribute__((ext_vector_type(4))) short s16x4;
typedef __attribute__((ext_vector_type(2))) unsigned short u16x2;
typedef __attribute__((ext_vector_type(4))) float f32x4;
typedef __attribute__((ext_vector_type(2))) float f32x2;
typedef __attribute__((ext_vector_type(4))) unsigned u32x4;
typedef __attribute__((ext_vector_type(2))) unsigned u32x2;
typedef GAS unsigned gu32;
typedef GAS unsigned long long gu64;

constexpr int S_ = 2048, D_ = 1024, F_ = 3584;

constexpr unsigned SY_CH = 0, SY_GO = 1024, SY_RE = 2048;
constexpr unsigned WS_FD  = 98304;
constexpr unsigned WS_ZF  = 8192;
constexpr unsigned WS_KVF = 16384;
constexpr unsigned WS_RS  = 131072;
constexpr unsigned WS_CS  = WS_RS + 16 * 16 * 1024;
constexpr unsigned WS_ZP  = WS_CS + 16 * 16 * 768;
constexpr unsigned WS_FB  = WS_ZP + 32 * 16 * 128;
constexpr unsigned WS_ST2 = WS_FB + 32 * 32 * 128;
constexpr unsigned WS_XS  = WS_ST2 + 32 * 8 * 1024;
constexpr unsigned WS_GU  = WS_XS + 64 * 2048;
constexpr unsigned WS_YP  = WS_GU + 256 * 1024;
constexpr unsigned WS_KVP = WS_YP + 2 * 8 * 64 * 512;
constexpr unsigned WS_O   = WS_KVP + 32 * 16 * 4096;
constexpr unsigned WS_END = WS_O + S_ * D_ * 2;
constexpr unsigned OOB = 0x80000000u;

template <int V> struct IC { static constexpr int v = V; };
template <int B, int E, class F> DI void sfor(F&& f) { if constexpr (B < E) { f(IC<B>{}); sfor<B + 1, E>(f); } }

DI float bflo(unsigned u) { return __uint_as_float(u << 16); }
DI float bfhi(unsigned u) { return __uint_as_float(u & 0xffff0000u); }
DI float rbf(float x) { return (float)(__bf16)x; }
DI unsigned pack2(float a, float b) { bf16x2 c; c[0] = (__bf16)a; c[1] = (__bf16)b; return __builtin_bit_cast(unsigned, c); }
DI unsigned pack2o(float a, float b) { unsigned p = pack2(a, b); asm("" : "+v"(p)); return p; }
DI f32x4 mfma16(u32x4 a, u32x4 b, f32x4 c) {
  return __builtin_amdgcn_mfma_f32_16x16x32_bf16(__builtin_bit_cast(bf16x8, a), __builtin_bit_cast(bf16x8, b), c, 0, 0, 0);
}
DI void mfma16i(u32x4 a, u32x4 b, f32x4& c) { asm volatile("v_mfma_f32_16x16x32_bf16 %0, %1, %2, %0" : "+v"(c) : "v"(a), "v"(b)); }
DI f32x2 unpk(unsigned u) { return (f32x2){bflo(u), bfhi(u)}; }
DI u32x2 trread(const char* p) {
  s16x4 v = __builtin_amdgcn_ds_read_tr16_b64_v4i16((LAS s16x4*)p);
  return __builtin_bit_cast(u32x2, v);
}
DI const char* uni(const void* p) {
  const u64 v = (u64)p;
  return (const char*)(((u64)(unsigned)__builtin_amdgcn_readfirstlane((int)(v >> 32)) << 32) | (unsigned)__builtin_amdgcn_readfirstlane((int)v));
}
#define RV4(a, b, c, d) "v" #a, "v" #b, "v" #c, "v" #d
#define RV10(p) "v" #p "0", "v" #p "1", "v" #p "2", "v" #p "3", "v" #p "4", "v" #p "5", "v" #p "6", "v" #p "7", "v" #p "8", "v" #p "9"
#define RSV2 RV4(224, 225, 226, 227), "v228", "v229", RV10(23), RV10(24), RV4(250, 251, 252, 253), "v254", "v255"
#define RSV1 RV4(192, 193, 194, 195), RV4(196, 197, 198, 199), RV10(20), RV10(21), RV4(220, 221, 222, 223), RSV2
#define RSV "v148", "v149", RV10(15), RV10(16), RV10(17), RV10(18), "v190", "v191", RSV1
#define NTW " nt"
#define GL_(v, s, o) "global_load_lds_dwordx4 " v ", " s " offset:" #o
#define GLDS4(SC, CL...) asm volatile("s_add_u32 m0, %5, %c6\n\ts_nop 3\n\t" GL_("%0", "%4", 0) SC "\n\t" GL_("%1", "%4", 1024) SC "\n\t" \
  GL_("%2", "%4", 2048) SC "\n\t" GL_("%3", "%4", 3072) SC :: "v"(v0), "v"(v1), "v"(v2), "v"(v3), "s"(uni(sbase)), "s"(lds), "i"(IMM) : "memory", "scc", ##CL)
#define GLDS3(SC, CL...) asm volatile("s_add_u32 m0, %4, %c5\n\ts_nop 3\n\t" GL_("%0", "%3", 0) SC "\n\t" GL_("%1", "%3", 1024) SC "\n\t" \
  GL_("%2", "%3", 2048) SC :: "v"(v0), "v"(v1), "v"(v2), "s"(uni(sbase)), "s"(lds), "i"(IMM) : "memory", "scc", ##CL)
template <int IMM> DI void glds4(unsigned v0, unsigned v1, unsigned v2, unsigned v3, const void* sbase, unsigned lds) { GLDS4(""); }
template <int IMM> DI void glds4r(unsigned v0, unsigned v1, unsigned v2, unsigned v3, const void* sbase, unsigned lds) { GLDS4("", RSV); }
template <int IMM> DI void glds3r(unsigned v0, unsigned v1, unsigned v2, const void* sbase, unsigned lds) { GLDS3("", RSV); }
template <int R> DI void sload(unsigned voff, const void* sbase) {
  asm volatile("s_nop 4\n\tglobal_load_dwordx4 v[%c0:%c1], %2, %3 sc1" ::"i"(R), "i"(R + 3), "v"(voff), "s"(uni(sbase)) : "memory", RSV);
}
DI float fadd(float a, float b) { float o; asm("v_add_f32 %0, %1, %2" : "=v"(o) : "v"(a), "v"(b)); return o; }
#define RA_(o, a, b) "v_add_f32 %" #o ", v[%c6+" #a "], v[%c6+" #b "]\n\t"
#define RB2_(o, a) "v_add_f32 %" #o ", v[%c6+" #a "], %" #o "\n\t"
#define RM_(o, a, b) "v_max3_u32 %" #o ", v[%c6+" #a "], v[%c6+" #b "], %" #o "\n\t"
template <int R> DI void rsum4(float* a, unsigned& m, unsigned& n) {
  asm volatile(RA_(0, 0, 4) RA_(1, 1, 5) RM_(4, 0, 1) RA_(2, 2, 6) RA_(3, 3, 7) RM_(5, 2, 3) RB2_(0, 8) RM_(4, 4, 5) RB2_(1, 9) RM_(5, 6, 7)
               RB2_(2, 10) RM_(4, 8, 9) RB2_(3, 11) RM_(5, 10, 11) RB2_(0, 12) RB2_(1, 13) RM_(4, 12, 13) RB2_(2, 14) RB2_(3, 15) RM_(5, 14, 15)
               : "=&v"(a[0]), "=&v"(a[1]), "=&v"(a[2]), "=&v"(a[3]), "+v"(m), "+v"(n) : "i"(R) : RSV);
}
template <int R> DI void wload2(unsigned voff, const void* sbase) {
  asm volatile("s_nop 4\n\tglobal_load_dwordx4 v[%c0:%c1], %2, %3" NTW ::"i"(R), "i"(R + 3), "v"(voff), "s"(uni(sbase)) : "memory", RSV2);
}
template <int R> DI void wload(unsigned voff, const void* sbase) {
  asm volatile("s_nop 4\n\tglobal_load_dwordx4 v[%c0:%c1], %2, %3" NTW ::"i"(R), "i"(R + 3), "v"(voff), "s"(uni(sbase)) : "memory", RSV);
}
template <int RA, int RB> DI unsigned wperm(unsigned sel) {
  unsigned o;
  if (RA < 192) asm volatile("v_perm_b32 %0, v%c2, v%c1, %3" : "=v"(o) : "i"(RA), "i"(RB), "s"(sel) : RSV);
  else if (RA < 224) asm volatile("v_perm_b32 %0, v%c2, v%c1, %3" : "=v"(o) : "i"(RA), "i"(RB), "s"(sel) : RSV1);
  else asm volatile("v_perm_b32 %0, v%c2, v%c1, %3" : "=v"(o) : "i"(RA), "i"(RB), "s"(sel) : RSV2);
  return o;
}
template <int R> DI unsigned rget() { unsigned o; asm volatile("v_mov_b32 %0, v%c1" : "=v"(o) : "i"(R) : RSV); return o; }
DI float xh_sum(float v) {
  auto r = __builtin_amdgcn_permlane32_swap(__float_as_uint(v), __float_as_uint(v), false, false);
  return __uint_as_float(r[0]) + __uint_as_float(r[1]);
}
template <int CTRL> DI unsigned dppu(unsigned v) { return (unsigned)__builtin_amdgcn_update_dpp(0, (int)v, CTRL, 0xf, 0xf, true); }
template <int CTRL> DI float dppf(float v) { return __uint_as_float(dppu<CTRL>(__float_as_uint(v))); }
constexpr int DPP_X1 = 0xB1, DPP_X2 = 0x4E, DPP_HMIR = 0x141, DPP_MIR = 0x140, DPP_ROR8 = 0x128;
DI float quad_sum(float v) { v += dppf<DPP_X1>(v); v += dppf<DPP_X2>(v); return v; }
DI float oct_sum(float v) { v = quad_sum(v); v += dppf<DPP_HMIR>(v); return v; }
DI float x16_sum(float v) {
  auto r = __builtin_amdgcn_permlane16_swap(__float_as_uint(v), __float_as_uint(v), false, false);
  return __uint_as_float(r[0]) + __uint_as_float(r[1]);
}
DI float sw16(float a, float b) {
  auto r = __builtin_amdgcn_permlane16_swap(__float_as_uint(a), __float_as_uint(b), false, false);
  return fadd(__uint_as_float(r[0]), __uint_as_float(r[1]));
}
DI float sw32(float a, float b) {
  auto r = __builtin_amdgcn_permlane32_swap(__float_as_uint(a), __float_as_uint(b), false, false);
  return fadd(__uint_as_float(r[0]), __uint_as_float(r[1]));
}
template <int N> struct Phi { unsigned tb[N]; f32x2 tr[N], e[N]; };
template <int N> DI void phi_a(const f32x2* t, Phi<N>& s) {
#pragma unroll
  for (int i = 0; i < N; ++i) s.tb[i] = pack2o(t[i][0], t[i][1]);
  __builtin_amdgcn_sched_barrier(0);
#pragma unroll
  for (int i = 0; i < N; ++i) s.tr[i] = unpk(s.tb[i]);
  __builtin_amdgcn_sched_barrier(0);
#pragma unroll
  for (int i = 0; i < N; ++i) s.e[i] = s.tr[i] * 1.44269504088896340736f;
  __builtin_amdgcn_sched_barrier(0);
}
template <int N> DI void phi_b(Phi<N>& s, unsigned* p) {
  u16x2 tm[2] = {{0, 0}, {0, 0}};
#pragma unroll
  for (int i = 0; i < N; ++i) {
    s.e[i][0] = __builtin_amdgcn_fmed3f(__builtin_amdgcn_exp2f(s.e[i][0]), 0.f, 1.f);
    tm[i & 1] = __builtin_elementwise_max(tm[i & 1], __builtin_bit_cast(u16x2, s.tb[i]));
    s.e[i][1] = __builtin_amdgcn_fmed3f(__builtin_amdgcn_exp2f(s.e[i][1]), 0.f, 1.f);
    __builtin_amdgcn_sched_barrier(0);
  }
#pragma unroll
  for (int i = 0; i < N; ++i) s.tr[i] += s.e[i];
  __builtin_amdgcn_sched_barrier(0);
#pragma unroll
  for (int i = 0; i < N; ++i) { s.tr[i][0] = fmaxf(s.tr[i][0], s.e[i][0]); s.tr[i][1] = fmaxf(s.tr[i][1], s.e[i][1]); }
  const u16x2 tx = __builtin_elementwise_max(tm[0], tm[1]);
  __builtin_amdgcn_sched_barrier(0);
#pragma unroll
  for (int i = 0; i < N; ++i) p[i] = pack2(s.tr[i][0], s.tr[i][1]);
  if (__any(tx[0] > 0xC0F0 || tx[1] > 0xC0F0)) {
#pragma unroll
    for (int i = 0; i < N; ++i) p[i] = pack2(bflo(p[i]) + 1e-6f, bfhi(p[i]) + 1e-6f);
  }
}
template <int N> DI void phi_n(const f32x2* t, unsigned* p) { Phi<N> s; phi_a<N>(t, s); phi_b<N>(s, p); }
#define RLX_AGENT __ATOMIC_RELAXED, __HIP_MEMORY_SCOPE_AGENT
DI void st64(char* ws, unsigned off, u64 v) { __hip_atomic_store((gu64*)(ws + off), v, RLX_AGENT); }
DI unsigned ld32(char* ws, unsigned off) { return __hip_atomic_load((gu32*)(ws + off), RLX_AGENT); }
#define WAIT_V(n) asm volatile("s_waitcnt vmcnt(%c0)" ::"i"(n) : "memory")
#define RAW_BAR() do { asm volatile("s_waitcnt lgkmcnt(0)" ::: "memory"); __builtin_amdgcn_s_barrier(); asm volatile("" ::: "memory"); } while (0)
#define SB0() __builtin_amdgcn_sched_barrier(0)
#define WAIT_VR(n) asm volatile("s_waitcnt vmcnt(%c0)" ::"i"(n) : "memory", RSV)
#define RAW_BARR() do { asm volatile("s_waitcnt lgkmcnt(0)" ::: "memory", RSV); __builtin_amdgcn_s_barrier(); asm volatile("" ::: "memory", RSV); } while (0)
#define KEEP2() asm volatile("" ::: "memory", RSV2)
#define RAW_BAR2() do { asm volatile("s_waitcnt lgkmcnt(0)" ::: "memory", RSV2); __builtin_amdgcn_s_barrier(); KEEP2(); } while (0)
constexpr unsigned SPIN_CAP = 1u << 18;
#define SPIN _Pragma("nounroll") for (unsigned s_ = 0; s_ < SPIN_CAP; ++s_)
constexpr unsigned SENT = 0xFFFFFFFFu;
DI unsigned sane1(unsigned v) { return min(v, 0xFFFFFFFEu); }
DI u32x4 sane(u32x4 v) { v[1] = sane1(v[1]); v[3] = sane1(v[3]); return v; }
DI unsigned chk(unsigned m, u32x4 v) { return max(max(m, v[1]), v[3]); }
DI bool wave_ok(unsigned m) { return !__any(m == SENT); }
DI unsigned key_idx(unsigned k) { return (0xFFFFu - (k & 0xFFFFu)) & (S_ - 1); }
DI float key_val(unsigned k) { const unsigned o = k >> 16; return __uint_as_float(((o & 0x8000u) ? (o & 0x7fffu) : (~o & 0xffffu)) << 16); }
DI void top2_mix(unsigned& b0, unsigned& b1, unsigned c0, unsigned c1) {
  const unsigned lo = min(b0, c0), mx = max(b1, c1);
  b0 = max(b0, c0); b1 = max(lo, mx);
}
DI void top2_lo(unsigned& b0, unsigned& b1) {
  top2_mix(b0, b1, dppu<DPP_X1>(b0), dppu<DPP_X1>(b1));
  top2_mix(b0, b1, dppu<DPP_X2>(b0), dppu<DPP_X2>(b1));
  top2_mix(b0, b1, dppu<DPP_HMIR>(b0), dppu<DPP_HMIR>(b1));
}
DI void top2_mid(unsigned& b0, unsigned& b1) {
  top2_mix(b0, b1, dppu<DPP_MIR>(b0), dppu<DPP_MIR>(b1));
  auto p = __builtin_amdgcn_permlane16_swap(b0, b0, false, false); auto q = __builtin_amdgcn_permlane16_swap(b1, b1, false, false);
  b0 = p[0]; b1 = q[0]; top2_mix(b0, b1, p[1], q[1]);
}
DI void top2_hi(unsigned& b0, unsigned& b1) {
  top2_mid(b0, b1);
  auto p = __builtin_amdgcn_permlane32_swap(b0, b0, false, false); auto q = __builtin_amdgcn_permlane32_swap(b1, b1, false, false);
  b0 = p[0]; b1 = q[0]; top2_mix(b0, b1, p[1], q[1]);
}

constexpr int K1_STAGE = 40960;
constexpr int LDS_TOTAL = 4 * K1_STAGE;
constexpr int L_STM = 0;
constexpr int L_MISC = 2048;
constexpr int L_XK = 5120;
constexpr int L_KX = 3328;
constexpr int L_ACT = 2304;
constexpr int L_CAND = 4096;
constexpr int L_WO = 16384;
constexpr int L_P = 16384, L_PK = 7 * 16384;
constexpr int L_RED = 65536;
constexpr int wo_slot(int j) { return L_WO + (j % 9) * 16384; }
constexpr int CODE_LINES = 12;
#define OW_ 8
constexpr int OW = OW_;
#define W1A_ 16
constexpr int W1A = W1A_;
#define GSCHED 5, 9, 13, 17, 21, 24, 27, 30, 31, 35, 39, 41, 45, 48, 48
constexpr int GS[15] = {GSCHED};
constexpr int gops(int g) { return (g / 3) * 10 + (g % 3 == 0 ? 0 : g % 3 == 1 ? 4 : 7); }
constexpr bool gs_ok() {
  bool ok = GS[0] >= 3 && GS[0] <= 9 && GS[1] >= 6 && GS[1] <= 12 && GS[0] <= GS[1] && GS[12] == 45 && GS[13] == 48 && GS[14] == 48;
  for (int t = 0; t < 12; ++t) ok = ok && GS[t + 2] <= 3 * (t + 5) && GS[t + 2] >= 3 * (t + 3) && GS[t + 2] >= GS[t + 1] + 1;
  return ok;
}
static_assert(gs_ok(), "staging schedule");
#define WSCHED 0, 0, 0, 0, 0, 0, 5, 5, 5, 10, 10, 10, 10
constexpr int WS_[13] = {WSCHED};
constexpr int wyoung(int t, int stages) {
  int e = 0;
  while (GS[e + 2] < 3 * stages) ++e;
  return WS_[t + 1] - WS_[e];
}
#define PACE 1
#define P_KVP 1
#define P_KVF 1
#define P_FB 8
#define P_C 14
#define P_D 2
#define P_GU 12
#define P_E 10
#define SLEEP(n) do { if (n) __builtin_amdgcn_s_sleep(n); } while (0)
#define STE 6
constexpr int VW = 160;

__global__ __launch_bounds__(512, 2) void fused(const u16* __restrict__ x, const u16* __restrict__ wqkv,
                                                const u16* __restrict__ wo, const u16* __restrict__ w_r,
                                                const u16* __restrict__ wg, const u16* __restrict__ wu,
                                                const u16* __restrict__ wd, u16* __restrict__ out, char* __restrict__ ws) {
  extern __shared__ __attribute__((aligned(16))) char smem[];
  u64 pc0; asm volatile("s_getpc_b64 %0" : "=s"(pc0));
  const int wave = __builtin_amdgcn_readfirstlane(threadIdx.x >> 6);
  const unsigned lds0 = (unsigned)(size_t)smem;
  if (wave == 0) {
    const int l = threadIdx.x;
    asm volatile("s_mov_b32 m0, %1\n\ts_nop 0\n\tglobal_load_lds_dword %0, off"
                 :: "v"((const char*)(pc0 - (pc0 & 127)) + ((blockIdx.x >> 3) + (l < CODE_LINES ? l : 0) * 32) * 128), "s"(__builtin_amdgcn_readfirstlane(lds0 + 3 * K1_STAGE)) : "memory");
  }
#define RB_(b) (((b) & 1) * 8 + (((b) >> 3) & 7))
#define HP_(b) ((((b) & 7) >> 1) * 4 + ((b) >> 6))
#define KROT_(b) ((4 * ((HP_(b) >> 2) + (RB_(b) >> 3) + 2 * ((RB_(b) >> 2) & 1))) & 15)
  unsigned voA0, voA1, voA2, voA3, voB0, voB1, voB2, voB3, voB4, voB5;
  const unsigned l4 = __builtin_amdgcn_readfirstlane(lds0 + (wave - 4) * 4096), l6 = __builtin_amdgcn_readfirstlane(lds0 + (wave - 4) * 6144);
  const int krot0 = KROT_(blockIdx.x);
  auto group = [&](auto G) {
    constexpr int t = G.v / 3, p = G.v % 3, sl = (t & 3) * K1_STAGE;
    const int kt = (t + krot0) & 15;
    if (p == 0) glds4r<sl>(voA0, voA1, voA2, voA3, (const char*)x + kt * 128, l4);
    else if (p == 1) glds3r<sl + 16384>(voB0, voB1, voB2, (const char*)wqkv + kt * 64 * (3 * D_ * 2), l6);
    else glds3r<sl + 16384 + 3072>(voB3, voB4, voB5, (const char*)wqkv + kt * 64 * (3 * D_ * 2), l6);
  };
  if (wave >= 4) {
    const int lane = threadIdx.x & 63, sw = wave - 4;
    const int r0 = RB_(blockIdx.x) * 128 + 32 * sw + (lane >> 3);
    voA0 = (unsigned)(r0 * D_ + ((lane & 7) ^ (lane >> 4)) * 8) * 2u;
    voA1 = ((voA0 + 8 * D_ * 2) ^ 64) - 1024; voA2 = voA0 + 16 * D_ * 2 - 2048; voA3 = voA1 + 16 * D_ * 2 - 2048;
    asm volatile("" : "+v"(voA0), "+v"(voA1), "+v"(voA2), "+v"(voA3));
    group(IC<0>{});
    int l2 = lane; asm volatile("" : "+v"(l2));
    voB0 = (unsigned)((16 * sw + 4 * (l2 >> 5) + ((l2 >> 2) & 3)) * (3 * D_) + HP_(blockIdx.x) * 64 + ((l2 >> 4) & 1) * 32 + (l2 & 3) * 8) * 2u;
    voB1 = voB0 + 1024; voB2 = voB0 + 2048;
    voB3 = (voB0 ^ 32) + 8 * 3 * D_ * 2; voB4 = voB3 + 1024; voB5 = voB3 + 2048;
    asm volatile("" : "+v"(voB0), "+v"(voB1), "+v"(voB2), "+v"(voB3), "+v"(voB4), "+v"(voB5));
    sfor<1, 3>(group);
  }
  int bid = blockIdx.x; asm volatile("" : "+s"(bid));
  const int xcd = bid & 7, idx = bid >> 3;
  const int rb = RB_(bid), hp = HP_(bid);
  const int rp = xcd * 4 + (idx >> 3), cb = idx & 7;
  const int hs_ = (hp + 4 + (int)(((size_t)wqkv >> 7) & 7)) & 7;
  const int g8 = ((hp >> 3) * 4 + (rb >> 2)) * 8 + hs_, kq = rb & 3;
  const int krot = KROT_(bid), posA = 1 + (hp & 3), posB = 1 + (rb & 3);
  const int wrot = 4 * rp + (xcd >> 1), wdir = (xcd & 1) ? -1 : 1;
  auto whead = [&](int t) { return (wrot + wdir * t) & 15; };
  const unsigned rs_slot = WS_RS + (rb * 16 + ((krot + posA) & 15)) * 1024, cs_slot = WS_CS + (hp * 16 + ((krot + posB) & 15)) * 768;
  const __amdgpu_buffer_rsrc_t wsr = __builtin_amdgcn_make_buffer_rsrc(ws, 0, WS_END, 0x00020000);
#define LDW(off) __builtin_amdgcn_raw_buffer_load_b128(wsr, (off), 0, 16)
#define STW(v, off) __builtin_amdgcn_raw_buffer_store_b128((v), wsr, (off), 0, 16)
#define STW1(v, off) __builtin_amdgcn_raw_buffer_store_b32((v), wsr, (off), 0, 16)
  float* stm = (float*)(smem + L_STM);
  volatile LAS int* misc = (volatile LAS int*)(smem + L_MISC);
#define LANES() int tid = threadIdx.x; asm volatile("" : "+v"(tid)); const int lane = tid & 63, r = lane & 31, h = lane >> 5; (void)r; (void)h
#define EPI() const int erow = tid >> 3, eseg = tid & 7; const size_t ego = (size_t)(rp * 64 + erow) * D_ + cb * 128 + eseg * 8
  u32x4 xv[2], wv[2];
  f32x2 xf[8], wf2[8];
  float sw_;
  auto unpack_xw = [&]() {
    f32x2 a = {0.f, 0.f};
#pragma unroll
    for (int i = 0; i < 8; ++i) { xf[i] = unpk(xv[i >> 2][i & 3]); wf2[i] = unpk(wv[i >> 2][i & 3]); a += wf2[i]; }
    sw_ = oct_sum(a[0] + a[1]);
#pragma unroll
    for (int i = 0; i < 8; ++i) asm volatile("" : "+v"(xf[i]), "+v"(wf2[i]));
    asm volatile("" : "+v"(sw_));
  };
  u32x4 wf[24];

  auto wtr = [&](auto I) {
    constexpr int m = I.v >> 3, c = I.v & 7, R = VW + 32 * m + (c >> 1);
    const unsigned sel = (c & 1) ? 0x07060302u : 0x05040100u;
    wf[I.v] = (u32x4){wperm<R, R + 4>(sel), wperm<R + 8, R + 12>(sel), wperm<R + 16, R + 20>(sel), wperm<R + 24, R + 28>(sel)};
  };

  if (wave < 4) {
    LANES();
    const int c16 = lane & 15, g4 = lane >> 4;
    const int trl = (c16 >> 2) * 64 + (c16 & 3) * 8;
    const int wr = wave >> 1, wc = wave & 1;
    const int head = 2 * hp + wc;
    const unsigned kvp_off = WS_KVP + (head * 16 + rb) * 4096 + wr * 2048 + lane * 16;
    const unsigned zp_off = WS_ZP + (head * 16 + rb) * 128;
    const unsigned o_off = WS_O + ((rb * 4 + 2 * wr) * 32 + head) * 2048 + lane * 16;
    const unsigned fb_off = lane < 8 ? WS_FB + ((rb * 2 + wr) * 32 + head) * 128 + lane * 16 : OOB;
    unsigned pfa, pfb = OOB;
    unsigned pfc = lane < 8 ? fb_off : lane < 24 ? rs_slot + wave * 256 + (lane - 8) * 16
        : lane < 40 ? (wave < 3 ? cs_slot + wave * 256 : WS_KVF + hp * 4096 + rb * 256) + (lane - 24) * 16
        : lane < 48 ? (wave < 2 ? zp_off + (lane & 7) * 16 : (wave == 2 && rb < 2) ? WS_ZF + (2 * hp + rb) * 128 + (lane & 7) * 16 : OOB) : OOB;
    if (wave == 0) pfa = WS_ST2 + (rp * 8 + cb) * 1024 + lane * 16;
    else if (wave == 1) pfa = WS_GU + (g8 * 4 + kq) * 1024 + lane * 16;
    else pfa = WS_YP + (((wave - 2) * 8 + 2 * kq + (lane >> 5)) * 64 + g8) * 512 + (lane & 31) * 16;
    if (wave == 0) pfb = lane < 32 ? WS_XS + (rp * 2 + (lane >> 4)) * 2048 + cb * 256 + (lane & 15) * 16 : lane < 40 ? WS_FD + (cb * 32 + rp) * 128 + (lane & 7) * 16 : OOB;
    u32x4 S4 = {SENT, SENT, SENT, SENT};
    asm volatile("" : "+v"(pfa), "+v"(pfb), "+v"(pfc), "+v"(S4));
    __builtin_amdgcn_s_setprio(1);
    f32x4 acc[4][6];
    int ao[2][2], bo[2][2], bv[2][2];
    f32x4 m4[4], i4[4]; float cs[4]; int st_ok;
    auto st_rd = [&](int q) {
      if (q == 0) { st_ok = misc[12 + (lane & 3)]; asm volatile("" ::: "memory"); }
      else if (q < 5) cs[(q - 1) & 3] = stm[256 + wc * 96 + 32 + 16 * (q < 3 ? q - 1 : (q - 1) ^ wr) + c16];
      else if (q < 9) m4[(q - 5) & 3] = *(const f32x4*)(stm + 64 * wr + 16 * (q - 5) + 4 * g4);
      else if (q < 13) i4[(q - 9) & 3] = *(const f32x4*)(stm + 128 + 64 * wr + 16 * (q - 9) + 4 * g4);
    };
    {
#pragma unroll
      for (int i = 0; i < 2; ++i) {
        ao[0][i] = (64 * wr + c16) * 128 + (((4 * i + g4) ^ (c16 >> 1)) * 16); ao[1][i] = ao[0][i] + 2 * K1_STAGE;
        bo[0][i] = 16384 + g4 * 3072 + wc * 256 + trl + ((i ^ (g4 & 1)) * 32); bo[1][i] = bo[0][i] + 2 * K1_STAGE;
        bv[0][i] = bo[0][i] ^ (wr * 32); bv[1][i] = bo[1][i] ^ (wr * 32);
        asm volatile("" : "+v"(ao[0][i]), "+v"(ao[1][i]), "+v"(bo[0][i]), "+v"(bo[1][i]), "+v"(bv[0][i]), "+v"(bv[1][i]));
      }
#pragma unroll
      for (int i = 0; i < 4; ++i)
#pragma unroll
        for (int j = 0; j < 6; ++j) { acc[i][j] = (f32x4){0.f, 0.f, 0.f, 0.f}; asm volatile("" : "+v"(acc[i][j])); }
      struct Frag { u32x4 a[4]; u32x2 bl[6], bh[6]; };
      Frag FA, FB;
#define K1_B(j) ((u32x4){C.bl[j][0], C.bl[j][1], C.bh[j][0], C.bh[j][1]})
#define K1_LA(i) N.a[i] = *(const u32x4*)(smem + ao[sn >> 1][hf] + (sn & 1) * K1_STAGE + (i) * 2048)
#define K1_LL(j) N.bl[j] = trread(smem + ((j) < 4 ? bo : bv)[sn >> 1][(j) & 1] + (sn & 1) * K1_STAGE + hf * 12288 + ((j) >> 1) * 1024)
#define K1_LH(j) N.bh[j] = trread(smem + ((j) < 4 ? bo : bv)[sn >> 1][(j) & 1] + (sn & 1) * K1_STAGE + hf * 12288 + ((j) >> 1) * 1024 + 512)
#define K1_M(i, j) if ((j) < 2) mfma16i(K1_B(j), C.a[i], acc[i][j]); else mfma16i(C.a[i], K1_B(j), acc[i][j]); SB0()
      auto sub = [&](Frag& C, Frag& N, int sn, int hf) {
        sfor<0, 24>([&](auto Q) {
          constexpr int q = Q.v;
          constexpr int j = q / 4, i = (j & 1) ? 3 - q % 4 : q % 4;
          if (q == 23) asm volatile("s_waitcnt lgkmcnt(0)" : "+v"(N.bh[5]));
          K1_M(i, j);
          if constexpr (q < 4) { K1_LA(q); } else if constexpr (q < 16) { if (q & 1) { K1_LH((q - 4) >> 1); } else { K1_LL((q - 4) >> 1); } }
          SB0();
        });
      };
      auto subkv = [&](Frag& C, Frag& N, int sn, int hf, bool ld) {
#define K1_QL(i, j, l) K1_M(i, j); if (ld) { l; } else { st_rd(qq); ++qq; } SB0()
        int qq = 0;
        K1_QL(0, 2, K1_LL(2)); K1_QL(1, 2, K1_LH(2)); K1_QL(0, 3, K1_LA(0)); K1_QL(1, 3, K1_LA(1)); K1_QL(2, 2, K1_LL(3)); K1_QL(3, 2, K1_LH(3));
        K1_QL(2, 3, K1_LA(2)); K1_QL(3, 3, K1_LA(3)); K1_QL(0, 4, K1_LL(4)); K1_QL(1, 4, K1_LH(4)); K1_QL(2, 4, K1_LL(5)); K1_QL(3, 4, K1_LH(5));
        K1_QL(0, 5, ); K1_M(1, 5); K1_M(2, 5); K1_M(3, 5);
      };
      WAIT_V(0);
      RAW_BAR();
      {
        Frag& N = FA; const int sn = 0, hf = 0;
        K1_LL(0); K1_LH(0); K1_LA(0); K1_LA(1); K1_LL(1); K1_LH(1); K1_LL(2); K1_LH(2); K1_LL(3); K1_LH(3); K1_LA(2); K1_LA(3); K1_LL(4); K1_LH(4); K1_LL(5); K1_LH(5);
      }
#pragma unroll 1
      for (int it = 0; it < 4; ++it) {
#pragma unroll
        for (int s = 0; s < 4; ++s) {
          if (s == 2 && it == 3) break;
          sub(FA, FB, s, 1);
          if (s == 0) WAIT_V(0);
          if (it == 0) { if (s == 1) STW(S4, pfc); if (s == 2) STW(S4, kvp_off); if (s == 3) STW(S4, kvp_off + 1024); }
          if (it == 1) { if (s == 0) STW(S4, pfa); if (s == 1) STW(S4, pfb); }
          if (s != 1 || it < 3) RAW_BAR();
          sub(FB, FA, (s + 1) & 3, 0);
        }
      }
      subkv(FA, FB, 2, 1, true);
      RAW_BAR();
      subkv(FB, FA, 3, 0, true);
      subkv(FA, FB, 3, 1, true);
      subkv(FB, FA, 0, 0, false);
    }
    asm volatile("s_nop 15" ::: "memory");
    {
      SPIN {
        if (__all(st_ok != 0)) break;
#pragma unroll
        for (int q = 0; q < 13; ++q) st_rd(q);
        SB0();
      }
      const u32x4 ONES4 = {0x3F803F80u, 0x3F803F80u, 0x3F803F80u, 0x3F803F80u};
      f32x2 tk[16], tv[16];
#define PR(v, i) ((f32x2){(v)[2 * (i)], (v)[2 * (i) + 1]})
#pragma unroll
      for (int i = 0; i < 16; ++i) {
        tk[i] = PR(acc[(i >> 1) & 3][2 + (i >> 3)], i & 1) - PR(m4[(i >> 1) & 3], i & 1) * cs[i >> 3];
        tv[i] = PR(acc[(i >> 1) & 3][4 + (i >> 3)], i & 1) - PR(m4[(i >> 1) & 3], i & 1) * cs[2 + (i >> 3)];
      }
      SB0();
#pragma unroll
      for (int i = 0; i < 16; ++i) { tk[i] *= PR(i4[(i >> 1) & 3], i & 1); tv[i] *= PR(i4[(i >> 1) & 3], i & 1); }
      SB0();
      unsigned kp[16], vp[16];
#pragma unroll
      for (int i = 0; i < 16; ++i) vp[i] = pack2o(tv[i][0], tv[i][1]);
      phi_n<16>(tk, kp);
#define F4_(p, i) ((u32x4){(p)[4 * (i)], (p)[4 * (i) + 1], (p)[4 * (i) + 2], (p)[4 * (i) + 3]})
      const f32x4 Z4 = {0.f, 0.f, 0.f, 0.f};
      const u32x4 vk[2] = {F4_(vp, 0), F4_(vp, 1)}, vg[2] = {F4_(vp, 2), F4_(vp, 3)};
      f32x4 keep[2], give[2], zq[2];
#pragma unroll
      for (int d = 0; d < 2; ++d) { give[d] = mfma16(F4_(kp, 2 * d), vg[0], Z4); zq[d] = mfma16(ONES4, F4_(kp, 2 * d), Z4); }
#pragma unroll
      for (int d = 0; d < 2; ++d) { give[d] = mfma16(F4_(kp, 2 * d + 1), vg[1], give[d]); zq[d] = mfma16(ONES4, F4_(kp, 2 * d + 1), zq[d]); }
#pragma unroll
      for (int d = 0; d < 2; ++d) keep[d] = mfma16(F4_(kp, 2 * d), vk[0], Z4);
#pragma unroll
      for (int d = 0; d < 2; ++d) keep[d] = mfma16(F4_(kp, 2 * d + 1), vk[1], keep[d]);
      SB0();
      const float zv = (lane & 16) ? zq[1][0] : zq[0][0];
      char* xk = smem + L_XK + wave * 2304 + lane * 16;
      const char* xp = smem + L_XK + (wave ^ 2) * 2304 + lane * 16;
      *(f32x4*)xk = give[0]; *(f32x4*)(xk + 1024) = give[1]; *(float*)(xk + 2048 - lane * 12) = zv;
      asm volatile("" ::: "memory");
      misc[16 + wave] = 1;
      f32x4 p0, p1; float pz;
      SPIN {
        const int ok = misc[16 + (wave ^ 2)];
        asm volatile("" ::: "memory");
        p0 = *(const f32x4*)xp; p1 = *(const f32x4*)(xp + 1024); pz = *(const float*)(xp + 2048 - lane * 12);
        SB0();
        if (ok == 1) break;
      }
      STW(sane(__builtin_bit_cast(u32x4, keep[0] + p0)), kvp_off);
      STW(sane(__builtin_bit_cast(u32x4, keep[1] + p1)), kvp_off + 1024);
      STW1(sane1(__float_as_uint(zv + pz)), (lane < 32 && wr == 0) ? zp_off + lane * 4 : OOB);
    }
    {
      u32x4 qa[4][4]; u32x2 ql[4][2], qh[4][2];
#pragma unroll
      for (int u = 0; u < 4; ++u) {
#pragma unroll
        for (int j = 0; j < 2; ++j) {
          ql[u][j] = trread(smem + bo[1][j] + (u >> 1) * K1_STAGE + (u & 1) * 12288);
          qh[u][j] = trread(smem + bo[1][j] + (u >> 1) * K1_STAGE + (u & 1) * 12288 + 512);
        }
#pragma unroll
        for (int i = 0; i < 4; ++i) qa[u][i] = *(const u32x4*)(smem + ao[1][u & 1] + (u >> 1) * K1_STAGE + i * 2048);
      }
      asm volatile("" ::: "memory");
      misc[8 + wave] = 3;
      SB0();
#pragma unroll
      for (int u = 0; u < 4; ++u)
#pragma unroll
        for (int j = 0; j < 2; ++j)
#pragma unroll
          for (int i = 0; i < 4; ++i) acc[i][j] = mfma16((u32x4){ql[u][j][0], ql[u][j][1], qh[u][j][0], qh[u][j][1]}, qa[u][i], acc[i][j]);
      SB0();
    }
    unsigned qp[16];
    Phi<16> qs;
    const unsigned po = WS_KVP + ((2 * hp + (rb >> 3)) * 16 + h) * 4096 + (2 * ((rb >> 2) & 1) + ((lane >> 4) & 1)) * 1024 + (16 * (rb & 3) + (lane & 15)) * 16;
    u32x4 pv[8] = {};
    {
      f32x4 c4[2]; float m_b[4], rinv[4];
#pragma unroll
      for (int i = 0; i < 2; ++i) c4[i] = *(const f32x4*)(stm + 256 + wc * 96 + 16 * i + 4 * g4);
#pragma unroll
      for (int i = 0; i < 4; ++i) { m_b[i] = stm[64 * wr + 16 * i + c16]; rinv[i] = stm[128 + 64 * wr + 16 * i + c16]; }
      SB0();
      f32x2 tq[16];
#pragma unroll
      for (int i = 0; i < 16; ++i) tq[i] = PR(acc[i >> 2][(i >> 1) & 1], i & 1) - PR(c4[(i >> 1) & 1], i & 1) * m_b[i >> 2];
      SB0();
#pragma unroll
      for (int i = 0; i < 16; ++i) tq[i] *= rinv[i >> 2];
      SB0();
      phi_a<16>(tq, qs);
#pragma unroll
      for (int i = 0; i < 16; ++i) asm volatile("" : "+v"(qs.tb[i]), "+v"(qs.tr[i]), "+v"(qs.e[i]));
    }
    if (wave == 0) {
      SLEEP(P_KVP);
#pragma unroll
      for (int p = 0; p < 8; ++p) pv[p] = LDW(po + p * 8192);
    }
    asm volatile("" ::: "memory");
    if (wave == 1 && rb < 2) {
      SLEEP(P_KVP);
      SPIN {
        unsigned m = 0;
#pragma unroll
        for (int p = 0; p < 2; ++p) { pv[p] = LDW(WS_ZP + (2 * hp + rb) * 2048 + p * 1024 + lane * 16); m = chk(max(max(m, pv[p][0]), pv[p][2]), pv[p]); }
        if (wave_ok(m)) break;
        asm volatile("" ::: "memory");
      }
      f32x4 s = __builtin_bit_cast(f32x4, pv[0]) + __builtin_bit_cast(f32x4, pv[1]);
#pragma unroll
      for (int i = 0; i < 4; ++i) { s[i] += dppf<DPP_ROR8>(s[i]); s[i] = xh_sum(x16_sum(s[i])); }
      if (lane < 16) st64(ws, WS_ZF + (2 * hp + rb) * 128 + (lane >> 3) * 64 + (lane & 3) * 16 + ((lane >> 2) & 1) * 8,
                          ((u64)sane1(pack2(s[2], s[3])) << 32) | pack2(s[0], s[1]));
    }
    if (wave == 0) {
      SPIN {
        unsigned m = 0;
#pragma unroll
        for (int p = 0; p < 8; ++p) m = chk(m, pv[p]);
        if (wave_ok(m)) break;
        asm volatile("" ::: "memory");
#pragma unroll
        for (int p = 0; p < 8; ++p) pv[p] = LDW(po + p * 8192);
      }
      f32x4 s = __builtin_bit_cast(f32x4, pv[0]);
#pragma unroll
      for (int p = 1; p < 8; ++p) s += __builtin_bit_cast(f32x4, pv[p]);
      const unsigned p0 = pack2(xh_sum(s[0]), xh_sum(s[1])), p1 = pack2(xh_sum(s[2]), xh_sum(s[3]));
      auto a = __builtin_amdgcn_permlane16_swap(p0, p0, false, false);
      auto b = __builtin_amdgcn_permlane16_swap(p1, p1, false, false);
      STW(sane((u32x4){a[0], b[0], a[1], b[1]}), lane < 16 ? WS_KVF + hp * 4096 + rb * 256 + lane * 16 : OOB);
    }
    phi_b<16>(qs, qp);
#pragma unroll
    for (int i = 0; i < 16; ++i) asm volatile("" : "+v"(qp[i]));
    if (wave == 0) misc[7] = 1;
    else SPIN { if (misc[7] == 1) break; __builtin_amdgcn_s_sleep(1); }
    SLEEP(P_KVF);
    {
      u32x4 k0, k1, z0;
      SPIN {
        k0 = LDW(WS_KVF + head * 2048 + lane * 16); k1 = LDW(WS_KVF + head * 2048 + 1024 + lane * 16);
        z0 = LDW(WS_ZF + head * 128 + g4 * 16);
        if (wave_ok(chk(chk(chk(0u, k0), k1), z0))) break;
        asm volatile("" ::: "memory");
      }
      const f32x4 Z4 = {0.f, 0.f, 0.f, 0.f};
      f32x4 X[8], Dn[4];
#pragma unroll
      for (int i = 0; i < 4; ++i) Dn[i] = mfma16(z0, F4_(qp, i), Z4);
#pragma unroll
      for (int i = 0; i < 8; ++i) X[i] = mfma16((i & 1) ? k1 : k0, F4_(qp, i >> 1), Z4);
      SB0();
      float ri[4]; unsigned a[16]; f32x2 f[16];
#pragma unroll
      for (int i = 0; i < 4; ++i) ri[i] = __builtin_amdgcn_rcpf(rbf(rbf(Dn[i][0]) + 1e-6f));
      SB0();
#pragma unroll
      for (int i = 0; i < 16; ++i) a[i] = pack2o(X[i >> 1][2 * (i & 1)], X[i >> 1][2 * (i & 1) + 1]);
      SB0();
#pragma unroll
      for (int i = 0; i < 16; ++i) f[i] = unpk(a[i]);
      SB0();
#pragma unroll
      for (int i = 0; i < 16; ++i) f[i] *= ri[i >> 2];
      SB0();
#pragma unroll
      for (int i = 0; i < 4; ++i) {
        STW(((u32x4){pack2(f[4 * i][0], f[4 * i][1]), pack2(f[4 * i + 1][0], f[4 * i + 1][1]), pack2(f[4 * i + 2][0], f[4 * i + 2][1]), pack2(f[4 * i + 3][0], f[4 * i + 3][1])}),
            o_off + (i >> 1) * 65536 + (i & 1) * 1024);
        SB0();
      }
    }
    {
      EPI();
      const int rt = wave & 1, kh = wave >> 1;
      const unsigned ob = WS_O + ((rp * 2 + rt) * 32 + 16 * kh) * 2048 + lane * 16;
#define LDO(t, st) __builtin_amdgcn_raw_buffer_load_b128(wsr, ob + (st) * 1024, whead(t) * 2048, 16)
      int woff[2];
#pragma unroll
      for (int i = 0; i < 2; ++i) woff[i] = kh * 8192 + (g4 >> 1) * 2048 + (g4 & 1) * 512 + trl + ((i ^ (g4 & 1)) * 32);
      struct Frag2 { u32x2 lo[8], hi[8]; };
      Frag2 G[2];
      auto ld2 = [&](Frag2& N, int sn, int nt, int hi) {
        const char* p = smem + sn + woff[nt & 1] + (nt >> 2) * 1024 + ((nt >> 1) & 1) * 256;
        if (hi) N.hi[nt] = trread(p + 4096); else N.lo[nt] = trread(p);
      };
      WAIT_V(0);
      STW(((u32x4){1u, 1u, 1u, 1u}), fb_off);
      RAW_BAR();
#pragma unroll
      for (int i = 0; i < 16; ++i) ld2(G[0], wo_slot(0), i >> 1, i & 1);
      f32x4 a2[2][8];
#pragma unroll
      for (int i = 0; i < 16; ++i) a2[i & 1][i >> 1] = (f32x4){0.f, 0.f, 0.f, 0.f};
      SLEEP(P_FB);
      SPIN { if (wave_ok(ld32(ws, WS_FB + (rp * 32 + 16 * kh + (lane & 15)) * 128))) break; }
      asm volatile("" ::: "memory");
      char* pr[2];
#pragma unroll
      for (int i = 0; i < 2; ++i) pr[i] = smem + L_P + kh * L_PK + (32 * rt + c16) * 512 + (((4 * i + g4) ^ (c16 & 7)) * 16);
      u32x4 of[16][2];
#pragma unroll
      for (int t = 0; t < OW; ++t) { of[t][0] = LDO(t, 0); of[t][1] = LDO(t, 1); }
      SB0();
      sfor<0, 16>([&](auto T) {
        constexpr int t = T.v;
        Frag2& C = G[t & 1]; Frag2& N = G[(t + 1) & 1];
#pragma unroll
        for (int i = 0; i < 16; ++i) {
          const int nt = i >> 1, st = i & 1;
          a2[st][nt] = mfma16((u32x4){C.lo[nt][0], C.lo[nt][1], C.hi[nt][0], C.hi[nt][1]}, of[t][st], a2[st][nt]);
          SB0();
          if ((t & 1) && t <= 11 && i == 8) {
            asm volatile("s_waitcnt lgkmcnt(8)" ::: "memory"); __builtin_amdgcn_s_barrier(); asm volatile("" ::: "memory");
          }
          if (t < 15) ld2(N, wo_slot(t + 1), nt, st);
          if (t > 0 && t - 1 + OW < 16 && (i == 3 || i == 11)) of[(t - 1 + OW) & 15][i >> 3] = LDO(t - 1 + OW, i >> 3);
          if (t - 1 + OW == 16 && (i == 3 || i == 11)) { xv[i >> 3] = *(const u32x4*)(x + ego + (i >> 3) * 64); wv[i >> 3] = *(const u32x4*)(w_r + cb * 128 + eseg * 8 + (i >> 3) * 64); }
          if (t == 15 && i >= 4) *(f32x4*)(pr[(nt - 2) & 1] + ((nt - 2) >> 1) * 128 + st * 8192) = a2[st][(nt - 2) & 7];
          SB0();
        }
      });
#pragma unroll
      for (int i = 12; i < 16; ++i) *(f32x4*)(pr[(i >> 1) & 1] + (i >> 2) * 128 + (i & 1) * 8192) = a2[i & 1][i >> 1];
    }
    unpack_xw();
    __builtin_amdgcn_s_setprio(0);
  } else {
    LANES();
    const int sw = wave - 4, t2 = tid - 256;
    __builtin_amdgcn_s_setprio(3);
    const bool real = hs_ != 7;
    unsigned hs_g0 = 0, hs_cur = 0;
    u32x4 ms_exp = {0, 0, 0, 0};
    bool hs_act = true, ms_act = !real && hp < 8 && rb == 0 && sw == 1;
    const unsigned hs_off = (lane == 1 ? SY_GO : SY_CH) + bid * 4;
    const unsigned re_off = t2 == 0 ? SY_RE + bid * 4 : OOB;
    const unsigned ms_off = lane * 16;
    const char* wsu = uni(ws);
#define HS_PROBE() asm volatile("s_nop 4\n\tglobal_load_dword v156, %0, %1 sc1" :: "v"(hs_off), "s"(wsu) : "memory", RSV)
#define MS_PROBE() asm volatile("s_nop 4\n\tglobal_load_dwordx4 v[152:155], %0, %1 offset:2048 sc1" :: "v"(ms_off), "s"(wsu) : "memory", RSV)
    auto hs_step = [&](bool first) {
      if (hs_act) {
        const unsigned hv_ = rget<156>();
        hs_act = __builtin_amdgcn_readlane(hv_, 1) == hs_g0;
        const unsigned c = __builtin_amdgcn_readlane(hv_, 0);
        if (sw == 0 && (first || c != hs_cur)) STW1(hv_, re_off);
        hs_cur = c;
        HS_PROBE();
      }
      if (ms_act) {
        const unsigned d = ((rget<152>() ^ ms_exp[0]) | (rget<153>() ^ ms_exp[1])) | ((rget<154>() ^ ms_exp[2]) | (rget<155>() ^ ms_exp[3]));
        ms_act = __any(d != 0u);
        STW(ms_exp, ms_act ? OOB : SY_GO + ms_off);
        MS_PROBE();
      }
    };
    HS_PROBE();
    if (ms_act) {
      MS_PROBE();
      asm volatile("s_nop 4\n\tglobal_load_dwordx4 v[148:151], %0, %1 offset:1024 sc1" :: "v"(ms_off), "s"(wsu) : "memory", RSV);
    }
    const int gr = (g8 >> 3) * 7 + min(hs_, 6);
    const unsigned voG = (unsigned)((lane >> 3) * 8 * F_ + (lane & 7) * 8) * 2u, voD = (unsigned)((lane >> 3) * 8 * D_ + (lane & 7) * 8) * 2u;
    const size_t gofs = ((size_t)(kq * 256 + sw * 64) * F_ + gr * 64) * 2;
    const char* pG = (const char*)wg + gofs;
    const char* pU = (const char*)wu + gofs;
    const char* pD = (const char*)(wd + (size_t)(gr * 64) * D_ + kq * 256 + sw * 64);
    auto wstep = [&](auto I) {
      constexpr int i = I.v;
      if (i < 8) wload<VW + 4 * i>(voG, pG + (i & 7) * (F_ * 2));
      else if (i < 16) wload<VW + 4 * i>(voG, pU + (i & 7) * (F_ * 2));
      else wload2<VW + 4 * i>(voD, pD + (i & 7) * (D_ * 2));
    };
    unsigned voW;
    {
      const int bi = lane >> 4, q = (lane >> 2) & 3, ch = lane & 3;
      voW = (unsigned)((4 * (bi >> 1) + q) * D_ + (bi & 1) * 32 + (ch ^ (bi & 2)) * 8) * 2u;
    }
    const char* baseW = (const char*)(wo + (size_t)((16 * (sw >> 1)) * 32 + 16 * (sw & 1)) * D_ + cb * 128) - 1024;
    auto stageW = [&](auto J, bool rsv) {
      const char* s_ = baseW + (size_t)whead(J.v) * (32 * D_ * 2);
      if (rsv) glds4r<wo_slot(J.v)>(voW + 1024, voW + 128, voW + 8 * D_ * 2 - 1024, voW + 8 * D_ * 2 + 128 - 2048, s_, l4);
      else glds4<wo_slot(J.v)>(voW + 1024, voW + 128, voW + 8 * D_ * 2 - 1024, voW + 8 * D_ * 2 + 128 - 2048, s_, l4);
    };
    int sta[2], stb[3];
    {
#pragma unroll
      for (int c = 0; c < 2; ++c) { sta[c] = (posA & 3) * K1_STAGE + (32 * sw + (lane & 15)) * 128 + (((4 * c + (lane >> 4)) ^ ((lane & 15) >> 1)) * 16); asm volatile("" : "+v"(sta[c]) :: RSV); }
#pragma unroll
      for (int i = 0; i < 3; ++i) {
        const int G = sw * 3 + i, hd = G >= 6 ? 1 : 0, mat = (G - 6 * hd) >> 1;
        stb[i] = (posB & 3) * K1_STAGE + 16384 + (lane >> 4) * 6144 + mat * 1024 + hd * 256 + ((lane & 15) >> 2) * 64 + (G & 1) * 32 + (lane & 3) * 8;
        asm volatile("" : "+v"(stb[i]) :: RSV);
      }
    }
    unsigned rs_dst = (lane >> 4) == ((lane & 15) >> 2) ? rs_slot + (32 * sw + (lane & 15)) * 8 : OOB, cs_dst = lane < 48 ? cs_slot + (sw * 48 + lane) * 4 : OOB;
    unsigned rs_off = WS_RS + rb * 16384 + (lane >> 4) * 1024 + sw * 256 + (lane & 15) * 16;
    unsigned cs_off = WS_CS + hp * 12288 + (lane >> 4) * 768 + sw * 192 + min(lane & 15, 11) * 16;
    asm volatile("" : "+v"(rs_dst), "+v"(cs_dst), "+v"(rs_off), "+v"(cs_off) :: RSV);
    int stm_dst = ((lane >> 5) * 128 + sw * 32 + (lane & 15) * 2 + ((lane >> 4) & 1)) * 4, cs_put = (lane & 15) < 12 ? (256 + sw * 48 + (lane & 15) * 4 + (lane >> 4)) * 4 : 1792 + lane * 4;
    asm volatile("" : "+v"(stm_dst), "+v"(cs_put) :: RSV);
    auto st_load = [&]() { sfor<0, 4>([&](auto I) { sload<224 + 4 * I.v>(rs_off, wsu + I.v * 4096); sload<240 + 4 * I.v>(cs_off, wsu + I.v * 3072); }); };
    sfor<3, GS[0]>(group);
    WAIT_VR(gops(GS[0]) - 10 + 1); RAW_BARR();
    sfor<GS[0], GS[1]>(group);
    WAIT_VR(gops(GS[1]) - 20); RAW_BARR();
    hs_g0 = __builtin_amdgcn_readlane(rget<156>(), 1);
    if (ms_act) {
      const u32x4 re = {rget<152>(), rget<153>(), rget<154>(), rget<155>()}, g0 = {rget<148>(), rget<149>(), rget<150>(), rget<151>()};
#pragma unroll
      for (int j = 0; j < 4; ++j) { const unsigned c = re[j] + 1u; ms_exp[j] = (c == g0[j]) ? c + 1u : c; }
      STW(ms_exp, SY_CH + ms_off);
    }
    u32x4 ra[4] = {}; u32x2 rc[3][4] = {};
    auto chunk = [&](auto C) {
      const u32x4 ONES4 = {0x3F803F80u, 0x3F803F80u, 0x3F803F80u, 0x3F803F80u};
      const f32x4 Z4 = {0.f, 0.f, 0.f, 0.f};
      if constexpr (C.v == 0) {
        asm volatile("" : "+v"(ra[0]), "+v"(ra[1]), "+v"(ra[2]), "+v"(ra[3]) :: RSV);
        f32x4 s[2], q[2];
#pragma unroll
        for (int i = 0; i < 2; ++i) { q[i] = mfma16(ra[2 * i], ra[2 * i], Z4); s[i] = mfma16(ONES4, ra[2 * i], Z4); }
#pragma unroll
        for (int i = 0; i < 2; ++i) { q[i] = mfma16(ra[2 * i + 1], ra[2 * i + 1], q[i]); s[i] = mfma16(ONES4, ra[2 * i + 1], s[i]); }
#pragma unroll
        for (int i = 0; i < 2; ++i) {
          float a = q[i][0], c = q[i][1], d = q[i][2], e = q[i][3];
          asm volatile("" : "+v"(a), "+v"(c), "+v"(d), "+v"(e) :: RSV);
          const float lo = (lane & 1) ? c : a, hi = (lane & 1) ? e : d;
          __builtin_amdgcn_raw_buffer_store_b64((u32x2){sane1(__float_as_uint(s[i][0])), sane1(__float_as_uint((lane & 2) ? hi : lo))}, wsr, rs_dst + i * 128, 0, 16);
        }
      } else {
        f32x4 c[3];
#pragma unroll
        for (int i = 0; i < 3; ++i) { asm volatile("" : "+v"(rc[i][0]), "+v"(rc[i][1]), "+v"(rc[i][2]), "+v"(rc[i][3]) :: RSV); c[i] = mfma16(ONES4, (u32x4){rc[i][0][0], rc[i][0][1], rc[i][1][0], rc[i][1][1]}, Z4); }
#pragma unroll
        for (int i = 0; i < 3; ++i) c[i] = mfma16(ONES4, (u32x4){rc[i][2][0], rc[i][2][1], rc[i][3][0], rc[i][3][1]}, c[i]);
        float c0 = c[0][0], c1 = c[1][0], c2 = c[2][0];
        asm volatile("" : "+v"(c0), "+v"(c1), "+v"(c2) :: RSV);
        STW1(sane1(__float_as_uint(lane < 16 ? c0 : lane < 32 ? c1 : c2)), cs_dst);
      }
    };
    sfor<0, 12>([&](auto T) {
      constexpr int t = T.v;
      if (t < 4 && posA == t + 1) {
#pragma unroll
        for (int c = 0; c < 4; ++c) ra[c] = *(const u32x4*)(smem + sta[c & 1] + (c >> 1) * 2048);
      }
      if (t < 4 && posB == t + 1) {
#pragma unroll
        for (int i = 0; i < 3; ++i)
#pragma unroll
          for (int j = 0; j < 4; ++j) rc[i][j] = trread(smem + (stb[i] ^ ((j >> 1) * 32)) + (j >> 1) * 3072 + (j & 1) * 512);
      }
      if (t == 2) HS_PROBE();
      if (t >= 4) hs_step(t == 4);
      sfor<GS[t + 1], GS[t + 2]>([&](auto G) {
        group(G);
        if constexpr ((t == 4 || t == 5) && G.v == GS[t + 1]) chunk(IC<t - 4>{});
      });
      if (real) sfor<WS_[t], WS_[t + 1]>(wstep);
      constexpr int need = t < 11 ? t + 3 : 15, nw = wyoung(t, need);
      if (nw && real) WAIT_VR(gops(GS[t + 2]) - 10 * need + nw); else WAIT_VR(gops(GS[t + 2]) - 10 * need);
      RAW_BARR();
    });
    hs_step(false);
    auto st_put = [&]() -> bool {
      float a[8]; unsigned m = 0, n = 0;
      rsum4<224>(a, m, n); rsum4<240>(a + 4, m, n);
      const float s1 = sw16(a[0], a[2]), q1 = sw16(a[1], a[3]);
      const float cs = sw32(sw16(a[4], a[5]), sw16(a[6], a[7]));
      const float mean = sw32(s1, s1) * (1.0f / 1024.0f);
      const float var = fmaxf(__builtin_fmaf(sw32(q1, q1), 1.0f / 1024.0f, -(mean * mean)), 0.f);
      const float rinv = __builtin_amdgcn_rcpf(rbf(__builtin_amdgcn_sqrtf(rbf(rbf(var) + 1e-5f))));
      *(float*)(smem + L_STM + stm_dst) = (lane & 32) ? rinv : rbf(mean);
      *(float*)(smem + L_STM + cs_put) = cs;
      return wave_ok(max(m, n));
    };
    st_load();
    if (real) { sfor<WS_[12], WS_[12] + STE>(wstep); WAIT_VR(STE); } else WAIT_VR(0);
    const bool st_fin = st_put() && !hs_act;
    if (lane == 0) { misc[8 + sw] = 0; misc[12 + sw] = st_fin; misc[16 + sw] = 0; misc[7] = 0; }
    if (real) sfor<WS_[12] + STE, W1A>(wstep);
    RAW_BARR();
    if (!st_fin) {
      if (hs_act) hs_act = __builtin_amdgcn_readlane(rget<156>(), 1) == hs_g0;
      if (ms_act) {
        SPIN {
          bool ok = true;
#pragma unroll
          for (int j = 0; j < 4; ++j) ok &= (ld32(ws, SY_RE + lane * 16 + j * 4) == ms_exp[j]);
          if (__all(ok)) break;
        }
        STW(ms_exp, SY_GO + lane * 16);
      }
      if (hs_act) {
        SPIN {
          if (ld32(ws, SY_GO + bid * 4) != hs_g0) break;
          const unsigned c = ld32(ws, SY_CH + bid * 4);
          if (c != hs_cur) { hs_cur = c; STW1(c, re_off); }
        }
      }
      SPIN { st_load(); WAIT_VR(0); if (st_put()) break; }
    }
    asm volatile("" ::: "memory", RSV);
    if (lane == 0) misc[12 + sw] = 1;
    __builtin_amdgcn_s_setprio(0);
    if (real) sfor<W1A, 16>(wstep);
    auto mwait = [&](int v) { SPIN { const int a = misc[8 + (lane & 3)]; if (__all(a >= v)) break; __builtin_amdgcn_s_sleep(1); } asm volatile("" ::: "memory"); };
    sfor<0, 9>([&](auto J) {
      if (J.v == 4) mwait(3);
      stageW(J, J.v < 2);
      if (J.v >= 1) WAIT_V(4 * PACE);
      if (J.v == 1) { WAIT_VR(8); sfor<0, 16>(wtr); }
    });
    EPI();
#pragma unroll
    for (int i = 0; i < 2; ++i) { xv[i] = *(const u32x4*)(x + ego + i * 64); wv[i] = *(const u32x4*)(w_r + cb * 128 + eseg * 8 + i * 64); }
    asm volatile("s_waitcnt vmcnt(0)" : "+v"(xv[0]), "+v"(xv[1]), "+v"(wv[0]), "+v"(wv[1]) :: "memory");
    unpack_xw();
    RAW_BAR();
    RAW_BAR(); stageW(IC<9>{}, false); stageW(IC<10>{}, false);
    RAW_BAR(); stageW(IC<11>{}, false); stageW(IC<12>{}, false);
    RAW_BAR(); stageW(IC<13>{}, false); stageW(IC<14>{}, false);
    WAIT_V(16); RAW_BAR(); stageW(IC<15>{}, false);
    WAIT_V(12); RAW_BAR();
    WAIT_V(0); RAW_BAR();
    if (real) sfor<16, 24>(wstep);
  }
  LANES(); EPI();
  const int grow = rp * 64 + erow;
  int pa0 = L_P + erow * 512 + ((2 * eseg) ^ (erow & 7)) * 16, pa1 = pa0 ^ 16;
  unsigned st_dst = eseg == 0 ? WS_ST2 + ((rp * 8 + cb) * 64 + erow) * 16 : OOB, st_src = WS_ST2 + rp * 8192 + lane * 16;
  unsigned fd_dst = lane < 8 ? WS_FD + (cb * 32 + rp) * 128 + lane * 16 : OOB, kidx = 0xFFFFu - grow;
  asm volatile("" : "+v"(pa0), "+v"(pa1), "+v"(st_dst), "+v"(st_src), "+v"(fd_dst), "+v"(kidx));
  RAW_BAR2();
  unsigned hb[8];
  f32x2 hv[8];
  {
    f32x4 pp[2][4];
#pragma unroll
    for (int k = 0; k < 2; ++k)
#pragma unroll
      for (int i = 0; i < 4; ++i) pp[k][i] = *(const f32x4*)(smem + ((i & 1) ? pa1 : pa0) + (i >> 1) * 256 + k * L_PK);
    SB0();
    f32x2 s = {0.f, 0.f}, q = {0.f, 0.f}, p2 = {0.f, 0.f};
#pragma unroll
    for (int i = 0; i < 8; ++i) {
      const f32x4 a = pp[0][i >> 1] + pp[1][i >> 1];
      const f32x2 u = xf[i] + unpk(pack2o(a[2 * (i & 1)], a[2 * (i & 1) + 1]));
      hb[i] = pack2o(u[0], u[1]);
      hv[i] = unpk(hb[i]);
      s += hv[i]; q += hv[i] * hv[i]; p2 += hv[i] * wf2[i];
    }
    STW(sane((u32x4){__float_as_uint(oct_sum(s[0] + s[1])), __float_as_uint(oct_sum(q[0] + q[1])), __float_as_uint(oct_sum(p2[0] + p2[1])), __float_as_uint(sw_)}), st_dst);
  }
  KEEP2();
  const int sw = wave - 4;
  const unsigned fd_src = WS_FD + xcd * 4096 + r * 128;
  u32x2 kpk = {SENT, SENT};
  if (wave == 0) {
    u32x4 pa[8];
    SLEEP(P_C);
    SPIN {
      unsigned m = 0;
#pragma unroll
      for (int j = 0; j < 8; ++j) { pa[j] = LDW(st_src + j * 1024); m = chk(m, pa[j]); }
      if (wave_ok(m)) break;
      KEEP2();
    }
#define F4(v) __builtin_bit_cast(f32x4, v)
    const f32x4 t = ((F4(pa[0]) + F4(pa[1])) + (F4(pa[2]) + F4(pa[3]))) + ((F4(pa[4]) + F4(pa[5])) + (F4(pa[6]) + F4(pa[7])));
    const float mean = t[0] * (1.0f / 1024.0f);
    const float var = fmaxf(__builtin_fmaf(t[1], 1.0f / 1024.0f, -(mean * mean)), 0.f);
    const float mb = rbf(mean), rs = __builtin_amdgcn_rcpf(rbf(__builtin_amdgcn_sqrtf(rbf(rbf(var) + 1e-5f))));
    *(f32x2*)(smem + L_KX + lane * 8) = (f32x2){mb, rs};
    const float sc = fmaxf((t[2] - mb * t[3]) * rs, -INFINITY);
    const int u = (int)(pack2(0.f, sc) & 0xffff0000u);
    unsigned b0 = ((unsigned)(u ^ ((u >> 31) | (int)0x80000000)) & 0xffff0000u) | (0xFFFFu - (rp * 64 + lane)), b1 = 0;
    top2_lo(b0, b1); top2_hi(b0, b1);
    STW(((u32x4){b0, b1, b0, b1}), fd_dst);
    if (lane == 0) *(u32x2*)(smem + L_MISC + 112) = (u32x2){b0, b1};
  } else if (wave >= 4) {
    asm volatile("s_waitcnt vmcnt(0)" ::: "memory", RSV2);
    sfor<16, 24>(wtr);
  }
  RAW_BAR();
  bool cand;
  u32x4 ob[2];
  {
    const f32x2 mr = *(const f32x2*)(smem + L_KX + erow * 8);
    const u32x2 kk = *(const u32x2*)(smem + L_MISC + 112);
    const float mb = mr[0], rs = mr[1];
    const unsigned b0 = kk[0], b1 = kk[1];
    u32x4 nq[2];
#pragma unroll
    for (int i = 0; i < 8; ++i) {
      const f32x2 d = hv[i] - mb;
      const f32x2 n = unpk(pack2o(d[0], d[1])) * rs;
      nq[i >> 2][i & 3] = pack2o(n[0], n[1]);
    }
    const bool c2 = (b1 & 0xffffu) == kidx;
    cand = c2 || (b0 & 0xffffu) == kidx;
    if (cand) {
      const unsigned xo = WS_XS + (rp * 2 + (c2 ? 1 : 0)) * 2048 + cb * 256 + eseg * 16;
      STW(sane(nq[0]), xo); STW(sane(nq[1]), xo + 128);
      char* cp = smem + L_CAND + (c2 ? 512 : 0) + eseg * 16;
      *(u32x4*)cp = (u32x4){hb[0], hb[1], hb[2], hb[3]}; *(u32x4*)(cp + 128) = (u32x4){hb[4], hb[5], hb[6], hb[7]};
      *(u32x4*)(cp + 256) = nq[0]; *(u32x4*)(cp + 384) = nq[1];
    }
    asm volatile("" ::: "memory");
    if (wave >= 4) { SLEEP(P_D); kpk = __builtin_amdgcn_raw_buffer_load_b64(wsr, fd_src, 0, 16); }
    asm volatile("" ::: "memory");
#pragma unroll
    for (int i = 0; i < 8; ++i) { const f32x2 o = hv[i] + unpk(nq[i >> 2][i & 3]); ob[i >> 2][i & 3] = pack2(o[0], o[1]); }
  }
#define OUT_ST() do { *(u32x4*)(out + ego) = ob[0]; *(u32x4*)(out + ego + 64) = ob[1]; } while (0)
  if (wave < 4) {
    if (!cand) OUT_ST();
    RAW_BAR();
    const unsigned w0 = misc[24], w1 = misc[25];
    if (cand && grow != (int)key_idx(w0) && grow != (int)key_idx(w1)) OUT_ST();
#pragma unroll 1
    for (int tk = 0; tk < 2; ++tk) {
      const unsigned wk = tk ? w1 : w0, slot = misc[26 + tk];
      if ((int)(slot >> 1) != rp) continue;
      u32x4 pv[7];
      const unsigned yo = WS_YP + ((tk * 8 + cb) * 64 + (lane >> 3) * 8) * 512 + wave * 128 + (lane & 7) * 16;
      const int col = (wave >> 1) * 64 + (lane & 7) * 8 + (wave & 1) * 4 + (lane >> 4);
      float sg = rbf(__builtin_amdgcn_rcpf(1.0f + __expf(-key_val(wk))));
      float hh = bflo(*(const u16*)(smem + L_CAND + (slot & 1) * 512 + col * 2)), nn = bflo(*(const u16*)(smem + L_CAND + (slot & 1) * 512 + 256 + col * 2));
      u16* op = out + (size_t)key_idx(wk) * D_ + cb * 128 + col;
      asm volatile("" : "+v"(sg), "+v"(hh), "+v"(nn), "+v"(op));
      if (tk == 0 || (int)(misc[26] >> 1) != rp) {
        SPIN { if (misc[30] == (int)w0) break; __builtin_amdgcn_s_sleep(1); }
        SLEEP(P_E);
      }
      SPIN {
        unsigned m = 0;
#pragma unroll
        for (int p = 0; p < 7; ++p) { pv[p] = LDW(yo + p * 512); m = chk(m, pv[p]); }
        if (wave_ok(m)) break;
        asm volatile("" ::: "memory");
      }
      f32x4 s = __builtin_bit_cast(f32x4, pv[0]);
#pragma unroll
      for (int p = 1; p < 7; ++p) s += __builtin_bit_cast(f32x4, pv[p]);
      float y = sw32(sw16(s[0], s[1]), sw16(s[2], s[3]));
      y += dppf<DPP_ROR8>(y);
      if (!(lane & 8)) *(GAS u16*)op = (u16)pack2(hh + rbf(nn + rbf(rbf(y) * sg)), 0.f);
    }
  } else {
    unsigned xl = WS_XS + (kq * 256 + sw * 64 + (lane >> 4) * 16 + ((lane >> 2) & 1) * 8) * 2;
    int red_dst = (lane < 8 || (lane >= 24 && lane < 32)) ? L_RED + sw * 2048 + ((lane >> 3) & 1) * 1024 + (lane & 7) * 16 : L_RED + 8192 + lane * 16;
    unsigned gu_src = WS_GU + g8 * 4096 + lane * 16;
    int act_dst = L_ACT + sw * 256 + ((lane & 7) * 8 + (lane >> 3)) * 2, act_src = L_ACT + sw * 256 + (lane & 1) * 128 + (lane >> 4) * 32 + ((lane >> 1) & 1) * 16;
    unsigned yp_dst = lane < 16 ? WS_YP + (((lane >> 3) * 8 + 2 * kq + (sw >> 1)) * 64 + g8) * 512 + (sw & 1) * 256 + (lane & 7) * 16 : OOB;
    asm volatile("" : "+v"(xl), "+v"(red_dst), "+v"(gu_src), "+v"(act_dst), "+v"(act_src), "+v"(yp_dst));
    unsigned w0, w1; int s0 = -1, s1 = -1;
    u32x4 xa = {SENT, SENT, SENT, SENT};
    SPIN {
      const bool all = wave_ok(kpk[1]);
      const unsigned k0 = kpk[1] == SENT ? 0u : kpk[0], k1 = kpk[1] == SENT ? 0u : kpk[1];
      w0 = k0; w1 = k1;
      top2_lo(w0, w1); top2_mid(w0, w1);
      const int n0 = 2 * __builtin_ctzll(__ballot(k0 == w0));
      const u64 m1 = __ballot(k0 == w1);
      const int n1 = m1 ? 2 * __builtin_ctzll(m1) : n0 + 1;
      const bool same = n0 == s0 && n1 == s1;
      s0 = n0; s1 = n1;
      asm volatile("" ::: "memory");
      if (all && same) break;
      if (!all) { kpk = __builtin_amdgcn_raw_buffer_load_b64(wsr, fd_src, 0, 16); }
      xa = LDW(xl + ((lane & 1) ? s1 : s0) * 2048);
      if (all) break;
    }
    SPIN {
      if (wave_ok(chk(0u, xa))) break;
      asm volatile("" ::: "memory");
      xa = LDW(xl + ((lane & 1) ? s1 : s0) * 2048);
    }
    {
      const u32x4 z4 = {0u, 0u, 0u, 0u};
      const u32x4 xg = (lane & 2) ? z4 : xa, xu = (lane & 2) ? xa : z4;
      f32x4 d[8];
#pragma unroll
      for (int c = 0; c < 8; ++c) d[c] = mfma16(xg, wf[c], (f32x4){0.f, 0.f, 0.f, 0.f});
      if (tid == 256) { *(u32x4*)(smem + L_MISC + 96) = (u32x4){w0, w1, (unsigned)s0, (unsigned)s1}; misc[30] = 0; }
      SB0();
#pragma unroll
      for (int c = 0; c < 10; ++c) {
        if (c < 8) d[c & 7] = mfma16(xu, wf[8 + (c & 7)], d[c & 7]);
        if (c >= 2) *(f32x4*)(smem + red_dst + (c - 2) * 128) = d[(c - 2) & 7];
        SB0();
      }
    }
    RAW_BAR();
    if (sw == 0) {
      f32x4 t[8];
#pragma unroll
      for (int p = 0; p < 8; ++p) t[p] = *(const f32x4*)(smem + L_RED + p * 1024 + lane * 16);
      SB0();
      STW(sane(__builtin_bit_cast(u32x4, ((t[0] + t[1]) + (t[2] + t[3])) + ((t[4] + t[5]) + (t[6] + t[7])))), gu_src + kq * 1024);
    } else __builtin_amdgcn_s_sleep(2);
    {
      u32x4 q[4];
      if (grow != (int)key_idx(w0) && grow != (int)key_idx(w1)) OUT_ST();
      asm volatile("" ::: "memory");
      SLEEP(P_GU);
      SPIN {
        unsigned m = 0;
#pragma unroll
        for (int j = 0; j < 4; ++j) { q[j] = LDW(gu_src + j * 1024); m = chk(m, q[j]); }
        if (wave_ok(m)) break;
        asm volatile("" ::: "memory");
      }
      const f32x4 tot = (__builtin_bit_cast(f32x4, q[0]) + __builtin_bit_cast(f32x4, q[1])) + (__builtin_bit_cast(f32x4, q[2]) + __builtin_bit_cast(f32x4, q[3]));
      const f32x2 gs = unpk(pack2o(tot[0], tot[1])), us = unpk(pack2o(tot[2], tot[3]));
      const f32x2 sg = unpk(pack2o(gs[0] * __builtin_amdgcn_rcpf(1.0f + __expf(-gs[0])), gs[1] * __builtin_amdgcn_rcpf(1.0f + __expf(-gs[1]))));
      const unsigned a2 = pack2o(sg[0] * us[0], sg[1] * us[1]);
      *(u16*)(smem + act_dst) = (u16)a2; *(u16*)(smem + act_dst + 128) = (u16)(a2 >> 16);
      __builtin_amdgcn_fence(__ATOMIC_RELEASE, "wavefront"); __builtin_amdgcn_wave_barrier(); __builtin_amdgcn_fence(__ATOMIC_ACQUIRE, "wavefront");
      const u32x4 aa = *(const u32x4*)(smem + act_src);
      f32x4 d[8];
#pragma unroll
      for (int c = 0; c < 8; ++c) d[c] = mfma16(aa, wf[16 + c], (f32x4){0.f, 0.f, 0.f, 0.f});
      SB0();
      u32x4 y[2];
#pragma unroll
      for (int c = 0; c < 8; ++c) {
        const float t0 = d[c][0] + dppf<DPP_ROR8>(d[c][2]), t1 = d[c][3] + dppf<DPP_ROR8>(d[c][1]);
        y[c >> 2][c & 3] = __float_as_uint((lane & 8) ? t1 : t0);
      }
      STW(sane(y[0]), yp_dst); STW(sane(y[1]), yp_dst + 128);
      if (tid == 256) misc[30] = w0;
    }
  }
}

extern "C" void kernel_launch(void* const* d_in, const int* in_sizes, int n_in, void* d_out, int out_size,
                              void* d_ws, size_t ws_size, hipStream_t stream) {
  (void)hipFuncSetAttribute((const void*)fused, hipFuncAttributeMaxDynamicSharedMemorySize, LDS_TOTAL);
  fused<<<256, 512, LDS_TOTAL, stream>>>((const u16*)d_in[0], (const u16*)d_in[1], (const u16*)d_in[2], (const u16*)d_in[4],
                                         (const u16*)d_in[5], (const u16*)d_in[6], (const u16*)d_in[7], (u16*)d_out, (char*)d_ws);
}
```

```cpp
#include <hip/hip_runtime.h>
#include <stdint.h>

#define LAS __attribute__((address_space(3)))
#define GAS __attribute__((address_space(1)))
#define DI __device__ __forceinline__

typedef unsigned short u16;
typedef unsigned long long u64;
typedef __attribute__((ext_vector_type(2))) __bf16 bf16x2;
typedef __attribute__((ext_vector_type(8))) __bf16 bf16x8;
typedef __attribute__((ext_vector_type(4))) short s16x4;
typedef __attribute__((ext_vector_type(2))) unsigned short u16x2;
typedef __attribute__((ext_vector_type(4))) float f32x4;
typedef __attribute__((ext_vector_type(2))) float f32x2;
typedef __attribute__((ext_vector_type(4))) unsigned u32x4;
typedef __attribute__((ext_vector_type(2))) unsigned u32x2;
typedef GAS unsigned gu32;
typedef GAS unsigned long long gu64;

constexpr int S_ = 2048, D_ = 1024, F_ = 3584;

constexpr unsigned SY_CH = 81920, SY_GO = SY_CH + 16, SY_RE = SY_CH + 8192;
constexpr unsigned WS_FD  = 98304;
constexpr unsigned WS_ZF  = 8192;
constexpr unsigned WS_KVF = 16384;
constexpr unsigned WS_RS  = 131072;
constexpr unsigned WS_CS  = WS_RS + 16 * 16 * 1024;
constexpr unsigned WS_ZP  = WS_CS + 16 * 16 * 768;
constexpr unsigned WS_FB  = WS_ZP + 32 * 16 * 128;
constexpr unsigned WS_ST2 = WS_FB + 32 * 32 * 128;
constexpr unsigned WS_XS  = WS_ST2 + 32 * 8 * 1024;
constexpr unsigned WS_GU  = WS_XS + 64 * 2048;
constexpr unsigned WS_YP  = WS_GU + 256 * 1024;
constexpr unsigned WS_KVP = WS_YP + 2 * 8 * 64 * 512;
constexpr unsigned WS_O   = WS_KVP + 32 * 16 * 4096;
constexpr unsigned WS_END = WS_O + S_ * D_ * 2;
constexpr unsigned OOB = 0x80000000u;
constexpr size_t WS_DIAG  = 64u << 20;
#define CNT(i) do {} while (0)
#define STAMPT(kid, i, T) do {} while (0)

#define TSL(idx) do {} while (0)
#define TSDUMP(k) do {} while (0)
template <int V> struct IC { static constexpr int v = V; };
template <int B, int E, class F> DI void sfor(F&& f) { if constexpr (B < E) { f(IC<B>{}); sfor<B + 1, E>(f); } }

DI float bflo(unsigned u) { return __uint_as_float(u << 16); }
DI float bfhi(unsigned u) { return __uint_as_float(u & 0xffff0000u); }
DI float rbf(float x) { return (float)(__bf16)x; }
DI unsigned pack2(float a, float b) { bf16x2 c; c[0] = (__bf16)a; c[1] = (__bf16)b; return __builtin_bit_cast(unsigned, c); }
DI unsigned pack2o(float a, float b) { unsigned p = pack2(a, b); asm("" : "+v"(p)); return p; }
DI f32x4 mfma16(u32x4 a, u32x4 b, f32x4 c) {
  return __builtin_amdgcn_mfma_f32_16x16x32_bf16(__builtin_bit_cast(bf16x8, a), __builtin_bit_cast(bf16x8, b), c, 0, 0, 0);
}
DI void mfma16i(u32x4 a, u32x4 b, f32x4& c) { asm volatile("v_mfma_f32_16x16x32_bf16 %0, %1, %2, %0" : "+v"(c) : "v"(a), "v"(b)); }
DI f32x2 unpk(unsigned u) { return (f32x2){bflo(u), bfhi(u)}; }
DI u32x2 trread(const char* p) {
  s16x4 v = __builtin_amdgcn_ds_read_tr16_b64_v4i16((LAS s16x4*)p);
  return __builtin_bit_cast(u32x2, v);
}
DI const char* uni(const void* p) {
  const u64 v = (u64)p;
  return (const char*)(((u64)(unsigned)__builtin_amdgcn_readfirstlane((int)(v >> 32)) << 32) | (unsigned)__builtin_amdgcn_readfirstlane((int)v));
}
#define RV4(a, b, c, d) "v" #a, "v" #b, "v" #c, "v" #d
#define RV10(p) "v" #p "0", "v" #p "1", "v" #p "2", "v" #p "3", "v" #p "4", "v" #p "5", "v" #p "6", "v" #p "7", "v" #p "8", "v" #p "9"
#define RSV2 RV4(224, 225, 226, 227), "v228", "v229", RV10(23), RV10(24), RV4(250, 251, 252, 253), "v254", "v255"
#define RSV1 RV4(192, 193, 194, 195), RV4(196, 197, 198, 199), RV10(20), RV10(21), RV4(220, 221, 222, 223), RSV2
#define RSV "v148", "v149", RV10(15), RV10(16), RV10(17), RV10(18), "v190", "v191", RSV1
#define NTW " nt"
#define GL_(v, s, o) "global_load_lds_dwordx4 " v ", " s " offset:" #o
#define GLDS4(SC, CL...) asm volatile("s_add_u32 m0, %5, %c6\n\ts_nop 3\n\t" GL_("%0", "%4", 0) SC "\n\t" GL_("%1", "%4", 1024) SC "\n\t" \
  GL_("%2", "%4", 2048) SC "\n\t" GL_("%3", "%4", 3072) SC :: "v"(v0), "v"(v1), "v"(v2), "v"(v3), "s"(uni(sbase)), "s"(lds), "i"(IMM) : "memory", "scc", ##CL)
#define GLDS3(SC, CL...) asm volatile("s_add_u32 m0, %4, %c5\n\ts_nop 3\n\t" GL_("%0", "%3", 0) SC "\n\t" GL_("%1", "%3", 1024) SC "\n\t" \
  GL_("%2", "%3", 2048) SC :: "v"(v0), "v"(v1), "v"(v2), "s"(uni(sbase)), "s"(lds), "i"(IMM) : "memory", "scc", ##CL)
template <int IMM> DI void glds4(unsigned v0, unsigned v1, unsigned v2, unsigned v3, const void* sbase, unsigned lds) { GLDS4(""); }
template <int IMM> DI void glds4r(unsigned v0, unsigned v1, unsigned v2, unsigned v3, const void* sbase, unsigned lds) { GLDS4("", RSV); }
template <int IMM> DI void glds3r(unsigned v0, unsigned v1, unsigned v2, const void* sbase, unsigned lds) { GLDS3("", RSV); }
template <int R> DI void sload(unsigned voff, const void* sbase) {
  asm volatile("s_nop 4\n\tglobal_load_dwordx4 v[%c0:%c1], %2, %3 sc1" ::"i"(R), "i"(R + 3), "v"(voff), "s"(uni(sbase)) : "memory", RSV);
}
DI float fadd(float a, float b) { float o; asm("v_add_f32 %0, %1, %2" : "=v"(o) : "v"(a), "v"(b)); return o; }
#define RA_(o, a, b) "v_add_f32 %" #o ", v[%c6+" #a "], v[%c6+" #b "]\n\t"
#define RB2_(o, a) "v_add_f32 %" #o ", v[%c6+" #a "], %" #o "\n\t"
#define RM_(o, a, b) "v_max3_u32 %" #o ", v[%c6+" #a "], v[%c6+" #b "], %" #o "\n\t"
template <int R> DI void rsum4(float* a, unsigned& m, unsigned& n) {
  asm volatile(RA_(0, 0, 4) RA_(1, 1, 5) RM_(4, 0, 1) RA_(2, 2, 6) RA_(3, 3, 7) RM_(5, 2, 3) RB2_(0, 8) RM_(4, 4, 5) RB2_(1, 9) RM_(5, 6, 7)
               RB2_(2, 10) RM_(4, 8, 9) RB2_(3, 11) RM_(5, 10, 11) RB2_(0, 12) RB2_(1, 13) RM_(4, 12, 13) RB2_(2, 14) RB2_(3, 15) RM_(5, 14, 15)
               : "=&v"(a[0]), "=&v"(a[1]), "=&v"(a[2]), "=&v"(a[3]), "+v"(m), "+v"(n) : "i"(R) : RSV);
}
template <int R> DI void wload2(unsigned voff, const void* sbase) {
  asm volatile("s_nop 4\n\tglobal_load_dwordx4 v[%c0:%c1], %2, %3" NTW ::"i"(R), "i"(R + 3), "v"(voff), "s"(uni(sbase)) : "memory", RSV2);
}
template <int R> DI void wload(unsigned voff, const void* sbase) {
  asm volatile("s_nop 4\n\tglobal_load_dwordx4 v[%c0:%c1], %2, %3" NTW ::"i"(R), "i"(R + 3), "v"(voff), "s"(uni(sbase)) : "memory", RSV);
}
template <int RA, int RB> DI unsigned wperm(unsigned sel) {
  unsigned o;
  if (RA < 192) asm volatile("v_perm_b32 %0, v%c2, v%c1, %3" : "=v"(o) : "i"(RA), "i"(RB), "s"(sel) : RSV);
  else if (RA < 224) asm volatile("v_perm_b32 %0, v%c2, v%c1, %3" : "=v"(o) : "i"(RA), "i"(RB), "s"(sel) : RSV1);
  else asm volatile("v_perm_b32 %0, v%c2, v%c1, %3" : "=v"(o) : "i"(RA), "i"(RB), "s"(sel) : RSV2);
  return o;
}
template <int R> DI unsigned rget() { unsigned o; asm volatile("v_mov_b32 %0, v%c1" : "=v"(o) : "i"(R) : RSV); return o; }
DI float xh_sum(float v) {
  auto r = __builtin_amdgcn_permlane32_swap(__float_as_uint(v), __float_as_uint(v), false, false);
  return __uint_as_float(r[0]) + __uint_as_float(r[1]);
}
template <int CTRL> DI unsigned dppu(unsigned v) { return (unsigned)__builtin_amdgcn_update_dpp(0, (int)v, CTRL, 0xf, 0xf, true); }
template <int CTRL> DI float dppf(float v) { return __uint_as_float(dppu<CTRL>(__float_as_uint(v))); }
constexpr int DPP_X1 = 0xB1, DPP_X2 = 0x4E, DPP_HMIR = 0x141, DPP_MIR = 0x140, DPP_ROR8 = 0x128;
DI float quad_sum(float v) { v += dppf<DPP_X1>(v); v += dppf<DPP_X2>(v); return v; }
DI float oct_sum(float v) { v = quad_sum(v); v += dppf<DPP_HMIR>(v); return v; }
DI float x16_sum(float v) {
  auto r = __builtin_amdgcn_permlane16_swap(__float_as_uint(v), __float_as_uint(v), false, false);
  return __uint_as_float(r[0]) + __uint_as_float(r[1]);
}
DI float sw16(float a, float b) {
  auto r = __builtin_amdgcn_permlane16_swap(__float_as_uint(a), __float_as_uint(b), false, false);
  return fadd(__uint_as_float(r[0]), __uint_as_float(r[1]));
}
DI float sw32(float a, float b) {
  auto r = __builtin_amdgcn_permlane32_swap(__float_as_uint(a), __float_as_uint(b), false, false);
  return fadd(__uint_as_float(r[0]), __uint_as_float(r[1]));
}
template <int N> struct Phi { unsigned tb[N]; f32x2 tr[N], e[N]; };
template <int N> DI void phi_a(const f32x2* t, Phi<N>& s) {
#pragma unroll
  for (int i = 0; i < N; ++i) s.tb[i] = pack2o(t[i][0], t[i][1]);
  __builtin_amdgcn_sched_barrier(0);
#pragma unroll
  for (int i = 0; i < N; ++i) s.tr[i] = unpk(s.tb[i]);
  __builtin_amdgcn_sched_barrier(0);
#pragma unroll
  for (int i = 0; i < N; ++i) s.e[i] = s.tr[i] * 1.44269504088896340736f;
  __builtin_amdgcn_sched_barrier(0);
}
template <int N> DI void phi_b(Phi<N>& s, unsigned* p) {
  u16x2 tm[2] = {{0, 0}, {0, 0}};
#pragma unroll
  for (int i = 0; i < N; ++i) {
    s.e[i][0] = __builtin_amdgcn_fmed3f(__builtin_amdgcn_exp2f(s.e[i][0]), 0.f, 1.f);
    tm[i & 1] = __builtin_elementwise_max(tm[i & 1], __builtin_bit_cast(u16x2, s.tb[i]));
    s.e[i][1] = __builtin_amdgcn_fmed3f(__builtin_amdgcn_exp2f(s.e[i][1]), 0.f, 1.f);
    __builtin_amdgcn_sched_barrier(0);
  }
#pragma unroll
  for (int i = 0; i < N; ++i) s.tr[i] += s.e[i];
  __builtin_amdgcn_sched_barrier(0);
#pragma unroll
  for (int i = 0; i < N; ++i) { s.tr[i][0] = fmaxf(s.tr[i][0], s.e[i][0]); s.tr[i][1] = fmaxf(s.tr[i][1], s.e[i][1]); }
  const u16x2 tx = __builtin_elementwise_max(tm[0], tm[1]);
  __builtin_amdgcn_sched_barrier(0);
#pragma unroll
  for (int i = 0; i < N; ++i) p[i] = pack2(s.tr[i][0], s.tr[i][1]);
  if (__any(tx[0] > 0xC0F0 || tx[1] > 0xC0F0)) {
#pragma unroll
    for (int i = 0; i < N; ++i) p[i] = pack2(bflo(p[i]) + 1e-6f, bfhi(p[i]) + 1e-6f);
  }
}
template <int N> DI void phi_n(const f32x2* t, unsigned* p) { Phi<N> s; phi_a<N>(t, s); phi_b<N>(s, p); }
#define RLX_AGENT __ATOMIC_RELAXED, __HIP_MEMORY_SCOPE_AGENT
DI void st64(char* ws, unsigned off, u64 v) { __hip_atomic_store((gu64*)(ws + off), v, RLX_AGENT); }
DI unsigned ld32(char* ws, unsigned off) { return __hip_atomic_load((gu32*)(ws + off), RLX_AGENT); }
#define WAIT_V(n) asm volatile("s_waitcnt vmcnt(%c0)" ::"i"(n) : "memory")
#define RAW_BAR() do { asm volatile("s_waitcnt lgkmcnt(0)" ::: "memory"); __builtin_amdgcn_s_barrier(); asm volatile("" ::: "memory"); } while (0)
#define SB0() __builtin_amdgcn_sched_barrier(0)
#define WAIT_VR(n) asm volatile("s_waitcnt vmcnt(%c0)" ::"i"(n) : "memory", RSV)
#define RAW_BARR() do { asm volatile("s_waitcnt lgkmcnt(0)" ::: "memory", RSV); __builtin_amdgcn_s_barrier(); asm volatile("" ::: "memory", RSV); } while (0)
#define KEEP2() asm volatile("" ::: "memory", RSV2)
#define RAW_BAR2() do { asm volatile("s_waitcnt lgkmcnt(0)" ::: "memory", RSV2); __builtin_amdgcn_s_barrier(); KEEP2(); } while (0)
constexpr unsigned SPIN_CAP = 1u << 18;
#define SPIN _Pragma("nounroll") for (unsigned s_ = 0; s_ < SPIN_CAP; ++s_)
constexpr unsigned SENT = 0xFFFFFFFFu;
DI unsigned sane1(unsigned v) { return min(v, 0xFFFFFFFEu); }
DI u32x4 sane(u32x4 v) { v[1] = sane1(v[1]); v[3] = sane1(v[3]); return v; }
DI unsigned chk(unsigned m, u32x4 v) { return max(max(m, v[1]), v[3]); }
DI bool wave_ok(unsigned m) { return !__any(m == SENT); }
DI unsigned key_idx(unsigned k) { return (0xFFFFu - (k & 0xFFFFu)) & (S_ - 1); }
DI float key_val(unsigned k) { const unsigned o = k >> 16; return __uint_as_float(((o & 0x8000u) ? (o & 0x7fffu) : (~o & 0xffffu)) << 16); }
DI void top2_mix(unsigned& b0, unsigned& b1, unsigned c0, unsigned c1) {
  const unsigned lo = min(b0, c0), mx = max(b1, c1);
  b0 = max(b0, c0); b1 = max(lo, mx);
}
DI void top2_lo(unsigned& b0, unsigned& b1) {
  top2_mix(b0, b1, dppu<DPP_X1>(b0), dppu<DPP_X1>(b1));
  top2_mix(b0, b1, dppu<DPP_X2>(b0), dppu<DPP_X2>(b1));
  top2_mix(b0, b1, dppu<DPP_HMIR>(b0), dppu<DPP_HMIR>(b1));
}
DI void top2_mid(unsigned& b0, unsigned& b1) {
  top2_mix(b0, b1, dppu<DPP_MIR>(b0), dppu<DPP_MIR>(b1));
  auto p = __builtin_amdgcn_permlane16_swap(b0, b0, false, false); auto q = __builtin_amdgcn_permlane16_swap(b1, b1, false, false);
  b0 = p[0]; b1 = q[0]; top2_mix(b0, b1, p[1], q[1]);
}
DI void top2_hi(unsigned& b0, unsigned& b1) {
  top2_mid(b0, b1);
  auto p = __builtin_amdgcn_permlane32_swap(b0, b0, false, false); auto q = __builtin_amdgcn_permlane32_swap(b1, b1, false, false);
  b0 = p[0]; b1 = q[0]; top2_mix(b0, b1, p[1], q[1]);
}

constexpr int K1_STAGE = 40960;
constexpr int LDS_TOTAL = 4 * K1_STAGE;
constexpr int L_STM = 0;
constexpr int L_MISC = 2048;
constexpr int L_XK = 5120;
constexpr int L_KX = 3328;
constexpr int L_ACT = 2304;
constexpr int L_CAND = 4096;
constexpr int L_WO = 16384;
constexpr int L_P = 16384, L_PK = 7 * 16384;
constexpr int L_RED = 65536;
constexpr int wo_slot(int j) { return L_WO + (j % 9) * 16384; }
constexpr int CODE_LINES = 12;
#define OW_ 8
constexpr int OW = OW_;
#define W1A_ 16
constexpr int W1A = W1A_;
#define GSCHED 5, 9, 13, 17, 21, 24, 27, 30, 31, 35, 39, 41, 45, 48, 48
constexpr int GS[15] = {GSCHED};
constexpr int gops(int g) { return (g / 3) * 10 + (g % 3 == 0 ? 0 : g % 3 == 1 ? 4 : 7); }
constexpr bool gs_ok() {
  bool ok = GS[0] >= 3 && GS[0] <= 9 && GS[1] >= 6 && GS[1] <= 12 && GS[0] <= GS[1] && GS[12] == 45 && GS[13] == 48 && GS[14] == 48;
  for (int t = 0; t < 12; ++t) ok = ok && GS[t + 2] <= 3 * (t + 5) && GS[t + 2] >= 3 * (t + 3) && GS[t + 2] >= GS[t + 1] + 1;
  return ok;
}
static_assert(gs_ok(), "staging schedule");
#define WSCHED 0, 0, 0, 0, 0, 0, 5, 5, 5, 10, 10, 10, 10
constexpr int WS_[13] = {WSCHED};
constexpr int wyoung(int t, int stages) {
  int e = 0;
  while (GS[e + 2] < 3 * stages) ++e;
  return WS_[t + 1] - WS_[e];
}
#define PACE 1
#define P_KVP 1
#define P_KVF 1
#define P_FB 8
#define P_C 14
#define P_D 2
#define P_GU 12
#define P_E 10
#define SLEEP(n) do { if (n) __builtin_amdgcn_s_sleep(n); } while (0)
#define STE 6
#define HS0 1
#define CP2 10
#define CP3 0
#define SLOWPF 1
#define SPX 0
constexpr int VW = 160;

__global__ __launch_bounds__(512, 2) void fused(const u16* __restrict__ x, const u16* __restrict__ wqkv,
                                                const u16* __restrict__ wo, const u16* __restrict__ w_r,
                                                const u16* __restrict__ wg, const u16* __restrict__ wu,
                                                const u16* __restrict__ wd, u16* __restrict__ out, char* __restrict__ ws) {
  extern __shared__ __attribute__((aligned(16))) char smem[];
  STAMPT(0, 0, 0);
  u64 pc0; asm volatile("s_getpc_b64 %0" : "=s"(pc0));
  const int wave = __builtin_amdgcn_readfirstlane(threadIdx.x >> 6);
  const unsigned lds0 = (unsigned)(size_t)smem;
  if (wave == 0) {
    const int l = threadIdx.x;
    asm volatile("s_mov_b32 m0, %1\n\ts_nop 0\n\tglobal_load_lds_dword %0, off"
                 :: "v"((const char*)(pc0 - (pc0 & 127)) + ((blockIdx.x >> 3) + (l < CODE_LINES ? l : 0) * 32) * 128), "s"(__builtin_amdgcn_readfirstlane(lds0 + 3 * K1_STAGE)) : "memory");
  }
#define RB_(b) (((b) & 1) * 8 + (((b) >> 3) & 7))
#define HP_(b) ((((b) & 7) >> 1) * 4 + ((b) >> 6))
#define KROT_(b) ((4 * ((HP_(b) >> 2) + (RB_(b) >> 3) + 2 * ((RB_(b) >> 2) & 1))) & 15)
  unsigned voA0, voA1, voA2, voA3, voB0, voB1, voB2, voB3, voB4, voB5;
  const unsigned l4 = __builtin_amdgcn_readfirstlane(lds0 + (wave - 4) * 4096), l6 = __builtin_amdgcn_readfirstlane(lds0 + (wave - 4) * 6144);
  const int krot0 = KROT_(blockIdx.x);
  auto group = [&](auto G) {
    constexpr int t = G.v / 3, p = G.v % 3, sl = (t & 3) * K1_STAGE;
    const int kt = (t + krot0) & 15;
    if (p == 0) glds4r<sl>(voA0, voA1, voA2, voA3, (const char*)x + kt * 128, l4);
    else if (p == 1) glds3r<sl + 16384>(voB0, voB1, voB2, (const char*)wqkv + kt * 64 * (3 * D_ * 2), l6);
    else glds3r<sl + 16384 + 3072>(voB3, voB4, voB5, (const char*)wqkv + kt * 64 * (3 * D_ * 2), l6);
  };
  if (wave >= 4) {
    const int lane = threadIdx.x & 63, sw = wave - 4;
    const int r0 = RB_(blockIdx.x) * 128 + 32 * sw + (lane >> 3);
    voA0 = (unsigned)(r0 * D_ + ((lane & 7) ^ (lane >> 4)) * 8) * 2u;
    voA1 = ((voA0 + 8 * D_ * 2) ^ 64) - 1024; voA2 = voA0 + 16 * D_ * 2 - 2048; voA3 = voA1 + 16 * D_ * 2 - 2048;
    asm volatile("" : "+v"(voA0), "+v"(voA1), "+v"(voA2), "+v"(voA3));
    STAMPT(1, 7, 256);
    group(IC<0>{});
    int l2 = lane; asm volatile("" : "+v"(l2));
    voB0 = (unsigned)((16 * sw + 4 * (l2 >> 5) + ((l2 >> 2) & 3)) * (3 * D_) + HP_(blockIdx.x) * 64 + ((l2 >> 4) & 1) * 32 + (l2 & 3) * 8) * 2u;
    voB1 = voB0 + 1024; voB2 = voB0 + 2048;
    voB3 = (voB0 ^ 32) + 8 * 3 * D_ * 2; voB4 = voB3 + 1024; voB5 = voB3 + 2048;
    asm volatile("" : "+v"(voB0), "+v"(voB1), "+v"(voB2), "+v"(voB3), "+v"(voB4), "+v"(voB5));
    sfor<1, 3>(group);
    STAMPT(1, 8, 256);
  }
  int bid = blockIdx.x; asm volatile("" : "+s"(bid));
  const int xcd = bid & 7, idx = bid >> 3;
  const int rb = RB_(bid), hp = HP_(bid);
  const int rp = xcd * 4 + (idx >> 3), cb = idx & 7;
  const int hs_ = (hp + 4 + (int)(((size_t)wqkv >> 7) & 7)) & 7;
  const int g8 = ((hp >> 3) * 4 + (rb >> 2)) * 8 + hs_, kq = rb & 3;
  const int krot = KROT_(bid), posA = 1 + (hp & 3), posB = 1 + (rb & 3);
  const int wrot = 4 * rp + (xcd >> 1), wdir = (xcd & 1) ? -1 : 1;
  auto whead = [&](int t) { return (wrot + wdir * t) & 15; };
  const unsigned rs_slot = WS_RS + (rb * 16 + ((krot + posA) & 15)) * 1024, cs_slot = WS_CS + (hp * 16 + ((krot + posB) & 15)) * 768;
  const __amdgpu_buffer_rsrc_t wsr = __builtin_amdgcn_make_buffer_rsrc(ws, 0, WS_END, 0x00020000);
#define LDW(off) __builtin_amdgcn_raw_buffer_load_b128(wsr, (off), 0, 16)
#define STW(v, off) __builtin_amdgcn_raw_buffer_store_b128((v), wsr, (off), 0, 16)
#define STW1(v, off) __builtin_amdgcn_raw_buffer_store_b32((v), wsr, (off), 0, 16)
  float* stm = (float*)(smem + L_STM);
  volatile LAS int* misc = (volatile LAS int*)(smem + L_MISC);
#define LANES() int tid = threadIdx.x; asm volatile("" : "+v"(tid)); const int lane = tid & 63, r = lane & 31, h = lane >> 5; (void)r; (void)h
#define EPI() const int erow = tid >> 3, eseg = tid & 7; const size_t ego = (size_t)(rp * 64 + erow) * D_ + cb * 128 + eseg * 8
  u32x4 xv[2], wv[2];
  f32x2 xf[8], wf2[8];
  float sw_;
  auto unpack_xw = [&]() {
    f32x2 a = {0.f, 0.f};
#pragma unroll
    for (int i = 0; i < 8; ++i) { xf[i] = unpk(xv[i >> 2][i & 3]); wf2[i] = unpk(wv[i >> 2][i & 3]); a += wf2[i]; }
    sw_ = oct_sum(a[0] + a[1]);
#pragma unroll
    for (int i = 0; i < 8; ++i) asm volatile("" : "+v"(xf[i]), "+v"(wf2[i]));
    asm volatile("" : "+v"(sw_));
  };
  unsigned tsv = 0; (void)tsv;
  u32x4 wf[24];

  auto wtr = [&](auto I) {
    constexpr int m = I.v >> 3, c = I.v & 7, R = VW + 32 * m + (c >> 1);
    const unsigned sel = (c & 1) ? 0x07060302u : 0x05040100u;
    wf[I.v] = (u32x4){wperm<R, R + 4>(sel), wperm<R + 8, R + 12>(sel), wperm<R + 16, R + 20>(sel), wperm<R + 24, R + 28>(sel)};
  };

  if (wave < 4) {
    LANES();
    const int c16 = lane & 15, g4 = lane >> 4;
    const int trl = (c16 >> 2) * 64 + (c16 & 3) * 8;
    const int wr = wave >> 1, wc = wave & 1;
    const int head = 2 * hp + wc;
    const unsigned kvp_off = WS_KVP + (head * 16 + rb) * 4096 + wr * 2048 + lane * 16;
    const unsigned zp_off = WS_ZP + (head * 16 + rb) * 128;
    const unsigned o_off = WS_O + ((rb * 4 + 2 * wr) * 32 + head) * 2048 + lane * 16;
    const unsigned fb_off = lane < 8 ? WS_FB + ((rb * 2 + wr) * 32 + head) * 128 + lane * 16 : OOB;
    unsigned pfa, pfb = OOB;
    unsigned pfc = lane < 8 ? fb_off : lane < 24 ? rs_slot + wave * 256 + (lane - 8) * 16
        : lane < 40 ? (wave < 3 ? cs_slot + wave * 256 : WS_KVF + hp * 4096 + rb * 256) + (lane - 24) * 16
        : lane < 48 ? (wave < 2 ? zp_off + (lane & 7) * 16 : (wave == 2 && rb < 2) ? WS_ZF + (2 * hp + rb) * 128 + (lane & 7) * 16 : OOB) : OOB;
    if (wave == 0) pfa = WS_ST2 + (rp * 8 + cb) * 1024 + lane * 16;
    else if (wave == 1) pfa = WS_GU + (g8 * 4 + kq) * 1024 + lane * 16;
    else pfa = WS_YP + (((wave - 2) * 8 + 2 * kq + (lane >> 5)) * 64 + g8) * 512 + (lane & 31) * 16;
    if (wave == 0) pfb = lane < 32 ? WS_XS + (rp * 2 + (lane >> 4)) * 2048 + cb * 256 + (lane & 15) * 16 : lane < 40 ? WS_FD + (cb * 32 + rp) * 128 + (lane & 7) * 16 : OOB;
    u32x4 S4 = {SENT, SENT, SENT, SENT};
    STW(S4, pfc);
    asm volatile("" : "+v"(pfa), "+v"(pfb), "+v"(S4));
    __builtin_amdgcn_s_setprio(1);
    f32x4 acc[4][6];
    int ao[2][2], bo[2][2], bv[2][2];
    f32x4 m4[4], i4[4]; float cs[4]; int st_ok;
    auto st_rd = [&](int q) {
      if (q == 0) { st_ok = misc[12 + (lane & 3)]; asm volatile("" ::: "memory"); }
      else if (q < 5) cs[(q - 1) & 3] = stm[256 + wc * 96 + 32 + 16 * (q < 3 ? q - 1 : (q - 1) ^ wr) + c16];
      else if (q < 9) m4[(q - 5) & 3] = *(const f32x4*)(stm + 64 * wr + 16 * (q - 5) + 4 * g4);
      else if (q < 13) i4[(q - 9) & 3] = *(const f32x4*)(stm + 128 + 64 * wr + 16 * (q - 9) + 4 * g4);
    };
    {
#pragma unroll
      for (int i = 0; i < 2; ++i) {
        ao[0][i] = (64 * wr + c16) * 128 + (((4 * i + g4) ^ (c16 >> 1)) * 16); ao[1][i] = ao[0][i] + 2 * K1_STAGE;
        bo[0][i] = 16384 + g4 * 3072 + wc * 256 + trl + ((i ^ (g4 & 1)) * 32); bo[1][i] = bo[0][i] + 2 * K1_STAGE;
        bv[0][i] = bo[0][i] ^ (wr * 32); bv[1][i] = bo[1][i] ^ (wr * 32);
        asm volatile("" : "+v"(ao[0][i]), "+v"(ao[1][i]), "+v"(bo[0][i]), "+v"(bo[1][i]), "+v"(bv[0][i]), "+v"(bv[1][i]));
      }
#pragma unroll
      for (int i = 0; i < 4; ++i)
#pragma unroll
        for (int j = 0; j < 6; ++j) { acc[i][j] = (f32x4){0.f, 0.f, 0.f, 0.f}; asm volatile("" : "+v"(acc[i][j])); }
      struct Frag { u32x4 a[4]; u32x2 bl[6], bh[6]; };
      Frag FA, FB;
#define K1_B(j) ((u32x4){C.bl[j][0], C.bl[j][1], C.bh[j][0], C.bh[j][1]})
#define K1_LA(i) N.a[i] = *(const u32x4*)(smem + ao[sn >> 1][hf] + (sn & 1) * K1_STAGE + (i) * 2048)
#define K1_LL(j) N.bl[j] = trread(smem + ((j) < 4 ? bo : bv)[sn >> 1][(j) & 1] + (sn & 1) * K1_STAGE + hf * 12288 + ((j) >> 1) * 1024)
#define K1_LH(j) N.bh[j] = trread(smem + ((j) < 4 ? bo : bv)[sn >> 1][(j) & 1] + (sn & 1) * K1_STAGE + hf * 12288 + ((j) >> 1) * 1024 + 512)
#define K1_M(i, j) if ((j) < 2) mfma16i(K1_B(j), C.a[i], acc[i][j]); else mfma16i(C.a[i], K1_B(j), acc[i][j]); SB0()
      auto sub = [&](Frag& C, Frag& N, int sn, int hf) {
        sfor<0, 24>([&](auto Q) {
          constexpr int q = Q.v;
          constexpr int j = q / 4, i = (j & 1) ? 3 - q % 4 : q % 4;
          if (q == 23) asm volatile("s_waitcnt lgkmcnt(0)" : "+v"(N.bh[5]));
          K1_M(i, j);
          if constexpr (q < 4) { K1_LA(q); } else if constexpr (q < 16) { if (q & 1) { K1_LH((q - 4) >> 1); } else { K1_LL((q - 4) >> 1); } }
          SB0();
        });
      };
      auto subkv = [&](Frag& C, Frag& N, int sn, int hf, bool ld) {
#define K1_QL(i, j, l) K1_M(i, j); if (ld) { l; } else { st_rd(qq); ++qq; } SB0()
        int qq = 0;
        K1_QL(0, 2, K1_LL(2)); K1_QL(1, 2, K1_LH(2)); K1_QL(0, 3, K1_LA(0)); K1_QL(1, 3, K1_LA(1)); K1_QL(2, 2, K1_LL(3)); K1_QL(3, 2, K1_LH(3));
        K1_QL(2, 3, K1_LA(2)); K1_QL(3, 3, K1_LA(3)); K1_QL(0, 4, K1_LL(4)); K1_QL(1, 4, K1_LH(4)); K1_QL(2, 4, K1_LL(5)); K1_QL(3, 4, K1_LH(5));
        K1_QL(0, 5, ); K1_M(1, 5); K1_M(2, 5); K1_M(3, 5);
      };
      WAIT_V(0);
      RAW_BAR();
      STAMPT(0, 1, 0);
      {
        Frag& N = FA; const int sn = 0, hf = 0;
        K1_LL(0); K1_LH(0); K1_LA(0); K1_LA(1); K1_LL(1); K1_LH(1); K1_LL(2); K1_LH(2); K1_LL(3); K1_LH(3); K1_LA(2); K1_LA(3); K1_LL(4); K1_LH(4); K1_LL(5); K1_LH(5);
      }
#pragma unroll 1
      for (int it = 0; it < 4; ++it) {
#pragma unroll
        for (int s = 0; s < 4; ++s) {
          if (s == 2 && it == 3) break;
          sub(FA, FB, s, 1);
          if (s == 0) WAIT_V(0);
          if (it == 0) { if (s == 2) STW(S4, kvp_off); if (s == 3) STW(S4, kvp_off + 1024); }
          if (it == 1) { if (s == 0) STW(S4, pfa); if (s == 1) STW(S4, pfb); }
          TSL(it * 4 + s);
          if (s != 1 || it < 3) RAW_BAR();
          sub(FB, FA, (s + 1) & 3, 0);
        }
      }
      subkv(FA, FB, 2, 1, true);
      TSL(14);
      RAW_BAR();
      subkv(FB, FA, 3, 0, true);
      subkv(FA, FB, 3, 1, true);
      TSL(15);
      subkv(FB, FA, 0, 0, false);
    }
    asm volatile("s_nop 15" ::: "memory");
    TSL(16); if (wave == 0) TSDUMP(0);
    STAMPT(0, 2, 0);
    {
      SPIN {
        if (__all(st_ok != 0)) break;
#pragma unroll
        for (int q = 0; q < 13; ++q) st_rd(q);
        SB0();
      }
      const u32x4 ONES4 = {0x3F803F80u, 0x3F803F80u, 0x3F803F80u, 0x3F803F80u};
      f32x2 tk[16], tv[16];
#define PR(v, i) ((f32x2){(v)[2 * (i)], (v)[2 * (i) + 1]})
#pragma unroll
      for (int i = 0; i < 16; ++i) {
        tk[i] = PR(acc[(i >> 1) & 3][2 + (i >> 3)], i & 1) - PR(m4[(i >> 1) & 3], i & 1) * cs[i >> 3];
        tv[i] = PR(acc[(i >> 1) & 3][4 + (i >> 3)], i & 1) - PR(m4[(i >> 1) & 3], i & 1) * cs[2 + (i >> 3)];
      }
      SB0();
#pragma unroll
      for (int i = 0; i < 16; ++i) { tk[i] *= PR(i4[(i >> 1) & 3], i & 1); tv[i] *= PR(i4[(i >> 1) & 3], i & 1); }
      SB0();
      unsigned kp[16], vp[16];
#pragma unroll
      for (int i = 0; i < 16; ++i) vp[i] = pack2o(tv[i][0], tv[i][1]);
      phi_n<16>(tk, kp);
#define F4_(p, i) ((u32x4){(p)[4 * (i)], (p)[4 * (i) + 1], (p)[4 * (i) + 2], (p)[4 * (i) + 3]})
      const f32x4 Z4 = {0.f, 0.f, 0.f, 0.f};
      const u32x4 vk[2] = {F4_(vp, 0), F4_(vp, 1)}, vg[2] = {F4_(vp, 2), F4_(vp, 3)};
      f32x4 keep[2], give[2], zq[2];
#pragma unroll
      for (int d = 0; d < 2; ++d) { give[d] = mfma16(F4_(kp, 2 * d), vg[0], Z4); zq[d] = mfma16(ONES4, F4_(kp, 2 * d), Z4); }
#pragma unroll
      for (int d = 0; d < 2; ++d) { give[d] = mfma16(F4_(kp, 2 * d + 1), vg[1], give[d]); zq[d] = mfma16(ONES4, F4_(kp, 2 * d + 1), zq[d]); }
#pragma unroll
      for (int d = 0; d < 2; ++d) keep[d] = mfma16(F4_(kp, 2 * d), vk[0], Z4);
#pragma unroll
      for (int d = 0; d < 2; ++d) keep[d] = mfma16(F4_(kp, 2 * d + 1), vk[1], keep[d]);
      SB0();
      const float zv = (lane & 16) ? zq[1][0] : zq[0][0];
      char* xk = smem + L_XK + wave * 2304 + lane * 16;
      const char* xp = smem + L_XK + (wave ^ 2) * 2304 + lane * 16;
      *(f32x4*)xk = give[0]; *(f32x4*)(xk + 1024) = give[1]; *(float*)(xk + 2048 - lane * 12) = zv;
      asm volatile("" ::: "memory");
      misc[16 + wave] = 1;
      f32x4 p0, p1; float pz;
      SPIN {
        const int ok = misc[16 + (wave ^ 2)];
        asm volatile("" ::: "memory");
        p0 = *(const f32x4*)xp; p1 = *(const f32x4*)(xp + 1024); pz = *(const float*)(xp + 2048 - lane * 12);
        SB0();
        if (ok == 1) break;
      }
      STW(sane(__builtin_bit_cast(u32x4, keep[0] + p0)), kvp_off);
      STW(sane(__builtin_bit_cast(u32x4, keep[1] + p1)), kvp_off + 1024);
      STW1(sane1(__float_as_uint(zv + pz)), (lane < 32 && wr == 0) ? zp_off + lane * 4 : OOB);
    }
    STAMPT(0, 3, 0);
    {
      u32x4 qa[4][4]; u32x2 ql[4][2], qh[4][2];
#pragma unroll
      for (int u = 0; u < 4; ++u) {
#pragma unroll
        for (int j = 0; j < 2; ++j) {
          ql[u][j] = trread(smem + bo[1][j] + (u >> 1) * K1_STAGE + (u & 1) * 12288);
          qh[u][j] = trread(smem + bo[1][j] + (u >> 1) * K1_STAGE + (u & 1) * 12288 + 512);
        }
#pragma unroll
        for (int i = 0; i < 4; ++i) qa[u][i] = *(const u32x4*)(smem + ao[1][u & 1] + (u >> 1) * K1_STAGE + i * 2048);
      }
      asm volatile("" ::: "memory");
      misc[8 + wave] = 3;
      SB0();
#pragma unroll
      for (int u = 0; u < 4; ++u)
#pragma unroll
        for (int j = 0; j < 2; ++j)
#pragma unroll
          for (int i = 0; i < 4; ++i) acc[i][j] = mfma16((u32x4){ql[u][j][0], ql[u][j][1], qh[u][j][0], qh[u][j][1]}, qa[u][i], acc[i][j]);
      SB0();
    }
    unsigned qp[16];
    Phi<16> qs;
    const unsigned po = WS_KVP + ((2 * hp + (rb >> 3)) * 16 + h) * 4096 + (2 * ((rb >> 2) & 1) + ((lane >> 4) & 1)) * 1024 + (16 * (rb & 3) + (lane & 15)) * 16;
    u32x4 pv[8] = {};
    {
      f32x4 c4[2]; float m_b[4], rinv[4];
#pragma unroll
      for (int i = 0; i < 2; ++i) c4[i] = *(const f32x4*)(stm + 256 + wc * 96 + 16 * i + 4 * g4);
#pragma unroll
      for (int i = 0; i < 4; ++i) { m_b[i] = stm[64 * wr + 16 * i + c16]; rinv[i] = stm[128 + 64 * wr + 16 * i + c16]; }
      SB0();
      f32x2 tq[16];
#pragma unroll
      for (int i = 0; i < 16; ++i) tq[i] = PR(acc[i >> 2][(i >> 1) & 1], i & 1) - PR(c4[(i >> 1) & 1], i & 1) * m_b[i >> 2];
      SB0();
#pragma unroll
      for (int i = 0; i < 16; ++i) tq[i] *= rinv[i >> 2];
      SB0();
      phi_a<16>(tq, qs);
#pragma unroll
      for (int i = 0; i < 16; ++i) asm volatile("" : "+v"(qs.tb[i]), "+v"(qs.tr[i]), "+v"(qs.e[i]));
    }
    if (wave == 0) {
      SLEEP(P_KVP);
#pragma unroll
      for (int p = 0; p < 8; ++p) pv[p] = LDW(po + p * 8192);
    }
    asm volatile("" ::: "memory");
    STAMPT(0, 4, 0);
    if (wave == 1 && rb < 2) {
      SLEEP(P_KVP);
      SPIN {
        unsigned m = 0;
#pragma unroll
        for (int p = 0; p < 2; ++p) { pv[p] = LDW(WS_ZP + (2 * hp + rb) * 2048 + p * 1024 + lane * 16); m = chk(max(max(m, pv[p][0]), pv[p][2]), pv[p]); }
        if (wave_ok(m)) break;
        asm volatile("" ::: "memory");
      }
      f32x4 s = __builtin_bit_cast(f32x4, pv[0]) + __builtin_bit_cast(f32x4, pv[1]);
#pragma unroll
      for (int i = 0; i < 4; ++i) { s[i] += dppf<DPP_ROR8>(s[i]); s[i] = xh_sum(x16_sum(s[i])); }
      if (lane < 16) st64(ws, WS_ZF + (2 * hp + rb) * 128 + (lane >> 3) * 64 + (lane & 3) * 16 + ((lane >> 2) & 1) * 8,
                          ((u64)sane1(pack2(s[2], s[3])) << 32) | pack2(s[0], s[1]));
    }
    if (wave == 0) {
      SPIN {
        unsigned m = 0;
#pragma unroll
        for (int p = 0; p < 8; ++p) m = chk(m, pv[p]);
        CNT(0);
        if (wave_ok(m)) break;
        asm volatile("" ::: "memory");
#pragma unroll
        for (int p = 0; p < 8; ++p) pv[p] = LDW(po + p * 8192);
      }
      f32x4 s = __builtin_bit_cast(f32x4, pv[0]);
#pragma unroll
      for (int p = 1; p < 8; ++p) s += __builtin_bit_cast(f32x4, pv[p]);
      const unsigned p0 = pack2(xh_sum(s[0]), xh_sum(s[1])), p1 = pack2(xh_sum(s[2]), xh_sum(s[3]));
      auto a = __builtin_amdgcn_permlane16_swap(p0, p0, false, false);
      auto b = __builtin_amdgcn_permlane16_swap(p1, p1, false, false);
      STW(sane((u32x4){a[0], b[0], a[1], b[1]}), lane < 16 ? WS_KVF + hp * 4096 + rb * 256 + lane * 16 : OOB);
      STAMPT(1, 2, 0);
    }
    phi_b<16>(qs, qp);
#pragma unroll
    for (int i = 0; i < 16; ++i) asm volatile("" : "+v"(qp[i]));
    if (wave == 0) misc[7] = 1;
    else SPIN { if (misc[7] == 1) break; __builtin_amdgcn_s_sleep(1); }
    SLEEP(P_KVF);
    {
      u32x4 k0, k1, z0;
      SPIN {
        k0 = LDW(WS_KVF + head * 2048 + lane * 16); k1 = LDW(WS_KVF + head * 2048 + 1024 + lane * 16);
        z0 = LDW(WS_ZF + head * 128 + g4 * 16);
        CNT(1);
        if (wave_ok(chk(chk(chk(0u, k0), k1), z0))) break;
        asm volatile("" ::: "memory");
      }
      STAMPT(0, 5, 0);
      const f32x4 Z4 = {0.f, 0.f, 0.f, 0.f};
      f32x4 X[8], Dn[4];
#pragma unroll
      for (int i = 0; i < 4; ++i) Dn[i] = mfma16(z0, F4_(qp, i), Z4);
#pragma unroll
      for (int i = 0; i < 8; ++i) X[i] = mfma16((i & 1) ? k1 : k0, F4_(qp, i >> 1), Z4);
      SB0();
      float ri[4]; unsigned a[16]; f32x2 f[16];
#pragma unroll
      for (int i = 0; i < 4; ++i) ri[i] = __builtin_amdgcn_rcpf(rbf(rbf(Dn[i][0]) + 1e-6f));
      SB0();
#pragma unroll
      for (int i = 0; i < 16; ++i) a[i] = pack2o(X[i >> 1][2 * (i & 1)], X[i >> 1][2 * (i & 1) + 1]);
      SB0();
#pragma unroll
      for (int i = 0; i < 16; ++i) f[i] = unpk(a[i]);
      SB0();
#pragma unroll
      for (int i = 0; i < 16; ++i) f[i] *= ri[i >> 2];
      SB0();
#pragma unroll
      for (int i = 0; i < 4; ++i) {
        STW(((u32x4){pack2(f[4 * i][0], f[4 * i][1]), pack2(f[4 * i + 1][0], f[4 * i + 1][1]), pack2(f[4 * i + 2][0], f[4 * i + 2][1]), pack2(f[4 * i + 3][0], f[4 * i + 3][1])}),
            o_off + (i >> 1) * 65536 + (i & 1) * 1024);
        SB0();
      }
    }
    STAMPT(0, 6, 0);
    {
      EPI();
      const int rt = wave & 1, kh = wave >> 1;
      const unsigned ob = WS_O + ((rp * 2 + rt) * 32 + 16 * kh) * 2048 + lane * 16;
#define LDO(t, st) __builtin_amdgcn_raw_buffer_load_b128(wsr, ob + (st) * 1024, whead(t) * 2048, 16)
      int woff[2];
#pragma unroll
      for (int i = 0; i < 2; ++i) woff[i] = kh * 8192 + (g4 >> 1) * 2048 + (g4 & 1) * 512 + trl + ((i ^ (g4 & 1)) * 32);
      struct Frag2 { u32x2 lo[8], hi[8]; };
      Frag2 G[2];
      auto ld2 = [&](Frag2& N, int sn, int nt, int hi) {
        const char* p = smem + sn + woff[nt & 1] + (nt >> 2) * 1024 + ((nt >> 1) & 1) * 256;
        if (hi) N.hi[nt] = trread(p + 4096); else N.lo[nt] = trread(p);
      };
      WAIT_V(0);
      STW(((u32x4){1u, 1u, 1u, 1u}), fb_off);
      RAW_BAR();
#pragma unroll
      for (int i = 0; i < 16; ++i) ld2(G[0], wo_slot(0), i >> 1, i & 1);
      f32x4 a2[2][8];
#pragma unroll
      for (int i = 0; i < 16; ++i) a2[i & 1][i >> 1] = (f32x4){0.f, 0.f, 0.f, 0.f};
      SLEEP(P_FB);
      SPIN { CNT(2); if (wave_ok(ld32(ws, WS_FB + (rp * 32 + 16 * kh + (lane & 15)) * 128))) break; }
      asm volatile("" ::: "memory");
      STAMPT(0, 7, 0);
      char* pr[2];
#pragma unroll
      for (int i = 0; i < 2; ++i) pr[i] = smem + L_P + kh * L_PK + (32 * rt + c16) * 512 + (((4 * i + g4) ^ (c16 & 7)) * 16);
      u32x4 of[16][2];
#pragma unroll
      for (int t = 0; t < OW; ++t) { of[t][0] = LDO(t, 0); of[t][1] = LDO(t, 1); }
      SB0();
      sfor<0, 16>([&](auto T) {
        constexpr int t = T.v;
        Frag2& C = G[t & 1]; Frag2& N = G[(t + 1) & 1];
#pragma unroll
        for (int i = 0; i < 16; ++i) {
          const int nt = i >> 1, st = i & 1;
          a2[st][nt] = mfma16((u32x4){C.lo[nt][0], C.lo[nt][1], C.hi[nt][0], C.hi[nt][1]}, of[t][st], a2[st][nt]);
          SB0();
          if ((t & 1) && t <= 11 && i == 8) {
            asm volatile("s_waitcnt lgkmcnt(8)" ::: "memory"); __builtin_amdgcn_s_barrier(); asm volatile("" ::: "memory");
          }
          if (t < 15) ld2(N, wo_slot(t + 1), nt, st);
          if (t > 0 && t - 1 + OW < 16 && (i == 3 || i == 11)) of[(t - 1 + OW) & 15][i >> 3] = LDO(t - 1 + OW, i >> 3);
          if (t - 1 + OW == 16 && (i == 3 || i == 11)) { xv[i >> 3] = *(const u32x4*)(x + ego + (i >> 3) * 64); wv[i >> 3] = *(const u32x4*)(w_r + cb * 128 + eseg * 8 + (i >> 3) * 64); }
          if (t == 15 && i >= 4) *(f32x4*)(pr[(nt - 2) & 1] + ((nt - 2) >> 1) * 128 + st * 8192) = a2[st][(nt - 2) & 7];
          SB0();
        }
      });
      STAMPT(0, 8, 0);
#pragma unroll
      for (int i = 12; i < 16; ++i) *(f32x4*)(pr[(i >> 1) & 1] + (i >> 2) * 128 + (i & 1) * 8192) = a2[i & 1][i >> 1];
    }
    unpack_xw();
    __builtin_amdgcn_s_setprio(0);
  } else {
    LANES();
    const int sw = wave - 4, t2 = tid - 256;
    __builtin_amdgcn_s_setprio(3);
    const bool real = hs_ != 7;
    unsigned hs_g0 = 0, hs_cur = 0;
    u32x4 ms_exp = {0, 0, 0, 0};
    bool hs_act = true, ms_act = !real && hp < 8 && rb == 0 && sw == 1;
    const unsigned sy_off = (bid & 63) * 128 + (bid >> 6) * 4;
    const unsigned hs_off = (lane == 1 ? SY_GO : SY_CH) + sy_off;
    const unsigned re_off = t2 == 0 ? SY_RE + sy_off : OOB;
    const unsigned ms_off = lane * 128;
    const char* wsu = uni(ws);
#define HS_PROBE() asm volatile("s_nop 4\n\tglobal_load_dword v156, %0, %1 sc1" :: "v"(hs_off), "s"(wsu) : "memory", RSV)
#define MS_PROBE() asm volatile("s_nop 4\n\tglobal_load_dwordx4 v[152:155], %0, %1 sc1" :: "v"(SY_RE + ms_off), "s"(wsu) : "memory", RSV)
    auto hs_step = [&](bool first) {
      if (hs_act) {
        const unsigned hv_ = rget<156>();
        hs_act = __builtin_amdgcn_readlane(hv_, 1) == hs_g0;
        const unsigned c = __builtin_amdgcn_readlane(hv_, 0);
        if (sw == 0 && (first || c != hs_cur)) STW1(hv_, re_off);
        hs_cur = c;
        HS_PROBE();
      }
      if (ms_act) {
        const unsigned d = ((rget<152>() ^ ms_exp[0]) | (rget<153>() ^ ms_exp[1])) | ((rget<154>() ^ ms_exp[2]) | (rget<155>() ^ ms_exp[3]));
        ms_act = __any(d != 0u);
        if (ms_act) MS_PROBE(); else STW(ms_exp, SY_GO + ms_off);
      }
    };
    HS_PROBE();
    if (ms_act) {
      MS_PROBE();
      asm volatile("s_nop 4\n\tglobal_load_dwordx4 v[148:151], %0, %1 sc1" :: "v"(SY_GO + ms_off), "s"(wsu) : "memory", RSV);
    }
    auto slowpf = [&]() {
      const int hq = (hp & ~3) + ((hp + 7 - hs_) & 3);
      if ((sw == 1 || sw == 2) && ((hq + 4 + (int)(((size_t)wqkv >> 7) & 7)) & 7) == 7)
        asm volatile("s_nop 4\n\tglobal_load_dword v157, %0, %1" :: "v"(min(lane, 47) * 2048), "s"(uni((const char*)wqkv + hq * 128 + (size_t)((((rb & 7) * 4 + (hp & 3) + SPX * (rb >> 3)) & 31) * 96 + (sw - 1) * 48) * 2048)) : "memory", RSV);
    };
    if (SLOWPF == 1) slowpf();
    const int gr = (g8 >> 3) * 7 + min(hs_, 6);
    const unsigned voG = (unsigned)((lane >> 3) * 8 * F_ + (lane & 7) * 8) * 2u, voD = (unsigned)((lane >> 3) * 8 * D_ + (lane & 7) * 8) * 2u;
    const size_t gofs = ((size_t)(kq * 256 + sw * 64) * F_ + gr * 64) * 2;
    const char* pG = (const char*)wg + gofs;
    const char* pU = (const char*)wu + gofs;
    const char* pD = (const char*)(wd + (size_t)(gr * 64) * D_ + kq * 256 + sw * 64);
    auto wstep = [&](auto I) {
      constexpr int i = I.v;
      if (i < 8) wload<VW + 4 * i>(voG, pG + (i & 7) * (F_ * 2));
      else if (i < 16) wload<VW + 4 * i>(voG, pU + (i & 7) * (F_ * 2));
      else wload2<VW + 4 * i>(voD, pD + (i & 7) * (D_ * 2));
    };
    unsigned voW;
    {
      const int bi = lane >> 4, q = (lane >> 2) & 3, ch = lane & 3;
      voW = (unsigned)((4 * (bi >> 1) + q) * D_ + (bi & 1) * 32 + (ch ^ (bi & 2)) * 8) * 2u;
    }
    const char* baseW = (const char*)(wo + (size_t)((16 * (sw >> 1)) * 32 + 16 * (sw & 1)) * D_ + cb * 128) - 1024;
    auto stageW = [&](auto J, bool rsv) {
      const char* s_ = baseW + (size_t)whead(J.v) * (32 * D_ * 2);
      if (rsv) glds4r<wo_slot(J.v)>(voW + 1024, voW + 128, voW + 8 * D_ * 2 - 1024, voW + 8 * D_ * 2 + 128 - 2048, s_, l4);
      else glds4<wo_slot(J.v)>(voW + 1024, voW + 128, voW + 8 * D_ * 2 - 1024, voW + 8 * D_ * 2 + 128 - 2048, s_, l4);
    };
    auto code_adr = [&]() { return (const char*)(pc0 - (pc0 & 127)) + (idx + (lane < CODE_LINES ? lane : 0) * 32) * 128; };
    int sta[2], stb[3];
    {
#pragma unroll
      for (int c = 0; c < 2; ++c) { sta[c] = (posA & 3) * K1_STAGE + (32 * sw + (lane & 15)) * 128 + (((4 * c + (lane >> 4)) ^ ((lane & 15) >> 1)) * 16); asm volatile("" : "+v"(sta[c]) :: RSV); }
#pragma unroll
      for (int i = 0; i < 3; ++i) {
        const int G = sw * 3 + i, hd = G >= 6 ? 1 : 0, mat = (G - 6 * hd) >> 1;
        stb[i] = (posB & 3) * K1_STAGE + 16384 + (lane >> 4) * 6144 + mat * 1024 + hd * 256 + ((lane & 15) >> 2) * 64 + (G & 1) * 32 + (lane & 3) * 8;
        asm volatile("" : "+v"(stb[i]) :: RSV);
      }
    }
    unsigned rs_dst = (lane >> 4) == ((lane & 15) >> 2) ? rs_slot + (32 * sw + (lane & 15)) * 8 : OOB, cs_dst = lane < 48 ? cs_slot + (sw * 48 + lane) * 4 : OOB;
    unsigned rs_off = WS_RS + rb * 16384 + (lane >> 4) * 1024 + sw * 256 + (lane & 15) * 16;
    unsigned cs_off = WS_CS + hp * 12288 + (lane >> 4) * 768 + sw * 192 + min(lane & 15, 11) * 16;
    asm volatile("" : "+v"(rs_dst), "+v"(cs_dst), "+v"(rs_off), "+v"(cs_off) :: RSV);
    int stm_dst = ((lane >> 5) * 128 + sw * 32 + (lane & 15) * 2 + ((lane >> 4) & 1)) * 4, cs_put = (lane & 15) < 12 ? (256 + sw * 48 + (lane & 15) * 4 + (lane >> 4)) * 4 : 1792 + lane * 4;
    asm volatile("" : "+v"(stm_dst), "+v"(cs_put) :: RSV);
    auto st_load = [&]() { sfor<0, 4>([&](auto I) { sload<224 + 4 * I.v>(rs_off, wsu + I.v * 4096); sload<240 + 4 * I.v>(cs_off, wsu + I.v * 3072); }); };
    sfor<3, GS[0]>(group);
    if (ms_act) {
      WAIT_VR(gops(GS[0]) - 10);
      const u32x4 re = {rget<152>(), rget<153>(), rget<154>(), rget<155>()}, g0 = {rget<148>(), rget<149>(), rget<150>(), rget<151>()};
#pragma unroll
      for (int j = 0; j < 4; ++j) { const unsigned c = re[j] + 1u; ms_exp[j] = (c == g0[j]) ? c + 1u : c; }
      STW(ms_exp, SY_CH + ms_off);
    }
    WAIT_VR(gops(GS[0]) - 10 + 1); RAW_BARR();
    TSL(14);
    if (SLOWPF == 2) slowpf();
    sfor<GS[0], GS[1]>(group);
    WAIT_VR(gops(GS[1]) - 20); RAW_BARR();
    hs_g0 = __builtin_amdgcn_readlane(rget<156>(), 1);
    if (SLOWPF == 3) slowpf();
    u32x4 ra[4] = {}; u32x2 rc[3][4] = {};
    auto chunk = [&](auto C) {
      const u32x4 ONES4 = {0x3F803F80u, 0x3F803F80u, 0x3F803F80u, 0x3F803F80u};
      const f32x4 Z4 = {0.f, 0.f, 0.f, 0.f};
      if constexpr (C.v == 0) {
        asm volatile("" : "+v"(ra[0]), "+v"(ra[1]), "+v"(ra[2]), "+v"(ra[3]) :: RSV);
        f32x4 s[2], q[2];
#pragma unroll
        for (int i = 0; i < 2; ++i) { q[i] = mfma16(ra[2 * i], ra[2 * i], Z4); s[i] = mfma16(ONES4, ra[2 * i], Z4); }
#pragma unroll
        for (int i = 0; i < 2; ++i) { q[i] = mfma16(ra[2 * i + 1], ra[2 * i + 1], q[i]); s[i] = mfma16(ONES4, ra[2 * i + 1], s[i]); }
#pragma unroll
        for (int i = 0; i < 2; ++i) {
          float a = q[i][0], c = q[i][1], d = q[i][2], e = q[i][3];
          asm volatile("" : "+v"(a), "+v"(c), "+v"(d), "+v"(e) :: RSV);
          const float lo = (lane & 1) ? c : a, hi = (lane & 1) ? e : d;
          __builtin_amdgcn_raw_buffer_store_b64((u32x2){sane1(__float_as_uint(s[i][0])), sane1(__float_as_uint((lane & 2) ? hi : lo))}, wsr, rs_dst + i * 128, 0, 16);
        }
      } else {
        f32x4 c[3];
#pragma unroll
        for (int i = 0; i < 3; ++i) { asm volatile("" : "+v"(rc[i][0]), "+v"(rc[i][1]), "+v"(rc[i][2]), "+v"(rc[i][3]) :: RSV); c[i] = mfma16(ONES4, (u32x4){rc[i][0][0], rc[i][0][1], rc[i][1][0], rc[i][1][1]}, Z4); }
#pragma unroll
        for (int i = 0; i < 3; ++i) c[i] = mfma16(ONES4, (u32x4){rc[i][2][0], rc[i][2][1], rc[i][3][0], rc[i][3][1]}, c[i]);
        float c0 = c[0][0], c1 = c[1][0], c2 = c[2][0];
        asm volatile("" : "+v"(c0), "+v"(c1), "+v"(c2) :: RSV);
        STW1(sane1(__float_as_uint(lane < 16 ? c0 : lane < 32 ? c1 : c2)), cs_dst);
      }
    };
    sfor<0, 12>([&](auto T) {
      constexpr int t = T.v;
      TSL(t);
      if (t == 4) STAMPT(1, 9, 256);
      if (t < 4 && posA == t + 1) {
#pragma unroll
        for (int c = 0; c < 4; ++c) ra[c] = *(const u32x4*)(smem + sta[c & 1] + (c >> 1) * 2048);
      }
      if (t < 4 && posB == t + 1) {
#pragma unroll
        for (int i = 0; i < 3; ++i)
#pragma unroll
          for (int j = 0; j < 4; ++j) rc[i][j] = trread(smem + (stb[i] ^ ((j >> 1) * 32)) + (j >> 1) * 3072 + (j & 1) * 512);
      }
      if (t == HS0 - 1) HS_PROBE();
      if (t >= HS0) hs_step(t == HS0);
      sfor<GS[t + 1], GS[t + 2]>([&](auto G) {
        group(G);
        if constexpr ((t == 4 || t == 5) && G.v == GS[t + 1]) chunk(IC<t - 4>{});
      });
      if (t == CP2 && sw == 3) asm volatile("global_load_dword v157, %0, off" :: "v"(code_adr()) : "memory", RSV);
      if (real) sfor<WS_[t], WS_[t + 1]>(wstep);
      TSL(16 + t);
      constexpr int need = t < 11 ? t + 3 : 15, nw = wyoung(t, need);
      if (nw && real) WAIT_VR(gops(GS[t + 2]) - 10 * need + nw); else WAIT_VR(gops(GS[t + 2]) - 10 * need);
      TSL(32 + t);
      RAW_BARR();
    });
    TSL(12);
    STAMPT(1, 10, 256);
    hs_step(false);
    auto st_put = [&]() -> bool {
      float a[8]; unsigned m = 0, n = 0;
      rsum4<224>(a, m, n); rsum4<240>(a + 4, m, n);
      const float s1 = sw16(a[0], a[2]), q1 = sw16(a[1], a[3]);
      const float cs = sw32(sw16(a[4], a[5]), sw16(a[6], a[7]));
      const float mean = sw32(s1, s1) * (1.0f / 1024.0f);
      const float var = fmaxf(__builtin_fmaf(sw32(q1, q1), 1.0f / 1024.0f, -(mean * mean)), 0.f);
      const float rinv = __builtin_amdgcn_rcpf(rbf(__builtin_amdgcn_sqrtf(rbf(rbf(var) + 1e-5f))));
      *(float*)(smem + L_STM + stm_dst) = (lane & 32) ? rinv : rbf(mean);
      *(float*)(smem + L_STM + cs_put) = cs;
      return wave_ok(max(m, n));
    };
    st_load();
    if (real) { sfor<WS_[12], WS_[12] + STE>(wstep); WAIT_VR(STE); } else WAIT_VR(0);
    const bool st_fin = st_put() && !hs_act;
    if (lane == 0) { misc[8 + sw] = 0; misc[12 + sw] = st_fin; misc[16 + sw] = 0; misc[7] = 0; }
    if (real) sfor<WS_[12] + STE, W1A>(wstep);
    TSL(44);
    RAW_BARR();
    TSL(13); TSDUMP(1 + sw);
    STAMPT(1, 0, 256);
    if (!st_fin) {
      if (hs_act) hs_act = __builtin_amdgcn_readlane(rget<156>(), 1) == hs_g0;
      if (ms_act) {
        SPIN {
          bool ok = true;
#pragma unroll
          for (int j = 0; j < 4; ++j) ok &= (ld32(ws, SY_RE + ms_off + j * 4) == ms_exp[j]);
          if (__all(ok)) break;
        }
        STW(ms_exp, SY_GO + ms_off);
      }
      if (hs_act) {
        SPIN {
          if (ld32(ws, SY_GO + sy_off) != hs_g0) break;
          const unsigned c = ld32(ws, SY_CH + sy_off);
          if (c != hs_cur) { hs_cur = c; STW1(c, re_off); }
        }
      }
      SPIN { st_load(); WAIT_VR(0); if (st_put()) break; }
    }
    asm volatile("" ::: "memory", RSV);
    if (lane == 0) misc[12 + sw] = 1;
    __builtin_amdgcn_s_setprio(0);
    if (real) sfor<W1A, 16>(wstep);
    auto mwait = [&](int v) { SPIN { const int a = misc[8 + (lane & 3)]; if (__all(a >= v)) break; __builtin_amdgcn_s_sleep(1); } asm volatile("" ::: "memory"); };
    sfor<0, 9>([&](auto J) {
      if (J.v == 4) mwait(3);
      stageW(J, J.v < 2);
      if (J.v >= 1) WAIT_V(4 * PACE);
      if (J.v == 1) { WAIT_VR(8); sfor<0, 16>(wtr); }
    });
    STAMPT(1, 1, 256);
    EPI();
#pragma unroll
    for (int i = 0; i < 2; ++i) { xv[i] = *(const u32x4*)(x + ego + i * 64); wv[i] = *(const u32x4*)(w_r + cb * 128 + eseg * 8 + i * 64); }
    if (CP3 && sw == 3) { unsigned d_; asm volatile("global_load_dword %0, %1, off\n\ts_waitcnt vmcnt(0)" : "=v"(d_) : "v"(code_adr()) : "memory"); }
    asm volatile("s_waitcnt vmcnt(0)" : "+v"(xv[0]), "+v"(xv[1]), "+v"(wv[0]), "+v"(wv[1]) :: "memory");
    unpack_xw();
    RAW_BAR();
    RAW_BAR(); stageW(IC<9>{}, false); stageW(IC<10>{}, false);
    RAW_BAR(); stageW(IC<11>{}, false); stageW(IC<12>{}, false);
    RAW_BAR(); stageW(IC<13>{}, false); stageW(IC<14>{}, false);
    WAIT_V(16); RAW_BAR(); stageW(IC<15>{}, false);
    WAIT_V(12); RAW_BAR();
    WAIT_V(0); RAW_BAR();
    if (real) sfor<16, 24>(wstep);
  }
  LANES(); EPI();
  const int grow = rp * 64 + erow;
  int pa0 = L_P + erow * 512 + ((2 * eseg) ^ (erow & 7)) * 16, pa1 = pa0 ^ 16;
  unsigned st_dst = eseg == 0 ? WS_ST2 + ((rp * 8 + cb) * 64 + erow) * 16 : OOB, st_src = WS_ST2 + rp * 8192 + lane * 16;
  unsigned fd_dst = lane < 8 ? WS_FD + (cb * 32 + rp) * 128 + lane * 16 : OOB, kidx = 0xFFFFu - grow;
  asm volatile("" : "+v"(pa0), "+v"(pa1), "+v"(st_dst), "+v"(st_src), "+v"(fd_dst), "+v"(kidx));
  RAW_BAR2();
  STAMPT(0, 9, 0);
  unsigned hb[8];
  f32x2 hv[8];
  {
    f32x4 pp[2][4];
#pragma unroll
    for (int k = 0; k < 2; ++k)
#pragma unroll
      for (int i = 0; i < 4; ++i) pp[k][i] = *(const f32x4*)(smem + ((i & 1) ? pa1 : pa0) + (i >> 1) * 256 + k * L_PK);
    SB0();
    f32x2 s = {0.f, 0.f}, q = {0.f, 0.f}, p2 = {0.f, 0.f};
#pragma unroll
    for (int i = 0; i < 8; ++i) {
      const f32x4 a = pp[0][i >> 1] + pp[1][i >> 1];
      const f32x2 u = xf[i] + unpk(pack2o(a[2 * (i & 1)], a[2 * (i & 1) + 1]));
      hb[i] = pack2o(u[0], u[1]);
      hv[i] = unpk(hb[i]);
      s += hv[i]; q += hv[i] * hv[i]; p2 += hv[i] * wf2[i];
    }
    STW(sane((u32x4){__float_as_uint(oct_sum(s[0] + s[1])), __float_as_uint(oct_sum(q[0] + q[1])), __float_as_uint(oct_sum(p2[0] + p2[1])), __float_as_uint(sw_)}), st_dst);
  }
  KEEP2();
  STAMPT(0, 10, 0);
  const int sw = wave - 4;
  const unsigned fd_src = WS_FD + xcd * 4096 + r * 128;
  u32x2 kpk = {SENT, SENT};
  if (wave == 0) {
    u32x4 pa[8];
    SLEEP(P_C);
    SPIN {
      unsigned m = 0;
#pragma unroll
      for (int j = 0; j < 8; ++j) { pa[j] = LDW(st_src + j * 1024); m = chk(m, pa[j]); }
      CNT(3);
      if (wave_ok(m)) break;
      KEEP2();
    }
    STAMPT(0, 11, 0);
#define F4(v) __builtin_bit_cast(f32x4, v)
    const f32x4 t = ((F4(pa[0]) + F4(pa[1])) + (F4(pa[2]) + F4(pa[3]))) + ((F4(pa[4]) + F4(pa[5])) + (F4(pa[6]) + F4(pa[7])));
    const float mean = t[0] * (1.0f / 1024.0f);
    const float var = fmaxf(__builtin_fmaf(t[1], 1.0f / 1024.0f, -(mean * mean)), 0.f);
    const float mb = rbf(mean), rs = __builtin_amdgcn_rcpf(rbf(__builtin_amdgcn_sqrtf(rbf(rbf(var) + 1e-5f))));
    *(f32x2*)(smem + L_KX + lane * 8) = (f32x2){mb, rs};
    const float sc = fmaxf((t[2] - mb * t[3]) * rs, -INFINITY);
    const int u = (int)(pack2(0.f, sc) & 0xffff0000u);
    unsigned b0 = ((unsigned)(u ^ ((u >> 31) | (int)0x80000000)) & 0xffff0000u) | (0xFFFFu - (rp * 64 + lane)), b1 = 0;
    top2_lo(b0, b1); top2_hi(b0, b1);
    STW(((u32x4){b0, b1, b0, b1}), fd_dst);
    if (lane == 0) *(u32x2*)(smem + L_MISC + 112) = (u32x2){b0, b1};
  } else if (wave >= 4) {
    asm volatile("s_waitcnt vmcnt(0)" ::: "memory", RSV2);
    sfor<16, 24>(wtr);
  }
  RAW_BAR();
  bool cand;
  u32x4 ob[2];
  {
    const f32x2 mr = *(const f32x2*)(smem + L_KX + erow * 8);
    const u32x2 kk = *(const u32x2*)(smem + L_MISC + 112);
    const float mb = mr[0], rs = mr[1];
    const unsigned b0 = kk[0], b1 = kk[1];
    u32x4 nq[2];
#pragma unroll
    for (int i = 0; i < 8; ++i) {
      const f32x2 d = hv[i] - mb;
      const f32x2 n = unpk(pack2o(d[0], d[1])) * rs;
      nq[i >> 2][i & 3] = pack2o(n[0], n[1]);
    }
    const bool c2 = (b1 & 0xffffu) == kidx;
    cand = c2 || (b0 & 0xffffu) == kidx;
    if (cand) {
      const unsigned xo = WS_XS + (rp * 2 + (c2 ? 1 : 0)) * 2048 + cb * 256 + eseg * 16;
      STW(sane(nq[0]), xo); STW(sane(nq[1]), xo + 128);
      char* cp = smem + L_CAND + (c2 ? 512 : 0) + eseg * 16;
      *(u32x4*)cp = (u32x4){hb[0], hb[1], hb[2], hb[3]}; *(u32x4*)(cp + 128) = (u32x4){hb[4], hb[5], hb[6], hb[7]};
      *(u32x4*)(cp + 256) = nq[0]; *(u32x4*)(cp + 384) = nq[1];
    }
    asm volatile("" ::: "memory");
    if (wave >= 4) { SLEEP(P_D); kpk = __builtin_amdgcn_raw_buffer_load_b64(wsr, fd_src, 0, 16); }
    asm volatile("" ::: "memory");
#pragma unroll
    for (int i = 0; i < 8; ++i) { const f32x2 o = hv[i] + unpk(nq[i >> 2][i & 3]); ob[i >> 2][i & 3] = pack2(o[0], o[1]); }
  }
#define OUT_ST() do { *(u32x4*)(out + ego) = ob[0]; *(u32x4*)(out + ego + 64) = ob[1]; } while (0)
  STAMPT(0, 12, 0);
  if (wave < 4) {
    if (!cand) OUT_ST();
    RAW_BAR();
    const unsigned w0 = misc[24], w1 = misc[25];
    if (cand && grow != (int)key_idx(w0) && grow != (int)key_idx(w1)) OUT_ST();
#pragma unroll 1
    for (int tk = 0; tk < 2; ++tk) {
      const unsigned wk = tk ? w1 : w0, slot = misc[26 + tk];
      if ((int)(slot >> 1) != rp) continue;
      u32x4 pv[7];
      const unsigned yo = WS_YP + ((tk * 8 + cb) * 64 + (lane >> 3) * 8) * 512 + wave * 128 + (lane & 7) * 16;
      const int col = (wave >> 1) * 64 + (lane & 7) * 8 + (wave & 1) * 4 + (lane >> 4);
      float sg = rbf(__builtin_amdgcn_rcpf(1.0f + __expf(-key_val(wk))));
      float hh = bflo(*(const u16*)(smem + L_CAND + (slot & 1) * 512 + col * 2)), nn = bflo(*(const u16*)(smem + L_CAND + (slot & 1) * 512 + 256 + col * 2));
      u16* op = out + (size_t)key_idx(wk) * D_ + cb * 128 + col;
      asm volatile("" : "+v"(sg), "+v"(hh), "+v"(nn), "+v"(op));
      if (tk == 0 || (int)(misc[26] >> 1) != rp) {
        SPIN { if (misc[30] == (int)w0) break; __builtin_amdgcn_s_sleep(1); }
        SLEEP(P_E);
      }
      SPIN {
        unsigned m = 0;
#pragma unroll
        for (int p = 0; p < 7; ++p) { pv[p] = LDW(yo + p * 512); m = chk(m, pv[p]); }
        CNT(6);
        if (wave_ok(m)) break;
        asm volatile("" ::: "memory");
      }
      STAMPT(0, 13, 0);
      f32x4 s = __builtin_bit_cast(f32x4, pv[0]);
#pragma unroll
      for (int p = 1; p < 7; ++p) s += __builtin_bit_cast(f32x4, pv[p]);
      float y = sw32(sw16(s[0], s[1]), sw16(s[2], s[3]));
      y += dppf<DPP_ROR8>(y);
      if (!(lane & 8)) *(GAS u16*)op = (u16)pack2(hh + rbf(nn + rbf(rbf(y) * sg)), 0.f);
      STAMPT(0, 14, 0);
    }
  } else {
    unsigned xl = WS_XS + (kq * 256 + sw * 64 + (lane >> 4) * 16 + ((lane >> 2) & 1) * 8) * 2;
    int red_dst = (lane < 8 || (lane >= 24 && lane < 32)) ? L_RED + sw * 2048 + ((lane >> 3) & 1) * 1024 + (lane & 7) * 16 : L_RED + 8192 + lane * 16;
    unsigned gu_src = WS_GU + g8 * 4096 + lane * 16;
    int act_dst = L_ACT + sw * 256 + ((lane & 7) * 8 + (lane >> 3)) * 2, act_src = L_ACT + sw * 256 + (lane & 1) * 128 + (lane >> 4) * 32 + ((lane >> 1) & 1) * 16;
    unsigned yp_dst = lane < 16 ? WS_YP + (((lane >> 3) * 8 + 2 * kq + (sw >> 1)) * 64 + g8) * 512 + (sw & 1) * 256 + (lane & 7) * 16 : OOB;
    asm volatile("" : "+v"(xl), "+v"(red_dst), "+v"(gu_src), "+v"(act_dst), "+v"(act_src), "+v"(yp_dst));
    unsigned w0, w1; int s0 = -1, s1 = -1;
    u32x4 xa = {SENT, SENT, SENT, SENT};
    SPIN {
      CNT(4);
      const bool all = wave_ok(kpk[1]);
      const unsigned k0 = kpk[1] == SENT ? 0u : kpk[0], k1 = kpk[1] == SENT ? 0u : kpk[1];
      w0 = k0; w1 = k1;
      top2_lo(w0, w1); top2_mid(w0, w1);
      const int n0 = 2 * __builtin_ctzll(__ballot(k0 == w0));
      const u64 m1 = __ballot(k0 == w1);
      const int n1 = m1 ? 2 * __builtin_ctzll(m1) : n0 + 1;
      const bool same = n0 == s0 && n1 == s1;
      s0 = n0; s1 = n1;
      asm volatile("" ::: "memory");
      if (all && same) break;
      if (!all) { kpk = __builtin_amdgcn_raw_buffer_load_b64(wsr, fd_src, 0, 16); }
      xa = LDW(xl + ((lane & 1) ? s1 : s0) * 2048);
      if (all) break;
    }
    SPIN {
      if (wave_ok(chk(0u, xa))) break;
      asm volatile("" ::: "memory");
      xa = LDW(xl + ((lane & 1) ? s1 : s0) * 2048);
    }
    STAMPT(1, 3, 256);
    {
      const u32x4 z4 = {0u, 0u, 0u, 0u};
      const u32x4 xg = (lane & 2) ? z4 : xa, xu = (lane & 2) ? xa : z4;
      f32x4 d[8];
#pragma unroll
      for (int c = 0; c < 8; ++c) d[c] = mfma16(xg, wf[c], (f32x4){0.f, 0.f, 0.f, 0.f});
      if (tid == 256) { *(u32x4*)(smem + L_MISC + 96) = (u32x4){w0, w1, (unsigned)s0, (unsigned)s1}; misc[30] = 0; }
      SB0();
#pragma unroll
      for (int c = 0; c < 10; ++c) {
        if (c < 8) d[c & 7] = mfma16(xu, wf[8 + (c & 7)], d[c & 7]);
        if (c >= 2) *(f32x4*)(smem + red_dst + (c - 2) * 128) = d[(c - 2) & 7];
        SB0();
      }
    }
    RAW_BAR();
    if (sw == 0) {
      f32x4 t[8];
#pragma unroll
      for (int p = 0; p < 8; ++p) t[p] = *(const f32x4*)(smem + L_RED + p * 1024 + lane * 16);
      SB0();
      STW(sane(__builtin_bit_cast(u32x4, ((t[0] + t[1]) + (t[2] + t[3])) + ((t[4] + t[5]) + (t[6] + t[7])))), gu_src + kq * 1024);
    } else __builtin_amdgcn_s_sleep(2);
    STAMPT(1, 4, 256);
    {
      u32x4 q[4];
      if (grow != (int)key_idx(w0) && grow != (int)key_idx(w1)) OUT_ST();
      asm volatile("" ::: "memory");
      SLEEP(P_GU);
      SPIN {
        unsigned m = 0;
#pragma unroll
        for (int j = 0; j < 4; ++j) { q[j] = LDW(gu_src + j * 1024); m = chk(m, q[j]); }
        CNT(5);
        if (wave_ok(m)) break;
        asm volatile("" ::: "memory");
      }
      STAMPT(1, 5, 256);
      const f32x4 tot = (__builtin_bit_cast(f32x4, q[0]) + __builtin_bit_cast(f32x4, q[1])) + (__builtin_bit_cast(f32x4, q[2]) + __builtin_bit_cast(f32x4, q[3]));
      const f32x2 gs = unpk(pack2o(tot[0], tot[1])), us = unpk(pack2o(tot[2], tot[3]));
      const f32x2 sg = unpk(pack2o(gs[0] * __builtin_amdgcn_rcpf(1.0f + __expf(-gs[0])), gs[1] * __builtin_amdgcn_rcpf(1.0f + __expf(-gs[1]))));
      const unsigned a2 = pack2o(sg[0] * us[0], sg[1] * us[1]);
      *(u16*)(smem + act_dst) = (u16)a2; *(u16*)(smem + act_dst + 128) = (u16)(a2 >> 16);
      __builtin_amdgcn_fence(__ATOMIC_RELEASE, "wavefront"); __builtin_amdgcn_wave_barrier(); __builtin_amdgcn_fence(__ATOMIC_ACQUIRE, "wavefront");
      const u32x4 aa = *(const u32x4*)(smem + act_src);
      f32x4 d[8];
#pragma unroll
      for (int c = 0; c < 8; ++c) d[c] = mfma16(aa, wf[16 + c], (f32x4){0.f, 0.f, 0.f, 0.f});
      SB0();
      u32x4 y[2];
#pragma unroll
      for (int c = 0; c < 8; ++c) {
        const float t0 = d[c][0] + dppf<DPP_ROR8>(d[c][2]), t1 = d[c][3] + dppf<DPP_ROR8>(d[c][1]);
        y[c >> 2][c & 3] = __float_as_uint((lane & 8) ? t1 : t0);
      }
      STW(sane(y[0]), yp_dst); STW(sane(y[1]), yp_dst + 128);
      if (tid == 256) misc[30] = w0;
    }
    STAMPT(1, 6, 256);
  }
}

extern "C" void kernel_launch(void* const* d_in, const int* in_sizes, int n_in, void* d_out, int out_size,
                              void* d_ws, size_t ws_size, hipStream_t stream) {
  (void)hipFuncSetAttribute((const void*)fused, hipFuncAttributeMaxDynamicSharedMemorySize, LDS_TOTAL);
  fused<<<256, 512, LDS_TOTAL, stream>>>((const u16*)d_in[0], (const u16*)d_in[1], (const u16*)d_in[2], (const u16*)d_in[4],
                                         (const u16*)d_in[5], (const u16*)d_in[6], (const u16*)d_in[7], (u16*)d_out, (char*)d_ws);
}
```

```cpp
#include <hip/hip_runtime.h>
#include <stdint.h>

#define LAS __attribute__((address_space(3)))
#define GAS __attribute__((address_space(1)))
#define DI __device__ __forceinline__

typedef unsigned short u16;
typedef unsigned long long u64;
typedef __attribute__((ext_vector_type(2))) __bf16 bf16x2;
typedef __attribute__((ext_vector_type(8))) __bf16 bf16x8;
typedef __attribute__((ext_vector_type(4))) short s16x4;
typedef __attribute__((ext_vector_type(2))) unsigned short u16x2;
typedef __attribute__((ext_vector_type(4))) float f32x4;
typedef __attribute__((ext_vector_type(2))) float f32x2;
typedef __attribute__((ext_vector_type(4))) unsigned u32x4;
typedef __attribute__((ext_vector_type(2))) unsigned u32x2;
typedef GAS unsigned gu32;
typedef GAS unsigned long long gu64;

constexpr int S_ = 2048, D_ = 1024, F_ = 3584;

constexpr unsigned SY_CH = 81920, SY_GO = SY_CH + 16, SY_RE = SY_CH + 8192;
constexpr unsigned WS_FD  = 98304;
constexpr unsigned WS_ZF  = 8192;
constexpr unsigned WS_KVF = 16384;
constexpr unsigned WS_RS  = 131072;
constexpr unsigned WS_CS  = WS_RS + 16 * 16 * 1024;
constexpr unsigned WS_ZP  = WS_CS + 16 * 16 * 768;
constexpr unsigned WS_FB  = WS_ZP + 32 * 16 * 128;
constexpr unsigned WS_ST2 = WS_FB + 32 * 32 * 128;
constexpr unsigned WS_XS  = WS_ST2 + 32 * 8 * 1024;
constexpr unsigned WS_GU  = WS_XS + 64 * 2048;
constexpr unsigned WS_YP  = WS_GU + 256 * 1024;
constexpr unsigned WS_KVP = WS_YP + 2 * 8 * 64 * 512;
constexpr unsigned WS_O   = WS_KVP + 32 * 16 * 4096;
constexpr unsigned WS_END = WS_O + S_ * D_ * 2;
constexpr unsigned OOB = 0x80000000u;

template <int V> struct IC { static constexpr int v = V; };
template <int B, int E, class F> DI void sfor(F&& f) { if constexpr (B < E) { f(IC<B>{}); sfor<B + 1, E>(f); } }

DI float bflo(unsigned u) { return __uint_as_float(u << 16); }
DI float bfhi(unsigned u) { return __uint_as_float(u & 0xffff0000u); }
DI float rbf(float x) { return (float)(__bf16)x; }
DI unsigned pack2(float a, float b) { bf16x2 c; c[0] = (__bf16)a; c[1] = (__bf16)b; return __builtin_bit_cast(unsigned, c); }
DI unsigned pack2o(float a, float b) { unsigned p = pack2(a, b); asm("" : "+v"(p)); return p; }
DI f32x4 mfma16(u32x4 a, u32x4 b, f32x4 c) {
  return __builtin_amdgcn_mfma_f32_16x16x32_bf16(__builtin_bit_cast(bf16x8, a), __builtin_bit_cast(bf16x8, b), c, 0, 0, 0);
}
DI void mfma16i(u32x4 a, u32x4 b, f32x4& c) { asm volatile("v_mfma_f32_16x16x32_bf16 %0, %1, %2, %0" : "+v"(c) : "v"(a), "v"(b)); }
DI f32x2 unpk(unsigned u) { return (f32x2){bflo(u), bfhi(u)}; }
DI u32x2 trread(const char* p) {
  s16x4 v = __builtin_amdgcn_ds_read_tr16_b64_v4i16((LAS s16x4*)p);
  return __builtin_bit_cast(u32x2, v);
}
DI const char* uni(const void* p) {
  const u64 v = (u64)p;
  return (const char*)(((u64)(unsigned)__builtin_amdgcn_readfirstlane((int)(v >> 32)) << 32) | (unsigned)__builtin_amdgcn_readfirstlane((int)v));
}
#define RV4(a, b, c, d) "v" #a, "v" #b, "v" #c, "v" #d
#define RV10(p) "v" #p "0", "v" #p "1", "v" #p "2", "v" #p "3", "v" #p "4", "v" #p "5", "v" #p "6", "v" #p "7", "v" #p "8", "v" #p "9"
#define RSV2 RV4(224, 225, 226, 227), "v228", "v229", RV10(23), RV10(24), RV4(250, 251, 252, 253), "v254", "v255"
#define RSV1 RV4(192, 193, 194, 195), RV4(196, 197, 198, 199), RV10(20), RV10(21), RV4(220, 221, 222, 223), RSV2
#define RSV "v148", "v149", RV10(15), RV10(16), RV10(17), RV10(18), "v190", "v191", RSV1
#define NTW " nt"
#define GL_(v, s, o) "global_load_lds_dwordx4 " v ", " s " offset:" #o
#define GLDS4(SC, CL...) asm volatile("s_add_u32 m0, %5, %c6\n\ts_nop 3\n\t" GL_("%0", "%4", 0) SC "\n\t" GL_("%1", "%4", 1024) SC "\n\t" \
  GL_("%2", "%4", 2048) SC "\n\t" GL_("%3", "%4", 3072) SC :: "v"(v0), "v"(v1), "v"(v2), "v"(v3), "s"(uni(sbase)), "s"(lds), "i"(IMM) : "memory", "scc", ##CL)
#define GLDS3(SC, CL...) asm volatile("s_add_u32 m0, %4, %c5\n\ts_nop 3\n\t" GL_("%0", "%3", 0) SC "\n\t" GL_("%1", "%3", 1024) SC "\n\t" \
  GL_("%2", "%3", 2048) SC :: "v"(v0), "v"(v1), "v"(v2), "s"(uni(sbase)), "s"(lds), "i"(IMM) : "memory", "scc", ##CL)
template <int IMM> DI void glds4(unsigned v0, unsigned v1, unsigned v2, unsigned v3, const void* sbase, unsigned lds) { GLDS4(""); }
template <int IMM> DI void glds4r(unsigned v0, unsigned v1, unsigned v2, unsigned v3, const void* sbase, unsigned lds) { GLDS4("", RSV); }
template <int IMM> DI void glds3r(unsigned v0, unsigned v1, unsigned v2, const void* sbase, unsigned lds) { GLDS3("", RSV); }
template <int R> DI void sload(unsigned voff, const void* sbase) {
  asm volatile("s_nop 4\n\tglobal_load_dwordx4 v[%c0:%c1], %2, %3 sc1" ::"i"(R), "i"(R + 3), "v"(voff), "s"(uni(sbase)) : "memory", RSV);
}
DI float fadd(float a, float b) { float o; asm("v_add_f32 %0, %1, %2" : "=v"(o) : "v"(a), "v"(b)); return o; }
#define RA_(o, a, b) "v_add_f32 %" #o ", v[%c6+" #a "], v[%c6+" #b "]\n\t"
#define RB2_(o, a) "v_add_f32 %" #o ", v[%c6+" #a "], %" #o "\n\t"
#define RM_(o, a, b) "v_max3_u32 %" #o ", v[%c6+" #a "], v[%c6+" #b "], %" #o "\n\t"
template <int R> DI void rsum4(float* a, unsigned& m, unsigned& n) {
  asm volatile(RA_(0, 0, 4) RA_(1, 1, 5) RM_(4, 0, 1) RA_(2, 2, 6) RA_(3, 3, 7) RM_(5, 2, 3) RB2_(0, 8) RM_(4, 4, 5) RB2_(1, 9) RM_(5, 6, 7)
               RB2_(2, 10) RM_(4, 8, 9) RB2_(3, 11) RM_(5, 10, 11) RB2_(0, 12) RB2_(1, 13) RM_(4, 12, 13) RB2_(2, 14) RB2_(3, 15) RM_(5, 14, 15)
               : "=&v"(a[0]), "=&v"(a[1]), "=&v"(a[2]), "=&v"(a[3]), "+v"(m), "+v"(n) : "i"(R) : RSV);
}
template <int R> DI void wload2(unsigned voff, const void* sbase) {
  asm volatile("s_nop 4\n\tglobal_load_dwordx4 v[%c0:%c1], %2, %3" NTW ::"i"(R), "i"(R + 3), "v"(voff), "s"(uni(sbase)) : "memory", RSV2);
}
template <int R> DI void wload(unsigned voff, const void* sbase) {
  asm volatile("s_nop 4\n\tglobal_load_dwordx4 v[%c0:%c1], %2, %3" NTW ::"i"(R), "i"(R + 3), "v"(voff), "s"(uni(sbase)) : "memory", RSV);
}
template <int RA, int RB> DI unsigned wperm(unsigned sel) {
  unsigned o;
  if (RA < 192) asm volatile("v_perm_b32 %0, v%c2, v%c1, %3" : "=v"(o) : "i"(RA), "i"(RB), "s"(sel) : RSV);
  else if (RA < 224) asm volatile("v_perm_b32 %0, v%c2, v%c1, %3" : "=v"(o) : "i"(RA), "i"(RB), "s"(sel) : RSV1);
  else asm volatile("v_perm_b32 %0, v%c2, v%c1, %3" : "=v"(o) : "i"(RA), "i"(RB), "s"(sel) : RSV2);
  return o;
}
template <int R> DI unsigned rget() { unsigned o; asm volatile("v_mov_b32 %0, v%c1" : "=v"(o) : "i"(R) : RSV); return o; }
DI float xh_sum(float v) {
  auto r = __builtin_amdgcn_permlane32_swap(__float_as_uint(v), __float_as_uint(v), false, false);
  return __uint_as_float(r[0]) + __uint_as_float(r[1]);
}
template <int CTRL> DI unsigned dppu(unsigned v) { return (unsigned)__builtin_amdgcn_update_dpp(0, (int)v, CTRL, 0xf, 0xf, true); }
template <int CTRL> DI float dppf(float v) { return __uint_as_float(dppu<CTRL>(__float_as_uint(v))); }
constexpr int DPP_X1 = 0xB1, DPP_X2 = 0x4E, DPP_HMIR = 0x141, DPP_MIR = 0x140, DPP_ROR8 = 0x128;
DI float quad_sum(float v) { v += dppf<DPP_X1>(v); v += dppf<DPP_X2>(v); return v; }
DI float oct_sum(float v) { v = quad_sum(v); v += dppf<DPP_HMIR>(v); return v; }
DI float x16_sum(float v) {
  auto r = __builtin_amdgcn_permlane16_swap(__float_as_uint(v), __float_as_uint(v), false, false);
  return __uint_as_float(r[0]) + __uint_as_float(r[1]);
}
DI float sw16(float a, float b) {
  auto r = __builtin_amdgcn_permlane16_swap(__float_as_uint(a), __float_as_uint(b), false, false);
  return fadd(__uint_as_float(r[0]), __uint_as_float(r[1]));
}
DI float sw32(float a, float b) {
  auto r = __builtin_amdgcn_permlane32_swap(__float_as_uint(a), __float_as_uint(b), false, false);
  return fadd(__uint_as_float(r[0]), __uint_as_float(r[1]));
}
template <int N> struct Phi { unsigned tb[N]; f32x2 tr[N], e[N]; };
template <int N> DI void phi_a(const f32x2* t, Phi<N>& s) {
#pragma unroll
  for (int i = 0; i < N; ++i) s.tb[i] = pack2o(t[i][0], t[i][1]);
  __builtin_amdgcn_sched_barrier(0);
#pragma unroll
  for (int i = 0; i < N; ++i) s.tr[i] = unpk(s.tb[i]);
  __builtin_amdgcn_sched_barrier(0);
#pragma unroll
  for (int i = 0; i < N; ++i) s.e[i] = s.tr[i] * 1.44269504088896340736f;
  __builtin_amdgcn_sched_barrier(0);
}
template <int N> DI void phi_b(Phi<N>& s, unsigned* p) {
  u16x2 tm[2] = {{0, 0}, {0, 0}};
#pragma unroll
  for (int i = 0; i < N; ++i) {
    s.e[i][0] = __builtin_amdgcn_fmed3f(__builtin_amdgcn_exp2f(s.e[i][0]), 0.f, 1.f);
    tm[i & 1] = __builtin_elementwise_max(tm[i & 1], __builtin_bit_cast(u16x2, s.tb[i]));
    s.e[i][1] = __builtin_amdgcn_fmed3f(__builtin_amdgcn_exp2f(s.e[i][1]), 0.f, 1.f);
    __builtin_amdgcn_sched_barrier(0);
  }
#pragma unroll
  for (int i = 0; i < N; ++i) s.tr[i] += s.e[i];
  __builtin_amdgcn_sched_barrier(0);
#pragma unroll
  for (int i = 0; i < N; ++i) { s.tr[i][0] = fmaxf(s.tr[i][0], s.e[i][0]); s.tr[i][1] = fmaxf(s.tr[i][1], s.e[i][1]); }
  const u16x2 tx = __builtin_elementwise_max(tm[0], tm[1]);
  __builtin_amdgcn_sched_barrier(0);
#pragma unroll
  for (int i = 0; i < N; ++i) p[i] = pack2(s.tr[i][0], s.tr[i][1]);
  if (__any(tx[0] > 0xC0F0 || tx[1] > 0xC0F0)) {
#pragma unroll
    for (int i = 0; i < N; ++i) p[i] = pack2(bflo(p[i]) + 1e-6f, bfhi(p[i]) + 1e-6f);
  }
}
template <int N> DI void phi_n(const f32x2* t, unsigned* p) { Phi<N> s; phi_a<N>(t, s); phi_b<N>(s, p); }
#define RLX_AGENT __ATOMIC_RELAXED, __HIP_MEMORY_SCOPE_AGENT
DI void st64(char* ws, unsigned off, u64 v) { __hip_atomic_store((gu64*)(ws + off), v, RLX_AGENT); }
DI unsigned ld32(char* ws, unsigned off) { return __hip_atomic_load((gu32*)(ws + off), RLX_AGENT); }
#define WAIT_V(n) asm volatile("s_waitcnt vmcnt(%c0)" ::"i"(n) : "memory")
#define RAW_BAR() do { asm volatile("s_waitcnt lgkmcnt(0)" ::: "memory"); __builtin_amdgcn_s_barrier(); asm volatile("" ::: "memory"); } while (0)
#define SB0() __builtin_amdgcn_sched_barrier(0)
#define WAIT_VR(n) asm volatile("s_waitcnt vmcnt(%c0)" ::"i"(n) : "memory", RSV)
#define RAW_BARR() do { asm volatile("s_waitcnt lgkmcnt(0)" ::: "memory", RSV); __builtin_amdgcn_s_barrier(); asm volatile("" ::: "memory", RSV); } while (0)
#define KEEP2() asm volatile("" ::: "memory", RSV2)
#define RAW_BAR2() do { asm volatile("s_waitcnt lgkmcnt(0)" ::: "memory", RSV2); __builtin_amdgcn_s_barrier(); KEEP2(); } while (0)
constexpr unsigned SPIN_CAP = 1u << 18;
#define SPIN _Pragma("nounroll") for (unsigned s_ = 0; s_ < SPIN_CAP; ++s_)
constexpr unsigned SENT = 0xFFFFFFFFu;
DI unsigned sane1(unsigned v) { return min(v, 0xFFFFFFFEu); }
DI u32x4 sane(u32x4 v) { v[1] = sane1(v[1]); v[3] = sane1(v[3]); return v; }
DI unsigned chk(unsigned m, u32x4 v) { return max(max(m, v[1]), v[3]); }
DI bool wave_ok(unsigned m) { return !__any(m == SENT); }
DI unsigned key_idx(unsigned k) { return (0xFFFFu - (k & 0xFFFFu)) & (S_ - 1); }
DI float key_val(unsigned k) { const unsigned o = k >> 16; return __uint_as_float(((o & 0x8000u) ? (o & 0x7fffu) : (~o & 0xffffu)) << 16); }
DI void top2_mix(unsigned& b0, unsigned& b1, unsigned c0, unsigned c1) {
  const unsigned lo = min(b0, c0), mx = max(b1, c1);
  b0 = max(b0, c0); b1 = max(lo, mx);
}
DI void top2_lo(unsigned& b0, unsigned& b1) {
  top2_mix(b0, b1, dppu<DPP_X1>(b0), dppu<DPP_X1>(b1));
  top2_mix(b0, b1, dppu<DPP_X2>(b0), dppu<DPP_X2>(b1));
  top2_mix(b0, b1, dppu<DPP_HMIR>(b0), dppu<DPP_HMIR>(b1));
}
DI void top2_mid(unsigned& b0, unsigned& b1) {
  top2_mix(b0, b1, dppu<DPP_MIR>(b0), dppu<DPP_MIR>(b1));
  auto p = __builtin_amdgcn_permlane16_swap(b0, b0, false, false); auto q = __builtin_amdgcn_permlane16_swap(b1, b1, false, false);
  b0 = p[0]; b1 = q[0]; top2_mix(b0, b1, p[1], q[1]);
}
DI void top2_hi(unsigned& b0, unsigned& b1) {
  top2_mid(b0, b1);
  auto p = __builtin_amdgcn_permlane32_swap(b0, b0, false, false); auto q = __builtin_amdgcn_permlane32_swap(b1, b1, false, false);
  b0 = p[0]; b1 = q[0]; top2_mix(b0, b1, p[1], q[1]);
}

constexpr int K1_STAGE = 40960;
constexpr int LDS_TOTAL = 4 * K1_STAGE;
constexpr int L_STM = 0;
constexpr int L_MISC = 2048;
constexpr int L_XK = 5120;
constexpr int L_KX = 3328;
constexpr int L_ACT = 2304;
constexpr int L_CAND = 4096;
constexpr int L_WO = 16384;
constexpr int L_P = 16384, L_PK = 7 * 16384;
constexpr int L_RED = 65536;
constexpr int wo_slot(int j) { return L_WO + (j % 9) * 16384; }
constexpr int CODE_LINES = 12;
#define OW_ 8
constexpr int OW = OW_;
#define W1A_ 16
constexpr int W1A = W1A_;
#define GSCHED 5, 9, 13, 17, 21, 24, 27, 30, 31, 35, 39, 41, 45, 48, 48
constexpr int GS[15] = {GSCHED};
constexpr int gops(int g) { return (g / 3) * 10 + (g % 3 == 0 ? 0 : g % 3 == 1 ? 4 : 7); }
constexpr bool gs_ok() {
  bool ok = GS[0] >= 3 && GS[0] <= 9 && GS[1] >= 6 && GS[1] <= 12 && GS[0] <= GS[1] && GS[12] == 45 && GS[13] == 48 && GS[14] == 48;
  for (int t = 0; t < 12; ++t) ok = ok && GS[t + 2] <= 3 * (t + 5) && GS[t + 2] >= 3 * (t + 3) && GS[t + 2] >= GS[t + 1] + 1;
  return ok;
}
static_assert(gs_ok(), "staging schedule");
#define WSCHED 0, 0, 0, 0, 0, 0, 5, 5, 5, 10, 10, 10, 10
constexpr int WS_[13] = {WSCHED};
constexpr int wyoung(int t, int stages) {
  int e = 0;
  while (GS[e + 2] < 3 * stages) ++e;
  return WS_[t + 1] - WS_[e];
}
#define PACE 1
#define P_KVP 1
#define P_KVF 1
#define P_FB 8
#define P_C 14
#define P_D 2
#define P_GU 12
#define P_E 10
#define SLEEP(n) do { if (n) __builtin_amdgcn_s_sleep(n); } while (0)
#define STE 6
#define HS0 1
#define CP2 10
#define CP3 0
#define SLOWPF 1
#define SPX 0
constexpr int VW = 160;

__global__ __launch_bounds__(512, 2) void fused(const u16* __restrict__ x, const u16* __restrict__ wqkv,
                                                const u16* __restrict__ wo, const u16* __restrict__ w_r,
                                                const u16* __restrict__ wg, const u16* __restrict__ wu,
                                                const u16* __restrict__ wd, u16* __restrict__ out, char* __restrict__ ws) {
  extern __shared__ __attribute__((aligned(16))) char smem[];
  u64 pc0; asm volatile("s_getpc_b64 %0" : "=s"(pc0));
  const int wave = __builtin_amdgcn_readfirstlane(threadIdx.x >> 6);
  const unsigned lds0 = (unsigned)(size_t)smem;
  if (wave == 0) {
    const int l = threadIdx.x;
    asm volatile("s_mov_b32 m0, %1\n\ts_nop 0\n\tglobal_load_lds_dword %0, off"
                 :: "v"((const char*)(pc0 - (pc0 & 127)) + ((blockIdx.x >> 3) + (l < CODE_LINES ? l : 0) * 32) * 128), "s"(__builtin_amdgcn_readfirstlane(lds0 + 3 * K1_STAGE)) : "memory");
  }
#define RB_(b) (((b) & 1) * 8 + (((b) >> 3) & 7))
#define HP_(b) ((((b) & 7) >> 1) * 4 + ((b) >> 6))
#define KROT_(b) ((4 * ((HP_(b) >> 2) + (RB_(b) >> 3) + 2 * ((RB_(b) >> 2) & 1))) & 15)
  unsigned voA0, voA1, voA2, voA3, voB0, voB1, voB2, voB3, voB4, voB5;
  const unsigned l4 = __builtin_amdgcn_readfirstlane(lds0 + (wave - 4) * 4096), l6 = __builtin_amdgcn_readfirstlane(lds0 + (wave - 4) * 6144);
  const int krot0 = KROT_(blockIdx.x);
  auto group = [&](auto G) {
    constexpr int t = G.v / 3, p = G.v % 3, sl = (t & 3) * K1_STAGE;
    const int kt = (t + krot0) & 15;
    if (p == 0) glds4r<sl>(voA0, voA1, voA2, voA3, (const char*)x + kt * 128, l4);
    else if (p == 1) glds3r<sl + 16384>(voB0, voB1, voB2, (const char*)wqkv + kt * 64 * (3 * D_ * 2), l6);
    else glds3r<sl + 16384 + 3072>(voB3, voB4, voB5, (const char*)wqkv + kt * 64 * (3 * D_ * 2), l6);
  };
  if (wave >= 4) {
    const int lane = threadIdx.x & 63, sw = wave - 4;
    const int r0 = RB_(blockIdx.x) * 128 + 32 * sw + (lane >> 3);
    voA0 = (unsigned)(r0 * D_ + ((lane & 7) ^ (lane >> 4)) * 8) * 2u;
    voA1 = ((voA0 + 8 * D_ * 2) ^ 64) - 1024; voA2 = voA0 + 16 * D_ * 2 - 2048; voA3 = voA1 + 16 * D_ * 2 - 2048;
    asm volatile("" : "+v"(voA0), "+v"(voA1), "+v"(voA2), "+v"(voA3));
    group(IC<0>{});
    int l2 = lane; asm volatile("" : "+v"(l2));
    voB0 = (unsigned)((16 * sw + 4 * (l2 >> 5) + ((l2 >> 2) & 3)) * (3 * D_) + HP_(blockIdx.x) * 64 + ((l2 >> 4) & 1) * 32 + (l2 & 3) * 8) * 2u;
    voB1 = voB0 + 1024; voB2 = voB0 + 2048;
    voB3 = (voB0 ^ 32) + 8 * 3 * D_ * 2; voB4 = voB3 + 1024; voB5 = voB3 + 2048;
    asm volatile("" : "+v"(voB0), "+v"(voB1), "+v"(voB2), "+v"(voB3), "+v"(voB4), "+v"(voB5));
    sfor<1, 3>(group);
  }
  int bid = blockIdx.x; asm volatile("" : "+s"(bid));
  const int xcd = bid & 7, idx = bid >> 3;
  const int rb = RB_(bid), hp = HP_(bid);
  const int rp = xcd * 4 + (idx >> 3), cb = idx & 7;
  const int hs_ = (hp + 4 + (int)(((size_t)wqkv >> 7) & 7)) & 7;
  const int g8 = ((hp >> 3) * 4 + (rb >> 2)) * 8 + hs_, kq = rb & 3;
  const int krot = KROT_(bid), posA = 1 + (hp & 3), posB = 1 + (rb & 3);
  const int wrot = 4 * rp + (xcd >> 1), wdir = (xcd & 1) ? -1 : 1;
  auto whead = [&](int t) { return (wrot + wdir * t) & 15; };
  const unsigned rs_slot = WS_RS + (rb * 16 + ((krot + posA) & 15)) * 1024, cs_slot = WS_CS + (hp * 16 + ((krot + posB) & 15)) * 768;
  const __amdgpu_buffer_rsrc_t wsr = __builtin_amdgcn_make_buffer_rsrc(ws, 0, WS_END, 0x00020000);
#define LDW(off) __builtin_amdgcn_raw_buffer_load_b128(wsr, (off), 0, 16)
#define STW(v, off) __builtin_amdgcn_raw_buffer_store_b128((v), wsr, (off), 0, 16)
#define STW1(v, off) __builtin_amdgcn_raw_buffer_store_b32((v), wsr, (off), 0, 16)
  float* stm = (float*)(smem + L_STM);
  volatile LAS int* misc = (volatile LAS int*)(smem + L_MISC);
#define LANES() int tid = threadIdx.x; asm volatile("" : "+v"(tid)); const int lane = tid & 63, r = lane & 31, h = lane >> 5; (void)r; (void)h
#define EPI() const int erow = tid >> 3, eseg = tid & 7; const size_t ego = (size_t)(rp * 64 + erow) * D_ + cb * 128 + eseg * 8
  u32x4 xv[2], wv[2];
  f32x2 xf[8], wf2[8];
  float sw_;
  auto unpack_xw = [&]() {
    f32x2 a = {0.f, 0.f};
#pragma unroll
    for (int i = 0; i < 8; ++i) { xf[i] = unpk(xv[i >> 2][i & 3]); wf2[i] = unpk(wv[i >> 2][i & 3]); a += wf2[i]; }
    sw_ = oct_sum(a[0] + a[1]);
#pragma unroll
    for (int i = 0; i < 8; ++i) asm volatile("" : "+v"(xf[i]), "+v"(wf2[i]));
    asm volatile("" : "+v"(sw_));
  };
  u32x4 wf[24];

  auto wtr = [&](auto I) {
    constexpr int m = I.v >> 3, c = I.v & 7, R = VW + 32 * m + (c >> 1);
    const unsigned sel = (c & 1) ? 0x07060302u : 0x05040100u;
    wf[I.v] = (u32x4){wperm<R, R + 4>(sel), wperm<R + 8, R + 12>(sel), wperm<R + 16, R + 20>(sel), wperm<R + 24, R + 28>(sel)};
  };

  if (wave < 4) {
    LANES();
    const int c16 = lane & 15, g4 = lane >> 4;
    const int trl = (c16 >> 2) * 64 + (c16 & 3) * 8;
    const int wr = wave >> 1, wc = wave & 1;
    const int head = 2 * hp + wc;
    const unsigned kvp_off = WS_KVP + (head * 16 + rb) * 4096 + wr * 2048 + lane * 16;
    const unsigned zp_off = WS_ZP + (head * 16 + rb) * 128;
    const unsigned o_off = WS_O + ((rb * 4 + 2 * wr) * 32 + head) * 2048 + lane * 16;
    const unsigned fb_off = lane < 8 ? WS_FB + ((rb * 2 + wr) * 32 + head) * 128 + lane * 16 : OOB;
    unsigned pfa, pfb = OOB;
    unsigned pfc = lane < 8 ? fb_off : lane < 24 ? rs_slot + wave * 256 + (lane - 8) * 16
        : lane < 40 ? (wave < 3 ? cs_slot + wave * 256 : WS_KVF + hp * 4096 + rb * 256) + (lane - 24) * 16
        : lane < 48 ? (wave < 2 ? zp_off + (lane & 7) * 16 : (wave == 2 && rb < 2) ? WS_ZF + (2 * hp + rb) * 128 + (lane & 7) * 16 : OOB) : OOB;
    if (wave == 0) pfa = WS_ST2 + (rp * 8 + cb) * 1024 + lane * 16;
    else if (wave == 1) pfa = WS_GU + (g8 * 4 + kq) * 1024 + lane * 16;
    else pfa = WS_YP + (((wave - 2) * 8 + 2 * kq + (lane >> 5)) * 64 + g8) * 512 + (lane & 31) * 16;
    if (wave == 0) pfb = lane < 32 ? WS_XS + (rp * 2 + (lane >> 4)) * 2048 + cb * 256 + (lane & 15) * 16 : lane < 40 ? WS_FD + (cb * 32 + rp) * 128 + (lane & 7) * 16 : OOB;
    u32x4 S4 = {SENT, SENT, SENT, SENT};
    STW(S4, pfc);
    asm volatile("" : "+v"(pfa), "+v"(pfb), "+v"(S4));
    __builtin_amdgcn_s_setprio(1);
    f32x4 acc[4][6];
    int ao[2][2], bo[2][2], bv[2][2];
    f32x4 m4[4], i4[4]; float cs[4]; int st_ok;
    auto st_rd = [&](int q) {
      if (q == 0) { st_ok = misc[12 + (lane & 3)]; asm volatile("" ::: "memory"); }
      else if (q < 5) cs[(q - 1) & 3] = stm[256 + wc * 96 + 32 + 16 * (q < 3 ? q - 1 : (q - 1) ^ wr) + c16];
      else if (q < 9) m4[(q - 5) & 3] = *(const f32x4*)(stm + 64 * wr + 16 * (q - 5) + 4 * g4);
      else if (q < 13) i4[(q - 9) & 3] = *(const f32x4*)(stm + 128 + 64 * wr + 16 * (q - 9) + 4 * g4);
    };
    {
#pragma unroll
      for (int i = 0; i < 2; ++i) {
        ao[0][i] = (64 * wr + c16) * 128 + (((4 * i + g4) ^ (c16 >> 1)) * 16); ao[1][i] = ao[0][i] + 2 * K1_STAGE;
        bo[0][i] = 16384 + g4 * 3072 + wc * 256 + trl + ((i ^ (g4 & 1)) * 32); bo[1][i] = bo[0][i] + 2 * K1_STAGE;
        bv[0][i] = bo[0][i] ^ (wr * 32); bv[1][i] = bo[1][i] ^ (wr * 32);
        asm volatile("" : "+v"(ao[0][i]), "+v"(ao[1][i]), "+v"(bo[0][i]), "+v"(bo[1][i]), "+v"(bv[0][i]), "+v"(bv[1][i]));
      }
#pragma unroll
      for (int i = 0; i < 4; ++i)
#pragma unroll
        for (int j = 0; j < 6; ++j) { acc[i][j] = (f32x4){0.f, 0.f, 0.f, 0.f}; asm volatile("" : "+v"(acc[i][j])); }
      struct Frag { u32x4 a[4]; u32x2 bl[6], bh[6]; };
      Frag FA, FB;
#define K1_B(j) ((u32x4){C.bl[j][0], C.bl[j][1], C.bh[j][0], C.bh[j][1]})
#define K1_LA(i) N.a[i] = *(const u32x4*)(smem + ao[sn >> 1][hf] + (sn & 1) * K1_STAGE + (i) * 2048)
#define K1_LL(j) N.bl[j] = trread(smem + ((j) < 4 ? bo : bv)[sn >> 1][(j) & 1] + (sn & 1) * K1_STAGE + hf * 12288 + ((j) >> 1) * 1024)
#define K1_LH(j) N.bh[j] = trread(smem + ((j) < 4 ? bo : bv)[sn >> 1][(j) & 1] + (sn & 1) * K1_STAGE + hf * 12288 + ((j) >> 1) * 1024 + 512)
#define K1_M(i, j) if ((j) < 2) mfma16i(K1_B(j), C.a[i], acc[i][j]); else mfma16i(C.a[i], K1_B(j), acc[i][j]); SB0()
      auto sub = [&](Frag& C, Frag& N, int sn, int hf) {
        sfor<0, 24>([&](auto Q) {
          constexpr int q = Q.v;
          constexpr int j = q / 4, i = (j & 1) ? 3 - q % 4 : q % 4;
          if (q == 23) asm volatile("s_waitcnt lgkmcnt(0)" : "+v"(N.bh[5]));
          K1_M(i, j);
          if constexpr (q < 4) { K1_LA(q); } else if constexpr (q < 16) { if (q & 1) { K1_LH((q - 4) >> 1); } else { K1_LL((q - 4) >> 1); } }
          SB0();
        });
      };
      auto subkv = [&](Frag& C, Frag& N, int sn, int hf, bool ld) {
#define K1_QL(i, j, l) K1_M(i, j); if (ld) { l; } else { st_rd(qq); ++qq; } SB0()
        int qq = 0;
        K1_QL(0, 2, K1_LL(2)); K1_QL(1, 2, K1_LH(2)); K1_QL(0, 3, K1_LA(0)); K1_QL(1, 3, K1_LA(1)); K1_QL(2, 2, K1_LL(3)); K1_QL(3, 2, K1_LH(3));
        K1_QL(2, 3, K1_LA(2)); K1_QL(3, 3, K1_LA(3)); K1_QL(0, 4, K1_LL(4)); K1_QL(1, 4, K1_LH(4)); K1_QL(2, 4, K1_LL(5)); K1_QL(3, 4, K1_LH(5));
        K1_QL(0, 5, ); K1_M(1, 5); K1_M(2, 5); K1_M(3, 5);
      };
      WAIT_V(0);
      RAW_BAR();
      {
        Frag& N = FA; const int sn = 0, hf = 0;
        K1_LL(0); K1_LH(0); K1_LA(0); K1_LA(1); K1_LL(1); K1_LH(1); K1_LL(2); K1_LH(2); K1_LL(3); K1_LH(3); K1_LA(2); K1_LA(3); K1_LL(4); K1_LH(4); K1_LL(5); K1_LH(5);
      }
#pragma unroll 1
      for (int it = 0; it < 4; ++it) {
#pragma unroll
        for (int s = 0; s < 4; ++s) {
          if (s == 2 && it == 3) break;
          sub(FA, FB, s, 1);
          if (s == 0) WAIT_V(0);
          if (it == 0) { if (s == 2) STW(S4, kvp_off); if (s == 3) STW(S4, kvp_off + 1024); }
          if (it == 1) { if (s == 0) STW(S4, pfa); if (s == 1) STW(S4, pfb); }
          if (s != 1 || it < 3) RAW_BAR();
          sub(FB, FA, (s + 1) & 3, 0);
        }
      }
      subkv(FA, FB, 2, 1, true);
      RAW_BAR();
      subkv(FB, FA, 3, 0, true);
      subkv(FA, FB, 3, 1, true);
      subkv(FB, FA, 0, 0, false);
    }
    asm volatile("s_nop 15" ::: "memory");
    {
      SPIN {
        if (__all(st_ok != 0)) break;
#pragma unroll
        for (int q = 0; q < 13; ++q) st_rd(q);
        SB0();
      }
      const u32x4 ONES4 = {0x3F803F80u, 0x3F803F80u, 0x3F803F80u, 0x3F803F80u};
      f32x2 tk[16], tv[16];
#define PR(v, i) ((f32x2){(v)[2 * (i)], (v)[2 * (i) + 1]})
#pragma unroll
      for (int i = 0; i < 16; ++i) {
        tk[i] = PR(acc[(i >> 1) & 3][2 + (i >> 3)], i & 1) - PR(m4[(i >> 1) & 3], i & 1) * cs[i >> 3];
        tv[i] = PR(acc[(i >> 1) & 3][4 + (i >> 3)], i & 1) - PR(m4[(i >> 1) & 3], i & 1) * cs[2 + (i >> 3)];
      }
      SB0();
#pragma unroll
      for (int i = 0; i < 16; ++i) { tk[i] *= PR(i4[(i >> 1) & 3], i & 1); tv[i] *= PR(i4[(i >> 1) & 3], i & 1); }
      SB0();
      unsigned kp[16], vp[16];
#pragma unroll
      for (int i = 0; i < 16; ++i) vp[i] = pack2o(tv[i][0], tv[i][1]);
      phi_n<16>(tk, kp);
#define F4_(p, i) ((u32x4){(p)[4 * (i)], (p)[4 * (i) + 1], (p)[4 * (i) + 2], (p)[4 * (i) + 3]})
      const f32x4 Z4 = {0.f, 0.f, 0.f, 0.f};
      const u32x4 vk[2] = {F4_(vp, 0), F4_(vp, 1)}, vg[2] = {F4_(vp, 2), F4_(vp, 3)};
      f32x4 keep[2], give[2], zq[2];
#pragma unroll
      for (int d = 0; d < 2; ++d) { give[d] = mfma16(F4_(kp, 2 * d), vg[0], Z4); zq[d] = mfma16(ONES4, F4_(kp, 2 * d), Z4); }
#pragma unroll
      for (int d = 0; d < 2; ++d) { give[d] = mfma16(F4_(kp, 2 * d + 1), vg[1], give[d]); zq[d] = mfma16(ONES4, F4_(kp, 2 * d + 1), zq[d]); }
#pragma unroll
      for (int d = 0; d < 2; ++d) keep[d] = mfma16(F4_(kp, 2 * d), vk[0], Z4);
#pragma unroll
      for (int d = 0; d < 2; ++d) keep[d] = mfma16(F4_(kp, 2 * d + 1), vk[1], keep[d]);
      SB0();
      const float zv = (lane & 16) ? zq[1][0] : zq[0][0];
      char* xk = smem + L_XK + wave * 2304 + lane * 16;
      const char* xp = smem + L_XK + (wave ^ 2) * 2304 + lane * 16;
      *(f32x4*)xk = give[0]; *(f32x4*)(xk + 1024) = give[1]; *(float*)(xk + 2048 - lane * 12) = zv;
      asm volatile("" ::: "memory");
      misc[16 + wave] = 1;
      f32x4 p0, p1; float pz;
      SPIN {
        const int ok = misc[16 + (wave ^ 2)];
        asm volatile("" ::: "memory");
        p0 = *(const f32x4*)xp; p1 = *(const f32x4*)(xp + 1024); pz = *(const float*)(xp + 2048 - lane * 12);
        SB0();
        if (ok == 1) break;
      }
      STW(sane(__builtin_bit_cast(u32x4, keep[0] + p0)), kvp_off);
      STW(sane(__builtin_bit_cast(u32x4, keep[1] + p1)), kvp_off + 1024);
      STW1(sane1(__float_as_uint(zv + pz)), (lane < 32 && wr == 0) ? zp_off + lane * 4 : OOB);
    }
    {
      u32x4 qa[4][4]; u32x2 ql[4][2], qh[4][2];
#pragma unroll
      for (int u = 0; u < 4; ++u) {
#pragma unroll
        for (int j = 0; j < 2; ++j) {
          ql[u][j] = trread(smem + bo[1][j] + (u >> 1) * K1_STAGE + (u & 1) * 12288);
          qh[u][j] = trread(smem + bo[1][j] + (u >> 1) * K1_STAGE + (u & 1) * 12288 + 512);
        }
#pragma unroll
        for (int i = 0; i < 4; ++i) qa[u][i] = *(const u32x4*)(smem + ao[1][u & 1] + (u >> 1) * K1_STAGE + i * 2048);
      }
      asm volatile("" ::: "memory");
      misc[8 + wave] = 3;
      SB0();
#pragma unroll
      for (int u = 0; u < 4; ++u)
#pragma unroll
        for (int j = 0; j < 2; ++j)
#pragma unroll
          for (int i = 0; i < 4; ++i) acc[i][j] = mfma16((u32x4){ql[u][j][0], ql[u][j][1], qh[u][j][0], qh[u][j][1]}, qa[u][i], acc[i][j]);
      SB0();
    }
    unsigned qp[16];
    Phi<16> qs;
    const unsigned po = WS_KVP + ((2 * hp + (rb >> 3)) * 16 + h) * 4096 + (2 * ((rb >> 2) & 1) + ((lane >> 4) & 1)) * 1024 + (16 * (rb & 3) + (lane & 15)) * 16;
    u32x4 pv[8] = {};
    {
      f32x4 c4[2]; float m_b[4], rinv[4];
#pragma unroll
      for (int i = 0; i < 2; ++i) c4[i] = *(const f32x4*)(stm + 256 + wc * 96 + 16 * i + 4 * g4);
#pragma unroll
      for (int i = 0; i < 4; ++i) { m_b[i] = stm[64 * wr + 16 * i + c16]; rinv[i] = stm[128 + 64 * wr + 16 * i + c16]; }
      SB0();
      f32x2 tq[16];
#pragma unroll
      for (int i = 0; i < 16; ++i) tq[i] = PR(acc[i >> 2][(i >> 1) & 1], i & 1) - PR(c4[(i >> 1) & 1], i & 1) * m_b[i >> 2];
      SB0();
#pragma unroll
      for (int i = 0; i < 16; ++i) tq[i] *= rinv[i >> 2];
      SB0();
      phi_a<16>(tq, qs);
#pragma unroll
      for (int i = 0; i < 16; ++i) asm volatile("" : "+v"(qs.tb[i]), "+v"(qs.tr[i]), "+v"(qs.e[i]));
    }
    if (wave == 0) {
      SLEEP(P_KVP);
#pragma unroll
      for (int p = 0; p < 8; ++p) pv[p] = LDW(po + p * 8192);
    }
    asm volatile("" ::: "memory");
    if (wave == 1 && rb < 2) {
      SLEEP(P_KVP);
      SPIN {
        unsigned m = 0;
#pragma unroll
        for (int p = 0; p < 2; ++p) { pv[p] = LDW(WS_ZP + (2 * hp + rb) * 2048 + p * 1024 + lane * 16); m = chk(max(max(m, pv[p][0]), pv[p][2]), pv[p]); }
        if (wave_ok(m)) break;
        asm volatile("" ::: "memory");
      }
      f32x4 s = __builtin_bit_cast(f32x4, pv[0]) + __builtin_bit_cast(f32x4, pv[1]);
#pragma unroll
      for (int i = 0; i < 4; ++i) { s[i] += dppf<DPP_ROR8>(s[i]); s[i] = xh_sum(x16_sum(s[i])); }
      if (lane < 16) st64(ws, WS_ZF + (2 * hp + rb) * 128 + (lane >> 3) * 64 + (lane & 3) * 16 + ((lane >> 2) & 1) * 8,
                          ((u64)sane1(pack2(s[2], s[3])) << 32) | pack2(s[0], s[1]));
    }
    if (wave == 0) {
      SPIN {
        unsigned m = 0;
#pragma unroll
        for (int p = 0; p < 8; ++p) m = chk(m, pv[p]);
        if (wave_ok(m)) break;
        asm volatile("" ::: "memory");
#pragma unroll
        for (int p = 0; p < 8; ++p) pv[p] = LDW(po + p * 8192);
      }
      f32x4 s = __builtin_bit_cast(f32x4, pv[0]);
#pragma unroll
      for (int p = 1; p < 8; ++p) s += __builtin_bit_cast(f32x4, pv[p]);
      const unsigned p0 = pack2(xh_sum(s[0]), xh_sum(s[1])), p1 = pack2(xh_sum(s[2]), xh_sum(s[3]));
      auto a = __builtin_amdgcn_permlane16_swap(p0, p0, false, false);
      auto b = __builtin_amdgcn_permlane16_swap(p1, p1, false, false);
      STW(sane((u32x4){a[0], b[0], a[1], b[1]}), lane < 16 ? WS_KVF + hp * 4096 + rb * 256 + lane * 16 : OOB);
    }
    phi_b<16>(qs, qp);
#pragma unroll
    for (int i = 0; i < 16; ++i) asm volatile("" : "+v"(qp[i]));
    if (wave == 0) misc[7] = 1;
    else SPIN { if (misc[7] == 1) break; __builtin_amdgcn_s_sleep(1); }
    SLEEP(P_KVF);
    {
      u32x4 k0, k1, z0;
      SPIN {
        k0 = LDW(WS_KVF + head * 2048 + lane * 16); k1 = LDW(WS_KVF + head * 2048 + 1024 + lane * 16);
        z0 = LDW(WS_ZF + head * 128 + g4 * 16);
        if (wave_ok(chk(chk(chk(0u, k0), k1), z0))) break;
        asm volatile("" ::: "memory");
      }
      const f32x4 Z4 = {0.f, 0.f, 0.f, 0.f};
      f32x4 X[8], Dn[4];
#pragma unroll
      for (int i = 0; i < 4; ++i) Dn[i] = mfma16(z0, F4_(qp, i), Z4);
#pragma unroll
      for (int i = 0; i < 8; ++i) X[i] = mfma16((i & 1) ? k1 : k0, F4_(qp, i >> 1), Z4);
      SB0();
      float ri[4]; unsigned a[16]; f32x2 f[16];
#pragma unroll
      for (int i = 0; i < 4; ++i) ri[i] = __builtin_amdgcn_rcpf(rbf(rbf(Dn[i][0]) + 1e-6f));
      SB0();
#pragma unroll
      for (int i = 0; i < 16; ++i) a[i] = pack2o(X[i >> 1][2 * (i & 1)], X[i >> 1][2 * (i & 1) + 1]);
      SB0();
#pragma unroll
      for (int i = 0; i < 16; ++i) f[i] = unpk(a[i]);
      SB0();
#pragma unroll
      for (int i = 0; i < 16; ++i) f[i] *= ri[i >> 2];
      SB0();
#pragma unroll
      for (int i = 0; i < 4; ++i) {
        STW(((u32x4){pack2(f[4 * i][0], f[4 * i][1]), pack2(f[4 * i + 1][0], f[4 * i + 1][1]), pack2(f[4 * i + 2][0], f[4 * i + 2][1]), pack2(f[4 * i + 3][0], f[4 * i + 3][1])}),
            o_off + (i >> 1) * 65536 + (i & 1) * 1024);
        SB0();
      }
    }
    {
      EPI();
      const int rt = wave & 1, kh = wave >> 1;
      const unsigned ob = WS_O + ((rp * 2 + rt) * 32 + 16 * kh) * 2048 + lane * 16;
#define LDO(t, st) __builtin_amdgcn_raw_buffer_load_b128(wsr, ob + (st) * 1024, whead(t) * 2048, 16)
      int woff[2];
#pragma unroll
      for (int i = 0; i < 2; ++i) woff[i] = kh * 8192 + (g4 >> 1) * 2048 + (g4 & 1) * 512 + trl + ((i ^ (g4 & 1)) * 32);
      struct Frag2 { u32x2 lo[8], hi[8]; };
      Frag2 G[2];
      auto ld2 = [&](Frag2& N, int sn, int nt, int hi) {
        const char* p = smem + sn + woff[nt & 1] + (nt >> 2) * 1024 + ((nt >> 1) & 1) * 256;
        if (hi) N.hi[nt] = trread(p + 4096); else N.lo[nt] = trread(p);
      };
      WAIT_V(0);
      STW(((u32x4){1u, 1u, 1u, 1u}), fb_off);
      RAW_BAR();
#pragma unroll
      for (int i = 0; i < 16; ++i) ld2(G[0], wo_slot(0), i >> 1, i & 1);
      f32x4 a2[2][8];
#pragma unroll
      for (int i = 0; i < 16; ++i) a2[i & 1][i >> 1] = (f32x4){0.f, 0.f, 0.f, 0.f};
      SLEEP(P_FB);
      SPIN { if (wave_ok(ld32(ws, WS_FB + (rp * 32 + 16 * kh + (lane & 15)) * 128))) break; }
      asm volatile("" ::: "memory");
      char* pr[2];
#pragma unroll
      for (int i = 0; i < 2; ++i) pr[i] = smem + L_P + kh * L_PK + (32 * rt + c16) * 512 + (((4 * i + g4) ^ (c16 & 7)) * 16);
      u32x4 of[16][2];
#pragma unroll
      for (int t = 0; t < OW; ++t) { of[t][0] = LDO(t, 0); of[t][1] = LDO(t, 1); }
      SB0();
      sfor<0, 16>([&](auto T) {
        constexpr int t = T.v;
        Frag2& C = G[t & 1]; Frag2& N = G[(t + 1) & 1];
#pragma unroll
        for (int i = 0; i < 16; ++i) {
          const int nt = i >> 1, st = i & 1;
          a2[st][nt] = mfma16((u32x4){C.lo[nt][0], C.lo[nt][1], C.hi[nt][0], C.hi[nt][1]}, of[t][st], a2[st][nt]);
          SB0();
          if ((t & 1) && t <= 11 && i == 8) {
            asm volatile("s_waitcnt lgkmcnt(8)" ::: "memory"); __builtin_amdgcn_s_barrier(); asm volatile("" ::: "memory");
          }
          if (t < 15) ld2(N, wo_slot(t + 1), nt, st);
          if (t > 0 && t - 1 + OW < 16 && (i == 3 || i == 11)) of[(t - 1 + OW) & 15][i >> 3] = LDO(t - 1 + OW, i >> 3);
          if (t - 1 + OW == 16 && (i == 3 || i == 11)) { xv[i >> 3] = *(const u32x4*)(x + ego + (i >> 3) * 64); wv[i >> 3] = *(const u32x4*)(w_r + cb * 128 + eseg * 8 + (i >> 3) * 64); }
          if (t == 15 && i >= 4) *(f32x4*)(pr[(nt - 2) & 1] + ((nt - 2) >> 1) * 128 + st * 8192) = a2[st][(nt - 2) & 7];
          SB0();
        }
      });
#pragma unroll
      for (int i = 12; i < 16; ++i) *(f32x4*)(pr[(i >> 1) & 1] + (i >> 2) * 128 + (i & 1) * 8192) = a2[i & 1][i >> 1];
    }
    unpack_xw();
    __builtin_amdgcn_s_setprio(0);
  } else {
    LANES();
    const int sw = wave - 4, t2 = tid - 256;
    __builtin_amdgcn_s_setprio(3);
    const bool real = hs_ != 7;
    unsigned hs_g0 = 0, hs_cur = 0;
    u32x4 ms_exp = {0, 0, 0, 0};
    bool hs_act = true, ms_act = !real && hp < 8 && rb == 0 && sw == 1;
    const unsigned sy_off = (bid & 63) * 128 + (bid >> 6) * 4;
    const unsigned hs_off = (lane == 1 ? SY_GO : SY_CH) + sy_off;
    const unsigned re_off = t2 == 0 ? SY_RE + sy_off : OOB;
    const unsigned ms_off = lane * 128;
    const char* wsu = uni(ws);
#define HS_PROBE() asm volatile("s_nop 4\n\tglobal_load_dword v156, %0, %1 sc1" :: "v"(hs_off), "s"(wsu) : "memory", RSV)
#define MS_PROBE() asm volatile("s_nop 4\n\tglobal_load_dwordx4 v[152:155], %0, %1 sc1" :: "v"(SY_RE + ms_off), "s"(wsu) : "memory", RSV)
    auto hs_step = [&](bool first) {
      if (hs_act) {
        const unsigned hv_ = rget<156>();
        hs_act = __builtin_amdgcn_readlane(hv_, 1) == hs_g0;
        const unsigned c = __builtin_amdgcn_readlane(hv_, 0);
        if (sw == 0 && (first || c != hs_cur)) STW1(hv_, re_off);
        hs_cur = c;
        HS_PROBE();
      }
      if (ms_act) {
        const unsigned d = ((rget<152>() ^ ms_exp[0]) | (rget<153>() ^ ms_exp[1])) | ((rget<154>() ^ ms_exp[2]) | (rget<155>() ^ ms_exp[3]));
        ms_act = __any(d != 0u);
        if (ms_act) MS_PROBE(); else STW(ms_exp, SY_GO + ms_off);
      }
    };
    HS_PROBE();
    if (ms_act) {
      MS_PROBE();
      asm volatile("s_nop 4\n\tglobal_load_dwordx4 v[148:151], %0, %1 sc1" :: "v"(SY_GO + ms_off), "s"(wsu) : "memory", RSV);
    }
    auto slowpf = [&]() {
      const int hq = (hp & ~3) + ((hp + 7 - hs_) & 3);
      if ((sw == 1 || sw == 2) && ((hq + 4 + (int)(((size_t)wqkv >> 7) & 7)) & 7) == 7)
        asm volatile("s_nop 4\n\tglobal_load_dword v157, %0, %1" :: "v"(min(lane, 47) * 2048), "s"(uni((const char*)wqkv + hq * 128 + (size_t)((((rb & 7) * 4 + (hp & 3) + SPX * (rb >> 3)) & 31) * 96 + (sw - 1) * 48) * 2048)) : "memory", RSV);
    };
    if (SLOWPF == 1) slowpf();
    const int gr = (g8 >> 3) * 7 + min(hs_, 6);
    const unsigned voG = (unsigned)((lane >> 3) * 8 * F_ + (lane & 7) * 8) * 2u, voD = (unsigned)((lane >> 3) * 8 * D_ + (lane & 7) * 8) * 2u;
    const size_t gofs = ((size_t)(kq * 256 + sw * 64) * F_ + gr * 64) * 2;
    const char* pG = (const char*)wg + gofs;
    const char* pU = (const char*)wu + gofs;
    const char* pD = (const char*)(wd + (size_t)(gr * 64) * D_ + kq * 256 + sw * 64);
    auto wstep = [&](auto I) {
      constexpr int i = I.v;
      if (i < 8) wload<VW + 4 * i>(voG, pG + (i & 7) * (F_ * 2));
      else if (i < 16) wload<VW + 4 * i>(voG, pU + (i & 7) * (F_ * 2));
      else wload2<VW + 4 * i>(voD, pD + (i & 7) * (D_ * 2));
    };
    unsigned voW;
    {
      const int bi = lane >> 4, q = (lane >> 2) & 3, ch = lane & 3;
      voW = (unsigned)((4 * (bi >> 1) + q) * D_ + (bi & 1) * 32 + (ch ^ (bi & 2)) * 8) * 2u;
    }
    const char* baseW = (const char*)(wo + (size_t)((16 * (sw >> 1)) * 32 + 16 * (sw & 1)) * D_ + cb * 128) - 1024;
    auto stageW = [&](auto J, bool rsv) {
      const char* s_ = baseW + (size_t)whead(J.v) * (32 * D_ * 2);
      if (rsv) glds4r<wo_slot(J.v)>(voW + 1024, voW + 128, voW + 8 * D_ * 2 - 1024, voW + 8 * D_ * 2 + 128 - 2048, s_, l4);
      else glds4<wo_slot(J.v)>(voW + 1024, voW + 128, voW + 8 * D_ * 2 - 1024, voW + 8 * D_ * 2 + 128 - 2048, s_, l4);
    };
    auto code_adr = [&]() { return (const char*)(pc0 - (pc0 & 127)) + (idx + (lane < CODE_LINES ? lane : 0) * 32) * 128; };
    int sta[2], stb[3];
    {
#pragma unroll
      for (int c = 0; c < 2; ++c) { sta[c] = (posA & 3) * K1_STAGE + (32 * sw + (lane & 15)) * 128 + (((4 * c + (lane >> 4)) ^ ((lane & 15) >> 1)) * 16); asm volatile("" : "+v"(sta[c]) :: RSV); }
#pragma unroll
      for (int i = 0; i < 3; ++i) {
        const int G = sw * 3 + i, hd = G >= 6 ? 1 : 0, mat = (G - 6 * hd) >> 1;
        stb[i] = (posB & 3) * K1_STAGE + 16384 + (lane >> 4) * 6144 + mat * 1024 + hd * 256 + ((lane & 15) >> 2) * 64 + (G & 1) * 32 + (lane & 3) * 8;
        asm volatile("" : "+v"(stb[i]) :: RSV);
      }
    }
    unsigned rs_dst = (lane >> 4) == ((lane & 15) >> 2) ? rs_slot + (32 * sw + (lane & 15)) * 8 : OOB, cs_dst = lane < 48 ? cs_slot + (sw * 48 + lane) * 4 : OOB;
    unsigned rs_off = WS_RS + rb * 16384 + (lane >> 4) * 1024 + sw * 256 + (lane & 15) * 16;
    unsigned cs_off = WS_CS + hp * 12288 + (lane >> 4) * 768 + sw * 192 + min(lane & 15, 11) * 16;
    asm volatile("" : "+v"(rs_dst), "+v"(cs_dst), "+v"(rs_off), "+v"(cs_off) :: RSV);
    int stm_dst = ((lane >> 5) * 128 + sw * 32 + (lane & 15) * 2 + ((lane >> 4) & 1)) * 4, cs_put = (lane & 15) < 12 ? (256 + sw * 48 + (lane & 15) * 4 + (lane >> 4)) * 4 : 1792 + lane * 4;
    asm volatile("" : "+v"(stm_dst), "+v"(cs_put) :: RSV);
    auto st_load = [&]() { sfor<0, 4>([&](auto I) { sload<224 + 4 * I.v>(rs_off, wsu + I.v * 4096); sload<240 + 4 * I.v>(cs_off, wsu + I.v * 3072); }); };
    sfor<3, GS[0]>(group);
    if (ms_act) {
      WAIT_VR(gops(GS[0]) - 10);
      const u32x4 re = {rget<152>(), rget<153>(), rget<154>(), rget<155>()}, g0 = {rget<148>(), rget<149>(), rget<150>(), rget<151>()};
#pragma unroll
      for (int j = 0; j < 4; ++j) { const unsigned c = re[j] + 1u; ms_exp[j] = (c == g0[j]) ? c + 1u : c; }
      STW(ms_exp, SY_CH + ms_off);
    }
    WAIT_VR(gops(GS[0]) - 10 + 1); RAW_BARR();
    if (SLOWPF == 2) slowpf();
    sfor<GS[0], GS[1]>(group);
    WAIT_VR(gops(GS[1]) - 20); RAW_BARR();
    hs_g0 = __builtin_amdgcn_readlane(rget<156>(), 1);
    if (SLOWPF == 3) slowpf();
    u32x4 ra[4] = {}; u32x2 rc[3][4] = {};
    auto chunk = [&](auto C) {
      const u32x4 ONES4 = {0x3F803F80u, 0x3F803F80u, 0x3F803F80u, 0x3F803F80u};
      const f32x4 Z4 = {0.f, 0.f, 0.f, 0.f};
      if constexpr (C.v == 0) {
        asm volatile("" : "+v"(ra[0]), "+v"(ra[1]), "+v"(ra[2]), "+v"(ra[3]) :: RSV);
        f32x4 s[2], q[2];
#pragma unroll
        for (int i = 0; i < 2; ++i) { q[i] = mfma16(ra[2 * i], ra[2 * i], Z4); s[i] = mfma16(ONES4, ra[2 * i], Z4); }
#pragma unroll
        for (int i = 0; i < 2; ++i) { q[i] = mfma16(ra[2 * i + 1], ra[2 * i + 1], q[i]); s[i] = mfma16(ONES4, ra[2 * i + 1], s[i]); }
#pragma unroll
        for (int i = 0; i < 2; ++i) {
          float a = q[i][0], c = q[i][1], d = q[i][2], e = q[i][3];
          asm volatile("" : "+v"(a), "+v"(c), "+v"(d), "+v"(e) :: RSV);
          const float lo = (lane & 1) ? c : a, hi = (lane & 1) ? e : d;
          __builtin_amdgcn_raw_buffer_store_b64((u32x2){sane1(__float_as_uint(s[i][0])), sane1(__float_as_uint((lane & 2) ? hi : lo))}, wsr, rs_dst + i * 128, 0, 16);
        }
      } else {
        f32x4 c[3];
#pragma unroll
        for (int i = 0; i < 3; ++i) { asm volatile("" : "+v"(rc[i][0]), "+v"(rc[i][1]), "+v"(rc[i][2]), "+v"(rc[i][3]) :: RSV); c[i] = mfma16(ONES4, (u32x4){rc[i][0][0], rc[i][0][1], rc[i][1][0], rc[i][1][1]}, Z4); }
#pragma unroll
        for (int i = 0; i < 3; ++i) c[i] = mfma16(ONES4, (u32x4){rc[i][2][0], rc[i][2][1], rc[i][3][0], rc[i][3][1]}, c[i]);
        float c0 = c[0][0], c1 = c[1][0], c2 = c[2][0];
        asm volatile("" : "+v"(c0), "+v"(c1), "+v"(c2) :: RSV);
        STW1(sane1(__float_as_uint(lane < 16 ? c0 : lane < 32 ? c1 : c2)), cs_dst);
      }
    };
    sfor<0, 12>([&](auto T) {
      constexpr int t = T.v;
      if (t < 4 && posA == t + 1) {
#pragma unroll
        for (int c = 0; c < 4; ++c) ra[c] = *(const u32x4*)(smem + sta[c & 1] + (c >> 1) * 2048);
      }
      if (t < 4 && posB == t + 1) {
#pragma unroll
        for (int i = 0; i < 3; ++i)
#pragma unroll
          for (int j = 0; j < 4; ++j) rc[i][j] = trread(smem + (stb[i] ^ ((j >> 1) * 32)) + (j >> 1) * 3072 + (j & 1) * 512);
      }
      if (t == HS0 - 1) HS_PROBE();
      if (t >= HS0) hs_step(t == HS0);
      sfor<GS[t + 1], GS[t + 2]>([&](auto G) {
        group(G);
        if constexpr ((t == 4 || t == 5) && G.v == GS[t + 1]) chunk(IC<t - 4>{});
      });
      if (t == CP2 && sw == 3) asm volatile("global_load_dword v157, %0, off" :: "v"(code_adr()) : "memory", RSV);
      if (real) sfor<WS_[t], WS_[t + 1]>(wstep);
      constexpr int need = t < 11 ? t + 3 : 15, nw = wyoung(t, need);
      if (nw && real) WAIT_VR(gops(GS[t + 2]) - 10 * need + nw); else WAIT_VR(gops(GS[t + 2]) - 10 * need);
      RAW_BARR();
    });
    hs_step(false);
    auto st_put = [&]() -> bool {
      float a[8]; unsigned m = 0, n = 0;
      rsum4<224>(a, m, n); rsum4<240>(a + 4, m, n);
      const float s1 = sw16(a[0], a[2]), q1 = sw16(a[1], a[3]);
      const float cs = sw32(sw16(a[4], a[5]), sw16(a[6], a[7]));
      const float mean = sw32(s1, s1) * (1.0f / 1024.0f);
      const float var = fmaxf(__builtin_fmaf(sw32(q1, q1), 1.0f / 1024.0f, -(mean * mean)), 0.f);
      const float rinv = __builtin_amdgcn_rcpf(rbf(__builtin_amdgcn_sqrtf(rbf(rbf(var) + 1e-5f))));
      *(float*)(smem + L_STM + stm_dst) = (lane & 32) ? rinv : rbf(mean);
      *(float*)(smem + L_STM + cs_put) = cs;
      return wave_ok(max(m, n));
    };
    st_load();
    if (real) { sfor<WS_[12], WS_[12] + STE>(wstep); WAIT_VR(STE); } else WAIT_VR(0);
    const bool st_fin = st_put() && !hs_act;
    if (lane == 0) { misc[8 + sw] = 0; misc[12 + sw] = st_fin; misc[16 + sw] = 0; misc[7] = 0; }
    if (real) sfor<WS_[12] + STE, W1A>(wstep);
    RAW_BARR();
    if (!st_fin) {
      if (hs_act) hs_act = __builtin_amdgcn_readlane(rget<156>(), 1) == hs_g0;
      if (ms_act) {
        SPIN {
          bool ok = true;
#pragma unroll
          for (int j = 0; j < 4; ++j) ok &= (ld32(ws, SY_RE + ms_off + j * 4) == ms_exp[j]);
          if (__all(ok)) break;
        }
        STW(ms_exp, SY_GO + ms_off);
      }
      if (hs_act) {
        SPIN {
          if (ld32(ws, SY_GO + sy_off) != hs_g0) break;
          const unsigned c = ld32(ws, SY_CH + sy_off);
          if (c != hs_cur) { hs_cur = c; STW1(c, re_off); }
        }
      }
      SPIN { st_load(); WAIT_VR(0); if (st_put()) break; }
    }
    asm volatile("" ::: "memory", RSV);
    if (lane == 0) misc[12 + sw] = 1;
    __builtin_amdgcn_s_setprio(0);
    if (real) sfor<W1A, 16>(wstep);
    auto mwait = [&](int v) { SPIN { const int a = misc[8 + (lane & 3)]; if (__all(a >= v)) break; __builtin_amdgcn_s_sleep(1); } asm volatile("" ::: "memory"); };
    sfor<0, 9>([&](auto J) {
      if (J.v == 4) mwait(3);
      stageW(J, J.v < 2);
      if (J.v >= 1) WAIT_V(4 * PACE);
      if (J.v == 1) { WAIT_VR(8); sfor<0, 16>(wtr); }
    });
    EPI();
#pragma unroll
    for (int i = 0; i < 2; ++i) { xv[i] = *(const u32x4*)(x + ego + i * 64); wv[i] = *(const u32x4*)(w_r + cb * 128 + eseg * 8 + i * 64); }
    if (CP3 && sw == 3) { unsigned d_; asm volatile("global_load_dword %0, %1, off\n\ts_waitcnt vmcnt(0)" : "=v"(d_) : "v"(code_adr()) : "memory"); }
    asm volatile("s_waitcnt vmcnt(0)" : "+v"(xv[0]), "+v"(xv[1]), "+v"(wv[0]), "+v"(wv[1]) :: "memory");
    unpack_xw();
    RAW_BAR();
    RAW_BAR(); stageW(IC<9>{}, false); stageW(IC<10>{}, false);
    RAW_BAR(); stageW(IC<11>{}, false); stageW(IC<12>{}, false);
    RAW_BAR(); stageW(IC<13>{}, false); stageW(IC<14>{}, false);
    WAIT_V(16); RAW_BAR(); stageW(IC<15>{}, false);
    WAIT_V(12); RAW_BAR();
    WAIT_V(0); RAW_BAR();
    if (real) sfor<16, 24>(wstep);
  }
  LANES(); EPI();
  const int grow = rp * 64 + erow;
  int pa0 = L_P + erow * 512 + ((2 * eseg) ^ (erow & 7)) * 16, pa1 = pa0 ^ 16;
  unsigned st_dst = eseg == 0 ? WS_ST2 + ((rp * 8 + cb) * 64 + erow) * 16 : OOB, st_src = WS_ST2 + rp * 8192 + lane * 16;
  unsigned fd_dst = lane < 8 ? WS_FD + (cb * 32 + rp) * 128 + lane * 16 : OOB, kidx = 0xFFFFu - grow;
  asm volatile("" : "+v"(pa0), "+v"(pa1), "+v"(st_dst), "+v"(st_src), "+v"(fd_dst), "+v"(kidx));
  RAW_BAR2();
  unsigned hb[8];
  f32x2 hv[8];
  {
    f32x4 pp[2][4];
#pragma unroll
    for (int k = 0; k < 2; ++k)
#pragma unroll
      for (int i = 0; i < 4; ++i) pp[k][i] = *(const f32x4*)(smem + ((i & 1) ? pa1 : pa0) + (i >> 1) * 256 + k * L_PK);
    SB0();
    f32x2 s = {0.f, 0.f}, q = {0.f, 0.f}, p2 = {0.f, 0.f};
#pragma unroll
    for (int i = 0; i < 8; ++i) {
      const f32x4 a = pp[0][i >> 1] + pp[1][i >> 1];
      const f32x2 u = xf[i] + unpk(pack2o(a[2 * (i & 1)], a[2 * (i & 1) + 1]));
      hb[i] = pack2o(u[0], u[1]);
      hv[i] = unpk(hb[i]);
      s += hv[i]; q += hv[i] * hv[i]; p2 += hv[i] * wf2[i];
    }
    STW(sane((u32x4){__float_as_uint(oct_sum(s[0] + s[1])), __float_as_uint(oct_sum(q[0] + q[1])), __float_as_uint(oct_sum(p2[0] + p2[1])), __float_as_uint(sw_)}), st_dst);
  }
  KEEP2();
  const int sw = wave - 4;
  const unsigned fd_src = WS_FD + xcd * 4096 + r * 128;
  u32x2 kpk = {SENT, SENT};
  if (wave == 0) {
    u32x4 pa[8];
    SLEEP(P_C);
    SPIN {
      unsigned m = 0;
#pragma unroll
      for (int j = 0; j < 8; ++j) { pa[j] = LDW(st_src + j * 1024); m = chk(m, pa[j]); }
      if (wave_ok(m)) break;
      KEEP2();
    }
#define F4(v) __builtin_bit_cast(f32x4, v)
    const f32x4 t = ((F4(pa[0]) + F4(pa[1])) + (F4(pa[2]) + F4(pa[3]))) + ((F4(pa[4]) + F4(pa[5])) + (F4(pa[6]) + F4(pa[7])));
    const float mean = t[0] * (1.0f / 1024.0f);
    const float var = fmaxf(__builtin_fmaf(t[1], 1.0f / 1024.0f, -(mean * mean)), 0.f);
    const float mb = rbf(mean), rs = __builtin_amdgcn_rcpf(rbf(__builtin_amdgcn_sqrtf(rbf(rbf(var) + 1e-5f))));
    *(f32x2*)(smem + L_KX + lane * 8) = (f32x2){mb, rs};
    const float sc = fmaxf((t[2] - mb * t[3]) * rs, -INFINITY);
    const int u = (int)(pack2(0.f, sc) & 0xffff0000u);
    unsigned b0 = ((unsigned)(u ^ ((u >> 31) | (int)0x80000000)) & 0xffff0000u) | (0xFFFFu - (rp * 64 + lane)), b1 = 0;
    top2_lo(b0, b1); top2_hi(b0, b1);
    STW(((u32x4){b0, b1, b0, b1}), fd_dst);
    if (lane == 0) *(u32x2*)(smem + L_MISC + 112) = (u32x2){b0, b1};
  } else if (wave >= 4) {
    asm volatile("s_waitcnt vmcnt(0)" ::: "memory", RSV2);
    sfor<16, 24>(wtr);
  }
  RAW_BAR();
  bool cand;
  u32x4 ob[2];
  {
    const f32x2 mr = *(const f32x2*)(smem + L_KX + erow * 8);
    const u32x2 kk = *(const u32x2*)(smem + L_MISC + 112);
    const float mb = mr[0], rs = mr[1];
    const unsigned b0 = kk[0], b1 = kk[1];
    u32x4 nq[2];
#pragma unroll
    for (int i = 0; i < 8; ++i) {
      const f32x2 d = hv[i] - mb;
      const f32x2 n = unpk(pack2o(d[0], d[1])) * rs;
      nq[i >> 2][i & 3] = pack2o(n[0], n[1]);
    }
    const bool c2 = (b1 & 0xffffu) == kidx;
    cand = c2 || (b0 & 0xffffu) == kidx;
    if (cand) {
      const unsigned xo = WS_XS + (rp * 2 + (c2 ? 1 : 0)) * 2048 + cb * 256 + eseg * 16;
      STW(sane(nq[0]), xo); STW(sane(nq[1]), xo + 128);
      char* cp = smem + L_CAND + (c2 ? 512 : 0) + eseg * 16;
      *(u32x4*)cp = (u32x4){hb[0], hb[1], hb[2], hb[3]}; *(u32x4*)(cp + 128) = (u32x4){hb[4], hb[5], hb[6], hb[7]};
      *(u32x4*)(cp + 256) = nq[0]; *(u32x4*)(cp + 384) = nq[1];
    }
    asm volatile("" ::: "memory");
    if (wave >= 4) { SLEEP(P_D); kpk = __builtin_amdgcn_raw_buffer_load_b64(wsr, fd_src, 0, 16); }
    asm volatile("" ::: "memory");
#pragma unroll
    for (int i = 0; i < 8; ++i) { const f32x2 o = hv[i] + unpk(nq[i >> 2][i & 3]); ob[i >> 2][i & 3] = pack2(o[0], o[1]); }
  }
#define OUT_ST() do { *(u32x4*)(out + ego) = ob[0]; *(u32x4*)(out + ego + 64) = ob[1]; } while (0)
  if (wave < 4) {
    if (!cand) OUT_ST();
    RAW_BAR();
    const unsigned w0 = misc[24], w1 = misc[25];
    if (cand && grow != (int)key_idx(w0) && grow != (int)key_idx(w1)) OUT_ST();
#pragma unroll 1
    for (int tk = 0; tk < 2; ++tk) {
      const unsigned wk = tk ? w1 : w0, slot = misc[26 + tk];
      if ((int)(slot >> 1) != rp) continue;
      u32x4 pv[7];
      const unsigned yo = WS_YP + ((tk * 8 + cb) * 64 + (lane >> 3) * 8) * 512 + wave * 128 + (lane & 7) * 16;
      const int col = (wave >> 1) * 64 + (lane & 7) * 8 + (wave & 1) * 4 + (lane >> 4);
      float sg = rbf(__builtin_amdgcn_rcpf(1.0f + __expf(-key_val(wk))));
      float hh = bflo(*(const u16*)(smem + L_CAND + (slot & 1) * 512 + col * 2)), nn = bflo(*(const u16*)(smem + L_CAND + (slot & 1) * 512 + 256 + col * 2));
      u16* op = out + (size_t)key_idx(wk) * D_ + cb * 128 + col;
      asm volatile("" : "+v"(sg), "+v"(hh), "+v"(nn), "+v"(op));
      if (tk == 0 || (int)(misc[26] >> 1) != rp) {
        SPIN { if (misc[30] == (int)w0) break; __builtin_amdgcn_s_sleep(1); }
        SLEEP(P_E);
      }
      SPIN {
        unsigned m = 0;
#pragma unroll
        for (int p = 0; p < 7; ++p) { pv[p] = LDW(yo + p * 512); m = chk(m, pv[p]); }
        if (wave_ok(m)) break;
        asm volatile("" ::: "memory");
      }
      f32x4 s = __builtin_bit_cast(f32x4, pv[0]);
#pragma unroll
      for (int p = 1; p < 7; ++p) s += __builtin_bit_cast(f32x4, pv[p]);
      float y = sw32(sw16(s[0], s[1]), sw16(s[2], s[3]));
      y += dppf<DPP_ROR8>(y);
      if (!(lane & 8)) *(GAS u16*)op = (u16)pack2(hh + rbf(nn + rbf(rbf(y) * sg)), 0.f);
    }
  } else {
    unsigned xl = WS_XS + (kq * 256 + sw * 64 + (lane >> 4) * 16 + ((lane >> 2) & 1) * 8) * 2;
    int red_dst = (lane < 8 || (lane >= 24 && lane < 32)) ? L_RED + sw * 2048 + ((lane >> 3) & 1) * 1024 + (lane & 7) * 16 : L_RED + 8192 + lane * 16;
    unsigned gu_src = WS_GU + g8 * 4096 + lane * 16;
    int act_dst = L_ACT + sw * 256 + ((lane & 7) * 8 + (lane >> 3)) * 2, act_src = L_ACT + sw * 256 + (lane & 1) * 128 + (lane >> 4) * 32 + ((lane >> 1) & 1) * 16;
    unsigned yp_dst = lane < 16 ? WS_YP + (((lane >> 3) * 8 + 2 * kq + (sw >> 1)) * 64 + g8) * 512 + (sw & 1) * 256 + (lane & 7) * 16 : OOB;
    asm volatile("" : "+v"(xl), "+v"(red_dst), "+v"(gu_src), "+v"(act_dst), "+v"(act_src), "+v"(yp_dst));
    unsigned w0, w1; int s0 = -1, s1 = -1;
    u32x4 xa = {SENT, SENT, SENT, SENT};
    SPIN {
      const bool all = wave_ok(kpk[1]);
      const unsigned k0 = kpk[1] == SENT ? 0u : kpk[0], k1 = kpk[1] == SENT ? 0u : kpk[1];
      w0 = k0; w1 = k1;
      top2_lo(w0, w1); top2_mid(w0, w1);
      const int n0 = 2 * __builtin_ctzll(__ballot(k0 == w0));
      const u64 m1 = __ballot(k0 == w1);
      const int n1 = m1 ? 2 * __builtin_ctzll(m1) : n0 + 1;
      const bool same = n0 == s0 && n1 == s1;
      s0 = n0; s1 = n1;
      asm volatile("" ::: "memory");
      if (all && same) break;
      if (!all) { kpk = __builtin_amdgcn_raw_buffer_load_b64(wsr, fd_src, 0, 16); }
      xa = LDW(xl + ((lane & 1) ? s1 : s0) * 2048);
      if (all) break;
    }
    SPIN {
      if (wave_ok(chk(0u, xa))) break;
      asm volatile("" ::: "memory");
      xa = LDW(xl + ((lane & 1) ? s1 : s0) * 2048);
    }
    {
      const u32x4 z4 = {0u, 0u, 0u, 0u};
      const u32x4 xg = (lane & 2) ? z4 : xa, xu = (lane & 2) ? xa : z4;
      f32x4 d[8];
#pragma unroll
      for (int c = 0; c < 8; ++c) d[c] = mfma16(xg, wf[c], (f32x4){0.f, 0.f, 0.f, 0.f});
      if (tid == 256) { *(u32x4*)(smem + L_MISC + 96) = (u32x4){w0, w1, (unsigned)s0, (unsigned)s1}; misc[30] = 0; }
      SB0();
#pragma unroll
      for (int c = 0; c < 10; ++c) {
        if (c < 8) d[c & 7] = mfma16(xu, wf[8 + (c & 7)], d[c & 7]);
        if (c >= 2) *(f32x4*)(smem + red_dst + (c - 2) * 128) = d[(c - 2) & 7];
        SB0();
      }
    }
    RAW_BAR();
    if (sw == 0) {
      f32x4 t[8];
#pragma unroll
      for (int p = 0; p < 8; ++p) t[p] = *(const f32x4*)(smem + L_RED + p * 1024 + lane * 16);
      SB0();
      STW(sane(__builtin_bit_cast(u32x4, ((t[0] + t[1]) + (t[2] + t[3])) + ((t[4] + t[5]) + (t[6] + t[7])))), gu_src + kq * 1024);
    } else __builtin_amdgcn_s_sleep(2);
    {
      u32x4 q[4];
      if (grow != (int)key_idx(w0) && grow != (int)key_idx(w1)) OUT_ST();
      asm volatile("" ::: "memory");
      SLEEP(P_GU);
      SPIN {
        unsigned m = 0;
#pragma unroll
        for (int j = 0; j < 4; ++j) { q[j] = LDW(gu_src + j * 1024); m = chk(m, q[j]); }
        if (wave_ok(m)) break;
        asm volatile("" ::: "memory");
      }
      const f32x4 tot = (__builtin_bit_cast(f32x4, q[0]) + __builtin_bit_cast(f32x4, q[1])) + (__builtin_bit_cast(f32x4, q[2]) + __builtin_bit_cast(f32x4, q[3]));
      const f32x2 gs = unpk(pack2o(tot[0], tot[1])), us = unpk(pack2o(tot[2], tot[3]));
      const f32x2 sg = unpk(pack2o(gs[0] * __builtin_amdgcn_rcpf(1.0f + __expf(-gs[0])), gs[1] * __builtin_amdgcn_rcpf(1.0f + __expf(-gs[1]))));
      const unsigned a2 = pack2o(sg[0] * us[0], sg[1] * us[1]);
      *(u16*)(smem + act_dst) = (u16)a2; *(u16*)(smem + act_dst + 128) = (u16)(a2 >> 16);
      __builtin_amdgcn_fence(__ATOMIC_RELEASE, "wavefront"); __builtin_amdgcn_wave_barrier(); __builtin_amdgcn_fence(__ATOMIC_ACQUIRE, "wavefront");
      const u32x4 aa = *(const u32x4*)(smem + act_src);
      f32x4 d[8];
#pragma unroll
      for (int c = 0; c < 8; ++c) d[c] = mfma16(aa, wf[16 + c], (f32x4){0.f, 0.f, 0.f, 0.f});
      SB0();
      u32x4 y[2];
#pragma unroll
      for (int c = 0; c < 8; ++c) {
        const float t0 = d[c][0] + dppf<DPP_ROR8>(d[c][2]), t1 = d[c][3] + dppf<DPP_ROR8>(d[c][1]);
        y[c >> 2][c & 3] = __float_as_uint((lane & 8) ? t1 : t0);
      }
      STW(sane(y[0]), yp_dst); STW(sane(y[1]), yp_dst + 128);
      if (tid == 256) misc[30] = w0;
    }
  }
}

extern "C" void kernel_launch(void* const* d_in, const int* in_sizes, int n_in, void* d_out, int out_size,
                              void* d_ws, size_t ws_size, hipStream_t stream) {
  (void)hipFuncSetAttribute((const void*)fused, hipFuncAttributeMaxDynamicSharedMemorySize, LDS_TOTAL);
  fused<<<256, 512, LDS_TOTAL, stream>>>((const u16*)d_in[0], (const u16*)d_in[1], (const u16*)d_in[2], (const u16*)d_in[4],
                                         (const u16*)d_in[5], (const u16*)d_in[6], (const u16*)d_in[7], (u16*)d_out, (char*)d_ws);
}
```

```cpp
#include <hip/hip_runtime.h>
#include <stdint.h>

#define LAS __attribute__((address_space(3)))
#define GAS __attribute__((address_space(1)))
#define DI __device__ __forceinline__

typedef unsigned short u16;
typedef unsigned long long u64;
typedef __attribute__((ext_vector_type(2))) __bf16 bf16x2;
typedef __attribute__((ext_vector_type(8))) __bf16 bf16x8;
typedef __attribute__((ext_vector_type(4))) short s16x4;
typedef __attribute__((ext_vector_type(2))) unsigned short u16x2;
typedef __attribute__((ext_vector_type(4))) float f32x4;
typedef __attribute__((ext_vector_type(2))) float f32x2;
typedef __attribute__((ext_vector_type(4))) unsigned u32x4;
typedef __attribute__((ext_vector_type(2))) unsigned u32x2;
typedef GAS unsigned gu32;
typedef GAS unsigned long long gu64;

constexpr int S_ = 2048, D_ = 1024, F_ = 3584;

constexpr unsigned SY_CH = 81920, SY_GO = SY_CH + 16, SY_RE = SY_CH + 8192;
constexpr unsigned WS_FD  = 98304;
constexpr unsigned WS_ZF  = 8192;
constexpr unsigned WS_KVF = 16384;
constexpr unsigned WS_RS  = 131072;
constexpr unsigned WS_CS  = WS_RS + 16 * 16 * 1024;
constexpr unsigned WS_ZP  = WS_CS + 16 * 16 * 768;
constexpr unsigned WS_FB  = WS_ZP + 32 * 16 * 128;
constexpr unsigned WS_ST2 = WS_FB + 32 * 32 * 128;
constexpr unsigned WS_XS  = WS_ST2 + 32 * 8 * 1024;
constexpr unsigned WS_GU  = WS_XS + 64 * 2048;
constexpr unsigned WS_YP  = WS_GU + 256 * 1024;
constexpr unsigned WS_KVP = WS_YP + 2 * 8 * 64 * 512;
constexpr unsigned WS_O   = WS_KVP + 32 * 16 * 4096;
constexpr unsigned WS_END = WS_O + S_ * D_ * 2;
constexpr unsigned OOB = 0x80000000u;

template <int V> struct IC { static constexpr int v = V; };
template <int B, int E, class F> DI void sfor(F&& f) { if constexpr (B < E) { f(IC<B>{}); sfor<B + 1, E>(f); } }

DI float bflo(unsigned u) { return __uint_as_float(u << 16); }
DI float bfhi(unsigned u) { return __uint_as_float(u & 0xffff0000u); }
DI float rbf(float x) { return (float)(__bf16)x; }
DI unsigned pack2(float a, float b) { bf16x2 c; c[0] = (__bf16)a; c[1] = (__bf16)b; return __builtin_bit_cast(unsigned, c); }
DI unsigned pack2o(float a, float b) { unsigned p = pack2(a, b); asm("" : "+v"(p)); return p; }
DI f32x4 mfma16(u32x4 a, u32x4 b, f32x4 c) {
  return __builtin_amdgcn_mfma_f32_16x16x32_bf16(__builtin_bit_cast(bf16x8, a), __builtin_bit_cast(bf16x8, b), c, 0, 0, 0);
}
DI void mfma16i(u32x4 a, u32x4 b, f32x4& c) { asm volatile("v_mfma_f32_16x16x32_bf16 %0, %1, %2, %0" : "+v"(c) : "v"(a), "v"(b)); }
DI f32x2 unpk(unsigned u) { return (f32x2){bflo(u), bfhi(u)}; }
DI u32x2 trread(const char* p) {
  s16x4 v = __builtin_amdgcn_ds_read_tr16_b64_v4i16((LAS s16x4*)p);
  return __builtin_bit_cast(u32x2, v);
}
DI const char* uni(const void* p) {
  const u64 v = (u64)p;
  return (const char*)(((u64)(unsigned)__builtin_amdgcn_readfirstlane((int)(v >> 32)) << 32) | (unsigned)__builtin_amdgcn_readfirstlane((int)v));
}
#define RV4(a, b, c, d) "v" #a, "v" #b, "v" #c, "v" #d
#define RV10(p) "v" #p "0", "v" #p "1", "v" #p "2", "v" #p "3", "v" #p "4", "v" #p "5", "v" #p "6", "v" #p "7", "v" #p "8", "v" #p "9"
#define RSV2 RV4(224, 225, 226, 227), "v228", "v229", RV10(23), RV10(24), RV4(250, 251, 252, 253), "v254", "v255"
#define RSV1 RV4(192, 193, 194, 195), RV4(196, 197, 198, 199), RV10(20), RV10(21), RV4(220, 221, 222, 223), RSV2
#define RSV "v148", "v149", RV10(15), RV10(16), RV10(17), RV10(18), "v190", "v191", RSV1
#define NTW " nt"
#define GL_(v, s, o) "global_load_lds_dwordx4 " v ", " s " offset:" #o
#define GLDS4(SC, CL...) asm volatile("s_add_u32 m0, %5, %c6\n\ts_nop 3\n\t" GL_("%0", "%4", 0) SC "\n\t" GL_("%1", "%4", 1024) SC "\n\t" \
  GL_("%2", "%4", 2048) SC "\n\t" GL_("%3", "%4", 3072) SC :: "v"(v0), "v"(v1), "v"(v2), "v"(v3), "s"(uni(sbase)), "s"(lds), "i"(IMM) : "memory", "scc", ##CL)
#define GLDS3(SC, CL...) asm volatile("s_add_u32 m0, %4, %c5\n\ts_nop 3\n\t" GL_("%0", "%3", 0) SC "\n\t" GL_("%1", "%3", 1024) SC "\n\t" \
  GL_("%2", "%3", 2048) SC :: "v"(v0), "v"(v1), "v"(v2), "s"(uni(sbase)), "s"(lds), "i"(IMM) : "memory", "scc", ##CL)
template <int IMM> DI void glds4(unsigned v0, unsigned v1, unsigned v2, unsigned v3, const void* sbase, unsigned lds) { GLDS4(""); }
template <int IMM> DI void glds4r(unsigned v0, unsigned v1, unsigned v2, unsigned v3, const void* sbase, unsigned lds) { GLDS4("", RSV); }
template <int IMM> DI void glds3r(unsigned v0, unsigned v1, unsigned v2, const void* sbase, unsigned lds) { GLDS3("", RSV); }
template <int R> DI void sload(unsigned voff, const void* sbase) {
  asm volatile("s_nop 4\n\tglobal_load_dwordx4 v[%c0:%c1], %2, %3 sc1" ::"i"(R), "i"(R + 3), "v"(voff), "s"(uni(sbase)) : "memory", RSV);
}
DI float fadd(float a, float b) { float o; asm("v_add_f32 %0, %1, %2" : "=v"(o) : "v"(a), "v"(b)); return o; }
#define RA_(o, a, b) "v_add_f32 %" #o ", v[%c6+" #a "], v[%c6+" #b "]\n\t"
#define RB2_(o, a) "v_add_f32 %" #o ", v[%c6+" #a "], %" #o "\n\t"
#define RM_(o, a, b) "v_max3_u32 %" #o ", v[%c6+" #a "], v[%c6+" #b "], %" #o "\n\t"
template <int R> DI void rsum4(float* a, unsigned& m, unsigned& n) {
  asm volatile(RA_(0, 0, 4) RA_(1, 1, 5) RM_(4, 0, 1) RA_(2, 2, 6) RA_(3, 3, 7) RM_(5, 2, 3) RB2_(0, 8) RM_(4, 4, 5) RB2_(1, 9) RM_(5, 6, 7)
               RB2_(2, 10) RM_(4, 8, 9) RB2_(3, 11) RM_(5, 10, 11) RB2_(0, 12) RB2_(1, 13) RM_(4, 12, 13) RB2_(2, 14) RB2_(3, 15) RM_(5, 14, 15)
               : "=&v"(a[0]), "=&v"(a[1]), "=&v"(a[2]), "=&v"(a[3]), "+v"(m), "+v"(n) : "i"(R) : RSV);
}
template <int R> DI void wload2(unsigned voff, const void* sbase) {
  asm volatile("s_nop 4\n\tglobal_load_dwordx4 v[%c0:%c1], %2, %3" NTW ::"i"(R), "i"(R + 3), "v"(voff), "s"(uni(sbase)) : "memory", RSV2);
}
template <int R> DI void wload(unsigned voff, const void* sbase) {
  asm volatile("s_nop 4\n\tglobal_load_dwordx4 v[%c0:%c1], %2, %3" NTW ::"i"(R), "i"(R + 3), "v"(voff), "s"(uni(sbase)) : "memory", RSV);
}
template <int RA, int RB> DI unsigned wperm(unsigned sel) {
  unsigned o;
  if (RA < 192) asm volatile("v_perm_b32 %0, v%c2, v%c1, %3" : "=v"(o) : "i"(RA), "i"(RB), "s"(sel) : RSV);
  else if (RA < 224) asm volatile("v_perm_b32 %0, v%c2, v%c1, %3" : "=v"(o) : "i"(RA), "i"(RB), "s"(sel) : RSV1);
  else asm volatile("v_perm_b32 %0, v%c2, v%c1, %3" : "=v"(o) : "i"(RA), "i"(RB), "s"(sel) : RSV2);
  return o;
}
template <int R> DI unsigned rget() { unsigned o; asm volatile("v_mov_b32 %0, v%c1" : "=v"(o) : "i"(R) : RSV); return o; }
DI float xh_sum(float v) {
  auto r = __builtin_amdgcn_permlane32_swap(__float_as_uint(v), __float_as_uint(v), false, false);
  return __uint_as_float(r[0]) + __uint_as_float(r[1]);
}
template <int CTRL> DI unsigned dppu(unsigned v) { return (unsigned)__builtin_amdgcn_update_dpp(0, (int)v, CTRL, 0xf, 0xf, true); }
template <int CTRL> DI float dppf(float v) { return __uint_as_float(dppu<CTRL>(__float_as_uint(v))); }
constexpr int DPP_X1 = 0xB1, DPP_X2 = 0x4E, DPP_HMIR = 0x141, DPP_MIR = 0x140, DPP_ROR8 = 0x128;
DI float quad_sum(float v) { v += dppf<DPP_X1>(v); v += dppf<DPP_X2>(v); return v; }
DI float oct_sum(float v) { v = quad_sum(v); v += dppf<DPP_HMIR>(v); return v; }
DI float x16_sum(float v) {
  auto r = __builtin_amdgcn_permlane16_swap(__float_as_uint(v), __float_as_uint(v), false, false);
  return __uint_as_float(r[0]) + __uint_as_float(r[1]);
}
DI float sw16(float a, float b) {
  auto r = __builtin_amdgcn_permlane16_swap(__float_as_uint(a), __float_as_uint(b), false, false);
  return fadd(__uint_as_float(r[0]), __uint_as_float(r[1]));
}
DI float sw32(float a, float b) {
  auto r = __builtin_amdgcn_permlane32_swap(__float_as_uint(a), __float_as_uint(b), false, false);
  return fadd(__uint_as_float(r[0]), __uint_as_float(r[1]));
}
template <int N> struct Phi { unsigned tb[N]; f32x2 tr[N], e[N]; };
template <int N> DI void phi_a(const f32x2* t, Phi<N>& s) {
#pragma unroll
  for (int i = 0; i < N; ++i) s.tb[i] = pack2o(t[i][0], t[i][1]);
  __builtin_amdgcn_sched_barrier(0);
#pragma unroll
  for (int i = 0; i < N; ++i) s.tr[i] = unpk(s.tb[i]);
  __builtin_amdgcn_sched_barrier(0);
#pragma unroll
  for (int i = 0; i < N; ++i) s.e[i] = s.tr[i] * 1.44269504088896340736f;
  __builtin_amdgcn_sched_barrier(0);
}
template <int N> DI void phi_b(Phi<N>& s, unsigned* p) {
  u16x2 tm[2] = {{0, 0}, {0, 0}};
#pragma unroll
  for (int i = 0; i < N; ++i) {
    s.e[i][0] = __builtin_amdgcn_fmed3f(__builtin_amdgcn_exp2f(s.e[i][0]), 0.f, 1.f);
    tm[i & 1] = __builtin_elementwise_max(tm[i & 1], __builtin_bit_cast(u16x2, s.tb[i]));
    s.e[i][1] = __builtin_amdgcn_fmed3f(__builtin_amdgcn_exp2f(s.e[i][1]), 0.f, 1.f);
    __builtin_amdgcn_sched_barrier(0);
  }
#pragma unroll
  for (int i = 0; i < N; ++i) s.tr[i] += s.e[i];
  __builtin_amdgcn_sched_barrier(0);
#pragma unroll
  for (int i = 0; i < N; ++i) { s.tr[i][0] = fmaxf(s.tr[i][0], s.e[i][0]); s.tr[i][1] = fmaxf(s.tr[i][1], s.e[i][1]); }
  const u16x2 tx = __builtin_elementwise_max(tm[0], tm[1]);
  __builtin_amdgcn_sched_barrier(0);
#pragma unroll
  for (int i = 0; i < N; ++i) p[i] = pack2(s.tr[i][0], s.tr[i][1]);
  if (__any(tx[0] > 0xC0F0 || tx[1] > 0xC0F0)) {
#pragma unroll
    for (int i = 0; i < N; ++i) p[i] = pack2(bflo(p[i]) + 1e-6f, bfhi(p[i]) + 1e-6f);
  }
}
template <int N> DI void phi_n(const f32x2* t, unsigned* p) { Phi<N> s; phi_a<N>(t, s); phi_b<N>(s, p); }
#define RLX_AGENT __ATOMIC_RELAXED, __HIP_MEMORY_SCOPE_AGENT
DI void st64(char* ws, unsigned off, u64 v) { __hip_atomic_store((gu64*)(ws + off), v, RLX_AGENT); }
DI unsigned ld32(char* ws, unsigned off) { return __hip_atomic_load((gu32*)(ws + off), RLX_AGENT); }
#define WAIT_V(n) asm volatile("s_waitcnt vmcnt(%c0)" ::"i"(n) : "memory")
#define RAW_BAR() do { asm volatile("s_waitcnt lgkmcnt(0)" ::: "memory"); __builtin_amdgcn_s_barrier(); asm volatile("" ::: "memory"); } while (0)
#define SB0() __builtin_amdgcn_sched_barrier(0)
#define WAIT_VR(n) asm volatile("s_waitcnt vmcnt(%c0)" ::"i"(n) : "memory", RSV)
#define RAW_BARR() do { asm volatile("s_waitcnt lgkmcnt(0)" ::: "memory", RSV); __builtin_amdgcn_s_barrier(); asm volatile("" ::: "memory", RSV); } while (0)
#define KEEP2() asm volatile("" ::: "memory", RSV2)
#define RAW_BAR2() do { asm volatile("s_waitcnt lgkmcnt(0)" ::: "memory", RSV2); __builtin_amdgcn_s_barrier(); KEEP2(); } while (0)
constexpr unsigned SPIN_CAP = 1u << 18;
#define SPIN _Pragma("nounroll") for (unsigned s_ = 0; s_ < SPIN_CAP; ++s_)
constexpr unsigned SENT = 0xFFFFFFFFu;
DI unsigned sane1(unsigned v) { return min(v, 0xFFFFFFFEu); }
DI u32x4 sane(u32x4 v) { v[1] = sane1(v[1]); v[3] = sane1(v[3]); return v; }
DI unsigned chk(unsigned m, u32x4 v) { return max(max(m, v[1]), v[3]); }
DI bool wave_ok(unsigned m) { return !__any(m == SENT); }
DI unsigned key_idx(unsigned k) { return (0xFFFFu - (k & 0xFFFFu)) & (S_ - 1); }
DI float key_val(unsigned k) { const unsigned o = k >> 16; return __uint_as_float(((o & 0x8000u) ? (o & 0x7fffu) : (~o & 0xffffu)) << 16); }
DI void top2_mix(unsigned& b0, unsigned& b1, unsigned c0, unsigned c1) {
  const unsigned lo = min(b0, c0), mx = max(b1, c1);
  b0 = max(b0, c0); b1 = max(lo, mx);
}
DI void top2_lo(unsigned& b0, unsigned& b1) {
  top2_mix(b0, b1, dppu<DPP_X1>(b0), dppu<DPP_X1>(b1));
  top2_mix(b0, b1, dppu<DPP_X2>(b0), dppu<DPP_X2>(b1));
  top2_mix(b0, b1, dppu<DPP_HMIR>(b0), dppu<DPP_HMIR>(b1));
}
DI void top2_mid(unsigned& b0, unsigned& b1) {
  top2_mix(b0, b1, dppu<DPP_MIR>(b0), dppu<DPP_MIR>(b1));
  auto p = __builtin_amdgcn_permlane16_swap(b0, b0, false, false); auto q = __builtin_amdgcn_permlane16_swap(b1, b1, false, false);
  b0 = p[0]; b1 = q[0]; top2_mix(b0, b1, p[1], q[1]);
}
DI void top2_hi(unsigned& b0, unsigned& b1) {
  top2_mid(b0, b1);
  auto p = __builtin_amdgcn_permlane32_swap(b0, b0, false, false); auto q = __builtin_amdgcn_permlane32_swap(b1, b1, false, false);
  b0 = p[0]; b1 = q[0]; top2_mix(b0, b1, p[1], q[1]);
}

constexpr int K1_STAGE = 40960;
constexpr int LDS_TOTAL = 4 * K1_STAGE;
constexpr int L_STM = 0;
constexpr int L_MISC = 2048;
constexpr int L_XK = 5120;
constexpr int L_KX = 3328;
constexpr int L_ACT = 2304;
constexpr int L_CAND = 4096;
constexpr int L_WO = 16384;
constexpr int L_P = 16384, L_PK = 7 * 16384;
constexpr int L_RED = 65536;
constexpr int wo_slot(int j) { return L_WO + (j % 9) * 16384; }
#define CODE_NL 384
constexpr int CODE_LINES = CODE_NL;
#define OW_ 8
constexpr int OW = OW_;
#define W1A_ 16
constexpr int W1A = W1A_;
#define GSCHED 5, 9, 13, 17, 21, 24, 27, 30, 31, 35, 39, 41, 45, 48, 48
constexpr int GS[15] = {GSCHED};
constexpr int gops(int g) { return (g / 3) * 10 + (g % 3 == 0 ? 0 : g % 3 == 1 ? 4 : 7); }
constexpr bool gs_ok() {
  bool ok = GS[0] >= 3 && GS[1] >= 6 && GS[1] <= 9 && GS[0] <= GS[1] && GS[12] == 45 && GS[13] == 48 && GS[14] == 48;
  for (int t = 0; t < 12; ++t) ok = ok && GS[t + 2] <= 3 * (t + 5) && GS[t + 2] >= 3 * (t + 3) && GS[t + 2] >= GS[t + 1] + 1;
  return ok;
}
static_assert(gs_ok(), "staging schedule");
#define WSCHED 0, 0, 0, 0, 0, 0, 5, 5, 5, 10, 10, 10, 10
constexpr int WS_[13] = {WSCHED};
constexpr int wyoung(int t, int stages) {
  int e = 0;
  while (GS[e + 2] < 3 * stages) ++e;
  return WS_[t + 1] - WS_[e];
}
#define PACE 1
#define P_KVP 1
#define P_KVF 1
#define P_FB 8
#define P_C 14
#define P_D 2
#define P_GU 12
#define P_E 10
#define SLEEP(n) do { if (n) __builtin_amdgcn_s_sleep(n); } while (0)
#define STE 6
#define HS0 1
#define CP2 10
constexpr int VW = 160;

__global__ __launch_bounds__(512, 2) void fused(const u16* __restrict__ x, const u16* __restrict__ wqkv,
                                                const u16* __restrict__ wo, const u16* __restrict__ w_r,
                                                const u16* __restrict__ wg, const u16* __restrict__ wu,
                                                const u16* __restrict__ wd, u16* __restrict__ out, char* __restrict__ ws) {
  extern __shared__ __attribute__((aligned(16))) char smem[];
  u64 pc0; asm volatile("s_getpc_b64 %0" : "=s"(pc0));
  const int wave = __builtin_amdgcn_readfirstlane(threadIdx.x >> 6);
  const unsigned lds0 = (unsigned)(size_t)smem;
  if (wave == 0) {
    const int l = threadIdx.x;
    asm volatile("s_mov_b32 m0, %1\n\ts_nop 0\n\tglobal_load_lds_dword %0, off"
                 :: "v"((const char*)(pc0 - (pc0 & 127)) + ((blockIdx.x >> 3) + (l < CODE_LINES / 32 ? l : 0) * 32) * 128), "s"(__builtin_amdgcn_readfirstlane(lds0 + 3 * K1_STAGE)) : "memory");
  }
#define RB_(b) (((b) & 1) * 8 + (((b) >> 3) & 7))
#define HP_(b) ((((b) & 7) >> 1) * 4 + ((b) >> 6))
#define KROT_(b) ((4 * ((HP_(b) >> 2) + (RB_(b) >> 3) + 2 * ((RB_(b) >> 2) & 1))) & 15)
  unsigned voA0, voA1, voA2, voA3, voB0, voB1, voB2, voB3, voB4, voB5;
  const unsigned l4 = __builtin_amdgcn_readfirstlane(lds0 + (wave - 4) * 4096), l6 = __builtin_amdgcn_readfirstlane(lds0 + (wave - 4) * 6144);
  const int krot0 = KROT_(blockIdx.x);
  auto group = [&](auto G) {
    constexpr int t = G.v / 3, p = G.v % 3, sl = (t & 3) * K1_STAGE;
    const int kt = (t + krot0) & 15;
    if (p == 0) glds4r<sl>(voA0, voA1, voA2, voA3, (const char*)x + kt * 128, l4);
    else if (p == 1) glds3r<sl + 16384>(voB0, voB1, voB2, (const char*)wqkv + kt * 64 * (3 * D_ * 2), l6);
    else glds3r<sl + 16384 + 3072>(voB3, voB4, voB5, (const char*)wqkv + kt * 64 * (3 * D_ * 2), l6);
  };
  if (wave >= 4) {
    const int lane = threadIdx.x & 63, sw = wave - 4;
    const int r0 = RB_(blockIdx.x) * 128 + 32 * sw + (lane >> 3);
    voA0 = (unsigned)(r0 * D_ + ((lane & 7) ^ (lane >> 4)) * 8) * 2u;
    voA1 = ((voA0 + 8 * D_ * 2) ^ 64) - 1024; voA2 = voA0 + 16 * D_ * 2 - 2048; voA3 = voA1 + 16 * D_ * 2 - 2048;
    asm volatile("" : "+v"(voA0), "+v"(voA1), "+v"(voA2), "+v"(voA3));
    group(IC<0>{});
    int l2 = lane; asm volatile("" : "+v"(l2));
    voB0 = (unsigned)((16 * sw + 4 * (l2 >> 5) + ((l2 >> 2) & 3)) * (3 * D_) + HP_(blockIdx.x) * 64 + ((l2 >> 4) & 1) * 32 + (l2 & 3) * 8) * 2u;
    voB1 = voB0 + 1024; voB2 = voB0 + 2048;
    voB3 = (voB0 ^ 32) + 8 * 3 * D_ * 2; voB4 = voB3 + 1024; voB5 = voB3 + 2048;
    asm volatile("" : "+v"(voB0), "+v"(voB1), "+v"(voB2), "+v"(voB3), "+v"(voB4), "+v"(voB5));
    sfor<1, 3>(group);
  }
  int bid = blockIdx.x; asm volatile("" : "+s"(bid));
  const int xcd = bid & 7, idx = bid >> 3;
  const int rb = RB_(bid), hp = HP_(bid);
  const int rp = xcd * 4 + (idx >> 3), cb = idx & 7;
  const int hs_ = (hp + 4 + (int)(((size_t)wqkv >> 7) & 7)) & 7;
  const int g8 = ((hp >> 3) * 4 + (rb >> 2)) * 8 + hs_, kq = rb & 3;
  const int krot = KROT_(bid), posA = 1 + (hp & 3), posB = 1 + (rb & 3);
  const int wrot = 4 * rp + (xcd >> 1), wdir = (xcd & 1) ? -1 : 1;
  auto whead = [&](int t) { return (wrot + wdir * t) & 15; };
  const unsigned rs_slot = WS_RS + (rb * 16 + ((krot + posA) & 15)) * 1024, cs_slot = WS_CS + (hp * 16 + ((krot + posB) & 15)) * 768;
  const __amdgpu_buffer_rsrc_t wsr = __builtin_amdgcn_make_buffer_rsrc(ws, 0, WS_END, 0x00020000);
#define LDW(off) __builtin_amdgcn_raw_buffer_load_b128(wsr, (off), 0, 16)
#define STW(v, off) __builtin_amdgcn_raw_buffer_store_b128((v), wsr, (off), 0, 16)
#define STW1(v, off) __builtin_amdgcn_raw_buffer_store_b32((v), wsr, (off), 0, 16)
  float* stm = (float*)(smem + L_STM);
  volatile LAS int* misc = (volatile LAS int*)(smem + L_MISC);
#define LANES() int tid = threadIdx.x; asm volatile("" : "+v"(tid)); const int lane = tid & 63, r = lane & 31, h = lane >> 5; (void)r; (void)h
#define EPI() const int erow = tid >> 3, eseg = tid & 7; const size_t ego = (size_t)(rp * 64 + erow) * D_ + cb * 128 + eseg * 8
  u32x4 xv[2], wv[2];
  f32x2 xf[8], wf2[8];
  float sw_;
  auto unpack_xw = [&]() {
    f32x2 a = {0.f, 0.f};
#pragma unroll
    for (int i = 0; i < 8; ++i) { xf[i] = unpk(xv[i >> 2][i & 3]); wf2[i] = unpk(wv[i >> 2][i & 3]); a += wf2[i]; }
    sw_ = oct_sum(a[0] + a[1]);
#pragma unroll
    for (int i = 0; i < 8; ++i) asm volatile("" : "+v"(xf[i]), "+v"(wf2[i]));
    asm volatile("" : "+v"(sw_));
  };
  u32x4 wf[24];

  auto wtr = [&](auto I) {
    constexpr int m = I.v >> 3, c = I.v & 7, R = VW + 32 * m + (c >> 1);
    const unsigned sel = (c & 1) ? 0x07060302u : 0x05040100u;
    wf[I.v] = (u32x4){wperm<R, R + 4>(sel), wperm<R + 8, R + 12>(sel), wperm<R + 16, R + 20>(sel), wperm<R + 24, R + 28>(sel)};
  };

  if (wave < 4) {
    LANES();
    const int c16 = lane & 15, g4 = lane >> 4;
    const int trl = (c16 >> 2) * 64 + (c16 & 3) * 8;
    const int wr = wave >> 1, wc = wave & 1;
    const int head = 2 * hp + wc;
    const unsigned kvp_off = WS_KVP + (head * 16 + rb) * 4096 + wr * 2048 + lane * 16;
    const unsigned zp_off = WS_ZP + (head * 16 + rb) * 128;
    const unsigned o_off = WS_O + ((rb * 4 + 2 * wr) * 32 + head) * 2048 + lane * 16;
    const unsigned fb_off = lane < 8 ? WS_FB + ((rb * 2 + wr) * 32 + head) * 128 + lane * 16 : OOB;
    unsigned pfa, pfb = OOB;
    unsigned pfc = lane < 8 ? fb_off : lane < 24 ? rs_slot + wave * 256 + (lane - 8) * 16
        : lane < 40 ? (wave < 3 ? cs_slot + wave * 256 : WS_KVF + hp * 4096 + rb * 256) + (lane - 24) * 16
        : lane < 48 ? (wave < 2 ? zp_off + (lane & 7) * 16 : (wave == 2 && rb < 2) ? WS_ZF + (2 * hp + rb) * 128 + (lane & 7) * 16 : OOB) : OOB;
    if (wave == 0) pfa = WS_ST2 + (rp * 8 + cb) * 1024 + lane * 16;
    else if (wave == 1) pfa = WS_GU + (g8 * 4 + kq) * 1024 + lane * 16;
    else pfa = WS_YP + (((wave - 2) * 8 + 2 * kq + (lane >> 5)) * 64 + g8) * 512 + (lane & 31) * 16;
    if (wave == 0) pfb = lane < 32 ? WS_XS + (rp * 2 + (lane >> 4)) * 2048 + cb * 256 + (lane & 15) * 16 : lane < 40 ? WS_FD + (cb * 32 + rp) * 128 + (lane & 7) * 16 : OOB;
    u32x4 S4 = {SENT, SENT, SENT, SENT};
    STW(S4, pfc);
    asm volatile("" : "+v"(pfa), "+v"(pfb), "+v"(S4));
    __builtin_amdgcn_s_setprio(1);
    f32x4 acc[4][6];
    int ao[2][2], bo[2][2], bv[2][2];
    f32x4 m4[4], i4[4]; float cs[4]; int st_ok;
    auto st_rd = [&](int q) {
      if (q == 0) { st_ok = misc[12 + (lane & 3)]; asm volatile("" ::: "memory"); }
      else if (q < 5) cs[(q - 1) & 3] = stm[256 + wc * 96 + 32 + 16 * (q < 3 ? q - 1 : (q - 1) ^ wr) + c16];
      else if (q < 9) m4[(q - 5) & 3] = *(const f32x4*)(stm + 64 * wr + 16 * (q - 5) + 4 * g4);
      else if (q < 13) i4[(q - 9) & 3] = *(const f32x4*)(stm + 128 + 64 * wr + 16 * (q - 9) + 4 * g4);
    };
    {
#pragma unroll
      for (int i = 0; i < 2; ++i) {
        ao[0][i] = (64 * wr + c16) * 128 + (((4 * i + g4) ^ (c16 >> 1)) * 16); ao[1][i] = ao[0][i] + 2 * K1_STAGE;
        bo[0][i] = 16384 + g4 * 3072 + wc * 256 + trl + ((i ^ (g4 & 1)) * 32); bo[1][i] = bo[0][i] + 2 * K1_STAGE;
        bv[0][i] = bo[0][i] ^ (wr * 32); bv[1][i] = bo[1][i] ^ (wr * 32);
        asm volatile("" : "+v"(ao[0][i]), "+v"(ao[1][i]), "+v"(bo[0][i]), "+v"(bo[1][i]), "+v"(bv[0][i]), "+v"(bv[1][i]));
      }
#pragma unroll
      for (int i = 0; i < 4; ++i)
#pragma unroll
        for (int j = 0; j < 6; ++j) { acc[i][j] = (f32x4){0.f, 0.f, 0.f, 0.f}; asm volatile("" : "+v"(acc[i][j])); }
      struct Frag { u32x4 a[4]; u32x2 bl[6], bh[6]; };
      Frag FA, FB;
#define K1_B(j) ((u32x4){C.bl[j][0], C.bl[j][1], C.bh[j][0], C.bh[j][1]})
#define K1_LA(i) N.a[i] = *(const u32x4*)(smem + ao[sn >> 1][hf] + (sn & 1) * K1_STAGE + (i) * 2048)
#define K1_LL(j) N.bl[j] = trread(smem + ((j) < 4 ? bo : bv)[sn >> 1][(j) & 1] + (sn & 1) * K1_STAGE + hf * 12288 + ((j) >> 1) * 1024)
#define K1_LH(j) N.bh[j] = trread(smem + ((j) < 4 ? bo : bv)[sn >> 1][(j) & 1] + (sn & 1) * K1_STAGE + hf * 12288 + ((j) >> 1) * 1024 + 512)
#define K1_M(i, j) if ((j) < 2) mfma16i(K1_B(j), C.a[i], acc[i][j]); else mfma16i(C.a[i], K1_B(j), acc[i][j]); SB0()
      auto sub = [&](Frag& C, Frag& N, int sn, int hf) {
        sfor<0, 24>([&](auto Q) {
          constexpr int q = Q.v;
          constexpr int j = q / 4, i = (j & 1) ? 3 - q % 4 : q % 4;
          if (q == 23) asm volatile("s_waitcnt lgkmcnt(0)" : "+v"(N.bh[5]));
          K1_M(i, j);
          if constexpr (q < 4) { K1_LA(q); } else if constexpr (q < 16) { if (q & 1) { K1_LH((q - 4) >> 1); } else { K1_LL((q - 4) >> 1); } }
          SB0();
        });
      };
      auto subkv = [&](Frag& C, Frag& N, int sn, int hf, bool ld) {
#define K1_QL(i, j, l) K1_M(i, j); if (ld) { l; } else { st_rd(qq); ++qq; } SB0()
        int qq = 0;
        K1_QL(0, 2, K1_LL(2)); K1_QL(1, 2, K1_LH(2)); K1_QL(0, 3, K1_LA(0)); K1_QL(1, 3, K1_LA(1)); K1_QL(2, 2, K1_LL(3)); K1_QL(3, 2, K1_LH(3));
        K1_QL(2, 3, K1_LA(2)); K1_QL(3, 3, K1_LA(3)); K1_QL(0, 4, K1_LL(4)); K1_QL(1, 4, K1_LH(4)); K1_QL(2, 4, K1_LL(5)); K1_QL(3, 4, K1_LH(5));
        K1_QL(0, 5, ); K1_M(1, 5); K1_M(2, 5); K1_M(3, 5);
      };
      RAW_BAR();
      {
        Frag& N = FA; const int sn = 0, hf = 0;
        K1_LL(0); K1_LH(0); K1_LA(0); K1_LA(1); K1_LL(1); K1_LH(1); K1_LL(2); K1_LH(2); K1_LL(3); K1_LH(3); K1_LA(2); K1_LA(3); K1_LL(4); K1_LH(4); K1_LL(5); K1_LH(5);
      }
#pragma unroll 1
      for (int it = 0; it < 4; ++it) {
#pragma unroll
        for (int s = 0; s < 4; ++s) {
          if (s == 2 && it == 3) break;
          sub(FA, FB, s, 1);
          if (s == 0) WAIT_V(0);
          if (it == 0) { if (s == 2) STW(S4, kvp_off); if (s == 3) STW(S4, kvp_off + 1024); }
          if (it == 1) { if (s == 0) STW(S4, pfa); if (s == 1) STW(S4, pfb); }
          if (s != 1 || it < 3) RAW_BAR();
          sub(FB, FA, (s + 1) & 3, 0);
        }
      }
      subkv(FA, FB, 2, 1, true);
      RAW_BAR();
      subkv(FB, FA, 3, 0, true);
      subkv(FA, FB, 3, 1, true);
      subkv(FB, FA, 0, 0, false);
    }
    asm volatile("s_nop 15" ::: "memory");
    {
      SPIN {
        if (__all(st_ok != 0)) break;
#pragma unroll
        for (int q = 0; q < 13; ++q) st_rd(q);
        SB0();
      }
      const u32x4 ONES4 = {0x3F803F80u, 0x3F803F80u, 0x3F803F80u, 0x3F803F80u};
      f32x2 tk[16], tv[16];
#define PR(v, i) ((f32x2){(v)[2 * (i)], (v)[2 * (i) + 1]})
#pragma unroll
      for (int i = 0; i < 16; ++i) {
        tk[i] = PR(acc[(i >> 1) & 3][2 + (i >> 3)], i & 1) - PR(m4[(i >> 1) & 3], i & 1) * cs[i >> 3];
        tv[i] = PR(acc[(i >> 1) & 3][4 + (i >> 3)], i & 1) - PR(m4[(i >> 1) & 3], i & 1) * cs[2 + (i >> 3)];
      }
      SB0();
#pragma unroll
      for (int i = 0; i < 16; ++i) { tk[i] *= PR(i4[(i >> 1) & 3], i & 1); tv[i] *= PR(i4[(i >> 1) & 3], i & 1); }
      SB0();
      unsigned kp[16], vp[16];
#pragma unroll
      for (int i = 0; i < 16; ++i) vp[i] = pack2o(tv[i][0], tv[i][1]);
      phi_n<16>(tk, kp);
#define F4_(p, i) ((u32x4){(p)[4 * (i)], (p)[4 * (i) + 1], (p)[4 * (i) + 2], (p)[4 * (i) + 3]})
      const f32x4 Z4 = {0.f, 0.f, 0.f, 0.f};
      const u32x4 vk[2] = {F4_(vp, 0), F4_(vp, 1)}, vg[2] = {F4_(vp, 2), F4_(vp, 3)};
      f32x4 keep[2], give[2], zq[2];
#pragma unroll
      for (int d = 0; d < 2; ++d) { give[d] = mfma16(F4_(kp, 2 * d), vg[0], Z4); zq[d] = mfma16(ONES4, F4_(kp, 2 * d), Z4); }
#pragma unroll
      for (int d = 0; d < 2; ++d) { give[d] = mfma16(F4_(kp, 2 * d + 1), vg[1], give[d]); zq[d] = mfma16(ONES4, F4_(kp, 2 * d + 1), zq[d]); }
#pragma unroll
      for (int d = 0; d < 2; ++d) keep[d] = mfma16(F4_(kp, 2 * d), vk[0], Z4);
#pragma unroll
      for (int d = 0; d < 2; ++d) keep[d] = mfma16(F4_(kp, 2 * d + 1), vk[1], keep[d]);
      SB0();
      const float zv = (lane & 16) ? zq[1][0] : zq[0][0];
      char* xk = smem + L_XK + wave * 2304 + lane * 16;
      const char* xp = smem + L_XK + (wave ^ 2) * 2304 + lane * 16;
      *(f32x4*)xk = give[0]; *(f32x4*)(xk + 1024) = give[1]; *(float*)(xk + 2048 - lane * 12) = zv;
      asm volatile("" ::: "memory");
      misc[16 + wave] = 1;
      f32x4 p0, p1; float pz;
      SPIN {
        const int ok = misc[16 + (wave ^ 2)];
        asm volatile("" ::: "memory");
        p0 = *(const f32x4*)xp; p1 = *(const f32x4*)(xp + 1024); pz = *(const float*)(xp + 2048 - lane * 12);
        SB0();
        if (ok == 1) break;
      }
      STW(sane(__builtin_bit_cast(u32x4, keep[0] + p0)), kvp_off);
      STW(sane(__builtin_bit_cast(u32x4, keep[1] + p1)), kvp_off + 1024);
      STW1(sane1(__float_as_uint(zv + pz)), (lane < 32 && wr == 0) ? zp_off + lane * 4 : OOB);
    }
    {
      u32x4 qa[4][4]; u32x2 ql[4][2], qh[4][2];
#pragma unroll
      for (int u = 0; u < 4; ++u) {
#pragma unroll
        for (int j = 0; j < 2; ++j) {
          ql[u][j] = trread(smem + bo[1][j] + (u >> 1) * K1_STAGE + (u & 1) * 12288);
          qh[u][j] = trread(smem + bo[1][j] + (u >> 1) * K1_STAGE + (u & 1) * 12288 + 512);
        }
#pragma unroll
        for (int i = 0; i < 4; ++i) qa[u][i] = *(const u32x4*)(smem + ao[1][u & 1] + (u >> 1) * K1_STAGE + i * 2048);
      }
      asm volatile("" ::: "memory");
      misc[8 + wave] = 3;
      SB0();
#pragma unroll
      for (int u = 0; u < 4; ++u)
#pragma unroll
        for (int j = 0; j < 2; ++j)
#pragma unroll
          for (int i = 0; i < 4; ++i) acc[i][j] = mfma16((u32x4){ql[u][j][0], ql[u][j][1], qh[u][j][0], qh[u][j][1]}, qa[u][i], acc[i][j]);
      SB0();
    }
    unsigned qp[16];
    Phi<16> qs;
    const unsigned po = WS_KVP + ((2 * hp + (rb >> 3)) * 16 + h) * 4096 + (2 * ((rb >> 2) & 1) + ((lane >> 4) & 1)) * 1024 + (16 * (rb & 3) + (lane & 15)) * 16;
    u32x4 pv[8] = {};
    {
      f32x4 c4[2]; float m_b[4], rinv[4];
#pragma unroll
      for (int i = 0; i < 2; ++i) c4[i] = *(const f32x4*)(stm + 256 + wc * 96 + 16 * i + 4 * g4);
#pragma unroll
      for (int i = 0; i < 4; ++i) { m_b[i] = stm[64 * wr + 16 * i + c16]; rinv[i] = stm[128 + 64 * wr + 16 * i + c16]; }
      SB0();
      f32x2 tq[16];
#pragma unroll
      for (int i = 0; i < 16; ++i) tq[i] = PR(acc[i >> 2][(i >> 1) & 1], i & 1) - PR(c4[(i >> 1) & 1], i & 1) * m_b[i >> 2];
      SB0();
#pragma unroll
      for (int i = 0; i < 16; ++i) tq[i] *= rinv[i >> 2];
      SB0();
      phi_a<16>(tq, qs);
#pragma unroll
      for (int i = 0; i < 16; ++i) asm volatile("" : "+v"(qs.tb[i]), "+v"(qs.tr[i]), "+v"(qs.e[i]));
    }
    if (wave == 0) {
      SLEEP(P_KVP);
#pragma unroll
      for (int p = 0; p < 8; ++p) pv[p] = LDW(po + p * 8192);
    }
    asm volatile("" ::: "memory");
    if (wave == 1 && rb < 2) {
      SLEEP(P_KVP);
      SPIN {
        unsigned m = 0;
#pragma unroll
        for (int p = 0; p < 2; ++p) { pv[p] = LDW(WS_ZP + (2 * hp + rb) * 2048 + p * 1024 + lane * 16); m = chk(max(max(m, pv[p][0]), pv[p][2]), pv[p]); }
        if (wave_ok(m)) break;
        asm volatile("" ::: "memory");
      }
      f32x4 s = __builtin_bit_cast(f32x4, pv[0]) + __builtin_bit_cast(f32x4, pv[1]);
#pragma unroll
      for (int i = 0; i < 4; ++i) { s[i] += dppf<DPP_ROR8>(s[i]); s[i] = xh_sum(x16_sum(s[i])); }
      if (lane < 16) st64(ws, WS_ZF + (2 * hp + rb) * 128 + (lane >> 3) * 64 + (lane & 3) * 16 + ((lane >> 2) & 1) * 8,
                          ((u64)sane1(pack2(s[2], s[3])) << 32) | pack2(s[0], s[1]));
    }
    if (wave == 0) {
      SPIN {
        unsigned m = 0;
#pragma unroll
        for (int p = 0; p < 8; ++p) m = chk(m, pv[p]);
        if (wave_ok(m)) break;
        asm volatile("" ::: "memory");
#pragma unroll
        for (int p = 0; p < 8; ++p) pv[p] = LDW(po + p * 8192);
      }
      f32x4 s = __builtin_bit_cast(f32x4, pv[0]);
#pragma unroll
      for (int p = 1; p < 8; ++p) s += __builtin_bit_cast(f32x4, pv[p]);
      const unsigned p0 = pack2(xh_sum(s[0]), xh_sum(s[1])), p1 = pack2(xh_sum(s[2]), xh_sum(s[3]));
      auto a = __builtin_amdgcn_permlane16_swap(p0, p0, false, false);
      auto b = __builtin_amdgcn_permlane16_swap(p1, p1, false, false);
      STW(sane((u32x4){a[0], b[0], a[1], b[1]}), lane < 16 ? WS_KVF + hp * 4096 + rb * 256 + lane * 16 : OOB);
    }
    phi_b<16>(qs, qp);
#pragma unroll
    for (int i = 0; i < 16; ++i) asm volatile("" : "+v"(qp[i]));
    if (wave == 0) misc[7] = 1;
    else SPIN { if (misc[7] == 1) break; __builtin_amdgcn_s_sleep(1); }
    SLEEP(P_KVF);
    {
      u32x4 k0, k1, z0;
      SPIN {
        k0 = LDW(WS_KVF + head * 2048 + lane * 16); k1 = LDW(WS_KVF + head * 2048 + 1024 + lane * 16);
        z0 = LDW(WS_ZF + head * 128 + g4 * 16);
        if (wave_ok(chk(chk(chk(0u, k0), k1), z0))) break;
        asm volatile("" ::: "memory");
      }
      const f32x4 Z4 = {0.f, 0.f, 0.f, 0.f};
      f32x4 X[8], Dn[4];
#pragma unroll
      for (int i = 0; i < 4; ++i) Dn[i] = mfma16(z0, F4_(qp, i), Z4);
#pragma unroll
      for (int i = 0; i < 8; ++i) X[i] = mfma16((i & 1) ? k1 : k0, F4_(qp, i >> 1), Z4);
      SB0();
      float ri[4]; unsigned a[16]; f32x2 f[16];
#pragma unroll
      for (int i = 0; i < 4; ++i) ri[i] = __builtin_amdgcn_rcpf(rbf(rbf(Dn[i][0]) + 1e-6f));
      SB0();
#pragma unroll
      for (int i = 0; i < 16; ++i) a[i] = pack2o(X[i >> 1][2 * (i & 1)], X[i >> 1][2 * (i & 1) + 1]);
      SB0();
#pragma unroll
      for (int i = 0; i < 16; ++i) f[i] = unpk(a[i]);
      SB0();
#pragma unroll
      for (int i = 0; i < 16; ++i) f[i] *= ri[i >> 2];
      SB0();
#pragma unroll
      for (int i = 0; i < 4; ++i) {
        STW(((u32x4){pack2(f[4 * i][0], f[4 * i][1]), pack2(f[4 * i + 1][0], f[4 * i + 1][1]), pack2(f[4 * i + 2][0], f[4 * i + 2][1]), pack2(f[4 * i + 3][0], f[4 * i + 3][1])}),
            o_off + (i >> 1) * 65536 + (i & 1) * 1024);
        SB0();
      }
    }
    {
      EPI();
      const int rt = wave & 1, kh = wave >> 1;
      const unsigned ob = WS_O + ((rp * 2 + rt) * 32 + 16 * kh) * 2048 + lane * 16;
#define LDO(t, st) __builtin_amdgcn_raw_buffer_load_b128(wsr, ob + (st) * 1024, whead(t) * 2048, 16)
      int woff[2];
#pragma unroll
      for (int i = 0; i < 2; ++i) woff[i] = kh * 8192 + (g4 >> 1) * 2048 + (g4 & 1) * 512 + trl + ((i ^ (g4 & 1)) * 32);
      struct Frag2 { u32x2 lo[8], hi[8]; };
      Frag2 G[2];
      auto ld2 = [&](Frag2& N, int sn, int nt, int hi) {
        const char* p = smem + sn + woff[nt & 1] + (nt >> 2) * 1024 + ((nt >> 1) & 1) * 256;
        if (hi) N.hi[nt] = trread(p + 4096); else N.lo[nt] = trread(p);
      };
      WAIT_V(0);
      STW(((u32x4){1u, 1u, 1u, 1u}), fb_off);
      RAW_BAR();
#pragma unroll
      for (int i = 0; i < 16; ++i) ld2(G[0], wo_slot(0), i >> 1, i & 1);
      f32x4 a2[2][8];
#pragma unroll
      for (int i = 0; i < 16; ++i) a2[i & 1][i >> 1] = (f32x4){0.f, 0.f, 0.f, 0.f};
      SLEEP(P_FB);
      SPIN { if (wave_ok(ld32(ws, WS_FB + (rp * 32 + 16 * kh + (lane & 15)) * 128))) break; }
      asm volatile("" ::: "memory");
      char* pr[2];
#pragma unroll
      for (int i = 0; i < 2; ++i) pr[i] = smem + L_P + kh * L_PK + (32 * rt + c16) * 512 + (((4 * i + g4) ^ (c16 & 7)) * 16);
      u32x4 of[16][2];
#pragma unroll
      for (int t = 0; t < OW; ++t) { of[t][0] = LDO(t, 0); of[t][1] = LDO(t, 1); }
      SB0();
      sfor<0, 16>([&](auto T) {
        constexpr int t = T.v;
        Frag2& C = G[t & 1]; Frag2& N = G[(t + 1) & 1];
#pragma unroll
        for (int i = 0; i < 16; ++i) {
          const int nt = i >> 1, st = i & 1;
          a2[st][nt] = mfma16((u32x4){C.lo[nt][0], C.lo[nt][1], C.hi[nt][0], C.hi[nt][1]}, of[t][st], a2[st][nt]);
          SB0();
          if ((t & 1) && t <= 11 && i == 8) {
            asm volatile("s_waitcnt lgkmcnt(8)" ::: "memory"); __builtin_amdgcn_s_barrier(); asm volatile("" ::: "memory");
          }
          if (t < 15) ld2(N, wo_slot(t + 1), nt, st);
          if (t > 0 && t - 1 + OW < 16 && (i == 3 || i == 11)) of[(t - 1 + OW) & 15][i >> 3] = LDO(t - 1 + OW, i >> 3);
          if (t - 1 + OW == 16 && (i == 3 || i == 11)) { xv[i >> 3] = *(const u32x4*)(x + ego + (i >> 3) * 64); wv[i >> 3] = *(const u32x4*)(w_r + cb * 128 + eseg * 8 + (i >> 3) * 64); }
          if (t == 15 && i >= 4) *(f32x4*)(pr[(nt - 2) & 1] + ((nt - 2) >> 1) * 128 + st * 8192) = a2[st][(nt - 2) & 7];
          SB0();
        }
      });
#pragma unroll
      for (int i = 12; i < 16; ++i) *(f32x4*)(pr[(i >> 1) & 1] + (i >> 2) * 128 + (i & 1) * 8192) = a2[i & 1][i >> 1];
    }
    unpack_xw();
    __builtin_amdgcn_s_setprio(0);
  } else {
    LANES();
    const int sw = wave - 4, t2 = tid - 256;
    __builtin_amdgcn_s_setprio(3);
    const bool real = hs_ != 7;
    unsigned hs_g0 = 0, hs_cur = 0;
    u32x4 ms_exp = {0, 0, 0, 0};
    bool hs_act = true, ms_act = !real && hp < 8 && rb == 0 && sw == 1;
    const unsigned sy_off = (bid & 63) * 128 + (bid >> 6) * 4;
    const unsigned hs_off = (lane == 1 ? SY_GO : SY_CH) + sy_off;
    const unsigned re_off = t2 == 0 ? SY_RE + sy_off : OOB;
    const unsigned ms_off = lane * 128;
    const char* wsu = uni(ws);
#define HS_PROBE() asm volatile("s_nop 4\n\tglobal_load_dword v156, %0, %1 sc1" :: "v"(hs_off), "s"(wsu) : "memory", RSV)
#define MS_PROBE() asm volatile("s_nop 4\n\tglobal_load_dwordx4 v[152:155], %0, %1 sc1" :: "v"(SY_RE + ms_off), "s"(wsu) : "memory", RSV)
    auto hs_step = [&](bool first) {
      if (hs_act) {
        const unsigned hv_ = rget<156>();
        hs_act = __builtin_amdgcn_readlane(hv_, 1) == hs_g0;
        const unsigned c = __builtin_amdgcn_readlane(hv_, 0);
        if (sw == 0 && (first || c != hs_cur)) STW1(hv_, re_off);
        hs_cur = c;
        HS_PROBE();
      }
      if (ms_act) {
        const unsigned d = ((rget<152>() ^ ms_exp[0]) | (rget<153>() ^ ms_exp[1])) | ((rget<154>() ^ ms_exp[2]) | (rget<155>() ^ ms_exp[3]));
        ms_act = __any(d != 0u);
        if (ms_act) MS_PROBE(); else STW(ms_exp, SY_GO + ms_off);
      }
    };
    HS_PROBE();
    if (ms_act) {
      MS_PROBE();
      asm volatile("s_nop 4\n\tglobal_load_dwordx4 v[148:151], %0, %1 sc1" :: "v"(SY_GO + ms_off), "s"(wsu) : "memory", RSV);
    }
    auto slowpf = [&]() {
      const int hq = (hp & ~3) + ((hp + 7 - hs_) & 3);
      if ((sw == 1 || sw == 2) && ((hq + 4 + (int)(((size_t)wqkv >> 7) & 7)) & 7) == 7)
        asm volatile("s_nop 4\n\tglobal_load_dword v157, %0, %1" :: "v"(min(lane, 47) * 2048), "s"(uni((const char*)wqkv + hq * 128 + (size_t)(((rb & 7) * 4 + (hp & 3)) * 96 + (sw - 1) * 48) * 2048)) : "memory", RSV);
    };
    slowpf();
    const int gr = (g8 >> 3) * 7 + min(hs_, 6);
    const unsigned voG = (unsigned)((lane >> 3) * 8 * F_ + (lane & 7) * 8) * 2u, voD = (unsigned)((lane >> 3) * 8 * D_ + (lane & 7) * 8) * 2u;
    const size_t gofs = ((size_t)(kq * 256 + sw * 64) * F_ + gr * 64) * 2;
    const char* pG = (const char*)wg + gofs;
    const char* pU = (const char*)wu + gofs;
    const char* pD = (const char*)(wd + (size_t)(gr * 64) * D_ + kq * 256 + sw * 64);
    auto wstep = [&](auto I) {
      constexpr int i = I.v;
      if (i < 8) wload<VW + 4 * i>(voG, pG + (i & 7) * (F_ * 2));
      else if (i < 16) wload<VW + 4 * i>(voG, pU + (i & 7) * (F_ * 2));
      else wload2<VW + 4 * i>(voD, pD + (i & 7) * (D_ * 2));
    };
    unsigned voW;
    {
      const int bi = lane >> 4, q = (lane >> 2) & 3, ch = lane & 3;
      voW = (unsigned)((4 * (bi >> 1) + q) * D_ + (bi & 1) * 32 + (ch ^ (bi & 2)) * 8) * 2u;
    }
    const char* baseW = (const char*)(wo + (size_t)((16 * (sw >> 1)) * 32 + 16 * (sw & 1)) * D_ + cb * 128) - 1024;
    auto stageW = [&](auto J, bool rsv) {
      const char* s_ = baseW + (size_t)whead(J.v) * (32 * D_ * 2);
      if (rsv) glds4r<wo_slot(J.v)>(voW + 1024, voW + 128, voW + 8 * D_ * 2 - 1024, voW + 8 * D_ * 2 + 128 - 2048, s_, l4);
      else glds4<wo_slot(J.v)>(voW + 1024, voW + 128, voW + 8 * D_ * 2 - 1024, voW + 8 * D_ * 2 + 128 - 2048, s_, l4);
    };
    auto code_adr = [&]() { return (const char*)(pc0 - (pc0 & 127)) + (idx + (lane < CODE_LINES / 32 ? lane : 0) * 32) * 128; };
    int sta[2], stb[3];
    {
#pragma unroll
      for (int c = 0; c < 2; ++c) { sta[c] = (posA & 3) * K1_STAGE + (32 * sw + (lane & 15)) * 128 + (((4 * c + (lane >> 4)) ^ ((lane & 15) >> 1)) * 16); asm volatile("" : "+v"(sta[c]) :: RSV); }
#pragma unroll
      for (int i = 0; i < 3; ++i) {
        const int G = sw * 3 + i, hd = G >= 6 ? 1 : 0, mat = (G - 6 * hd) >> 1;
        stb[i] = (posB & 3) * K1_STAGE + 16384 + (lane >> 4) * 6144 + mat * 1024 + hd * 256 + ((lane & 15) >> 2) * 64 + (G & 1) * 32 + (lane & 3) * 8;
        asm volatile("" : "+v"(stb[i]) :: RSV);
      }
    }
    unsigned rs_dst = (lane >> 4) == ((lane & 15) >> 2) ? rs_slot + (32 * sw + (lane & 15)) * 8 : OOB, cs_dst = lane < 48 ? cs_slot + (sw * 48 + lane) * 4 : OOB;
    unsigned rs_off = WS_RS + rb * 16384 + (lane >> 4) * 1024 + sw * 256 + (lane & 15) * 16;
    unsigned cs_off = WS_CS + hp * 12288 + (lane >> 4) * 768 + sw * 192 + min(lane & 15, 11) * 16;
    asm volatile("" : "+v"(rs_dst), "+v"(cs_dst), "+v"(rs_off), "+v"(cs_off) :: RSV);
    int stm_dst = ((lane >> 5) * 128 + sw * 32 + (lane & 15) * 2 + ((lane >> 4) & 1)) * 4, cs_put = (lane & 15) < 12 ? (256 + sw * 48 + (lane & 15) * 4 + (lane >> 4)) * 4 : 1792 + lane * 4;
    asm volatile("" : "+v"(stm_dst), "+v"(cs_put) :: RSV);
    auto st_load = [&]() { sfor<0, 4>([&](auto I) { sload<224 + 4 * I.v>(rs_off, wsu + I.v * 4096); sload<240 + 4 * I.v>(cs_off, wsu + I.v * 3072); }); };
    sfor<3, GS[0]>(group);
    if (ms_act) {
      WAIT_VR(gops(GS[0]) - 10);
      const u32x4 re = {rget<152>(), rget<153>(), rget<154>(), rget<155>()}, g0 = {rget<148>(), rget<149>(), rget<150>(), rget<151>()};
#pragma unroll
      for (int j = 0; j < 4; ++j) { const unsigned c = re[j] + 1u; ms_exp[j] = (c == g0[j]) ? c + 1u : c; }
      STW(ms_exp, SY_CH + ms_off);
    }
    WAIT_VR(gops(GS[0]) - 10 + 1); RAW_BARR();
    sfor<GS[0], GS[1]>(group);
    WAIT_VR(gops(GS[1]) - 20); RAW_BARR();
    hs_g0 = __builtin_amdgcn_readlane(rget<156>(), 1);
    u32x4 ra[4] = {}; u32x2 rc[3][4] = {};
    auto chunk = [&](auto C) {
      const u32x4 ONES4 = {0x3F803F80u, 0x3F803F80u, 0x3F803F80u, 0x3F803F80u};
      const f32x4 Z4 = {0.f, 0.f, 0.f, 0.f};
      if constexpr (C.v == 0) {
        asm volatile("" : "+v"(ra[0]), "+v"(ra[1]), "+v"(ra[2]), "+v"(ra[3]) :: RSV);
        f32x4 s[2], q[2];
#pragma unroll
        for (int i = 0; i < 2; ++i) { q[i] = mfma16(ra[2 * i], ra[2 * i], Z4); s[i] = mfma16(ONES4, ra[2 * i], Z4); }
#pragma unroll
        for (int i = 0; i < 2; ++i) { q[i] = mfma16(ra[2 * i + 1], ra[2 * i + 1], q[i]); s[i] = mfma16(ONES4, ra[2 * i + 1], s[i]); }
#pragma unroll
        for (int i = 0; i < 2; ++i) {
          float a = q[i][0], c = q[i][1], d = q[i][2], e = q[i][3];
          asm volatile("" : "+v"(a), "+v"(c), "+v"(d), "+v"(e) :: RSV);
          const float lo = (lane & 1) ? c : a, hi = (lane & 1) ? e : d;
          __builtin_amdgcn_raw_buffer_store_b64((u32x2){sane1(__float_as_uint(s[i][0])), sane1(__float_as_uint((lane & 2) ? hi : lo))}, wsr, rs_dst + i * 128, 0, 16);
        }
      } else {
        f32x4 c[3];
#pragma unroll
        for (int i = 0; i < 3; ++i) { asm volatile("" : "+v"(rc[i][0]), "+v"(rc[i][1]), "+v"(rc[i][2]), "+v"(rc[i][3]) :: RSV); c[i] = mfma16(ONES4, (u32x4){rc[i][0][0], rc[i][0][1], rc[i][1][0], rc[i][1][1]}, Z4); }
#pragma unroll
        for (int i = 0; i < 3; ++i) c[i] = mfma16(ONES4, (u32x4){rc[i][2][0], rc[i][2][1], rc[i][3][0], rc[i][3][1]}, c[i]);
        float c0 = c[0][0], c1 = c[1][0], c2 = c[2][0];
        asm volatile("" : "+v"(c0), "+v"(c1), "+v"(c2) :: RSV);
        STW1(sane1(__float_as_uint(lane < 16 ? c0 : lane < 32 ? c1 : c2)), cs_dst);
      }
    };
    sfor<0, 12>([&](auto T) {
      constexpr int t = T.v;
      if (t < 4 && posA == t + 1) {
#pragma unroll
        for (int c = 0; c < 4; ++c) ra[c] = *(const u32x4*)(smem + sta[c & 1] + (c >> 1) * 2048);
      }
      if (t < 4 && posB == t + 1) {
#pragma unroll
        for (int i = 0; i < 3; ++i)
#pragma unroll
          for (int j = 0; j < 4; ++j) rc[i][j] = trread(smem + (stb[i] ^ ((j >> 1) * 32)) + (j >> 1) * 3072 + (j & 1) * 512);
      }
      if (t == HS0 - 1) HS_PROBE();
      if (t >= HS0) hs_step(t == HS0);
      sfor<GS[t + 1], GS[t + 2]>([&](auto G) {
        group(G);
        if constexpr ((t == 4 || t == 5) && G.v == GS[t + 1]) chunk(IC<t - 4>{});
      });
      if (t == CP2 && sw == 3) asm volatile("global_load_dword v157, %0, off" :: "v"(code_adr()) : "memory", RSV);
      if (real) sfor<WS_[t], WS_[t + 1]>(wstep);
      constexpr int need = t < 11 ? t + 3 : 15, nw = wyoung(t, need);
      if (nw && real) WAIT_VR(gops(GS[t + 2]) - 10 * need + nw); else WAIT_VR(gops(GS[t + 2]) - 10 * need);
      RAW_BARR();
    });
    hs_step(false);
    auto st_put = [&]() -> bool {
      float a[8]; unsigned m = 0, n = 0;
      rsum4<224>(a, m, n); rsum4<240>(a + 4, m, n);
      const float s1 = sw16(a[0], a[2]), q1 = sw16(a[1], a[3]);
      const float cs = sw32(sw16(a[4], a[5]), sw16(a[6], a[7]));
      const float mean = sw32(s1, s1) * (1.0f / 1024.0f);
      const float var = fmaxf(__builtin_fmaf(sw32(q1, q1), 1.0f / 1024.0f, -(mean * mean)), 0.f);
      const float rinv = __builtin_amdgcn_rcpf(rbf(__builtin_amdgcn_sqrtf(rbf(rbf(var) + 1e-5f))));
      *(float*)(smem + L_STM + stm_dst) = (lane & 32) ? rinv : rbf(mean);
      *(float*)(smem + L_STM + cs_put) = cs;
      return wave_ok(max(m, n));
    };
    st_load();
    if (real) { sfor<WS_[12], WS_[12] + STE>(wstep); WAIT_VR(STE); } else WAIT_VR(0);
    const bool st_fin = st_put() && !hs_act;
    if (lane == 0) { misc[8 + sw] = 0; misc[12 + sw] = st_fin; misc[16 + sw] = 0; misc[7] = 0; }
    if (real) sfor<WS_[12] + STE, W1A>(wstep);
    RAW_BARR();
    if (!st_fin) {
      if (hs_act) hs_act = __builtin_amdgcn_readlane(rget<156>(), 1) == hs_g0;
      if (ms_act) {
        SPIN {
          bool ok = true;
#pragma unroll
          for (int j = 0; j < 4; ++j) ok &= (ld32(ws, SY_RE + ms_off + j * 4) == ms_exp[j]);
          if (__all(ok)) break;
        }
        STW(ms_exp, SY_GO + ms_off);
      }
      if (hs_act) {
        SPIN {
          if (ld32(ws, SY_GO + sy_off) != hs_g0) break;
          const unsigned c = ld32(ws, SY_CH + sy_off);
          if (c != hs_cur) { hs_cur = c; STW1(c, re_off); }
        }
      }
      SPIN { st_load(); WAIT_VR(0); if (st_put()) break; }
    }
    asm volatile("" ::: "memory", RSV);
    if (lane == 0) misc[12 + sw] = 1;
    __builtin_amdgcn_s_setprio(0);
    if (real) sfor<W1A, 16>(wstep);
    auto mwait = [&](int v) { SPIN { const int a = misc[8 + (lane & 3)]; if (__all(a >= v)) break; __builtin_amdgcn_s_sleep(1); } asm volatile("" ::: "memory"); };
    sfor<0, 9>([&](auto J) {
      if (J.v == 4) mwait(3);
      stageW(J, J.v < 2);
      if (J.v >= 1) WAIT_V(4 * PACE);
      if (J.v == 1) { WAIT_VR(8); sfor<0, 16>(wtr); }
    });
    EPI();
#pragma unroll
    for (int i = 0; i < 2; ++i) { xv[i] = *(const u32x4*)(x + ego + i * 64); wv[i] = *(const u32x4*)(w_r + cb * 128 + eseg * 8 + i * 64); }
    asm volatile("s_waitcnt vmcnt(0)" : "+v"(xv[0]), "+v"(xv[1]), "+v"(wv[0]), "+v"(wv[1]) :: "memory");
    unpack_xw();
    RAW_BAR();
    RAW_BAR(); stageW(IC<9>{}, false); stageW(IC<10>{}, false);
    RAW_BAR(); stageW(IC<11>{}, false); stageW(IC<12>{}, false);
    RAW_BAR(); stageW(IC<13>{}, false); stageW(IC<14>{}, false);
    WAIT_V(16); RAW_BAR(); stageW(IC<15>{}, false);
    WAIT_V(12); RAW_BAR();
    WAIT_V(0); RAW_BAR();
    if (real) sfor<16, 24>(wstep);
  }
  LANES(); EPI();
  const int grow = rp * 64 + erow;
  int pa0 = L_P + erow * 512 + ((2 * eseg) ^ (erow & 7)) * 16, pa1 = pa0 ^ 16;
  unsigned st_dst = eseg == 0 ? WS_ST2 + ((rp * 8 + cb) * 64 + erow) * 16 : OOB, st_src = WS_ST2 + rp * 8192 + lane * 16;
  unsigned fd_dst = lane < 8 ? WS_FD + (cb * 32 + rp) * 128 + lane * 16 : OOB, kidx = 0xFFFFu - grow;
  asm volatile("" : "+v"(pa0), "+v"(pa1), "+v"(st_dst), "+v"(st_src), "+v"(fd_dst), "+v"(kidx));
  RAW_BAR2();
  unsigned hb[8];
  f32x2 hv[8];
  {
    f32x4 pp[2][4];
#pragma unroll
    for (int k = 0; k < 2; ++k)
#pragma unroll
      for (int i = 0; i < 4; ++i) pp[k][i] = *(const f32x4*)(smem + ((i & 1) ? pa1 : pa0) + (i >> 1) * 256 + k * L_PK);
    SB0();
    f32x2 s = {0.f, 0.f}, q = {0.f, 0.f}, p2 = {0.f, 0.f};
#pragma unroll
    for (int i = 0; i < 8; ++i) {
      const f32x4 a = pp[0][i >> 1] + pp[1][i >> 1];
      const f32x2 u = xf[i] + unpk(pack2o(a[2 * (i & 1)], a[2 * (i & 1) + 1]));
      hb[i] = pack2o(u[0], u[1]);
      hv[i] = unpk(hb[i]);
      s += hv[i]; q += hv[i] * hv[i]; p2 += hv[i] * wf2[i];
    }
    STW(sane((u32x4){__float_as_uint(oct_sum(s[0] + s[1])), __float_as_uint(oct_sum(q[0] + q[1])), __float_as_uint(oct_sum(p2[0] + p2[1])), __float_as_uint(sw_)}), st_dst);
  }
  KEEP2();
  const int sw = wave - 4;
  const unsigned fd_src = WS_FD + xcd * 4096 + r * 128;
  u32x2 kpk = {SENT, SENT};
  if (wave == 0) {
    u32x4 pa[8];
    SLEEP(P_C);
    SPIN {
      unsigned m = 0;
#pragma unroll
      for (int j = 0; j < 8; ++j) { pa[j] = LDW(st_src + j * 1024); m = chk(m, pa[j]); }
      if (wave_ok(m)) break;
      KEEP2();
    }
#define F4(v) __builtin_bit_cast(f32x4, v)
    const f32x4 t = ((F4(pa[0]) + F4(pa[1])) + (F4(pa[2]) + F4(pa[3]))) + ((F4(pa[4]) + F4(pa[5])) + (F4(pa[6]) + F4(pa[7])));
    const float mean = t[0] * (1.0f / 1024.0f);
    const float var = fmaxf(__builtin_fmaf(t[1], 1.0f / 1024.0f, -(mean * mean)), 0.f);
    const float mb = rbf(mean), rs = __builtin_amdgcn_rcpf(rbf(__builtin_amdgcn_sqrtf(rbf(rbf(var) + 1e-5f))));
    *(f32x2*)(smem + L_KX + lane * 8) = (f32x2){mb, rs};
    const float sc = fmaxf((t[2] - mb * t[3]) * rs, -INFINITY);
    const int u = (int)(pack2(0.f, sc) & 0xffff0000u);
    unsigned b0 = ((unsigned)(u ^ ((u >> 31) | (int)0x80000000)) & 0xffff0000u) | (0xFFFFu - (rp * 64 + lane)), b1 = 0;
    top2_lo(b0, b1); top2_hi(b0, b1);
    STW(((u32x4){b0, b1, b0, b1}), fd_dst);
    if (lane == 0) *(u32x2*)(smem + L_MISC + 112) = (u32x2){b0, b1};
  } else if (wave >= 4) {
    asm volatile("s_waitcnt vmcnt(0)" ::: "memory", RSV2);
    sfor<16, 24>(wtr);
  }
  RAW_BAR();
  bool cand;
  u32x4 ob[2];
  {
    const f32x2 mr = *(const f32x2*)(smem + L_KX + erow * 8);
    const u32x2 kk = *(const u32x2*)(smem + L_MISC + 112);
    const float mb = mr[0], rs = mr[1];
    const unsigned b0 = kk[0], b1 = kk[1];
    u32x4 nq[2];
#pragma unroll
    for (int i = 0; i < 8; ++i) {
      const f32x2 d = hv[i] - mb;
      const f32x2 n = unpk(pack2o(d[0], d[1])) * rs;
      nq[i >> 2][i & 3] = pack2o(n[0], n[1]);
    }
    const bool c2 = (b1 & 0xffffu) == kidx;
    cand = c2 || (b0 & 0xffffu) == kidx;
    if (cand) {
      const unsigned xo = WS_XS + (rp * 2 + (c2 ? 1 : 0)) * 2048 + cb * 256 + eseg * 16;
      STW(sane(nq[0]), xo); STW(sane(nq[1]), xo + 128);
      char* cp = smem + L_CAND + (c2 ? 512 : 0) + eseg * 16;
      *(u32x4*)cp = (u32x4){hb[0], hb[1], hb[2], hb[3]}; *(u32x4*)(cp + 128) = (u32x4){hb[4], hb[5], hb[6], hb[7]};
      *(u32x4*)(cp + 256) = nq[0]; *(u32x4*)(cp + 384) = nq[1];
    }
    asm volatile("" ::: "memory");
    if (wave >= 4) { SLEEP(P_D); kpk = __builtin_amdgcn_raw_buffer_load_b64(wsr, fd_src, 0, 16); }
    asm volatile("" ::: "memory");
#pragma unroll
    for (int i = 0; i < 8; ++i) { const f32x2 o = hv[i] + unpk(nq[i >> 2][i & 3]); ob[i >> 2][i & 3] = pack2(o[0], o[1]); }
  }
#define OUT_ST() do { *(u32x4*)(out + ego) = ob[0]; *(u32x4*)(out + ego + 64) = ob[1]; } while (0)
  if (wave < 4) {
    if (!cand) OUT_ST();
    RAW_BAR();
    const unsigned w0 = misc[24], w1 = misc[25];
    if (cand && grow != (int)key_idx(w0) && grow != (int)key_idx(w1)) OUT_ST();
#pragma unroll 1
    for (int tk = 0; tk < 2; ++tk) {
      const unsigned wk = tk ? w1 : w0, slot = misc[26 + tk];
      if ((int)(slot >> 1) != rp) continue;
      u32x4 pv[7];
      const unsigned yo = WS_YP + ((tk * 8 + cb) * 64 + (lane >> 3) * 8) * 512 + wave * 128 + (lane & 7) * 16;
      const int col = (wave >> 1) * 64 + (lane & 7) * 8 + (wave & 1) * 4 + (lane >> 4);
      float sg = rbf(__builtin_amdgcn_rcpf(1.0f + __expf(-key_val(wk))));
      float hh = bflo(*(const u16*)(smem + L_CAND + (slot & 1) * 512 + col * 2)), nn = bflo(*(const u16*)(smem + L_CAND + (slot & 1) * 512 + 256 + col * 2));
      u16* op = out + (size_t)key_idx(wk) * D_ + cb * 128 + col;
      asm volatile("" : "+v"(sg), "+v"(hh), "+v"(nn), "+v"(op));
      if (tk == 0 || (int)(misc[26] >> 1) != rp) {
        SPIN { if (misc[30] == (int)w0) break; __builtin_amdgcn_s_sleep(1); }
        SLEEP(P_E);
      }
      SPIN {
        unsigned m = 0;
#pragma unroll
        for (int p = 0; p < 7; ++p) { pv[p] = LDW(yo + p * 512); m = chk(m, pv[p]); }
        if (wave_ok(m)) break;
        asm volatile("" ::: "memory");
      }
      f32x4 s = __builtin_bit_cast(f32x4, pv[0]);
#pragma unroll
      for (int p = 1; p < 7; ++p) s += __builtin_bit_cast(f32x4, pv[p]);
      float y = sw32(sw16(s[0], s[1]), sw16(s[2], s[3]));
      y += dppf<DPP_ROR8>(y);
      if (!(lane & 8)) *(GAS u16*)op = (u16)pack2(hh + rbf(nn + rbf(rbf(y) * sg)), 0.f);
    }
  } else {
    unsigned xl = WS_XS + (kq * 256 + sw * 64 + (lane >> 4) * 16 + ((lane >> 2) & 1) * 8) * 2;
    int red_dst = (lane < 8 || (lane >= 24 && lane < 32)) ? L_RED + sw * 2048 + ((lane >> 3) & 1) * 1024 + (lane & 7) * 16 : L_RED + 8192 + lane * 16;
    unsigned gu_src = WS_GU + g8 * 4096 + lane * 16;
    int act_dst = L_ACT + sw * 256 + ((lane & 7) * 8 + (lane >> 3)) * 2, act_src = L_ACT + sw * 256 + (lane & 1) * 128 + (lane >> 4) * 32 + ((lane >> 1) & 1) * 16;
    unsigned yp_dst = lane < 16 ? WS_YP + (((lane >> 3) * 8 + 2 * kq + (sw >> 1)) * 64 + g8) * 512 + (sw & 1) * 256 + (lane & 7) * 16 : OOB;
    asm volatile("" : "+v"(xl), "+v"(red_dst), "+v"(gu_src), "+v"(act_dst), "+v"(act_src), "+v"(yp_dst));
    unsigned w0, w1; int s0 = -1, s1 = -1;
    u32x4 xa = {SENT, SENT, SENT, SENT};
    SPIN {
      const bool all = wave_ok(kpk[1]);
      const unsigned k0 = kpk[1] == SENT ? 0u : kpk[0], k1 = kpk[1] == SENT ? 0u : kpk[1];
      w0 = k0; w1 = k1;
      top2_lo(w0, w1); top2_mid(w0, w1);
      const int n0 = 2 * __builtin_ctzll(__ballot(k0 == w0));
      const u64 m1 = __ballot(k0 == w1);
      const int n1 = m1 ? 2 * __builtin_ctzll(m1) : n0 + 1;
      const bool same = n0 == s0 && n1 == s1;
      s0 = n0; s1 = n1;
      asm volatile("" ::: "memory");
      if (all && same) break;
      if (!all) { kpk = __builtin_amdgcn_raw_buffer_load_b64(wsr, fd_src, 0, 16); }
      xa = LDW(xl + ((lane & 1) ? s1 : s0) * 2048);
      if (all) break;
    }
    SPIN {
      if (wave_ok(chk(0u, xa))) break;
      asm volatile("" ::: "memory");
      xa = LDW(xl + ((lane & 1) ? s1 : s0) * 2048);
    }
    {
      const u32x4 z4 = {0u, 0u, 0u, 0u};
      const u32x4 xg = (lane & 2) ? z4 : xa, xu = (lane & 2) ? xa : z4;
      f32x4 d[8];
#pragma unroll
      for (int c = 0; c < 8; ++c) d[c] = mfma16(xg, wf[c], (f32x4){0.f, 0.f, 0.f, 0.f});
      if (tid == 256) { *(u32x4*)(smem + L_MISC + 96) = (u32x4){w0, w1, (unsigned)s0, (unsigned)s1}; misc[30] = 0; }
      SB0();
#pragma unroll
      for (int c = 0; c < 10; ++c) {
        if (c < 8) d[c & 7] = mfma16(xu, wf[8 + (c & 7)], d[c & 7]);
        if (c >= 2) *(f32x4*)(smem + red_dst + (c - 2) * 128) = d[(c - 2) & 7];
        SB0();
      }
    }
    RAW_BAR();
    if (sw == 0) {
      f32x4 t[8];
#pragma unroll
      for (int p = 0; p < 8; ++p) t[p] = *(const f32x4*)(smem + L_RED + p * 1024 + lane * 16);
      SB0();
      STW(sane(__builtin_bit_cast(u32x4, ((t[0] + t[1]) + (t[2] + t[3])) + ((t[4] + t[5]) + (t[6] + t[7])))), gu_src + kq * 1024);
    } else __builtin_amdgcn_s_sleep(2);
    {
      u32x4 q[4];
      if (grow != (int)key_idx(w0) && grow != (int)key_idx(w1)) OUT_ST();
      asm volatile("" ::: "memory");
      SLEEP(P_GU);
      SPIN {
        unsigned m = 0;
#pragma unroll
        for (int j = 0; j < 4; ++j) { q[j] = LDW(gu_src + j * 1024); m = chk(m, q[j]); }
        if (wave_ok(m)) break;
        asm volatile("" ::: "memory");
      }
      const f32x4 tot = (__builtin_bit_cast(f32x4, q[0]) + __builtin_bit_cast(f32x4, q[1])) + (__builtin_bit_cast(f32x4, q[2]) + __builtin_bit_cast(f32x4, q[3]));
      const f32x2 gs = unpk(pack2o(tot[0], tot[1])), us = unpk(pack2o(tot[2], tot[3]));
      const f32x2 sg = unpk(pack2o(gs[0] * __builtin_amdgcn_rcpf(1.0f + __expf(-gs[0])), gs[1] * __builtin_amdgcn_rcpf(1.0f + __expf(-gs[1]))));
      const unsigned a2 = pack2o(sg[0] * us[0], sg[1] * us[1]);
      *(u16*)(smem + act_dst) = (u16)a2; *(u16*)(smem + act_dst + 128) = (u16)(a2 >> 16);
      __builtin_amdgcn_fence(__ATOMIC_RELEASE, "wavefront"); __builtin_amdgcn_wave_barrier(); __builtin_amdgcn_fence(__ATOMIC_ACQUIRE, "wavefront");
      const u32x4 aa = *(const u32x4*)(smem + act_src);
      f32x4 d[8];
#pragma unroll
      for (int c = 0; c < 8; ++c) d[c] = mfma16(aa, wf[16 + c], (f32x4){0.f, 0.f, 0.f, 0.f});
      SB0();
      u32x4 y[2];
#pragma unroll
      for (int c = 0; c < 8; ++c) {
        const float t0 = d[c][0] + dppf<DPP_ROR8>(d[c][2]), t1 = d[c][3] + dppf<DPP_ROR8>(d[c][1]);
        y[c >> 2][c & 3] = __float_as_uint((lane & 8) ? t1 : t0);
      }
      STW(sane(y[0]), yp_dst); STW(sane(y[1]), yp_dst + 128);
      if (tid == 256) misc[30] = w0;
    }
  }
}

extern "C" void kernel_launch(void* const* d_in, const int* in_sizes, int n_in, void* d_out, int out_size,
                              void* d_ws, size_t ws_size, hipStream_t stream) {
  (void)hipFuncSetAttribute((const void*)fused, hipFuncAttributeMaxDynamicSharedMemorySize, LDS_TOTAL);
  fused<<<256, 512, LDS_TOTAL, stream>>>((const u16*)d_in[0], (const u16*)d_in[1], (const u16*)d_in[2], (const u16*)d_in[4],
                                         (const u16*)d_in[5], (const u16*)d_in[6], (const u16*)d_in[7], (u16*)d_out, (char*)d_ws);
}
```
